```python
import torch

def _softmax(x, axis=-1):
    return torch.softmax(x, dim=axis)
def _gelu(x):
    return torch.nn.functional.gelu(x, approximate="tanh")
def _silu(x):
    return torch.nn.functional.silu(x)
def _sigmoid(x):
    return torch.sigmoid(x)
def _layernorm(x, axis=-1, eps=1e-5):
    m = x.mean(dim=axis, keepdim=True)
    v = x.var(dim=axis, keepdim=True, unbiased=False)
    return (x - m) / torch.sqrt(v + eps)
def _scatter_add(tgt, idx, src, axis=0):
    return tgt.clone().index_add_(axis, idx, src)
def _segment_sum(src, seg, n):
    out = torch.zeros((n,) + src.shape[1:], dtype=src.dtype)
    return out.index_add_(0, seg, src)
def _pad(x, pw):
    flat = tuple(v for pair in reversed(pw) for v in pair)
    return torch.nn.functional.pad(x, flat)

kernel_name = 'sequential_preLN__linear_kernel_hedgehog__glu_gated_geglu__prompt'

def setup_inputs():

    _w = (lambda a: (lambda t: t.to(torch.bfloat16) if t.is_floating_point() else t)(torch.as_tensor(a)))
    def _A_setup():
        B, S, D, H = 1, 2048, 448, 16
        Dh = D // H
        return {
            "x": _w((lambda *s: torch.randn(*s, dtype=torch.bfloat16) * 0.05)(B, S, D)),
            "wqkv": _w((lambda *s: torch.randn(*s, dtype=torch.bfloat16) * 0.2)(D, 3 * D)),
            "w_phi": _w((lambda *s: torch.randn(*s, dtype=torch.bfloat16) * 0.2)(H, Dh, Dh) * (Dh ** -0.5)),
            "wo": _w((lambda *s: torch.randn(*s, dtype=torch.bfloat16) * 0.05)(D, D)),
        }
    _A_in = _A_setup()

    def _B_setup():
        B, S, Dm, Df = 1, 2048, 448, 1792
        return {
            "x": _w((lambda *s: torch.randn(*s, dtype=torch.bfloat16) * 0.05)(B, S, Dm)),
            "w_gate": _w((lambda *s: torch.randn(*s, dtype=torch.bfloat16) * 0.4)(Dm, Df) * (Dm ** -0.5)),
            "w_up": _w((lambda *s: torch.randn(*s, dtype=torch.bfloat16) * 0.4)(Dm, Df) * (Dm ** -0.5)),
            "w_down": _w((lambda *s: torch.randn(*s, dtype=torch.bfloat16) * 0.4)(Df, Dm) * (Df ** -0.5)),
        }
    _B_in = _B_setup()

    return {
        "A_x": _A_in["x"],
        "A_wqkv": _A_in["wqkv"],
        "A_w_phi": _A_in["w_phi"],
        "A_wo": _A_in["wo"],
        "B_x": _B_in["x"],
        "B_w_gate": _B_in["w_gate"],
        "B_w_up": _B_in["w_up"],
        "B_w_down": _B_in["w_down"],
    }

def reference(A_x, A_wqkv, A_w_phi, A_wo, B_x, B_w_gate, B_w_up, B_w_down):
    def _A_ref(x, wqkv, w_phi, wo):
        B, S, D = x.shape
        H = 16; Dh = D // H
        qkv = (lambda x, *d: x.permute(*d))((x @ wqkv).reshape(B, S, 3, H, Dh), 2, 0, 3, 1, 4)
        q, k, v = qkv[0], qkv[1], qkv[2]
        pq = torch.einsum("bhsd,hdf->bhsf", q, w_phi)
        pk = torch.einsum("bhsd,hdf->bhsf", k, w_phi)
        phi_q = torch.concatenate([_softmax(pq, axis=-1), _softmax(-pq, axis=-1)], axis=-1)
        phi_k = torch.concatenate([_softmax(pk, axis=-1), _softmax(-pk, axis=-1)], axis=-1)
        kv = torch.einsum("bhsf,bhsd->bhfd", phi_k, v)
        z  = torch.einsum("bhsf->bhf", phi_k)
        num = torch.einsum("bhqf,bhfd->bhqd", phi_q, kv)
        den = torch.einsum("bhqf,bhf->bhq", phi_q, z)[..., None] + 1e-6
        o = num / den
        return (lambda x, *d: x.permute(*d))(o, 0, 2, 1, 3).reshape(B, S, D) @ wo

    def _B_ref(x, w_gate, w_up, w_down):
        g = torch.einsum("bsd,df->bsf", x, w_gate)
        u = torch.einsum("bsd,df->bsf", x, w_up)
        h = _gelu(g) * u
        return torch.einsum("bsf,fd->bsd", h, w_down)

    x = A_x
    h = x + _A_ref(_layernorm(x, axis=-1), A_wqkv, A_w_phi, A_wo)
    h2 = h + _B_ref(_layernorm(h, axis=-1), B_w_gate, B_w_up, B_w_down)
    return h2
```

```cpp
#include <hip/hip_runtime.h>
#include <hip/hip_bf16.h>
typedef __hip_bfloat16 bf16;
#define S 2048
#define D 448
#define H 16
#define DH 28
#define F 1792
__device__ __forceinline__ float b2f(bf16 x){return __bfloat162float(x);}
__device__ __forceinline__ float rb(float x){return __bfloat162float(__float2bfloat16(x));}

__global__ void k_ln(const bf16* x, float* xn){
  int r=blockIdx.x, t=threadIdx.x; float v[7]; float s=0;
  for(int i=0;i<7;i++){v[i]=b2f(x[r*D+t+64*i]); s+=v[i];}
  for(int o=32;o;o>>=1) s+=__shfl_xor(s,o);
  float m=s/D; float q=0; for(int i=0;i<7;i++){float d=v[i]-m;q+=d*d;}
  for(int o=32;o;o>>=1) q+=__shfl_xor(q,o);
  float rs=rsqrtf(q/D+1e-5f);
  for(int i=0;i<7;i++) xn[r*D+t+64*i]=rb((v[i]-m)*rs);
}
__global__ void k_gemm(const float* A,const bf16* B,float* C,int N,int K){
  __shared__ float sa[16][17], sb[16][17];
  int tx=threadIdx.x&15, ty=threadIdx.x>>4; int r=blockIdx.y*16+ty, c=blockIdx.x*16+tx; float acc=0;
  for(int k0=0;k0<K;k0+=16){ sa[ty][tx]=A[(size_t)r*K+k0+tx]; sb[ty][tx]=b2f(B[(size_t)(k0+ty)*N+c]); __syncthreads();
    for(int k=0;k<16;k++) acc+=sa[ty][k]*sb[k][tx]; __syncthreads(); }
  C[(size_t)r*N+c]=rb(acc);
}
__global__ void k_phi(const float* qkv,const bf16* wphi,float* phiq,float* phik){
  int idx=blockIdx.x*blockDim.x+threadIdx.x; if(idx>=S*H*2) return;
  int w=idx&1, h=(idx>>1)&15, s=idx>>5;
  const float* q=qkv+(size_t)s*3*D+w*D+h*DH; float* out=(w?phik:phiq)+((size_t)s*H+h)*56;
  float mx=-1e30f,mn=1e30f;
  for(int f=0;f<DH;f++){float a=0;for(int d=0;d<DH;d++)a+=q[d]*b2f(wphi[(h*DH+d)*DH+f]); a=rb(a); out[f]=a; mx=fmaxf(mx,a); mn=fminf(mn,a);}
  float s1=0,s2=0;
  for(int f=0;f<DH;f++){float a=out[f]; float e1=__expf(a-mx), e2=__expf(mn-a); out[f]=e1; out[28+f]=e2; s1+=e1;s2+=e2;}
  for(int f=0;f<DH;f++){out[f]=rb(out[f]/s1); out[28+f]=rb(out[28+f]/s2);}
}
__global__ void k_kv(const float* qkv,const float* phik,float* kv){
  int h=blockIdx.x/56, f=blockIdx.x%56; int t=threadIdx.x; int d=t&31, half=t>>5; float a=0;
  if(d<=28) for(int s=half;s<S;s+=2){ float p=phik[((size_t)s*H+h)*56+f]; a+= d<28? p*qkv[(size_t)s*3*D+2*D+h*DH+d] : p; }
  a+=__shfl_xor(a,32);
  if(half==0) kv[(h*56+f)*32+d]=rb(a);
}
__global__ void k_o(const float* phiq,const float* kv,float* o){
  int idx=blockIdx.x*blockDim.x+threadIdx.x; if(idx>=S*D) return;
  int s=idx/D, c=idx%D, h=c/DH, d=c%DH; float num=0,den=0;
  for(int f=0;f<56;f++){float p=phiq[((size_t)s*H+h)*56+f]; num+=p*kv[(h*56+f)*32+d]; den+=p*kv[(h*56+f)*32+28];}
  o[idx]=rb(rb(num)/rb(rb(den)+1e-6f));
}
__global__ void k_res1(const bf16* x,const float* a,bf16* hb){int i=blockIdx.x*blockDim.x+threadIdx.x; if(i<S*D) hb[i]=__float2bfloat16(b2f(x[i])+a[i]);}
__global__ void k_act(const float* g,const float* u,float* hh){int i=blockIdx.x*blockDim.x+threadIdx.x; if(i<S*F){float x=g[i]; float t=tanhf(0.7978845608f*(x+0.044715f*x*x*x)); hh[i]=rb(rb(0.5f*x*(1+t))*u[i]);}}
__global__ void k_res2(const bf16* h,const float* a,bf16* out){int i=blockIdx.x*blockDim.x+threadIdx.x; if(i<S*D) out[i]=__float2bfloat16(b2f(h[i])+a[i]);}

extern "C" void kernel_launch(void* const* d_in, const int* in_sizes, int n_in, void* d_out, int out_size, void* d_ws, size_t ws_size, hipStream_t stream) {
  const bf16* x=(const bf16*)d_in[0]; const bf16* wqkv=(const bf16*)d_in[1]; const bf16* wphi=(const bf16*)d_in[2]; const bf16* wo=(const bf16*)d_in[3];
  const bf16* wg=(const bf16*)d_in[5]; const bf16* wu=(const bf16*)d_in[6]; const bf16* wd=(const bf16*)d_in[7];
  float* ws=(float*)d_ws;
  float* xn=ws; float* qkv=xn+S*D; float* phiq=qkv+S*3*D; float* phik=phiq+S*H*56; float* kv=phik+S*H*56; float* o=kv+H*56*32; float* a=o+S*D; float* g=a+S*D; float* u=g+S*F; float* hh=u+S*F; bf16* hb=(bf16*)(hh+S*F);
  k_ln<<<S,64,0,stream>>>(x,xn);
  k_gemm<<<dim3(3*D/16,S/16),256,0,stream>>>(xn,wqkv,qkv,3*D,D);
  k_phi<<<S*H*2/256,256,0,stream>>>(qkv,wphi,phiq,phik);
  k_kv<<<H*56,64,0,stream>>>(qkv,phik,kv);
  k_o<<<S*D/256,256,0,stream>>>(phiq,kv,o);
  k_gemm<<<dim3(D/16,S/16),256,0,stream>>>(o,wo,a,D,D);
  k_res1<<<S*D/256,256,0,stream>>>(x,a,hb);
  k_ln<<<S,64,0,stream>>>(hb,xn);
  k_gemm<<<dim3(F/16,S/16),256,0,stream>>>(xn,wg,g,F,D);
  k_gemm<<<dim3(F/16,S/16),256,0,stream>>>(xn,wu,u,F,D);
  k_act<<<S*F/256,256,0,stream>>>(g,u,hh);
  k_gemm<<<dim3(D/16,S/16),256,0,stream>>>(hh,wd,a,D,F);
  k_res2<<<S*D/256,256,0,stream>>>(hb,a,(bf16*)d_out);
}
```

```cpp
#include <hip/hip_runtime.h>
#include <hip/hip_bf16.h>
#include <stdint.h>

typedef unsigned u32;
typedef unsigned long long u64;
typedef u32 u32x4 __attribute__((ext_vector_type(4)));
typedef u32 u32x2 __attribute__((ext_vector_type(2)));
typedef float f32x2 __attribute__((ext_vector_type(2)));
typedef float f32x4 __attribute__((ext_vector_type(4)));
typedef float f32x16 __attribute__((ext_vector_type(16)));
typedef short s16x4 __attribute__((ext_vector_type(4)));
typedef __bf16 bf16x2 __attribute__((ext_vector_type(2)));
typedef __bf16 bf16x8 __attribute__((ext_vector_type(8)));
#define LAS __attribute__((address_space(3)))
#define GAS __attribute__((address_space(1)))
#define DI __device__ __forceinline__
#define RLX_AGENT __ATOMIC_RELAXED, __HIP_MEMORY_SCOPE_AGENT

constexpr int WS_E = 0;
constexpr int WS_FREP = 4096;
constexpr int WS_FKV = 8192;
constexpr int WS_FO = WS_FKV + 32768;
constexpr int WS_FH = WS_FO + 32768;
constexpr int WS_FHH = WS_FH + 32768;
constexpr int WS_CS = WS_FHH + 131072;
constexpr int WS_ST = WS_CS + 200704;
constexpr int WS_WO = WS_ST + 229376;
constexpr int WS_WGU = WS_WO + 458752;
constexpr int WS_WD = WS_WGU + 3211264;
constexpr int WS_KVP = WS_WD + 1605632;
constexpr int WS_O = WS_KVP + 2097152;
constexpr int WS_H = WS_O + 2097152;
constexpr int WS_HH = WS_H + 1835008;
constexpr int WS_STAMP = WS_HH + 7340032;
constexpr int WS_USED = WS_STAMP + 256 * 2 * 32 * 8;
constexpr int SLOT = 28672;
constexpr int L_SCR = 3 * SLOT;
constexpr int L_WO = 3 * SLOT;
constexpr int L_EX = 5 * SLOT;
constexpr int L_KVA = 151552;
constexpr int L_CS = 159744;
constexpr int L_FLG = 160768;
constexpr int L_JUNK = 161024;
constexpr int L_STAMP = 161280;
constexpr int L_RED = 2 * SLOT;
constexpr int LDS_BYTES = 163840;
constexpr int CODE_PF = 24576;
constexpr u32 SPIN_CAP = 1u << 16;

#define STAMP(i) do {} while (0)

DI f32x4 mfma16(u32x4 a, u32x4 b, f32x4 c) { return __builtin_amdgcn_mfma_f32_16x16x32_bf16(__builtin_bit_cast(bf16x8, a), __builtin_bit_cast(bf16x8, b), c, 0, 0, 0); }
DI f32x16 mfma32(u32x4 a, u32x4 b, f32x16 c) { return __builtin_amdgcn_mfma_f32_32x32x16_bf16(__builtin_bit_cast(bf16x8, a), __builtin_bit_cast(bf16x8, b), c, 0, 0, 0); }
DI u32 pk(float lo, float hi) { f32x2 p = {lo, hi}; return __builtin_bit_cast(u32, __builtin_convertvector(p, bf16x2)); }
DI float blo(u32 v) { return __builtin_bit_cast(float, v << 16); }
DI float bhi(u32 v) { return __builtin_bit_cast(float, v & 0xffff0000u); }
DI float dot2(u32 a, u32 b, float c) { return __builtin_amdgcn_fdot2_f32_bf16(__builtin_bit_cast(bf16x2, a), __builtin_bit_cast(bf16x2, b), c, false); }
DI u32x2 trread(LAS char* p) { return __builtin_bit_cast(u32x2, __builtin_amdgcn_ds_read_tr16_b64_v4i16((LAS s16x4*)p)); }
DI u32x4 trfrag(LAS char* p, int step) { u32x2 a = trread(p), b = trread(p + step); return u32x4{a.x, a.y, b.x, b.y}; }
DI float fexp2(float x) { return __builtin_amdgcn_exp2f(x); }
DI float frcp(float x) { return __builtin_amdgcn_rcpf(x); }
template <int C> DI float dppror(float x) { return __builtin_bit_cast(float, __builtin_amdgcn_update_dpp(0, __builtin_bit_cast(int, x), 0x120 + C, 0xf, 0xf, false)); }
DI float row_max(float x) { x = fmaxf(x, dppror<8>(x)); x = fmaxf(x, dppror<4>(x)); x = fmaxf(x, dppror<2>(x)); return fmaxf(x, dppror<1>(x)); }
DI float row_min(float x) { x = fminf(x, dppror<8>(x)); x = fminf(x, dppror<4>(x)); x = fminf(x, dppror<2>(x)); return fminf(x, dppror<1>(x)); }
DI float row_sum(float x) { x += dppror<8>(x); x += dppror<4>(x); x += dppror<2>(x); return x + dppror<1>(x); }
DI f32x2 sw16(float x) { u32 u = __builtin_bit_cast(u32, x); auto r = __builtin_amdgcn_permlane16_swap(u, u, false, false); return f32x2{__builtin_bit_cast(float, (u32)r[0]), __builtin_bit_cast(float, (u32)r[1])}; }
DI f32x2 sw32(float x) { u32 u = __builtin_bit_cast(u32, x); auto r = __builtin_amdgcn_permlane32_swap(u, u, false, false); return f32x2{__builtin_bit_cast(float, (u32)r[0]), __builtin_bit_cast(float, (u32)r[1])}; }
DI float grp_sum(float x) { f32x2 a = sw16(x); x = a.x + a.y; a = sw32(x); return a.x + a.y; }
DI float grp_max(float x) { f32x2 a = sw16(x); x = fmaxf(a.x, a.y); a = sw32(x); return fmaxf(a.x, a.y); }
DI float grp_min(float x) { f32x2 a = sw16(x); x = fminf(a.x, a.y); a = sw32(x); return fminf(a.x, a.y); }

#define BAR() asm volatile("s_waitcnt lgkmcnt(0)\n\ts_barrier" ::: "memory")
#define WAITVM(n) asm volatile("s_waitcnt vmcnt(" #n ")" ::: "memory")
#define DRAIN() do { asm volatile("s_waitcnt vmcnt(0)" ::: "memory"); __builtin_amdgcn_s_waitcnt(0x0F70); } while (0)
#define SCHED() __builtin_amdgcn_sched_barrier(0)
DI void dma16(const void* base, u32 voff, u32 lds) {
  u32 keep;
  asm volatile("s_mov_b32 %0, m0\n\ts_mov_b32 m0, %3\n\ts_nop 0\n\tglobal_load_lds_dwordx4 %1, %2\n\ts_mov_b32 m0, %0" : "=&s"(keep) : "v"(voff), "s"(base), "s"(lds) : "memory");
}
DI void dma16c(const void* base, u32 voff, u32 lds) {
  u32 keep;
  asm volatile("s_mov_b32 %0, m0\n\ts_mov_b32 m0, %3\n\ts_nop 0\n\tglobal_load_lds_dwordx4 %1, %2 sc1\n\ts_mov_b32 m0, %0" : "=&s"(keep) : "v"(voff), "s"(base), "s"(lds) : "memory");
}
DI void dma4(const void* base, u32 voff, u32 lds) {
  u32 keep;
  asm volatile("s_mov_b32 %0, m0\n\ts_mov_b32 m0, %3\n\ts_nop 0\n\tglobal_load_lds_dword %1, %2\n\ts_mov_b32 m0, %0" : "=&s"(keep) : "v"(voff), "s"(base), "s"(lds) : "memory");
}
#define LD16(rs, off) __builtin_bit_cast(u32x4, __builtin_amdgcn_raw_buffer_load_b128(rs, off, 0, 16))
#define ST16(rs, v, off, soff) __builtin_amdgcn_raw_buffer_store_b128(__builtin_bit_cast(u32x4, v), rs, off, soff, 16)

DI void poll_flags(const char* ws, u32 off, u64 tag, u32 need) {
  for (u32 sp = 0; sp < SPIN_CAP; ++sp) {
    u64 d = __hip_atomic_load((const GAS u64*)(ws + off), RLX_AGENT) - tag;
    if (__all(d >= need && d <= 8)) break;
  }
  __builtin_amdgcn_fence(__ATOMIC_ACQUIRE, "wavefront");
}
DI void lds_wait(LAS char* L, int off, u32 need) {
  for (u32 sp = 0; sp < (SPIN_CAP << 4); ++sp) {
    if (__builtin_amdgcn_readfirstlane(*(volatile LAS u32*)(L + off)) >= need) break;
    __builtin_amdgcn_s_sleep(1);
  }
  asm volatile("" ::: "memory");
}
DI void lds_signal(LAS char* L, int off, int lane) {
  asm volatile("s_waitcnt lgkmcnt(0)" ::: "memory");
  if (lane == 0) __hip_atomic_fetch_add((LAS u32*)(L + off), 1u, __ATOMIC_RELAXED, __HIP_MEMORY_SCOPE_WORKGROUP);
}

__global__ void __launch_bounds__(512, 2) fused_block(const char* __restrict__ x, const char* __restrict__ wqkv, const char* __restrict__ wphi, const char* __restrict__ wo,
                                                      const char* __restrict__ wg, const char* __restrict__ wu, const char* __restrict__ wd, char* __restrict__ out, char* ws) {
  extern __shared__ __attribute__((aligned(16))) char smem[];
  LAS char* L = (LAS char*)smem;
  const int tid = threadIdx.x, lane = tid & 63, wave = __builtin_amdgcn_readfirstlane(tid >> 6);
  const int b = blockIdx.x, li = b >> 3, tile = 2 * (b & 7) + (li >> 4), r = li & 15;
  const int l15 = lane & 15, g = lane >> 4, q4 = l15 >> 2, p4 = lane & 3, hh = lane >> 5;
  const int head = r, off4 = (head & 1) * 4;
  const int tg = r >= 7 ? 1 : 0, cg = r - 7 * tg;
  asm volatile("" :: "s"(x), "s"(wqkv), "s"(wphi), "s"(wo), "s"(wg), "s"(wu), "s"(wd), "s"(out), "s"(ws));
  u64 epoch;
  asm volatile("s_load_dwordx2 %0, %1, 0x0" : "=s"(epoch) : "s"(ws) : "memory");
  STAMP(0);
  if (wave == 7) {
    u64 pc; asm volatile("s_getpc_b64 %0" : "=s"(pc));
    dma4((const void*)(pc & ~127ull), min((li & 3) * 8192 + lane * 128, CODE_PF), L_JUNK);
  }
  const int task = b + 256 * (wave - 1);
  const bool has_task = wave >= 1 && wave <= 3 && task < 644;
  int LB = 0, TS = 0, FA = 0, FB = 0, FC = 0, SA = 4096, SB = 2048, SC = 1024, SBASE = 0, csoff = -1;
  const int scr = L_SCR + (wave - 1) * 8192;
  if (has_task) {
    const char *pa, *pb, *pl; int stride;
    if (task < 392) {
      const int kp = task / 28, fl = task - 28 * kp; stride = 3584;
      pa = wg + kp * 32 * 3584 + fl * 128; pb = wu + kp * 32 * 3584 + fl * 128; pl = pb + 24 * 3584;
      LB = (g & 1) * 4096 + (8 * hh + q4) * 128 + 8 * p4; TS = 512; FA = 2048; FB = 64; FC = 32;
      SA = 1024; SB = 2 * SLOT; SC = SLOT; SBASE = WS_WGU + 4 * fl * SLOT + 2 * kp * 1024;
      csoff = WS_CS + kp * 14336 + fl * 256;
    } else if (task < 588) {
      const int t2 = task - 392, fq = t2 / 7, c7 = t2 - 7 * fq; stride = 896;
      pa = wd + fq * 64 * 896 + c7 * 128; pb = pa + 32 * 896; pl = pa + 56 * 896;
      LB = (4 * hh + q4) * 128 + 32 * (g & 1) + 8 * p4; TS = 1024; FA = 4096; FB = 2048; FC = 64;
      SBASE = WS_WD + (c7 * 112 + 4 * fq) * 2048;
    } else {
      const int t2 = task - 588, hp = t2 / 7, c7 = t2 - 7 * hp; stride = 896;
      pa = wo + hp * 56 * 896 + c7 * 128; pb = pa + 32 * 896; pl = pa + 48 * 896;
      LB = (4 * g + q4) * 128 + 16 * p4; TS = 2048; FA = 3072; FB = 64; FC = 8;
      SBASE = WS_WO + (c7 * 16 + 2 * hp) * 4096;
    }
    const u32 vo = (lane >> 3) * stride + (lane & 7) * 16;
#pragma unroll
    for (int i = 0; i < 4; ++i) dma16(pa + i * 8 * stride, vo, scr + i * 1024);
#pragma unroll
    for (int i = 0; i < 4; ++i) dma16(i < 3 ? pb + i * 8 * stride : pl, vo, scr + 4096 + i * 1024);
  }
  u32 wp[8];
  {
    const int fcl = l15 < 14 ? l15 : 13;
#pragma unroll
    for (int j = 0; j < 8; ++j) {
      int d = (j < 4 ? 4 * g + j : 12 + 4 * g + j) - off4;
      d = d < 0 ? 0 : (d > 27 ? 27 : d);
      const u32 vo = (head * 28 + d) * 56 + fcl * 4;
      asm volatile("global_load_dword %0, %1, %2" : "=v"(wp[j]) : "v"(vo), "s"(wphi) : "memory");
    }
  }
  u32x4 xf[14];
  {
    const u32 vo = (tile * 128 + wave * 16 + l15) * 896 + g * 16;
#pragma unroll
    for (int s = 0; s < 14; ++s) asm volatile("global_load_dwordx4 %0, %1, %2 offset:%c3" : "=v"(xf[s]) : "v"(vo), "s"(x), "i"(s * 64) : "memory");
  }
  const u32 wvo = (lane >> 2) * 2688 + (((lane & 3) ^ (2 * (lane >> 5))) * 16);
  const char* wbase = wqkv + head * 56 - off4 * 2;
#pragma unroll
  for (int i = 0; i < 7; ++i)
    dma16(wbase + (1 + (wave >> 2)) * 896 + (64 * i + 16 * (wave & 3)) * 2688, wvo, (wave >> 2) * SLOT + (4 * i + (wave & 3)) * 1024);
  asm volatile("s_waitcnt lgkmcnt(0)" : "+s"(epoch)::"memory");
  const u64 tag = epoch * 6364136223846793005ull + 1442695040888963407ull;
  if (tid < 16) *(LAS u32*)(L + L_FLG + tid * 4) = 0u;
  const __amdgpu_buffer_rsrc_t rs = __builtin_amdgcn_make_buffer_rsrc((void*)ws, 0, WS_USED, 0x00020000);
  STAMP(1);
#define XW_OPS "+v"(xf[0]), "+v"(xf[1]), "+v"(xf[2]), "+v"(xf[3]), "+v"(xf[4]), "+v"(xf[5]), "+v"(xf[6]), "+v"(xf[7]), "+v"(xf[8]), "+v"(xf[9]), "+v"(xf[10]), "+v"(xf[11]), "+v"(xf[12]), "+v"(xf[13]), \
               "+v"(wp[0]), "+v"(wp[1]), "+v"(wp[2]), "+v"(wp[3]), "+v"(wp[4]), "+v"(wp[5]), "+v"(wp[6]), "+v"(wp[7])
  if (has_task) {
    WAITVM(29);
    float cs4[4] = {0.f, 0.f, 0.f, 0.f};
#pragma unroll
    for (int n = 0; n < 8; ++n) {
      const u32x4 f = trfrag(L + scr + LB + (n >> 2) * FA + ((n >> 1) & 1) * FB + (n & 1) * FC, TS);
      ST16(rs, f, lane * 16, SBASE + (n >> 2) * SA + ((n >> 1) & 1) * SB + (n & 1) * SC);
      cs4[n & 3] = dot2(f.x, 0x3f803f80u, dot2(f.y, 0x3f803f80u, dot2(f.z, 0x3f803f80u, dot2(f.w, 0x3f803f80u, cs4[n & 3]))));
    }
    if (csoff >= 0) {
#pragma unroll
      for (int m = 0; m < 2; ++m) {
        auto sw = __builtin_amdgcn_permlane32_swap(__builtin_bit_cast(u32, cs4[2 * m]), __builtin_bit_cast(u32, cs4[2 * m + 1]), false, false);
        const float t = __builtin_bit_cast(float, (u32)sw[0]) + __builtin_bit_cast(float, (u32)sw[1]);
        __builtin_amdgcn_raw_buffer_store_b32(__builtin_bit_cast(u32, t), rs, (g & 1) * 7168 + (2 * m + hh) * 64 + l15 * 4, csoff, 16);
      }
    }
    WAITVM(15);
  } else {
    WAITVM(7);
  }
  asm volatile("" : XW_OPS::"memory");
  STAMP(3);
#pragma unroll
  for (int i = 0; i < 4; ++i)
    if (i < 3 || wave < 4) dma16(wbase + (128 * i + 16 * wave) * 2688, wvo, 2 * SLOT + (8 * i + wave) * 1024);
  {
    float s1 = 0.f, s2 = 0.f;
#pragma unroll
    for (int s = 0; s < 14; ++s)
#pragma unroll
      for (int e = 0; e < 4; ++e) { s1 = dot2(xf[s][e], 0x3f803f80u, s1); s2 = dot2(xf[s][e], xf[s][e], s2); }
    s1 = grp_sum(s1); s2 = grp_sum(s2);
    const float mu = s1 * (1.f / 448.f), var = fmaxf(s2 * (1.f / 448.f) - mu * mu, 0.f);
    const float rstd = __builtin_amdgcn_rsqf(var + 1e-5f), nb = -mu * rstd;
#pragma unroll
    for (int s = 0; s < 14; ++s)
#pragma unroll
      for (int e = 0; e < 4; ++e) xf[s][e] = pk(fmaf(blo(xf[s][e]), rstd, nb), fmaf(bhi(xf[s][e]), rstd, nb));
  }
  u32x4 wpf[2];
  {
#pragma unroll
    for (int j = 0; j < 8; ++j) {
      const int d = (j < 4 ? 4 * g + j : 12 + 4 * g + j) - off4;
      if (d < 0 || d > 27 || l15 >= 14) wp[j] = 0u;
    }
#pragma unroll
    for (int e = 0; e < 4; ++e) {
      wpf[0][e] = __builtin_amdgcn_perm(wp[2 * e + 1], wp[2 * e], 0x05040100u);
      wpf[1][e] = __builtin_amdgcn_perm(wp[2 * e + 1], wp[2 * e], 0x07060302u);
    }
  }
  asm volatile("" : "+v"(xf[0]), "+v"(xf[1]), "+v"(xf[2]), "+v"(xf[3]), "+v"(xf[4]), "+v"(xf[5]), "+v"(xf[6]), "+v"(xf[7]), "+v"(xf[8]), "+v"(xf[9]), "+v"(xf[10]), "+v"(xf[11]), "+v"(xf[12]), "+v"(xf[13]), "+v"(wpf[0]), "+v"(wpf[1]));
  STAMP(4);
  WAITVM(3);
  BAR();
  STAMP(5);
  if (tid == 448) __hip_atomic_store((GAS u64*)(ws + WS_FREP + b * 8), tag + 1, RLX_AGENT);
  LAS char* wr0 = L + (8 * g + q4) * 64 + 32 * (g & 1) + 8 * p4;
  LAS char* wr1 = L + (8 * g + q4) * 64 + 32 * ((g & 1) ^ 1) + 8 * p4;
  constexpr float LOG2E = 1.4426950408889634f;
  {
    f32x4 kt[2] = {}, vv[2] = {};
#pragma unroll
    for (int s = 0; s < 14; ++s) {
      kt[0] = mfma16(trfrag(wr0 + s * 2048, 256), xf[s], kt[0]);
      kt[1] = mfma16(trfrag(wr1 + s * 2048, 256), xf[s], kt[1]);
      vv[0] = mfma16(xf[s], trfrag(wr0 + SLOT + s * 2048, 256), vv[0]);
      vv[1] = mfma16(xf[s], trfrag(wr1 + SLOT + s * 2048, 256), vv[1]);
    }
    STAMP(6);
    const u32x4 ka = {pk(kt[0][0], kt[0][1]), pk(kt[0][2], kt[0][3]), pk(kt[1][0], kt[1][1]), pk(kt[1][2], kt[1][3])};
    const f32x4 z4 = {0.f, 0.f, 0.f, 0.f};
    const f32x4 p0 = mfma16(ka, wpf[0], z4), p1 = mfma16(ka, wpf[1], z4);
    const bool valid = l15 < 14;
    float ep[2][4], em[2][4];
#pragma unroll
    for (int i = 0; i < 4; ++i) {
      const float mx = row_max(valid ? fmaxf(p0[i], p1[i]) : -3.0e38f), mn = row_min(valid ? fminf(p0[i], p1[i]) : 3.0e38f);
      ep[0][i] = valid ? fexp2((p0[i] - mx) * LOG2E) : 0.f; ep[1][i] = valid ? fexp2((p1[i] - mx) * LOG2E) : 0.f;
      em[0][i] = valid ? fexp2((mn - p0[i]) * LOG2E) : 0.f; em[1][i] = valid ? fexp2((mn - p1[i]) * LOG2E) : 0.f;
      const float rp = frcp(row_sum(ep[0][i] + ep[1][i])), rm = frcp(row_sum(em[0][i] + em[1][i]));
      ep[0][i] *= rp; ep[1][i] *= rp; em[0][i] *= rm; em[1][i] *= rm;
    }
    LAS char* pa = L + L_SCR + (wave >> 1) * 1024 + lane * 16 + (wave & 1) * 8;
#pragma unroll
    for (int k = 0; k < 2; ++k) {
      *(LAS u32x2*)(pa + k * 4096) = u32x2{pk(ep[k][0], ep[k][1]), pk(ep[k][2], ep[k][3])};
      *(LAS u32x2*)(pa + 8192 + k * 4096) = u32x2{pk(em[k][0], em[k][1]), pk(em[k][2], em[k][3])};
      const int d = 16 * k + l15 - off4;
      u32x2 vb = {pk(vv[k][0], vv[k][1]), pk(vv[k][2], vv[k][3])};
      if (d < 0 || d > 27) { const u32 c = (d == 28 || d == -4) ? 0x3f803f80u : 0u; vb = u32x2{c, c}; }
      *(LAS u32x2*)(pa + 16384 + k * 4096) = vb;
    }
  }
  u64 repd[4];
  if (wave == 0) {
#pragma unroll
    for (int i = 0; i < 4; ++i) repd[i] = __hip_atomic_load((const GAS u64*)(ws + WS_FREP + i * 512 + lane * 8), RLX_AGENT);
  }
  BAR();
  {
    f32x4 kvp = {0.f, 0.f, 0.f, 0.f};
#pragma unroll
    for (int m = 0; m < 4; ++m)
      kvp = mfma16(*(LAS u32x4*)(L + L_SCR + (wave >> 1) * 4096 + m * 1024 + lane * 16), *(LAS u32x4*)(L + L_SCR + 16384 + (wave & 1) * 4096 + m * 1024 + lane * 16), kvp);
    ST16(rs, kvp, lane * 16, WS_KVP + ((head * 16 + tile) * 8 + wave) * 1024);
  }
  STAMP(7);
  if (wave == 0) {
    const bool ok = __all(repd[0] == tag + 1 && repd[1] == tag + 1 && repd[2] == tag + 1 && repd[3] == tag + 1);
    if (lane == 0) *(LAS u32*)(L + L_FLG + 16) = ok ? 1u : 0u;
  }
  DRAIN();
  BAR();
  if (tid == 448) __hip_atomic_store((GAS u64*)(ws + WS_FKV + (head * 16 + tile) * 128), tag + 1, RLX_AGENT);
  STAMP(8);
  if (__builtin_amdgcn_readfirstlane(*(volatile LAS u32*)(L + L_FLG + 16)) == 0u) {
    if (wave == 0) {
#pragma unroll
      for (int i = 0; i < 4; ++i) poll_flags(ws, WS_FREP + i * 512 + lane * 8, tag, 1);
    }
    BAR();
  }
  if (wave > 0) {
    if (r < 14) for (int i = wave - 1; i < 64; i += 7) dma16c(ws + WS_WO + cg * 65536 + i * 1024, lane * 16, L_WO + i * 1024);
    for (int i = wave - 1; i < 56; i += 7) dma16c(ws + WS_WGU + 7 * r * SLOT + i * 1024, lane * 16, i * 1024);
  }
  u32x4 pq[2];
  {
    f32x4 qt[2] = {};
#pragma unroll
    for (int s = 0; s < 14; ++s) {
      qt[0] = mfma16(trfrag(wr0 + 2 * SLOT + s * 2048, 256), xf[s], qt[0]);
      qt[1] = mfma16(trfrag(wr1 + 2 * SLOT + s * 2048, 256), xf[s], qt[1]);
    }
    const u32x4 qb = {pk(qt[0][0], qt[0][1]), pk(qt[0][2], qt[0][3]), pk(qt[1][0], qt[1][1]), pk(qt[1][2], qt[1][3])};
    const f32x4 z4 = {0.f, 0.f, 0.f, 0.f};
    const f32x4 p0 = mfma16(wpf[0], qb, z4), p1 = mfma16(wpf[1], qb, z4);
    const bool v23 = g < 3;
    float mx = fmaxf(fmaxf(p0[0], p1[0]), fmaxf(p0[1], p1[1])), mn = fminf(fminf(p0[0], p1[0]), fminf(p0[1], p1[1]));
    if (v23) { mx = fmaxf(mx, fmaxf(fmaxf(p0[2], p1[2]), fmaxf(p0[3], p1[3]))); mn = fminf(mn, fminf(fminf(p0[2], p1[2]), fminf(p0[3], p1[3]))); }
    mx = grp_max(mx); mn = grp_min(mn);
    float ep[8], em[8], sp = 0.f, sm = 0.f;
#pragma unroll
    for (int i = 0; i < 8; ++i) {
      const float v = i < 4 ? p0[i] : p1[i - 4];
      const bool ok = (i & 3) < 2 || v23;
      ep[i] = ok ? fexp2((v - mx) * LOG2E) : 0.f; em[i] = ok ? fexp2((mn - v) * LOG2E) : 0.f;
      sp += ep[i]; sm += em[i];
    }
    const float rp = frcp(grp_sum(sp)), rm = frcp(grp_sum(sm));
#pragma unroll
    for (int e = 0; e < 4; ++e) { pq[0][e] = pk(ep[2 * e] * rp, ep[2 * e + 1] * rp); pq[1][e] = pk(em[2 * e] * rm, em[2 * e + 1] * rm); }
  }
  STAMP(9);
  if (wave == 0) poll_flags(ws, WS_FKV + (head * 16 + l15) * 128, tag, 1);
  BAR();
  STAMP(10);
  {
    u32x4 t[16];
#pragma unroll
    for (int i = 0; i < 16; ++i) t[i] = LD16(rs, WS_KVP + (head * 128 + wave) * 1024 + lane * 16 + i * 8192);
    SCHED();
    f32x4 s = __builtin_bit_cast(f32x4, t[0]);
#pragma unroll
    for (int i = 1; i < 16; ++i) s += __builtin_bit_cast(f32x4, t[i]);
    *(LAS u32x2*)(L + L_KVA + (wave & 1) * 2048 + (wave >> 2) * 1024 + lane * 16 + ((wave >> 1) & 1) * 8) = u32x2{pk(s[0], s[1]), pk(s[2], s[3])};
  }
  DRAIN();
  BAR();
  STAMP(11);
  {
    f32x4 o[2];
#pragma unroll
    for (int k = 0; k < 2; ++k) {
      const f32x4 z4 = {0.f, 0.f, 0.f, 0.f};
      o[k] = mfma16(*(LAS u32x4*)(L + L_KVA + k * 2048 + lane * 16), pq[0], z4);
      o[k] = mfma16(*(LAS u32x4*)(L + L_KVA + k * 2048 + 1024 + lane * 16), pq[1], o[k]);
    }
    const bool padg0 = off4 && g == 0, padg1 = !off4 && g == 3;
    const float den = grp_sum(padg0 ? o[0][0] : (padg1 ? o[1][0] : 0.f)) + 1e-6f;
    const float rd = 1.0f / den;
    u32x4 ob = {pk(o[0][0] * rd, o[0][1] * rd), pk(o[0][2] * rd, o[0][3] * rd), pk(o[1][0] * rd, o[1][1] * rd), pk(o[1][2] * rd, o[1][3] * rd)};
    if (padg0) { ob.x = 0u; ob.y = 0u; }
    if (padg1) { ob.z = 0u; ob.w = 0u; }
    ST16(rs, ob, lane * 16, WS_O + ((tile * 8 + wave) * 16 + head) * 1024);
  }
  STAMP(12);
  const int tb = wave & 3, kh = wave >> 2;
  u32x4 hres = {0u, 0u, 0u, 0u};
  const int tok = tile * 128 + tg * 64 + tb * 16 + l15;
  if (r < 14) {
    u32x4 wof[8][4];
#pragma unroll
    for (int i = 0; i < 8; ++i)
#pragma unroll
      for (int k = 0; k < 4; ++k) wof[i][k] = *(LAS u32x4*)(L + L_WO + ((kh * 8 + i) * 4 + k) * 1024 + lane * 16);
    DRAIN();
    BAR();
    if (tid == 448) __hip_atomic_store((GAS u64*)(ws + WS_FO + (tile * 16 + head) * 128), tag + 1, RLX_AGENT);
    STAMP(13);
    const u32x4 xr = *(const u32x4*)(x + tok * 896 + cg * 128 + kh * 64 + g * 16);
    if (wave > 0) for (int i = 56 + wave - 1; i < 140; i += 7) dma16c(ws + WS_WGU + 7 * r * SLOT + i * 1024, lane * 16, i * 1024);
    if (wave == 0) poll_flags(ws, WS_FO + (tile * 16 + l15) * 128, tag, 1);
    BAR();
    STAMP(14);
    u32x4 of[8];
#pragma unroll
    for (int i = 0; i < 8; ++i) of[i] = LD16(rs, WS_O + ((tile * 8 + tg * 4 + tb) * 16 + kh * 8 + i) * 1024 + lane * 16);
    SCHED();
    f32x4 a[4] = {};
#pragma unroll
    for (int i = 0; i < 8; ++i)
#pragma unroll
      for (int k = 0; k < 4; ++k) a[k] = mfma16(wof[i][k], of[i], a[k]);
    *(LAS f32x4*)(L + L_EX + wave * 2048 + lane * 16) = kh ? a[0] : a[2];
    *(LAS f32x4*)(L + L_EX + wave * 2048 + 1024 + lane * 16) = kh ? a[1] : a[3];
    BAR();
    STAMP(15);
    const f32x4 c0 = (kh ? a[2] : a[0]) + *(LAS f32x4*)(L + L_EX + (wave ^ 4) * 2048 + lane * 16);
    const f32x4 c1 = (kh ? a[3] : a[1]) + *(LAS f32x4*)(L + L_EX + (wave ^ 4) * 2048 + 1024 + lane * 16);
    hres = u32x4{pk(blo(xr.x) + c0[0], bhi(xr.x) + c0[1]), pk(blo(xr.y) + c0[2], bhi(xr.y) + c0[3]), pk(blo(xr.z) + c1[0], bhi(xr.z) + c1[1]), pk(blo(xr.w) + c1[2], bhi(xr.w) + c1[3])};
    const int slot = 8 * cg + 4 * kh + g;
    ST16(rs, hres, WS_H + ((tok >> 5) * 28 + (slot >> 1)) * 1024 + ((slot & 1) * 32 + (tok & 31)) * 16, 0);
    float s1 = 0.f, s2 = 0.f;
#pragma unroll
    for (int e = 0; e < 4; ++e) { s1 = dot2(hres[e], 0x3f803f80u, s1); s2 = dot2(hres[e], hres[e], s2); }
    s1 = grp_sum(s1); s2 = grp_sum(s2);
    if (g == 0) __builtin_amdgcn_raw_buffer_store_b64(u32x2{__builtin_bit_cast(u32, s1), __builtin_bit_cast(u32, s2)}, rs, WS_ST + ((2 * cg + kh) * 2048 + tok) * 8, 0, 16);
    DRAIN();
    BAR();
    if (tid == 448) __hip_atomic_store((GAS u64*)(ws + WS_FH + (tile * 16 + r) * 128), tag + 1, RLX_AGENT);
  } else {
    DRAIN();
    BAR();
    if (tid == 448) __hip_atomic_store((GAS u64*)(ws + WS_FO + (tile * 16 + head) * 128), tag + 1, RLX_AGENT);
    if (wave > 0) for (int i = 56 + wave - 1; i < 140; i += 7) dma16c(ws + WS_WGU + 7 * r * SLOT + i * 1024, lane * 16, i * 1024);
    DRAIN();
  }
  STAMP(16);
  if (tid >= 256 && tid < 480) {
    const int t2 = tid - 256, row = t2 & 31;
    const int co = WS_CS + (row >> 4) * 7168 + (112 * r + 16 * (t2 >> 5) + (row & 15)) * 4;
    float c[14];
#pragma unroll
    for (int i = 0; i < 14; ++i) c[i] = __builtin_bit_cast(float, __builtin_amdgcn_raw_buffer_load_b32(rs, co + i * 14336, 0, 16));
    float s = 0.f;
#pragma unroll
    for (int i = 0; i < 14; ++i) s += c[i];
    *(LAS float*)(L + L_CS + t2 * 4) = s;
  }
  const int w2 = wave & 3;
  u32x4 wdf[2][8];
  const int wdo = WS_WD + (cg * 112 + 28 * w2) * 2048 + lane * 16;
  if (wave >= 4 && r < 14) {
#pragma unroll
    for (int j = 0; j < 2; ++j)
#pragma unroll
      for (int i = 0; i < 8; ++i) wdf[j][i] = LD16(rs, wdo + (7 * (i >> 1) + j) * 2048 + (i & 1) * 1024);
    SCHED();
  }
  if (wave == 0) poll_flags(ws, WS_FH + (tile * 16 + (l15 < 14 ? l15 : 0)) * 128, tag, 1);
  BAR();
  STAMP(17);
  if (wave < 4) {
    const int T32 = tile * 4 + wave;
    f32x2 st[7];
#pragma unroll
    for (int i = 0; i < 7; ++i) st[i] = __builtin_bit_cast(f32x2, __builtin_amdgcn_raw_buffer_load_b64(rs, WS_ST + ((7 * hh + i) * 2048 + T32 * 32 + (lane & 31)) * 8, 0, 16));
    u32x4 hb[28];
#pragma unroll
    for (int s = 0; s < 28; ++s) hb[s] = LD16(rs, WS_H + (T32 * 28 + s) * 1024 + lane * 16);
    SCHED();
    float s1 = 0.f, s2 = 0.f;
#pragma unroll
    for (int i = 0; i < 7; ++i) { s1 += st[i].x; s2 += st[i].y; }
    { f32x2 a = sw32(s1); s1 = a.x + a.y; a = sw32(s2); s2 = a.x + a.y; }
    const float mu = s1 * (1.f / 448.f), var = fmaxf(s2 * (1.f / 448.f) - mu * mu, 0.f);
    const float rstd = __builtin_amdgcn_rsqf(var + 1e-5f);
    GAS u64* prog = (GAS u64*)(ws + WS_FHH + ((tile * 16 + r) * 4 + wave) * 128);
    __builtin_amdgcn_s_setprio(2);
    for (int j = 0; j < 7; ++j) {
      if (j == 5) lds_wait(L, L_FLG + 4, 4);
      LAS char* wsl = L + (j < 5 ? j : j - 5) * SLOT + lane * 16;
      f32x16 acc;
#pragma unroll
      for (int k = 0; k < 4; ++k) {
        const f32x4 c = *(LAS f32x4*)(L + L_CS + j * 128 + k * 32 + hh * 16);
#pragma unroll
        for (int e = 0; e < 4; ++e) acc[4 * k + e] = -mu * c[e];
      }
#pragma unroll
      for (int s = 0; s < 28; ++s) acc = mfma32(*(LAS u32x4*)(wsl + s * 1024), hb[s], acc);
      if (j == 1 || j == 4) lds_signal(L, L_FLG + (j == 1 ? 0 : 8), lane);
      if (j > 0) { DRAIN(); if (lane == 0) __hip_atomic_store(prog, tag + j, RLX_AGENT); }
      float y[8];
#pragma unroll
      for (int i = 0; i < 8; ++i) {
        const float gg = acc[i] * rstd;
        const float a = gg * fmaf(gg * gg, -0.10294324f, -2.30220819f);
        y[i] = gg * frcp(1.0f + fexp2(a)) * (acc[i + 8] * rstd);
      }
      const u32x4 yb = {pk(y[0], y[1]), pk(y[2], y[3]), pk(y[4], y[5]), pk(y[6], y[7])};
      ST16(rs, yb, lane * 16, WS_HH + (T32 * 112 + 7 * r + j) * 1024);
      if (j == 0) STAMP(18);
    }
    DRAIN();
    if (lane == 0) __hip_atomic_store(prog, tag + 7, RLX_AGENT);
    __builtin_amdgcn_s_setprio(0);
    STAMP(19);
  } else {
    lds_wait(L, L_FLG + 0, 4);
    for (int i = w2; i < 56; i += 4) dma16c(ws + WS_WGU + (7 * r + 5) * SLOT + i * 1024, lane * 16, i * 1024);
    if (r >= 14) { DRAIN(); lds_signal(L, L_FLG + 4, lane); }
    else {
      const u32 po = WS_FHH + ((tile * 16 + 4 * w2 + ((lane >> 1) & 3)) * 4 + 2 * tg + (lane & 1)) * 128;
      const int ho = WS_HH + ((tile * 4 + 2 * tg) * 112 + 28 * w2) * 1024 + lane * 16;
      f32x16 acc[2][2] = {};
      u32x4 hf[2][8];
      u32 have = 0;
#define NEED(n) do { if (have < (n)) { for (u32 sp = 0; sp < SPIN_CAP; ++sp) { const u64 d = __hip_atomic_load((const GAS u64*)(ws + po), RLX_AGENT) - tag; \
        u32 c = 0; _Pragma("unroll") for (u32 k = (n); k <= 7; ++k) c += __all(d >= k && d <= 8) ? 1u : 0u; if (c) { have = (n) - 1 + c; break; } } \
        __builtin_amdgcn_fence(__ATOMIC_ACQUIRE, "wavefront"); } } while (0)
      NEED(1);
      lds_signal(L, L_FLG + 4, lane);
#pragma unroll
      for (int i = 0; i < 8; ++i) hf[0][i] = LD16(rs, ho + (i & 1) * 114688 + (7 * (i >> 1)) * 1024);
      SCHED();
#pragma unroll
      for (int j = 0; j < 7; ++j) {
        if (j < 6) {
          NEED(j + 2);
#pragma unroll
          for (int i = 0; i < 8; ++i) hf[(j + 1) & 1][i] = LD16(rs, ho + (i & 1) * 114688 + (7 * (i >> 1) + j + 1) * 1024);
          SCHED();
        }
#pragma unroll
        for (int i = 0; i < 4; ++i)
#pragma unroll
          for (int T = 0; T < 2; ++T)
#pragma unroll
            for (int k = 0; k < 2; ++k) acc[T][k] = mfma32(wdf[j & 1][2 * i + k], hf[j & 1][2 * i + T], acc[T][k]);
        SCHED();
        if (j < 5) {
#pragma unroll
          for (int i = 0; i < 8; ++i) wdf[j & 1][i] = LD16(rs, wdo + (7 * (i >> 1) + j + 2) * 2048 + (i & 1) * 1024);
          SCHED();
        }
        if (j == 0) STAMP(18);
        if (j == 5) STAMP(19);
      }
      lds_wait(L, L_FLG + 8, 4);
#pragma unroll
      for (int T = 0; T < 2; ++T)
#pragma unroll
        for (int k = 0; k < 2; ++k)
#pragma unroll
          for (int q = 0; q < 4; ++q)
            *(LAS f32x4*)(L + L_RED + (((w2 * 2 + T) * 2 + k) * 4 + q) * 1024 + lane * 16) = f32x4{acc[T][k][4 * q], acc[T][k][4 * q + 1], acc[T][k][4 * q + 2], acc[T][k][4 * q + 3]};
      STAMP(20);
    }
  }
  if (r < 14) {
    BAR();
    f32x4 c0 = {blo(hres.x), bhi(hres.x), blo(hres.y), bhi(hres.y)}, c1 = {blo(hres.z), bhi(hres.z), blo(hres.w), bhi(hres.w)};
    LAS char* rp = L + L_RED + ((((tb >> 1) * 2 + kh) * 4 + g) * 64 + (tb & 1) * 16 + l15) * 16;
#pragma unroll
    for (int w = 0; w < 4; ++w) { c0 += *(LAS f32x4*)(rp + w * 16384); c1 += *(LAS f32x4*)(rp + w * 16384 + 512); }
    *(u32x4*)(out + tok * 896 + cg * 128 + kh * 64 + g * 16) = u32x4{pk(c0[0], c0[1]), pk(c0[2], c0[3]), pk(c1[0], c1[1]), pk(c1[2], c1[3])};
    if (b == 0 && tid == 0) *(u64*)(ws + WS_E) = tag;
  }
  STAMP(21);
}

extern "C" void kernel_launch(void* const* d_in, const int* in_sizes, int n_in, void* d_out, int out_size, void* d_ws, size_t ws_size, hipStream_t stream) {
  (void)hipFuncSetAttribute((const void*)fused_block, hipFuncAttributeMaxDynamicSharedMemorySize, LDS_BYTES);
  fused_block<<<256, 512, LDS_BYTES, stream>>>((const char*)d_in[0], (const char*)d_in[1], (const char*)d_in[2], (const char*)d_in[3], (const char*)d_in[5], (const char*)d_in[6],
                                               (const char*)d_in[7], (char*)d_out, (char*)d_ws);
}
```

```cpp
#include <hip/hip_runtime.h>
#include <hip/hip_bf16.h>
#include <stdint.h>

typedef unsigned u32;
typedef unsigned long long u64;
typedef u32 u32x4 __attribute__((ext_vector_type(4)));
typedef u32 u32x2 __attribute__((ext_vector_type(2)));
typedef float f32x2 __attribute__((ext_vector_type(2)));
typedef float f32x4 __attribute__((ext_vector_type(4)));
typedef float f32x16 __attribute__((ext_vector_type(16)));
typedef short s16x4 __attribute__((ext_vector_type(4)));
typedef __bf16 bf16x2 __attribute__((ext_vector_type(2)));
typedef __bf16 bf16x8 __attribute__((ext_vector_type(8)));
#define LAS __attribute__((address_space(3)))
#define GAS __attribute__((address_space(1)))
#define DI __device__ __forceinline__
#define RLX_AGENT __ATOMIC_RELAXED, __HIP_MEMORY_SCOPE_AGENT

constexpr int WS_E = 0;
constexpr int WS_FKV = 8192;
constexpr int WS_FO = WS_FKV + 32768;
constexpr int WS_FH = WS_FO + 32768;
constexpr int WS_FQ = WS_FH + 32768;
constexpr int WS_FG = WS_FQ + 43008;
constexpr int WS_FD = WS_FG + 114688;
constexpr int WS_FHH = WS_FD + 57344;
constexpr int WS_CS = WS_FHH + 131072;
constexpr int WS_CSQ = WS_CS + 200704;
constexpr int WS_ST = WS_CSQ + 75264;
constexpr int WS_WQKV = WS_ST + 229376;
constexpr int WS_WGU = WS_WQKV + 1376256;
constexpr int WS_WD = WS_WGU + 3211264;
constexpr int WS_KVP = WS_WD + 1605632;
constexpr int WS_O = WS_KVP + 1048576;
constexpr int WS_H = WS_O + 2097152;
constexpr int WS_HH = WS_H + 1835008;
constexpr int WS_STAMP = WS_HH + 7340032;
constexpr int WS_LOG = WS_STAMP + 256 * 8 * 32 * 8;
constexpr int WS_USED = WS_LOG + 4096;
constexpr int SLOT = 28672;
constexpr int L_WK = 4 * SLOT, L_WV = 0, L_WQ = SLOT;
constexpr int L_PKV = 4 * SLOT;
constexpr int L_WO = 2 * SLOT;
constexpr int L_EX = 5 * SLOT;
constexpr int L_KVA = 5 * SLOT;
constexpr int L_ST1 = 5 * SLOT + 4096;
constexpr int L_WPHI = 5 * SLOT + 5120;
constexpr int L_CS1 = 5 * SLOT + 7168;
constexpr int L_CS = 159744;
constexpr int L_FLG = 160768;
constexpr int L_JUNK = 161280;
constexpr int L_STAMP = 161536;
constexpr int L_RED = SLOT;
constexpr int LDS_BYTES = 163840;
constexpr int gu_slot(int j) { return j == 0 || j == 5 || j > 6 ? 0 : (j == 1 || j == 6 ? 4 : j - 1); }
constexpr int CODE_PF = 24576;
constexpr u32 SPIN_CAP = 1u << 16;

#define STAMP(i) do {} while (0)

DI f32x4 mfma16(u32x4 a, u32x4 b, f32x4 c) { return __builtin_amdgcn_mfma_f32_16x16x32_bf16(__builtin_bit_cast(bf16x8, a), __builtin_bit_cast(bf16x8, b), c, 0, 0, 0); }
DI f32x16 mfma32(u32x4 a, u32x4 b, f32x16 c) { return __builtin_amdgcn_mfma_f32_32x32x16_bf16(__builtin_bit_cast(bf16x8, a), __builtin_bit_cast(bf16x8, b), c, 0, 0, 0); }
DI u32 pk(float lo, float hi) { f32x2 p = {lo, hi}; return __builtin_bit_cast(u32, __builtin_convertvector(p, bf16x2)); }
DI float blo(u32 v) { return __builtin_bit_cast(float, v << 16); }
DI float bhi(u32 v) { return __builtin_bit_cast(float, v & 0xffff0000u); }
DI float dot2(u32 a, u32 b, float c) { return __builtin_amdgcn_fdot2_f32_bf16(__builtin_bit_cast(bf16x2, a), __builtin_bit_cast(bf16x2, b), c, false); }
DI u32x2 trread(LAS char* p) { return __builtin_bit_cast(u32x2, __builtin_amdgcn_ds_read_tr16_b64_v4i16((LAS s16x4*)p)); }
DI u32x4 trfrag(LAS char* p, int step) { u32x2 a = trread(p), b = trread(p + step); return u32x4{a.x, a.y, b.x, b.y}; }
DI float fexp2(float x) { return __builtin_amdgcn_exp2f(x); }
DI float frcp(float x) { return __builtin_amdgcn_rcpf(x); }
template <int C> DI float dppror(float x) { return __builtin_bit_cast(float, __builtin_amdgcn_update_dpp(0, __builtin_bit_cast(int, x), 0x120 + C, 0xf, 0xf, false)); }
DI float row_max(float x) { x = fmaxf(x, dppror<8>(x)); x = fmaxf(x, dppror<4>(x)); x = fmaxf(x, dppror<2>(x)); return fmaxf(x, dppror<1>(x)); }
DI float row_min(float x) { x = fminf(x, dppror<8>(x)); x = fminf(x, dppror<4>(x)); x = fminf(x, dppror<2>(x)); return fminf(x, dppror<1>(x)); }
DI float row_sum(float x) { x += dppror<8>(x); x += dppror<4>(x); x += dppror<2>(x); return x + dppror<1>(x); }
DI f32x2 sw16(float x) { u32 u = __builtin_bit_cast(u32, x); auto r = __builtin_amdgcn_permlane16_swap(u, u, false, false); return f32x2{__builtin_bit_cast(float, (u32)r[0]), __builtin_bit_cast(float, (u32)r[1])}; }
DI f32x2 sw32(float x) { u32 u = __builtin_bit_cast(u32, x); auto r = __builtin_amdgcn_permlane32_swap(u, u, false, false); return f32x2{__builtin_bit_cast(float, (u32)r[0]), __builtin_bit_cast(float, (u32)r[1])}; }
DI float grp_sum(float x) { f32x2 a = sw16(x); x = a.x + a.y; a = sw32(x); return a.x + a.y; }
DI float grp_max(float x) { f32x2 a = sw16(x); x = fmaxf(a.x, a.y); a = sw32(x); return fmaxf(a.x, a.y); }
DI float grp_min(float x) { f32x2 a = sw16(x); x = fminf(a.x, a.y); a = sw32(x); return fminf(a.x, a.y); }

#define BAR() asm volatile("s_waitcnt lgkmcnt(0)\n\ts_barrier" ::: "memory")
#define WAITVM(n) asm volatile("s_waitcnt vmcnt(" #n ")" ::: "memory")
#define DRAIN() do { asm volatile("s_waitcnt vmcnt(0)" ::: "memory"); __builtin_amdgcn_s_waitcnt(0x0F70); } while (0)
#define SCHED() __builtin_amdgcn_sched_barrier(0)
DI void dma16(const void* base, u32 voff, u32 lds) {
  u32 keep;
  asm volatile("s_mov_b32 %0, m0\n\ts_mov_b32 m0, %3\n\ts_nop 0\n\tglobal_load_lds_dwordx4 %1, %2\n\ts_mov_b32 m0, %0" : "=&s"(keep) : "v"(voff), "s"(base), "s"(lds) : "memory");
}
DI void dma16c(const void* base, u32 voff, u32 lds) {
  u32 keep;
  asm volatile("s_mov_b32 %0, m0\n\ts_mov_b32 m0, %3\n\ts_nop 0\n\tglobal_load_lds_dwordx4 %1, %2 sc1\n\ts_mov_b32 m0, %0" : "=&s"(keep) : "v"(voff), "s"(base), "s"(lds) : "memory");
}
DI void dma4(const void* base, u32 voff, u32 lds) {
  u32 keep;
  asm volatile("s_mov_b32 %0, m0\n\ts_mov_b32 m0, %3\n\ts_nop 0\n\tglobal_load_lds_dword %1, %2\n\ts_mov_b32 m0, %0" : "=&s"(keep) : "v"(voff), "s"(base), "s"(lds) : "memory");
}
#define LD16(rs, off) __builtin_bit_cast(u32x4, __builtin_amdgcn_raw_buffer_load_b128(rs, off, 0, 16))
#define ST16(rs, v, off, soff) do { const u32x4 d_ = __builtin_bit_cast(u32x4, v); __builtin_amdgcn_raw_buffer_store_b128(d_, rs, off, soff, 16); asm volatile("s_nop 1" :: "v"(d_) : "memory"); } while (0)

#define FLAG(vo, n) __builtin_amdgcn_raw_buffer_store_b64(__builtin_bit_cast(u32x2, tag + (n)), rs, vo, 0, 16)
constexpr int NOWHERE = 0x7ffffff0;
DI void poll_flags(const char* ws, u32 off, u64 tag, u32 need) {
#pragma nounroll
  for (u32 sp = 0; sp < SPIN_CAP; ++sp) {
    u64 d = __hip_atomic_load((const GAS u64*)(ws + off), RLX_AGENT) - tag;
    if (__all(d >= need && d <= 8)) break;
  }
  __builtin_amdgcn_fence(__ATOMIC_ACQUIRE, "wavefront");
}
DI void lds_wait(LAS char* L, int sig) {
#pragma nounroll
  for (u32 sp = 0; sp < (SPIN_CAP << 4); ++sp) {
    const u32x4 v = *(volatile LAS u32x4*)(L + L_FLG + sig * 16);
    if (__builtin_amdgcn_readfirstlane(v.x & v.y & v.z & v.w)) break;
    __builtin_amdgcn_s_sleep(1);
  }
  asm volatile("" ::: "memory");
}
DI void lds_signal(LAS char* L, int sig, int w) {
  asm volatile("" ::: "memory");
  *(volatile LAS u32*)(L + L_FLG + sig * 16 + w * 4) = 1u;
}

template <int N> struct IC { static constexpr int value = N; };
constexpr float LOG2E = 1.4426950408889634f;
template <class F> DI void project(LAS char* L, u32 wb, int sec, const u32x4 (&xf)[2][14], int tp, int lane, f32x4 (&t)[2][2], F&& mid) {
  asm volatile("" : "+v"(wb));
  u32x4 ring[7];
#pragma unroll
  for (int n = 0; n < 7; ++n) ring[n] = *(LAS u32x4*)(L + wb + n * 1024);
#pragma unroll
  for (int i = 0; i < 4; ++i) t[i >> 1][i & 1] = f32x4{0.f, 0.f, 0.f, 0.f};
  SCHED();
#pragma unroll
  for (int n = 0; n < 28; ++n) {
    const u32x4 f = ring[n % 7];
    t[0][n & 1] = mfma16(f, xf[0][n >> 1], t[0][n & 1]);
    t[1][n & 1] = mfma16(f, xf[1][n >> 1], t[1][n & 1]);
    if (n + 7 < 28) ring[n % 7] = *(LAS u32x4*)(L + wb + (n + 7) * 1024);
    mid(n);
    SCHED();
  }
  f32x4 cs[2];
#pragma unroll
  for (int k = 0; k < 2; ++k) cs[k] = *(LAS f32x4*)(L + L_CS1 + sec * 128 + k * 64 + (lane >> 4) * 16);
#pragma unroll
  for (int tb = 0; tb < 2; ++tb) {
    const f32x2 st = *(LAS f32x2*)(L + L_ST1 + (tp * 32 + tb * 16 + (lane & 15)) * 8);
#pragma unroll
    for (int k = 0; k < 2; ++k) {
      t[tb][k] = (t[tb][k] + cs[k] * st.x) * st.y;
      t[tb][k][3] = (lane & 16) ? 0.f : t[tb][k][3];
    }
  }
}
DI f32x2 swp16(float a, float b) { auto r = __builtin_amdgcn_permlane16_swap(__builtin_bit_cast(u32, a), __builtin_bit_cast(u32, b), false, false); return f32x2{__builtin_bit_cast(float, (u32)r[0]), __builtin_bit_cast(float, (u32)r[1])}; }
DI f32x2 swp32(float a, float b) { auto r = __builtin_amdgcn_permlane32_swap(__builtin_bit_cast(u32, a), __builtin_bit_cast(u32, b), false, false); return f32x2{__builtin_bit_cast(float, (u32)r[0]), __builtin_bit_cast(float, (u32)r[1])}; }
DI float swadd32(float a, float b) { const f32x2 s = swp32(a, b); return s.x + s.y; }
DI float swadd16(float a, float b) { const f32x2 s = swp16(a, b); return s.x + s.y; }
DI float mx2(float a, float b) { float r; asm("v_max_f32 %0, %1, %2" : "=v"(r) : "v"(a), "v"(b)); return r; }
DI float mx3(float a, float b, float c) { return fmaxf(fmaxf(a, b), c); }
DI float mn3(float a, float b, float c) { return fminf(fminf(a, b), c); }
DI void feature_map(const f32x4 (&t)[2][2], const u32x4 (&wpf)[2], int g, u32x4 (&P)[2][2]) {
  const f32x4 z4 = {0.f, 0.f, 0.f, 0.f};
  const bool v23 = g < 3;
  float p[2][8], m[4];
#pragma unroll
  for (int tb = 0; tb < 2; ++tb) {
    const u32x4 tq = {pk(t[tb][0][0], t[tb][0][1]), pk(t[tb][0][2], t[tb][0][3]), pk(t[tb][1][0], t[tb][1][1]), pk(t[tb][1][2], t[tb][1][3])};
    const f32x4 p0 = mfma16(wpf[0], tq, z4), p1 = mfma16(wpf[1], tq, z4);
#pragma unroll
    for (int i = 0; i < 8; ++i) { const float v = i < 4 ? p0[i] : p1[i - 4]; p[tb][i] = ((i & 3) < 2 || v23) ? v : p0[0]; }
    m[2 * tb] = mx3(mx3(p[tb][0], p[tb][1], p[tb][2]), mx3(p[tb][3], p[tb][4], p[tb][5]), mx3(p[tb][6], p[tb][7], p[tb][0]));
    m[2 * tb + 1] = -mn3(mn3(p[tb][0], p[tb][1], p[tb][2]), mn3(p[tb][3], p[tb][4], p[tb][5]), mn3(p[tb][6], p[tb][7], p[tb][0]));
  }
  {
    const f32x2 a = swp16(m[0], m[1]), b = swp16(m[2], m[3]);
    const f32x2 c = swp32(mx2(a.x, a.y), mx2(b.x, b.y));
    const float d = mx2(c.x, c.y) * -LOG2E;
    const f32x2 e = swp32(d, d), f0 = swp16(e.x, e.x), f1 = swp16(e.y, e.y);
    m[0] = f0.x; m[1] = f0.y; m[2] = f1.x; m[3] = f1.y;
  }
  float ex[4][8], sm[4];
#pragma unroll
  for (int c = 0; c < 4; ++c) {
#pragma unroll
    for (int i = 0; i < 8; ++i) {
      ex[c][i] = fexp2(fmaf(p[c >> 1][i], (c & 1) ? -LOG2E : LOG2E, m[c]));
      if ((i & 3) >= 2) ex[c][i] = v23 ? ex[c][i] : 0.f;
    }
    sm[c] = ((ex[c][0] + ex[c][1]) + (ex[c][2] + ex[c][3])) + ((ex[c][4] + ex[c][5]) + (ex[c][6] + ex[c][7]));
  }
  {
    const float d = frcp(swadd32(swadd16(sm[0], sm[1]), swadd16(sm[2], sm[3])));
    const f32x2 e = swp32(d, d), f0 = swp16(e.x, e.x), f1 = swp16(e.y, e.y);
    sm[0] = f0.x; sm[1] = f0.y; sm[2] = f1.x; sm[3] = f1.y;
  }
#pragma unroll
  for (int c = 0; c < 4; ++c)
#pragma unroll
    for (int e = 0; e < 4; ++e) P[c >> 1][c & 1][e] = pk(ex[c][2 * e] * sm[c], ex[c][2 * e + 1] * sm[c]);
}
DI u32x4 piece(const u32x2 (&rp)[8], int e) {
  u32x4 f;
#pragma unroll
  for (int m = 0; m < 4; ++m) f[m] = __builtin_amdgcn_perm(rp[2 * m + 1][e >> 1], rp[2 * m][e >> 1], (e & 1) ? 0x07060302u : 0x05040100u);
  return f;
}
#define RP_OPS "+v"(rp[0]), "+v"(rp[1]), "+v"(rp[2]), "+v"(rp[3]), "+v"(rp[4]), "+v"(rp[5]), "+v"(rp[6]), "+v"(rp[7])
#define RP_LOAD(lvo, addr) _Pragma("unroll") for (int j = 0; j < 8; ++j) asm volatile("global_load_dwordx2 %0, %1, %2" : "=v"(rp[j]) : "v"(lvo), "s"(addr) : "memory")
#define PF1 56
#define PF2 112
#define DLY2 8
#define DLY3 8
#define GUPRIO 2
enum { S_FREE01 = 0, S_REFILL, S_DONE4, S_WIN3, S_WK, S_WV, S_WQ, S_WSEEN, S_KDONE, S_V, S_PHIK, S_KVP, S_KVSEEN, S_KVA, S_REP, S_WO, S_REPSEEN, S_HSEEN };
DI u32 gu_dst(int i) { return i < 28 ? i * 1024 : (i < 56 ? 3 * SLOT + i * 1024 : i * 1024 - SLOT); }

__global__ void __launch_bounds__(512, 2) fused_block(const char* __restrict__ x, const char* __restrict__ wqkv, const char* __restrict__ wphi, const char* __restrict__ wo,
                                                      const char* __restrict__ wg, const char* __restrict__ wu, const char* __restrict__ wd, char* __restrict__ out, char* ws) {
  extern __shared__ __attribute__((aligned(16))) char smem[];
  LAS char* L = (LAS char*)smem;
  const int tid = threadIdx.x, lane = tid & 63, wave = __builtin_amdgcn_readfirstlane(tid >> 6), w2 = wave & 3;
  const int b = blockIdx.x, li = b >> 3, xp = (b & 7) >> 1, xh = b & 1;
  const int tile = 4 * xp + (li >> 3), r = 8 * xh + (li & 7);
  const int tile2 = 4 * xp + 2 * xh + (li >> 4), r2 = li & 15;
  const int tg = r2 >= 7 ? 1 : 0, cg = r2 - 7 * tg;
  const int l15 = lane & 15, g = lane >> 4, q4 = l15 >> 2, p4 = lane & 3, hh = lane >> 5;
  const int head = r;
  asm volatile("" :: "s"(x), "s"(wqkv), "s"(wphi), "s"(wo), "s"(wg), "s"(wu), "s"(wd), "s"(out), "s"(ws));
  STAMP(0);
  const __amdgpu_buffer_rsrc_t rs = __builtin_amdgcn_make_buffer_rsrc((void*)ws, 0, WS_USED, 0x00020000);
  const u32x4 ones = {0x3f803f80u, 0x3f803f80u, 0x3f803f80u, 0x3f803f80u};
  const f32x4 z4 = {0.f, 0.f, 0.f, 0.f};
  auto role = [&](auto AC) __attribute__((always_inline)) {
  constexpr bool A = AC.value;
  u64 epoch;
  if (A) asm volatile("s_load_dwordx2 %0, %1, 0x0 ; role A" : "=s"(epoch) : "s"(ws) : "memory");
  else asm volatile("s_load_dwordx2 %0, %1, 0x0 ; role B" : "=s"(epoch) : "s"(ws) : "memory");
  __builtin_amdgcn_s_waitcnt(0x0F70);
  u32x2 rp[8];
  const int uidx = A ? b * 2 + (wave & 1) : b * 4 + w2, unit = uidx * 49 >> 6;
  const bool has_unit = (((uidx + 1) * 49) >> 6) != unit;
  const int umat = unit & 1, ukp = (unit >> 1) / 28, ufl = (unit >> 1) - 28 * ukp, un = unit / 7, uc = unit - 7 * un;
  if (A && wave < 2) {
    const int u = b + 256 * wave;
    if (wave == 1) { dma16(wphi + head * 1568, lane * 16, L_WPHI); dma16(wphi + head * 1568, min(lane + 64, 97) * 16, L_WPHI + 1024); }
    if (u < 294) {
      const int sl = u / 14, kp = u - 14 * sl, sl2 = sl >= 14 ? sl - 14 : sl + 7, sec = sl2 / 7, ln = sl2 - 7 * sec;
      const u32 lvo = g * 8 * 2688 + l15 * 8;
      RP_LOAD(lvo, wqkv + (kp * 32 + j) * 2688 + sl2 * 128);
      const int Q = 16 * ln + l15, h2 = Q / 7;
      const int svo = h2 * 86016 + g * 256 + (Q - 7 * h2) * 16;
      asm volatile("s_waitcnt vmcnt(0) lgkmcnt(0)" : RP_OPS, "+s"(epoch)::"memory");
      f32x4 c;
#pragma unroll
      for (int e = 0; e < 4; ++e) {
        const u32x4 f = piece(rp, e);
        ST16(rs, f, svo + (e >> 1) * 1024 + (e & 1) * 128, WS_WQKV + sec * SLOT + kp * 2048);
        c[e] = mfma16(ones, f, z4)[0];
      }
      ST16(rs, c, g == 0 ? l15 * 16 : 0x7ffffff0, WS_CSQ + kp * 5376 + sl2 * 256);
      DRAIN();
      if (lane == 0) __hip_atomic_store((GAS u64*)(ws + WS_FQ + (sl2 * 16 + kp) * 128), epoch * 6364136223846793005ull + 1442695040888963408ull, RLX_AGENT);
    } else {
      WAITVM(0);
    }
    STAMP(1);
  } else {
    {
      u64 pc; asm volatile("s_getpc_b64 %0" : "=s"(pc));
      if (A) dma4(x + (xp * 512 + li * 16) * 896 + (wave & 1) * 7168, min(lane, 55) * 128, L_JUNK);
      else if (wave == 4) dma4((const char*)(pc & ~127ull), min((li & 3) * 8192 + lane * 128, CODE_PF), L_JUNK);
    }
    const u32 vo = (lane >> 3) * 896 + (((lane & 7) ^ (lane >> 3)) * 16);
    for (int n = wave - 2; n < 112; n += 6) {
      const int gr = n / 14, s = n - 14 * gr;
      dma16(x + (tile * 128 + gr * 16 + (s & 1) * 8) * 896 + (s >> 1) * 128, vo, n * 1024);
    }
    if (!A) { const u32 lvo = g * 8 * 3584 + l15 * 8; RP_LOAD(lvo, (umat ? wu : wg) + (ukp * 32 + j) * 3584 + ufl * 128); }
    else { const u32 lvo = (16 * (g & 1) + (g >> 1)) * 896 + l15 * 8; RP_LOAD(lvo, wd + (64 * (un >> 1) + 2 * (un & 1) + (j & 3) * 4 + (j >> 2) * 32) * 896 + uc * 128); }
    if (!A && wave >= 6) dma4(wo + li * 12544 + (wave & 1) * 6272, min(lane, 48) * 128, L_JUNK);
    if (!A && wave == 7) { *(LAS u32*)(L + L_FLG + lane * 4) = 0u; *(LAS u32*)(L + L_FLG + 256 + lane * 4) = 0u; }
    STAMP(1);
    if (!A && wave >= 6) WAITVM(9); else WAITVM(8);
  }
  asm volatile("s_waitcnt lgkmcnt(0)\n\ts_barrier" : "+s"(epoch)::"memory");
  STAMP(2);
  const u64 tag = epoch * 6364136223846793005ull + 1442695040888963407ull;
  const char* gusrc = ws + WS_WGU + 7 * r * SLOT;
  const int tb = w2; constexpr int kh = A ? 0 : 1;
  u32x4 wof[8][4];
  u32 wb0 = L_WO + (kh * 224 + (g >> 1) * 7 + (g & 1) * 4 + ((q4 == 3 && (g & 1)) ? 2 : q4)) * 128;
  wb0 += 16 * p4 + ((wb0 >> 2) & 64);
#define WOF_READ() do { u32 wa_ = wb0, wc_ = wb0 ^ 64; asm volatile("" : "+v"(wa_), "+v"(wc_)); \
    _Pragma("unroll") for (int i = 0; i < 8; ++i) _Pragma("unroll") for (int k = 0; k < 4; ++k) { \
      const u32x2 a_ = trread(L + (k >> 1 ? wc_ : wa_) + 28 * i * 128 + (k & 1) * 8), c_ = trread(L + (k >> 1 ? wa_ : wc_) + (28 * i + 14) * 128 + (k & 1) * 8); \
      wof[i][k] = u32x4{a_.x, a_.y, c_.x, c_.y}; } } while (0)
  {
    u32x4 xf[2][14];
    {
      LAS char* xr0 = L + w2 * 28672 + l15 * 128 + ((g ^ (l15 & 7)) * 16);
      LAS char* xr1 = L + w2 * 28672 + l15 * 128 + (((4 + g) ^ (l15 & 7)) * 16);
#pragma unroll
      for (int s = 0; s < 28; ++s) xf[s / 14][s % 14] = *(LAS u32x4*)((s & 1 ? xr1 : xr0) + (s >> 1) * 2048);
    }
    u64 fq0, fq1;
    const u32 fqo = WS_FQ + ((((g & 1) ? 28 * head + 27 : 28 * head) >> 6) * 16 + min(l15, 13)) * 128;
    if (A && wave == 0) asm volatile("global_load_dwordx2 %0, %2, %4 sc1\n\tglobal_load_dwordx2 %1, %3, %4 sc1" : "=&v"(fq0), "=&v"(fq1) : "v"(fqo + (1 + hh) * 14336), "v"(fqo), "s"(ws) : "memory");
    BAR();
    STAMP(3);
    if (A && wave == 0) {
      asm volatile("s_waitcnt vmcnt(0)" : "+v"(fq0), "+v"(fq1)::"memory");
      if (!__all(fq0 == tag + 1 && fq1 == tag + 1)) { poll_flags(ws, fqo + (1 + hh) * 14336, tag, 1); poll_flags(ws, fqo, tag, 1); }
      __builtin_amdgcn_fence(__ATOMIC_ACQUIRE, "wavefront");
      *(volatile LAS u32x4*)(L + L_FLG + S_WSEEN * 16) = u32x4{1u, 1u, 1u, 1u};
      STAMP(27);
    }
    float csq[14];
    {
      const char* wsrc = ws + WS_WQKV + head * 86016 + w2 * 1024 + (A ? 1 : 2) * SLOT;
      if (A && wave == 0) {
#pragma unroll
        for (int i = 0; i < 7; ++i) dma16c(wsrc + i * 4096, lane * 16, L_WK + 4 * i * 1024);
      }
      f32x4 g1 = z4, g2 = z4;
#pragma unroll
      for (int s = 0; s < 14; ++s) { g1 = mfma16(ones, xf[A ? 0 : 1][s], g1); g2 = mfma16(xf[A ? 0 : 1][s], xf[A ? 0 : 1][s], g2); }
      asm volatile("" : "+v"(g1), "+v"(g2));
      if (!A || wave > 0) {
        lds_wait(L, S_WSEEN);
        if (!A) STAMP(8);
#pragma unroll
        for (int i = 0; i < 7; ++i) dma16c(wsrc + i * 4096, lane * 16, (A ? L_WK : L_WV) + (4 * i + w2) * 1024);
      }
      if (!A && wave < 6) {
        const u32 co = WS_CSQ + head * 112 + ((wave == 4 ? 1 + hh : 0) * 448 + min(4 * (l15 & 7) + 2 * (g & 1) + (l15 >> 3), 27)) * 4;
#pragma unroll
        for (int i = 0; i < 14; ++i) asm volatile("global_load_dword %0, %1, %2 sc1" : "=v"(csq[i]) : "v"(co), "s"(ws + i * 5376) : "memory");
      }
      const float d01 = (p4 & 1) ? g2[1] : g2[0], d23 = (p4 & 1) ? g2[3] : g2[2], s2 = (p4 & 2) ? d23 : d01;
      const float nmu = g1[0] * (-1.f / 448.f), var = fmaxf(s2 * (1.f / 448.f) - nmu * nmu, 0.f);
      if (g == q4) *(LAS f32x2*)(L + L_ST1 + (w2 * 32 + kh * 16 + l15) * 8) = f32x2{nmu, __builtin_amdgcn_rsqf(var + 1e-5f)};
    }
    u32x4 wpf[2];
    {
      u32 wp[8];
#pragma unroll
      for (int j = 0; j < 8; ++j) {
        wp[j] = *(LAS u32*)(L + L_WPHI + (16 * (g & 1) + (g >> 1)) * 56 + l15 * 4 + (4 * (j & 3) + 2 * (j >> 2)) * 56);
        if (l15 >= 14 || ((j & 3) == 3 && (g & 1))) wp[j] = 0u;
      }
#pragma unroll
      for (int e = 0; e < 4; ++e) {
        wpf[0][e] = __builtin_amdgcn_perm(wp[2 * e + 1], wp[2 * e], 0x05040100u);
        wpf[1][e] = __builtin_amdgcn_perm(wp[2 * e + 1], wp[2 * e], 0x07060302u);
      }
      asm volatile("" : "+v"(wpf[0]), "+v"(wpf[1]));
    }
    STAMP(4);
    LAS char* pw = L + L_PKV + (w2 * 32 + l15) * 224 + g * 8;
    if constexpr (A) {
      WAITVM(0);
      lds_signal(L, S_WK, w2);
      lds_wait(L, S_WK);
      STAMP(5);
      __builtin_amdgcn_s_setprio(2);
      f32x4 t[2][2];
      project(L, L_WK + lane * 16, 1, xf, w2, lane, t, [&](int n) __attribute__((always_inline)) { if (n == 27) { lds_signal(L, S_KDONE, w2); lds_wait(L, S_WV); } });
      STAMP(6);
      u32x4 P[2][2];
      feature_map(t, wpf, g, P);
      asm volatile("" : "+v"(P[0][0]), "+v"(P[0][1]), "+v"(P[1][0]), "+v"(P[1][1]));
      lds_wait(L, S_KDONE);
#pragma unroll
      for (int c = 0; c < 4; ++c) {
        LAS char* p = pw + (c >> 1) * 16 * 224 + (c & 1) * 64;
        *(LAS u32x2*)(p) = u32x2{P[c >> 1][c & 1].x, P[c >> 1][c & 1].y}; *(LAS u32x2*)(p + 32) = u32x2{P[c >> 1][c & 1].z, P[c >> 1][c & 1].w};
      }
      lds_signal(L, S_PHIK, w2);
      STAMP(7);
      u32 pr = L_PKV + (4 * g + q4) * 224 + 8 * p4;
      asm volatile("" : "+v"(pr));
      lds_wait(L, S_V);
      u32x4 pb[8];
#pragma unroll
      for (int m = 0; m < 8; ++m) pb[m] = trfrag(L + pr + (m >> 1) * 32 * 224 + 128 + (m & 1) * 32, 16 * 224);
      lds_wait(L, S_PHIK);
      u32x4 pa[4];
#pragma unroll
      for (int m = 0; m < 4; ++m) pa[m] = trfrag(L + pr + w2 * 32 + m * 32 * 224, 16 * 224);
      f32x4 kv[2] = {};
#pragma unroll
      for (int m = 0; m < 8; ++m) kv[m & 1] = mfma16(pa[m >> 1], pb[m], kv[m & 1]);
      const u32x4 kvb = {pk(kv[0][0], kv[0][1]), pk(kv[0][2], kv[0][3]), pk(kv[1][0], kv[1][1]), pk(kv[1][2], kv[1][3])};
      ST16(rs, kvb, lane * 16, WS_KVP + ((head * 16 + tile) * 4 + w2) * 1024);
      STAMP(8);
      DRAIN();
      lds_signal(L, S_KVP, w2);
      __builtin_amdgcn_s_setprio(0);
      if (wave == 0) {
        lds_wait(L, S_KVP);
        if (lane == 0) __hip_atomic_store((GAS u64*)(ws + WS_FKV + (head * 16 + tile) * 128), tag + 1, RLX_AGENT);
        STAMP(9);
      } else {
        if (wave == 1) {
          const int f0 = (7 * r) >> 2, f1 = (7 * r + 6) >> 2;
          poll_flags(ws, WS_FG + (((f0 + hh) * 2 + (g & 1)) * 16 + min(l15, 13)) * 128, tag, 1);
          if (f1 > f0 + 1) poll_flags(ws, WS_FG + ((f1 * 2 + (g & 1)) * 16 + min(l15, 13)) * 128, tag, 1);
          *(volatile LAS u32x4*)(L + L_FLG + S_REPSEEN * 16) = u32x4{1u, 1u, 1u, 1u};
        } else {
          asm volatile("" : RP_OPS::"memory");
          if (has_unit) {
#pragma unroll
            for (int e = 0; e < 4; ++e) ST16(rs, piece(rp, e), (g >> 1) * 2048 + (g & 1) * 512 + l15 * 16 + (e >> 1) * 1024 + (e & 1) * 256, WS_WD + (uc * 112 + 2 * un) * 2048);
          }
        }
        lds_wait(L, S_KVP);
        lds_wait(L, S_REPSEEN);
        for (int i = wave - 1; i < PF1; i += 3) dma16c(gusrc + i * 1024, lane * 16, gu_dst(i));
      }
      lds_wait(L, S_WO);
      WOF_READ();
      if (wave == 0) {
        poll_flags(ws, WS_FKV + (head * 16 + l15) * 128, tag, 1);
        *(volatile LAS u32x4*)(L + L_FLG + S_KVSEEN * 16) = u32x4{1u, 1u, 1u, 1u};
      } else {
        lds_wait(L, S_KVSEEN);
      }
      STAMP(10);
      u32x4 tt[16];
#pragma unroll
      for (int i = 0; i < 16; ++i) tt[i] = __builtin_bit_cast(u32x4, __builtin_amdgcn_raw_buffer_load_b128(rs, lane * 16, WS_KVP + head * 65536 + w2 * 1024 + i * 4096, 16));
      SCHED();
      const u32 one = g == q4 ? ((p4 & 1) ? 0x3f800000u : 0x3f80u) : 0u, s01 = p4 < 2 ? one : 0u, s23 = p4 < 2 ? 0u : one;
      const u32x4 sel0 = {s01, s23, 0u, 0u}, sel1 = {0u, 0u, s01, s23};
      f32x4 s0 = {}, s1 = {};
#pragma unroll
      for (int i = 0; i < 16; ++i) { s0 = mfma16(sel0, tt[i], s0); s1 = mfma16(sel1, tt[i], s1); }
      LAS char* kw = L + L_KVA + (w2 >> 1) * 1024 + lane * 16 + (w2 & 1) * 8;
      *(LAS u32x2*)(kw) = u32x2{pk(s0[0], s0[1]), pk(s0[2], s0[3])};
      *(LAS u32x2*)(kw + 2048) = u32x2{pk(s1[0], s1[1]), pk(s1[2], s1[3])};
      lds_signal(L, S_KVA, w2);
      if (wave >= 2) { WAITVM(0); FLAG(has_unit && lane == 0 ? WS_FD + (uc * 64 + un) * 128 : NOWHERE, 1); }
      STAMP(11);
    } else {
      u32 wvo[7];
#pragma unroll
      for (int n = 0; n < 7; ++n) {
        const int rho = 8 * n + (lane >> 3), h1 = rho >= 28 ? 1 : 0, sg = rho - 28 * h1, e = (sg * 37) >> 8;
        wvo[n] = (28 * h1 + 4 * (sg - 7 * e) + e) * 896 + (((lane & 7) ^ ((lane >> 2) & 4)) * 16);
      }
      if (wave < 6) WAITVM(21); else WAITVM(7);
      asm volatile("" : RP_OPS::"memory");
      {
        const int so = has_unit ? 0 : NOWHERE;
        f32x4 c;
#pragma unroll
        for (int e = 0; e < 4; ++e) {
          const u32x4 f = piece(rp, e);
          ST16(rs, f, so | ((g >> 1) * 1024 + (g & 1) * 512 + l15 * 16), WS_WGU + (4 * ufl + e) * SLOT + 2 * ukp * 1024 + umat * 256);
          c[e] = mfma16(ones, f, z4)[0];
        }
        ST16(rs, c, g == 0 ? so | (l15 * 16) : NOWHERE, WS_CS + ukp * 14336 + umat * 7168 + ufl * 256);
      }
      WAITVM(5);
      if (wave < 6) {
        asm volatile("" : "+v"(csq[0]), "+v"(csq[1]), "+v"(csq[2]), "+v"(csq[3]), "+v"(csq[4]), "+v"(csq[5]), "+v"(csq[6]), "+v"(csq[7]), "+v"(csq[8]), "+v"(csq[9]), "+v"(csq[10]), "+v"(csq[11]), "+v"(csq[12]), "+v"(csq[13]));
        *(LAS float*)(L + L_CS1 + (wave == 4 ? 1 + hh : 0) * 128 + (lane & 31) * 4) =
            (((csq[0] + csq[1]) + (csq[2] + csq[3])) + ((csq[4] + csq[5]) + (csq[6] + csq[7]))) + (((csq[8] + csq[9]) + (csq[10] + csq[11])) + (csq[12] + csq[13]));
      }
      lds_signal(L, S_WV, w2);
      lds_wait(L, S_WV);
      STAMP(5);
      f32x4 t[2][2];
      const char* wqs = ws + WS_WQKV + head * 86016 + w2 * 1024;
      const char* wos = wo + (r2 < 14 ? cg : 0) * 128 + w2 * 112 * 896;
      project(L, L_WV + lane * 16, 2, xf, w2, lane, t, [&](int n) __attribute__((always_inline)) {
        if (n < 7) dma16c(wqs + n * 4096, lane * 16, L_WQ + (4 * n + w2) * 1024);
        if (n == 12) { WAITVM(7); FLAG(has_unit && lane == 0 ? WS_FG + ((ufl * 2 + umat) * 16 + ukp) * 128 : NOWHERE, 1); }
        if (n == 27) { lds_wait(L, S_WK); lds_wait(L, S_KDONE); }
      });
      STAMP(6);
#pragma unroll
      for (int c = 0; c < 2; ++c) {
        LAS char* p = pw + c * 16 * 224 + 128;
        *(LAS u32x2*)(p) = u32x2{pk(t[c][0][0], t[c][0][1]), pk(t[c][0][2], g == 1 ? 1.0f : t[c][0][3])};
        *(LAS u32x2*)(p + 32) = u32x2{pk(t[c][1][0], t[c][1][1]), pk(t[c][1][2], t[c][1][3])};
      }
      lds_signal(L, S_V, w2);
      STAMP(7);
      WAITVM(1);
      lds_signal(L, S_WQ, w2);
      lds_wait(L, S_WQ);
      STAMP(9);
      project(L, L_WQ + lane * 16, 0, xf, w2, lane, t, [&](int n) __attribute__((always_inline)) {
        if (!(n & 1)) dma16(wos + (n >= 14 ? 56 * 896 : 0), wvo[(n >> 1) % 7], L_WO + (w2 * 14 + (n >> 1)) * 1024);
      });
      STAMP(10);
      u32x4 pq[2][2];
      feature_map(t, wpf, g, pq);
      asm volatile("" : "+v"(pq[0][0]), "+v"(pq[0][1]), "+v"(pq[1][0]), "+v"(pq[1][1]));
      WAITVM(0);
      lds_signal(L, S_WO, w2);
      STAMP(11);
      lds_wait(L, S_WO);
      WOF_READ();
      STAMP(24);
      {
        lds_wait(L, S_REPSEEN);
        const int t2 = tid - 256, fb = 7 * r + min(t2 >> 5, 6), cvo = (g & 1) * 7168 + (64 * (fb >> 2) + 4 * l15 + (fb & 3)) * 4;
        float c[14];
#pragma unroll
        for (int i = 0; i < 14; ++i) c[i] = __builtin_bit_cast(float, __builtin_amdgcn_raw_buffer_load_b32(rs, cvo, WS_CS + i * 14336, 16));
        const float s = (((c[0] + c[1]) + (c[2] + c[3])) + ((c[4] + c[5]) + (c[6] + c[7]))) + (((c[8] + c[9]) + (c[10] + c[11])) + (c[12] + c[13]));
        if (t2 < 224) *(LAS float*)(L + L_CS + t2 * 4) = s;
      }
      lds_wait(L, S_KVA);
      STAMP(27);
      u32x4 ka[4];
#pragma unroll
      for (int i = 0; i < 4; ++i) ka[i] = *(LAS u32x4*)(L + L_KVA + i * 1024 + lane * 16);
      f32x4 o[2][2];
#pragma unroll
      for (int c = 0; c < 4; ++c) o[c >> 1][c & 1] = mfma16(ka[2 * (c & 1) + 1], pq[c >> 1][1], mfma16(ka[2 * (c & 1)], pq[c >> 1][0], z4));
      const f32x2 da = swp16(o[0][0][3], o[0][0][3]), db = swp16(o[1][0][3], o[1][0][3]);
      const float den[2] = {swp32(da.y, da.y).x, swp32(db.y, db.y).x};
#pragma unroll
      for (int c = 0; c < 2; ++c) {
        const float rd = frcp(den[c] + 1e-6f), r3 = (g & 1) ? 0.f : rd;
        const u32x4 ob = {pk(o[c][0][0] * rd, o[c][0][1] * rd), pk(o[c][0][2] * rd, o[c][0][3] * r3), pk(o[c][1][0] * rd, o[c][1][1] * rd), pk(o[c][1][2] * rd, o[c][1][3] * r3)};
        ST16(rs, ob, lane * 16, WS_O + ((tile * 8 + 2 * w2 + c) * 16 + head) * 1024);
      }
      STAMP(12);
    }
  }
  DRAIN();
  BAR();
  if (!A && tid == 448) __hip_atomic_store((GAS u64*)(ws + WS_FO + (tile * 16 + head) * 128), tag + 1, RLX_AGENT);
  STAMP(13);
  u32x4 hres = {0u, 0u, 0u, 0u};
  const int tok = tile2 * 128 + tg * 64 + tb * 16 + l15;
  const int xo = tok * 896 + cg * 128 + kh * 64 + g * 16;
  if (wave > 0) for (int i = PF1 + wave - 1; i < PF2; i += 7) dma16c(gusrc + i * 1024, lane * 16, gu_dst(i));
  STAMP(23);
  if (r2 < 14) {
    const u32x4 xr = *(const u32x4*)(x + xo);
    if (A && wave == 0) { __builtin_amdgcn_s_sleep(DLY2); poll_flags(ws, WS_FO + (tile2 * 16 + l15) * 128, tag, 1); }
    BAR();
    STAMP(14);
    u32x4 of[8];
#pragma unroll
    for (int i = 0; i < 8; ++i) of[i] = __builtin_bit_cast(u32x4, __builtin_amdgcn_raw_buffer_load_b128(rs, lane * 16 + (i & 3) * 1024, WS_O + ((tile2 * 8 + tg * 4 + tb) * 16 + kh * 8 + (i & 4)) * 1024, 16));
    SCHED();
    f32x4 a[4] = {};
#pragma unroll
    for (int i = 0; i < 8; ++i)
#pragma unroll
      for (int k = 0; k < 4; ++k) a[k] = mfma16(wof[i][k], of[i], a[k]);
    *(LAS f32x4*)(L + L_EX + wave * 2048 + lane * 16) = kh ? a[0] : a[2];
    *(LAS f32x4*)(L + L_EX + wave * 2048 + 1024 + lane * 16) = kh ? a[1] : a[3];
    BAR();
    STAMP(15);
    const f32x4 c0 = (kh ? a[2] : a[0]) + *(LAS f32x4*)(L + L_EX + (wave ^ 4) * 2048 + lane * 16);
    const f32x4 c1 = (kh ? a[3] : a[1]) + *(LAS f32x4*)(L + L_EX + (wave ^ 4) * 2048 + 1024 + lane * 16);
    const f32x4 h0 = c0 + f32x4{blo(xr.x), bhi(xr.x), blo(xr.y), bhi(xr.y)}, h1 = c1 + f32x4{blo(xr.z), bhi(xr.z), blo(xr.w), bhi(xr.w)};
    hres = u32x4{pk(h0[0], h0[1]), pk(h0[2], h0[3]), pk(h1[0], h1[1]), pk(h1[2], h1[3])};
    ST16(rs, hres, WS_H + ((tok >> 5) * 28 + 4 * cg + 2 * kh + (g >> 1)) * 1024 + ((g & 1) * 32 + (tok & 31)) * 16, 0);
    const f32x4 r0 = {blo(hres.x), bhi(hres.x), blo(hres.y), bhi(hres.y)}, r1 = {blo(hres.z), bhi(hres.z), blo(hres.w), bhi(hres.w)};
    const f32x4 a1 = r0 + r1, a2q = r0 * r0 + r1 * r1;
    float s1 = (a1[0] + a1[1]) + (a1[2] + a1[3]), s2 = (a2q[0] + a2q[1]) + (a2q[2] + a2q[3]);
    { const float t = swadd16(s1, s2); const f32x2 a2 = sw32(t); s1 = a2.x + a2.y; }
    if (g < 2) __builtin_amdgcn_raw_buffer_store_b32(__builtin_bit_cast(u32, s1), rs, WS_ST + ((2 * cg + kh) * 2048 + tok) * 8 + g * 4, 0, 16);
    DRAIN();
    BAR();
    if (A && tid == 192) __hip_atomic_store((GAS u64*)(ws + WS_FH + (tile2 * 16 + r2) * 128), tag + 1, RLX_AGENT);
  } else {
    DRAIN();
    BAR();
  }
  STAMP(16);
  u32x4 wdf[2][8];
  const int wdo = WS_WD + (cg * 112 + 28 * w2) * 2048 + lane * 16;
  if (A) {
    if (wave == 0) {
      __builtin_amdgcn_s_sleep(DLY3);
      poll_flags(ws, WS_FH + (tile * 16 + (l15 < 14 ? l15 : 0)) * 128, tag, 1);
      *(volatile LAS u32x4*)(L + L_FLG + S_HSEEN * 16) = u32x4{1u, 1u, 1u, 1u};
    } else {
      lds_wait(L, S_HSEEN);
    }
  }
  STAMP(17);
  if constexpr (A) {
    const int T32 = tile * 4 + wave;
    f32x2 st[7];
#pragma unroll
    for (int i = 0; i < 7; ++i) st[i] = __builtin_bit_cast(f32x2, __builtin_amdgcn_raw_buffer_load_b64(rs, WS_ST + (7 * hh * 2048 + (lane & 31)) * 8, (i * 2048 + T32 * 32) * 8, 16));
    u32x4 hb[28];
#pragma unroll
    for (int s = 0; s < 28; ++s) hb[s] = __builtin_bit_cast(u32x4, __builtin_amdgcn_raw_buffer_load_b128(rs, lane * 16 + (s & 3) * 1024, WS_H + (T32 * 28 + (s & ~3)) * 1024, 16));
    u32 wb_[3] = {(u32)lane * 16, (u32)lane * 16 + 2 * SLOT, (u32)lane * 16 + 4 * SLOT};
    asm volatile("" : "+v"(wb_[1]), "+v"(wb_[2]));
#define WFRAG(sl, s) (*(LAS u32x4*)(L + wb_[(sl) >> 1] + ((sl) & 1) * SLOT + (s) * 1024))
    LAS char* csl = L + L_CS + hh * 16;
    u32x4 ring[7];
    f32x4 cq[4];
#pragma unroll
    for (int k = 0; k < 4; ++k) cq[k] = *(LAS f32x4*)(csl + k * 32);
#pragma unroll
    for (int s = 0; s < 7; ++s) ring[s] = WFRAG(0, s);
    SCHED();
    float s1 = ((st[0].x + st[1].x) + (st[2].x + st[3].x)) + ((st[4].x + st[5].x) + st[6].x);
    float s2 = ((st[0].y + st[1].y) + (st[2].y + st[3].y)) + ((st[4].y + st[5].y) + st[6].y);
    { f32x2 a = sw32(s1); s1 = a.x + a.y; a = sw32(s2); s2 = a.x + a.y; }
    const float nmu = s1 * (-1.f / 448.f), ve = fmaxf(s2 * (1.f / 448.f) - nmu * nmu, 0.f) + 1e-5f;
    const float rstd = __builtin_amdgcn_rsqf(ve);
    const float k0 = -2.30220819f * rstd, k1 = -0.10294324f * rstd * rstd * rstd;
    const int pvo = lane == 0 ? WS_FHH + ((tile * 16 + r) * 4 + wave) * 128 : 0x7ffffff0;
#define PROG(n) FLAG(pvo, n)
    const int hho = WS_HH + (T32 * 112 + 7 * r) * 1024;
    f32x16 acc[2];
    float y[8], ta = 0.f;
#pragma unroll
    for (int i = 0; i < 16; ++i) acc[0][i] = nmu * cq[i >> 2][i & 3];
    __builtin_amdgcn_s_setprio(GUPRIO);
    SCHED();
    auto gu_block = [&](auto JC) __attribute__((always_inline)) {
      constexpr int j = JC.value;
#pragma unroll
      for (int s = 0; s < 28; ++s) {
        acc[j & 1] = mfma32(ring[s % 7], hb[s], acc[j & 1]);
        if (s == 20 && (j + 1) * 28 <= PF2 && (j + 2) * 28 > PF2 && j < 4) lds_wait(L, S_WIN3);
        if (s == 20 && j == 4) lds_wait(L, S_REFILL);
        if (s + 7 < 28) ring[s % 7] = WFRAG(gu_slot(j), s + 7);
        else if (j < 6) ring[s % 7] = WFRAG(gu_slot(j + 1), s - 21);
        if (s == 0 && (j == 2 || j == 5)) lds_signal(L, j == 2 ? S_FREE01 : S_DONE4, wave);
        if (j > 0 && s >= 1 && s < 17) {
          const int i = (s - 1) >> 1;
          const float ga = acc[(j - 1) & 1][i];
          if ((s - 1) & 1) y[i] = ga * acc[(j - 1) & 1][i + 8] * frcp(fmaf(ta, ve, ve));
          else ta = fexp2(ga * fmaf(ga * ga, k1, k0));
        }
        if (j > 1 && s == 14) {
 WAITVM(0);
 PROG(j - 1); }
        if (j > 0 && s == 17) { const u32x4 yb = {pk(y[0], y[1]), pk(y[2], y[3]), pk(y[4], y[5]), pk(y[6], y[7])}; ST16(rs, yb, lane * 16, hho + (j - 1) * 1024); }
        if (j < 6 && s >= 16 && s < 20) cq[s - 16] = *(LAS f32x4*)(csl + (j + 1) * 128 + (s - 16) * 32);
        if (j < 6 && s >= 22 && s < 26) {
#pragma unroll
          for (int e = 0; e < 4; ++e) acc[(j + 1) & 1][4 * (s - 22) + e] = nmu * cq[s - 22][e];
        }
        SCHED();
      }
      if (j == 0) STAMP(18);
      if (j == 3) STAMP(26);
      if (j == 1) STAMP(28);
      if (j == 2) STAMP(29);
      if (j == 4) STAMP(30);
      if (j == 5) STAMP(31);
    };
    gu_block(IC<0>{}); gu_block(IC<1>{}); gu_block(IC<2>{}); gu_block(IC<3>{}); gu_block(IC<4>{}); gu_block(IC<5>{}); gu_block(IC<6>{});
#pragma unroll
    for (int i = 0; i < 8; ++i) { const float ga = acc[0][i]; y[i] = ga * acc[0][i + 8] * frcp(fmaf(fexp2(ga * fmaf(ga * ga, k1, k0)), ve, ve)); }
    { const u32x4 yb = {pk(y[0], y[1]), pk(y[2], y[3]), pk(y[4], y[5]), pk(y[6], y[7])}; ST16(rs, yb, lane * 16, hho + 6 * 1024); }
    WAITVM(1);
    PROG(6);
    WAITVM(1);
    PROG(7);
    __builtin_amdgcn_s_setprio(0);
    STAMP(19);
  } else {
    for (int i = PF2 + w2; i < 140; i += 4) dma16c(gusrc + i * 1024, lane * 16, gu_dst(i));
    if (r2 < 14) {
      poll_flags(ws, WS_FD + (cg * 64 + 14 * w2 + min(l15, 13)) * 128, tag, 1);
      lds_signal(L, S_WIN3, w2);
#pragma unroll
      for (int i = 0; i < 8; ++i) wdf[0][i] = LD16(rs, wdo + (7 * (i >> 1)) * 2048 + (i & 1) * 1024);
      SCHED();
      STAMP(25);
      lds_wait(L, S_HSEEN);
      __builtin_amdgcn_s_sleep(8);
#pragma unroll
      for (int i = 0; i < 8; ++i) wdf[1][i] = LD16(rs, wdo + (7 * (i >> 1) + 1) * 2048 + (i & 1) * 1024);
      SCHED();
    } else { WAITVM(0); lds_signal(L, S_WIN3, w2); }
    lds_wait(L, S_FREE01);
    for (int i = w2; i < 56; i += 4) dma16c(gusrc + 5 * SLOT + i * 1024, lane * 16, gu_dst(i));
    if (r2 >= 14) { WAITVM(0); lds_signal(L, S_REFILL, w2); }
    else {
      const u32 po = WS_FHH + ((tile2 * 16 + 4 * w2 + ((lane >> 1) & 3)) * 4 + 2 * tg + (lane & 1)) * 128;
      const int ho = WS_HH + ((tile2 * 4 + 2 * tg) * 112 + 28 * w2) * 1024 + lane * 16;
      f32x16 acc[2][2] = {};
      u32x4 hf[2][8];
      u32 have = 0;
#define NEED(n) do { if (have < (n)) { _Pragma("nounroll") for (u32 sp = 0; sp < SPIN_CAP; ++sp) { const u64 d = __hip_atomic_load((const GAS u64*)(ws + po), RLX_AGENT) - tag; \
        u32 c = d <= 8 ? (u32)d : 0u; c = min(c, (u32)__builtin_amdgcn_update_dpp(0, (int)c, 0x121, 0xf, 0xf, false)); c = min(c, (u32)__builtin_amdgcn_update_dpp(0, (int)c, 0x122, 0xf, 0xf, false)); \
        c = min(c, (u32)__builtin_amdgcn_update_dpp(0, (int)c, 0x124, 0xf, 0xf, false)); have = __builtin_amdgcn_readfirstlane(c); if ((n) == 1) lds_signal(L, S_REFILL, w2);   if (have >= (n)) break; } \
        __builtin_amdgcn_fence(__ATOMIC_ACQUIRE, "wavefront"); } } while (0)
      NEED(1);
#pragma unroll
      for (int i = 0; i < 8; ++i) hf[0][i] = LD16(rs, ho + (i & 1) * 114688 + (7 * (i >> 1)) * 1024);
      SCHED();
#pragma unroll
      for (int j = 0; j < 7; ++j) {
        if (j < 6) {
          NEED(j + 2);
#pragma unroll
          for (int i = 0; i < 8; ++i) hf[(j + 1) & 1][i] = LD16(rs, ho + (i & 1) * 114688 + (7 * (i >> 1) + j + 1) * 1024);
          SCHED();
        }
#pragma unroll
        for (int i = 0; i < 4; ++i)
#pragma unroll
          for (int T = 0; T < 2; ++T)
#pragma unroll
            for (int k = 0; k < 2; ++k) acc[T][k] = mfma32(wdf[j & 1][2 * i + k], hf[j & 1][2 * i + T], acc[T][k]);
        SCHED();
        if (j < 5) {
#pragma unroll
          for (int i = 0; i < 8; ++i) wdf[j & 1][i] = LD16(rs, wdo + (7 * (i >> 1) + j + 2) * 2048 + (i & 1) * 1024);
          SCHED();
        }
        if (j == 0) STAMP(18);
        if (j == 3) STAMP(26);
        if (j == 5) STAMP(19);
      }
      lds_wait(L, S_DONE4);
#pragma unroll
      for (int T = 0; T < 2; ++T)
#pragma unroll
        for (int k = 0; k < 2; ++k)
#pragma unroll
          for (int q = 0; q < 4; ++q)
            *(LAS f32x4*)(L + L_RED + (((w2 * 2 + T) * 2 + k) * 4 + q) * 1024 + lane * 16) = f32x4{acc[T][k][4 * q], acc[T][k][4 * q + 1], acc[T][k][4 * q + 2], acc[T][k][4 * q + 3]};
      STAMP(20);
    }
  }
  if (r2 < 14) {
    BAR();
    float c[8] = {blo(hres.x), bhi(hres.x), blo(hres.y), bhi(hres.y), blo(hres.z), bhi(hres.z), blo(hres.w), bhi(hres.w)};
    LAS char* rq = L + L_RED + ((tb >> 1) * 8 + kh) * 1024 + ((g >> 1) * 32 + (tb & 1) * 16 + l15) * 16 + (g & 1) * 8;
#pragma unroll
    for (int w = 0; w < 4; ++w)
#pragma unroll
      for (int e = 0; e < 4; ++e) { const f32x2 v = *(LAS f32x2*)(rq + w * 16384 + (e >> 1) * 4096 + (e & 1) * 2048); c[e] += v.x; c[4 + e] += v.y; }
    *(u32x4*)(out + xo) = u32x4{pk(c[0], c[1]), pk(c[2], c[3]), pk(c[4], c[5]), pk(c[6], c[7])};
    if (A && b == 0 && tid == 0) *(u64*)(ws + WS_E) = tag;
  }
  STAMP(21);
  };
  if (wave < 4) role(IC<1>{}); else role(IC<0>{});
}

extern "C" void kernel_launch(void* const* d_in, const int* in_sizes, int n_in, void* d_out, int out_size, void* d_ws, size_t ws_size, hipStream_t stream) {
  (void)hipFuncSetAttribute((const void*)fused_block, hipFuncAttributeMaxDynamicSharedMemorySize, LDS_BYTES);
  fused_block<<<256, 512, LDS_BYTES, stream>>>((const char*)d_in[0], (const char*)d_in[1], (const char*)d_in[2], (const char*)d_in[3], (const char*)d_in[5], (const char*)d_in[6],
                                               (const char*)d_in[7], (char*)d_out, (char*)d_ws);
}
```

```cpp
#define PROBE 0
#include <hip/hip_runtime.h>
#include <hip/hip_bf16.h>
#include <stdint.h>

typedef unsigned u32;
typedef unsigned long long u64;
typedef u32 u32x4 __attribute__((ext_vector_type(4)));
typedef u32 u32x2 __attribute__((ext_vector_type(2)));
typedef float f32x2 __attribute__((ext_vector_type(2)));
typedef float f32x4 __attribute__((ext_vector_type(4)));
typedef float f32x16 __attribute__((ext_vector_type(16)));
typedef short s16x4 __attribute__((ext_vector_type(4)));
typedef __bf16 bf16x2 __attribute__((ext_vector_type(2)));
typedef __bf16 bf16x8 __attribute__((ext_vector_type(8)));
#define LAS __attribute__((address_space(3)))
#define GAS __attribute__((address_space(1)))
#define DI __device__ __forceinline__
#define RLX_AGENT __ATOMIC_RELAXED, __HIP_MEMORY_SCOPE_AGENT

constexpr int WS_E = 0;
constexpr int WS_FKV = 8192;
constexpr int WS_FO = WS_FKV + 32768;
constexpr int WS_FH = WS_FO + 32768;
constexpr int WS_FQ = WS_FH + 32768;
constexpr int WS_FG = WS_FQ + 43008;
constexpr int WS_FD = WS_FG + 114688;
constexpr int WS_FHH = WS_FD + 57344;
constexpr int WS_CS = WS_FHH + 131072;
constexpr int WS_CSQ = WS_CS + 200704;
constexpr int WS_ST = WS_CSQ + 75264;
constexpr int WS_WQKV = WS_ST + 229376;
constexpr int WS_WGU = WS_WQKV + 1376256;
constexpr int WS_WD = WS_WGU + 3211264;
constexpr int WS_KVP = WS_WD + 1605632;
constexpr int WS_O = WS_KVP + 1048576;
constexpr int WS_H = WS_O + 2097152;
constexpr int WS_HH = WS_H + 1835008;
constexpr int WS_STAMP = WS_HH + 7340032;
constexpr int WS_LOG = WS_STAMP + 256 * 8 * 32 * 8;
constexpr int WS_USED = WS_LOG + 4096;
constexpr int SLOT = 28672;
constexpr int L_WK = 4 * SLOT, L_WV = 0, L_WQ = SLOT;
constexpr int L_PKV = 4 * SLOT;
constexpr int L_WO = 2 * SLOT;
constexpr int L_EX = 5 * SLOT;
constexpr int L_KVA = 5 * SLOT;
constexpr int L_ST1 = 5 * SLOT + 4096;
constexpr int L_WPHI = 5 * SLOT + 5120;
constexpr int L_CS1 = 5 * SLOT + 7168;
constexpr int L_CS = 159744;
constexpr int L_FLG = 160768;
constexpr int L_JUNK = 161280;
constexpr int L_STAMP = 161536;
constexpr int L_RED = SLOT;
constexpr int LDS_BYTES = 163840;
constexpr int gu_slot(int j) { return j == 0 || j == 5 || j > 6 ? 0 : (j == 1 || j == 6 ? 4 : j - 1); }
constexpr int CODE_PF = 38912;
constexpr u32 SPIN_CAP = 1u << 16;

#define STAMP(i) do { if ((i) == PROBE) pt_ = __builtin_amdgcn_s_memrealtime(); } while (0)

DI f32x4 mfma16(u32x4 a, u32x4 b, f32x4 c) { return __builtin_amdgcn_mfma_f32_16x16x32_bf16(__builtin_bit_cast(bf16x8, a), __builtin_bit_cast(bf16x8, b), c, 0, 0, 0); }
DI f32x16 mfma32(u32x4 a, u32x4 b, f32x16 c) { return __builtin_amdgcn_mfma_f32_32x32x16_bf16(__builtin_bit_cast(bf16x8, a), __builtin_bit_cast(bf16x8, b), c, 0, 0, 0); }
DI u32 pk(float lo, float hi) { f32x2 p = {lo, hi}; return __builtin_bit_cast(u32, __builtin_convertvector(p, bf16x2)); }
DI float blo(u32 v) { return __builtin_bit_cast(float, v << 16); }
DI float bhi(u32 v) { return __builtin_bit_cast(float, v & 0xffff0000u); }
DI float dot2(u32 a, u32 b, float c) { return __builtin_amdgcn_fdot2_f32_bf16(__builtin_bit_cast(bf16x2, a), __builtin_bit_cast(bf16x2, b), c, false); }
DI u32x2 trread(LAS char* p) { return __builtin_bit_cast(u32x2, __builtin_amdgcn_ds_read_tr16_b64_v4i16((LAS s16x4*)p)); }
DI u32x4 trfrag(LAS char* p, int step) { u32x2 a = trread(p), b = trread(p + step); return u32x4{a.x, a.y, b.x, b.y}; }
DI float fexp2(float x) { return __builtin_amdgcn_exp2f(x); }
DI float frcp(float x) { return __builtin_amdgcn_rcpf(x); }
template <int C> DI float dppror(float x) { return __builtin_bit_cast(float, __builtin_amdgcn_update_dpp(0, __builtin_bit_cast(int, x), 0x120 + C, 0xf, 0xf, false)); }
DI float row_max(float x) { x = fmaxf(x, dppror<8>(x)); x = fmaxf(x, dppror<4>(x)); x = fmaxf(x, dppror<2>(x)); return fmaxf(x, dppror<1>(x)); }
DI float row_min(float x) { x = fminf(x, dppror<8>(x)); x = fminf(x, dppror<4>(x)); x = fminf(x, dppror<2>(x)); return fminf(x, dppror<1>(x)); }
DI float row_sum(float x) { x += dppror<8>(x); x += dppror<4>(x); x += dppror<2>(x); return x + dppror<1>(x); }
DI f32x2 sw16(float x) { u32 u = __builtin_bit_cast(u32, x); auto r = __builtin_amdgcn_permlane16_swap(u, u, false, false); return f32x2{__builtin_bit_cast(float, (u32)r[0]), __builtin_bit_cast(float, (u32)r[1])}; }
DI f32x2 sw32(float x) { u32 u = __builtin_bit_cast(u32, x); auto r = __builtin_amdgcn_permlane32_swap(u, u, false, false); return f32x2{__builtin_bit_cast(float, (u32)r[0]), __builtin_bit_cast(float, (u32)r[1])}; }
DI float grp_sum(float x) { f32x2 a = sw16(x); x = a.x + a.y; a = sw32(x); return a.x + a.y; }
DI float grp_max(float x) { f32x2 a = sw16(x); x = fmaxf(a.x, a.y); a = sw32(x); return fmaxf(a.x, a.y); }
DI float grp_min(float x) { f32x2 a = sw16(x); x = fminf(a.x, a.y); a = sw32(x); return fminf(a.x, a.y); }

#define BAR() asm volatile("s_waitcnt lgkmcnt(0)\n\ts_barrier" ::: "memory")
#define WAITVM(n) asm volatile("s_waitcnt vmcnt(" #n ")" ::: "memory")
#define DRAIN() do { asm volatile("s_waitcnt vmcnt(0)" ::: "memory"); __builtin_amdgcn_s_waitcnt(0x0F70); } while (0)
#define SCHED() __builtin_amdgcn_sched_barrier(0)
DI void dma16(const void* base, u32 voff, u32 lds) {
  u32 keep;
  asm volatile("s_mov_b32 %0, m0\n\ts_mov_b32 m0, %3\n\ts_nop 0\n\tglobal_load_lds_dwordx4 %1, %2\n\ts_mov_b32 m0, %0" : "=&s"(keep) : "v"(voff), "s"(base), "s"(lds) : "memory");
}
DI void dma16c(const void* base, u32 voff, u32 lds) {
  u32 keep;
  asm volatile("s_mov_b32 %0, m0\n\ts_mov_b32 m0, %3\n\ts_nop 0\n\tglobal_load_lds_dwordx4 %1, %2 sc1\n\ts_mov_b32 m0, %0" : "=&s"(keep) : "v"(voff), "s"(base), "s"(lds) : "memory");
}
DI void dma4(const void* base, u32 voff, u32 lds) {
  u32 keep;
  asm volatile("s_mov_b32 %0, m0\n\ts_mov_b32 m0, %3\n\ts_nop 0\n\tglobal_load_lds_dword %1, %2\n\ts_mov_b32 m0, %0" : "=&s"(keep) : "v"(voff), "s"(base), "s"(lds) : "memory");
}
#define LD16(rs, off) __builtin_bit_cast(u32x4, __builtin_amdgcn_raw_buffer_load_b128(rs, off, 0, 16))
#define ST16(rs, v, off, soff) do { const u32x4 d_ = __builtin_bit_cast(u32x4, v); __builtin_amdgcn_raw_buffer_store_b128(d_, rs, off, soff, 16); asm volatile("s_nop 1" :: "v"(d_) : "memory"); } while (0)

#define FLAG(vo, n) __builtin_amdgcn_raw_buffer_store_b64(__builtin_bit_cast(u32x2, tag + (n)), rs, vo, 0, 16)
constexpr int NOWHERE = 0x7ffffff0;
DI void poll_flags(const char* ws, u32 off, u64 tag, u32 need) {
#pragma nounroll
  for (u32 sp = 0; sp < SPIN_CAP; ++sp) {
    u64 d = __hip_atomic_load((const GAS u64*)(ws + off), RLX_AGENT) - tag;
    if (__all(d >= need && d <= 8)) break;
  }
  __builtin_amdgcn_fence(__ATOMIC_ACQUIRE, "wavefront");
}
DI void lds_wait(LAS char* L, int sig) {
#pragma nounroll
  for (u32 sp = 0; sp < (SPIN_CAP << 4); ++sp) {
    const u32x4 v = *(volatile LAS u32x4*)(L + L_FLG + sig * 16);
    if (__builtin_amdgcn_readfirstlane(v.x & v.y & v.z & v.w)) break;
    __builtin_amdgcn_s_sleep(1);
  }
  asm volatile("" ::: "memory");
}
DI void lds_signal(LAS char* L, int sig, int w) {
  asm volatile("" ::: "memory");
  *(volatile LAS u32*)(L + L_FLG + sig * 16 + w * 4) = 1u;
}

template <int N> struct IC { static constexpr int value = N; };
constexpr float LOG2E = 1.4426950408889634f;
template <class F> DI void project(LAS char* L, u32 wb, int sec, const u32x4 (&xf)[2][14], int tp, int lane, f32x4 (&t)[2][2], F&& mid) {
  asm volatile("" : "+v"(wb));
  u32x4 ring[7];
#pragma unroll
  for (int n = 0; n < 7; ++n) ring[n] = *(LAS u32x4*)(L + wb + n * 1024);
#pragma unroll
  for (int i = 0; i < 4; ++i) t[i >> 1][i & 1] = f32x4{0.f, 0.f, 0.f, 0.f};
  SCHED();
#pragma unroll
  for (int n = 0; n < 28; ++n) {
    const u32x4 f = ring[n % 7];
    t[0][n & 1] = mfma16(f, xf[0][n >> 1], t[0][n & 1]);
    t[1][n & 1] = mfma16(f, xf[1][n >> 1], t[1][n & 1]);
    if (n + 7 < 28) ring[n % 7] = *(LAS u32x4*)(L + wb + (n + 7) * 1024);
    mid(n);
    SCHED();
  }
  f32x4 cs[2];
#pragma unroll
  for (int k = 0; k < 2; ++k) cs[k] = *(LAS f32x4*)(L + L_CS1 + sec * 128 + k * 64 + (lane >> 4) * 16);
#pragma unroll
  for (int tb = 0; tb < 2; ++tb) {
    const f32x2 st = *(LAS f32x2*)(L + L_ST1 + (tp * 32 + tb * 16 + (lane & 15)) * 8);
#pragma unroll
    for (int k = 0; k < 2; ++k) {
      t[tb][k] = (t[tb][k] + cs[k] * st.x) * st.y;
      t[tb][k][3] = (lane & 16) ? 0.f : t[tb][k][3];
    }
  }
}
DI f32x2 swp16(float a, float b) { auto r = __builtin_amdgcn_permlane16_swap(__builtin_bit_cast(u32, a), __builtin_bit_cast(u32, b), false, false); return f32x2{__builtin_bit_cast(float, (u32)r[0]), __builtin_bit_cast(float, (u32)r[1])}; }
DI f32x2 swp32(float a, float b) { auto r = __builtin_amdgcn_permlane32_swap(__builtin_bit_cast(u32, a), __builtin_bit_cast(u32, b), false, false); return f32x2{__builtin_bit_cast(float, (u32)r[0]), __builtin_bit_cast(float, (u32)r[1])}; }
DI float swadd32(float a, float b) { const f32x2 s = swp32(a, b); return s.x + s.y; }
DI float swadd16(float a, float b) { const f32x2 s = swp16(a, b); return s.x + s.y; }
DI float mx2(float a, float b) { float r; asm("v_max_f32 %0, %1, %2" : "=v"(r) : "v"(a), "v"(b)); return r; }
DI float mx3(float a, float b, float c) { return fmaxf(fmaxf(a, b), c); }
DI float mn3(float a, float b, float c) { return fminf(fminf(a, b), c); }
DI void feature_map(const f32x4 (&t)[2][2], const u32x4 (&wpf)[2], int g, u32x4 (&P)[2][2]) {
  const f32x4 z4 = {0.f, 0.f, 0.f, 0.f};
  const bool v23 = g < 3;
  float p[2][8], m[4];
#pragma unroll
  for (int tb = 0; tb < 2; ++tb) {
    const u32x4 tq = {pk(t[tb][0][0], t[tb][0][1]), pk(t[tb][0][2], t[tb][0][3]), pk(t[tb][1][0], t[tb][1][1]), pk(t[tb][1][2], t[tb][1][3])};
    const f32x4 p0 = mfma16(wpf[0], tq, z4), p1 = mfma16(wpf[1], tq, z4);
#pragma unroll
    for (int i = 0; i < 8; ++i) { const float v = i < 4 ? p0[i] : p1[i - 4]; p[tb][i] = ((i & 3) < 2 || v23) ? v : p0[0]; }
    m[2 * tb] = mx3(mx3(p[tb][0], p[tb][1], p[tb][2]), mx3(p[tb][3], p[tb][4], p[tb][5]), mx3(p[tb][6], p[tb][7], p[tb][0]));
    m[2 * tb + 1] = -mn3(mn3(p[tb][0], p[tb][1], p[tb][2]), mn3(p[tb][3], p[tb][4], p[tb][5]), mn3(p[tb][6], p[tb][7], p[tb][0]));
  }
  {
    const f32x2 a = swp16(m[0], m[1]), b = swp16(m[2], m[3]);
    const f32x2 c = swp32(mx2(a.x, a.y), mx2(b.x, b.y));
    const float d = mx2(c.x, c.y) * -LOG2E;
    const f32x2 e = swp32(d, d), f0 = swp16(e.x, e.x), f1 = swp16(e.y, e.y);
    m[0] = f0.x; m[1] = f0.y; m[2] = f1.x; m[3] = f1.y;
  }
  float ex[4][8], sm[4];
#pragma unroll
  for (int c = 0; c < 4; ++c) {
#pragma unroll
    for (int i = 0; i < 8; ++i) {
      ex[c][i] = fexp2(fmaf(p[c >> 1][i], (c & 1) ? -LOG2E : LOG2E, m[c]));
      if ((i & 3) >= 2) ex[c][i] = v23 ? ex[c][i] : 0.f;
    }
    sm[c] = ((ex[c][0] + ex[c][1]) + (ex[c][2] + ex[c][3])) + ((ex[c][4] + ex[c][5]) + (ex[c][6] + ex[c][7]));
  }
  {
    const float d = frcp(swadd32(swadd16(sm[0], sm[1]), swadd16(sm[2], sm[3])));
    const f32x2 e = swp32(d, d), f0 = swp16(e.x, e.x), f1 = swp16(e.y, e.y);
    sm[0] = f0.x; sm[1] = f0.y; sm[2] = f1.x; sm[3] = f1.y;
  }
#pragma unroll
  for (int c = 0; c < 4; ++c)
#pragma unroll
    for (int e = 0; e < 4; ++e) P[c >> 1][c & 1][e] = pk(ex[c][2 * e] * sm[c], ex[c][2 * e + 1] * sm[c]);
}
DI u32x4 piece(const u32x2 (&rp)[8], int e) {
  u32x4 f;
#pragma unroll
  for (int m = 0; m < 4; ++m) f[m] = __builtin_amdgcn_perm(rp[2 * m + 1][e >> 1], rp[2 * m][e >> 1], (e & 1) ? 0x07060302u : 0x05040100u);
  return f;
}
#define RP_OPS "+v"(rp[0]), "+v"(rp[1]), "+v"(rp[2]), "+v"(rp[3]), "+v"(rp[4]), "+v"(rp[5]), "+v"(rp[6]), "+v"(rp[7])
#define RP_LOAD(lvo, addr) _Pragma("unroll") for (int j = 0; j < 8; ++j) asm volatile("global_load_dwordx2 %0, %1, %2" : "=v"(rp[j]) : "v"(lvo), "s"(addr) : "memory")
#define PF1 56
#define PF2 112
#define DLY2 8
#define DLY3 8
#define GUPRIO 2
enum { S_FREE01 = 0, S_REFILL, S_DONE4, S_WIN3, S_WK, S_WV, S_WQ, S_WSEEN, S_KDONE, S_V, S_PHIK, S_KVP, S_KVSEEN, S_KVA, S_REP, S_WO, S_REPSEEN, S_HSEEN };
DI u32 gu_dst(int i) { return i < 28 ? i * 1024 : (i < 56 ? 3 * SLOT + i * 1024 : i * 1024 - SLOT); }

__global__ void __launch_bounds__(512, 2) fused_block(const char* __restrict__ x, const char* __restrict__ wqkv, const char* __restrict__ wphi, const char* __restrict__ wo,
                                                      const char* __restrict__ wg, const char* __restrict__ wu, const char* __restrict__ wd, char* __restrict__ out, char* ws) {
  extern __shared__ __attribute__((aligned(16))) char smem[];
  LAS char* L = (LAS char*)smem;
  const int tid = threadIdx.x, lane = tid & 63, wave = __builtin_amdgcn_readfirstlane(tid >> 6), w2 = wave & 3;
  const int b = blockIdx.x, li = b >> 3, xp = (b & 7) >> 1, xh = b & 1;
  const int tile = 4 * xp + (li >> 3), r = 8 * xh + (li & 7);
  const int tile2 = 4 * xp + 2 * xh + (li >> 4), r2 = li & 15;
  const int tg = r2 >= 7 ? 1 : 0, cg = r2 - 7 * tg;
  const int l15 = lane & 15, g = lane >> 4, q4 = l15 >> 2, p4 = lane & 3, hh = lane >> 5;
  const int head = r;
  asm volatile("" :: "s"(x), "s"(wqkv), "s"(wphi), "s"(wo), "s"(wg), "s"(wu), "s"(wd), "s"(out), "s"(ws));
  const u64 p0_ = __builtin_amdgcn_s_memrealtime(); u64 pt_ = p0_;
  STAMP(0);
  const __amdgpu_buffer_rsrc_t rs = __builtin_amdgcn_make_buffer_rsrc((void*)ws, 0, WS_USED, 0x00020000);
  const u32x4 ones = {0x3f803f80u, 0x3f803f80u, 0x3f803f80u, 0x3f803f80u};
  const f32x4 z4 = {0.f, 0.f, 0.f, 0.f};
  auto role = [&](auto AC) __attribute__((always_inline)) {
  constexpr bool A = AC.value;
  u64 epoch;
  if (A) asm volatile("s_load_dwordx2 %0, %1, 0x0 ; role A" : "=s"(epoch) : "s"(ws) : "memory");
  else asm volatile("s_load_dwordx2 %0, %1, 0x0 ; role B" : "=s"(epoch) : "s"(ws) : "memory");
  __builtin_amdgcn_s_waitcnt(0x0F70);
  u32x2 rp[8];
  const int uidx = A ? b * 2 + (wave & 1) : b * 4 + w2, unit = uidx * 49 >> 6;
  const bool has_unit = (((uidx + 1) * 49) >> 6) != unit;
  const int umat = unit & 1, ukp = (unit >> 1) / 28, ufl = (unit >> 1) - 28 * ukp, un = unit / 7, uc = unit - 7 * un;
  if (A && wave < 2) {
    const int u = b + 256 * wave;
    if (wave == 1) { dma16(wphi + head * 1568, lane * 16, L_WPHI); dma16(wphi + head * 1568, min(lane + 64, 97) * 16, L_WPHI + 1024); }
    if (u < 294) {
      const int sl = u / 14, kp = u - 14 * sl, sl2 = sl >= 14 ? sl - 14 : sl + 7, sec = sl2 / 7, ln = sl2 - 7 * sec;
      const u32 lvo = g * 8 * 2688 + l15 * 8;
      RP_LOAD(lvo, wqkv + (kp * 32 + j) * 2688 + sl2 * 128);
      const int Q = 16 * ln + l15, h2 = Q / 7;
      const int svo = h2 * 86016 + g * 256 + (Q - 7 * h2) * 16;
      asm volatile("s_waitcnt vmcnt(0) lgkmcnt(0)" : RP_OPS, "+s"(epoch)::"memory");
      f32x4 c;
#pragma unroll
      for (int e = 0; e < 4; ++e) {
        const u32x4 f = piece(rp, e);
        ST16(rs, f, svo + (e >> 1) * 1024 + (e & 1) * 128, WS_WQKV + sec * SLOT + kp * 2048);
        c[e] = mfma16(ones, f, z4)[0];
      }
      ST16(rs, c, g == 0 ? l15 * 16 : 0x7ffffff0, WS_CSQ + kp * 5376 + sl2 * 256);
      DRAIN();
      if (lane == 0) __hip_atomic_store((GAS u64*)(ws + WS_FQ + (sl2 * 16 + kp) * 128), epoch * 6364136223846793005ull + 1442695040888963408ull, RLX_AGENT);
    } else {
      WAITVM(0);
    }
    STAMP(1);
  } else {
    {
      u64 pc; asm volatile("s_getpc_b64 %0" : "=s"(pc));
      if (A) dma4(x + (xp * 512 + li * 16) * 896 + (wave & 1) * 7168, min(lane, 55) * 128, L_JUNK);
      else if (wave == 4) dma4((const char*)(pc & ~127ull), min((li % 5) * 8192 + lane * 128, CODE_PF), L_JUNK);
    }
    const u32 vo = (lane >> 3) * 896 + (((lane & 7) ^ (lane >> 3)) * 16);
    for (int n = wave - 2; n < 112; n += 6) {
      const int gr = n / 14, s = n - 14 * gr;
      dma16(x + (tile * 128 + gr * 16 + (s & 1) * 8) * 896 + (s >> 1) * 128, vo, n * 1024);
    }
    if (!A) { const u32 lvo = g * 8 * 3584 + l15 * 8; RP_LOAD(lvo, (umat ? wu : wg) + (ukp * 32 + j) * 3584 + ufl * 128); }
    else { const u32 lvo = (16 * (g & 1) + (g >> 1)) * 896 + l15 * 8; RP_LOAD(lvo, wd + (64 * (un >> 1) + 2 * (un & 1) + (j & 3) * 4 + (j >> 2) * 32) * 896 + uc * 128); }
    if (!A && wave >= 6) dma4(wo + li * 12544 + (wave & 1) * 6272, min(lane, 48) * 128, L_JUNK);
    if (!A && wave == 7) { *(LAS u32*)(L + L_FLG + lane * 4) = 0u; *(LAS u32*)(L + L_FLG + 256 + lane * 4) = 0u; }
    STAMP(1);
    if (!A && wave >= 6) WAITVM(9); else WAITVM(8);
  }
  asm volatile("s_waitcnt lgkmcnt(0)\n\ts_barrier" : "+s"(epoch)::"memory");
  STAMP(2);
  const u64 tag = epoch * 6364136223846793005ull + 1442695040888963407ull;
  const char* gusrc = ws + WS_WGU + 7 * r * SLOT;
  const int tb = w2; constexpr int kh = A ? 0 : 1;
  u32x4 wof[8][4];
  u32 wb0 = L_WO + (kh * 224 + (g >> 1) * 7 + (g & 1) * 4 + ((q4 == 3 && (g & 1)) ? 2 : q4)) * 128;
  wb0 += 16 * p4 + ((wb0 >> 2) & 64);
#define WOF_READ() do { u32 wa_ = wb0, wc_ = wb0 ^ 64; asm volatile("" : "+v"(wa_), "+v"(wc_)); \
    _Pragma("unroll") for (int i = 0; i < 8; ++i) _Pragma("unroll") for (int k = 0; k < 4; ++k) { \
      const u32x2 a_ = trread(L + (k >> 1 ? wc_ : wa_) + 28 * i * 128 + (k & 1) * 8), c_ = trread(L + (k >> 1 ? wa_ : wc_) + (28 * i + 14) * 128 + (k & 1) * 8); \
      wof[i][k] = u32x4{a_.x, a_.y, c_.x, c_.y}; } } while (0)
  {
    u32x4 xf[2][14];
    {
      LAS char* xr0 = L + w2 * 28672 + l15 * 128 + ((g ^ (l15 & 7)) * 16);
      LAS char* xr1 = L + w2 * 28672 + l15 * 128 + (((4 + g) ^ (l15 & 7)) * 16);
#pragma unroll
      for (int s = 0; s < 28; ++s) xf[s / 14][s % 14] = *(LAS u32x4*)((s & 1 ? xr1 : xr0) + (s >> 1) * 2048);
    }
    u64 fq0, fq1;
    const u32 fqo = WS_FQ + ((((g & 1) ? 28 * head + 27 : 28 * head) >> 6) * 16 + min(l15, 13)) * 128;
    if (A && wave == 0) asm volatile("global_load_dwordx2 %0, %2, %4 sc1\n\tglobal_load_dwordx2 %1, %3, %4 sc1" : "=&v"(fq0), "=&v"(fq1) : "v"(fqo + (1 + hh) * 14336), "v"(fqo), "s"(ws) : "memory");
    BAR();
    STAMP(3);
    if (A && wave == 0) {
      asm volatile("s_waitcnt vmcnt(0)" : "+v"(fq0), "+v"(fq1)::"memory");
      if (!__all(fq0 == tag + 1 && fq1 == tag + 1)) { poll_flags(ws, fqo + (1 + hh) * 14336, tag, 1); poll_flags(ws, fqo, tag, 1); }
      __builtin_amdgcn_fence(__ATOMIC_ACQUIRE, "wavefront");
      *(volatile LAS u32x4*)(L + L_FLG + S_WSEEN * 16) = u32x4{1u, 1u, 1u, 1u};
      STAMP(27);
    }
    float csq[14];
    {
      const char* wsrc = ws + WS_WQKV + head * 86016 + w2 * 1024 + (A ? 1 : 2) * SLOT;
      if (A && wave == 0) {
#pragma unroll
        for (int i = 0; i < 7; ++i) dma16c(wsrc + i * 4096, lane * 16, L_WK + 4 * i * 1024);
      }
      f32x4 g1 = z4, g2 = z4;
#pragma unroll
      for (int s = 0; s < 14; ++s) { g1 = mfma16(ones, xf[A ? 0 : 1][s], g1); g2 = mfma16(xf[A ? 0 : 1][s], xf[A ? 0 : 1][s], g2); }
      asm volatile("" : "+v"(g1), "+v"(g2));
      if (!A || wave > 0) {
        lds_wait(L, S_WSEEN);
        if (!A) STAMP(8);
#pragma unroll
        for (int i = 0; i < 7; ++i) dma16c(wsrc + i * 4096, lane * 16, (A ? L_WK : L_WV) + (4 * i + w2) * 1024);
      }
      if (!A && wave < 6) {
        const u32 co = WS_CSQ + head * 112 + ((wave == 4 ? 1 + hh : 0) * 448 + min(4 * (l15 & 7) + 2 * (g & 1) + (l15 >> 3), 27)) * 4;
#pragma unroll
        for (int i = 0; i < 14; ++i) asm volatile("global_load_dword %0, %1, %2 sc1" : "=v"(csq[i]) : "v"(co), "s"(ws + i * 5376) : "memory");
      }
      const float d01 = (p4 & 1) ? g2[1] : g2[0], d23 = (p4 & 1) ? g2[3] : g2[2], s2 = (p4 & 2) ? d23 : d01;
      const float nmu = g1[0] * (-1.f / 448.f), var = fmaxf(s2 * (1.f / 448.f) - nmu * nmu, 0.f);
      if (g == q4) *(LAS f32x2*)(L + L_ST1 + (w2 * 32 + kh * 16 + l15) * 8) = f32x2{nmu, __builtin_amdgcn_rsqf(var + 1e-5f)};
    }
    u32x4 wpf[2];
    {
      u32 wp[8];
#pragma unroll
      for (int j = 0; j < 8; ++j) {
        wp[j] = *(LAS u32*)(L + L_WPHI + (16 * (g & 1) + (g >> 1)) * 56 + l15 * 4 + (4 * (j & 3) + 2 * (j >> 2)) * 56);
        if (l15 >= 14 || ((j & 3) == 3 && (g & 1))) wp[j] = 0u;
      }
#pragma unroll
      for (int e = 0; e < 4; ++e) {
        wpf[0][e] = __builtin_amdgcn_perm(wp[2 * e + 1], wp[2 * e], 0x05040100u);
        wpf[1][e] = __builtin_amdgcn_perm(wp[2 * e + 1], wp[2 * e], 0x07060302u);
      }
      asm volatile("" : "+v"(wpf[0]), "+v"(wpf[1]));
    }
    STAMP(4);
    LAS char* pw = L + L_PKV + (w2 * 32 + l15) * 224 + g * 8;
    if constexpr (A) {
      WAITVM(0);
      lds_signal(L, S_WK, w2);
      lds_wait(L, S_WK);
      STAMP(5);
      __builtin_amdgcn_s_setprio(2);
      f32x4 t[2][2];
      project(L, L_WK + lane * 16, 1, xf, w2, lane, t, [&](int n) __attribute__((always_inline)) { if (n == 27) { lds_signal(L, S_KDONE, w2); lds_wait(L, S_WV); } });
      STAMP(6);
      u32x4 P[2][2];
      feature_map(t, wpf, g, P);
      asm volatile("" : "+v"(P[0][0]), "+v"(P[0][1]), "+v"(P[1][0]), "+v"(P[1][1]));
      lds_wait(L, S_KDONE);
#pragma unroll
      for (int c = 0; c < 4; ++c) {
        LAS char* p = pw + (c >> 1) * 16 * 224 + (c & 1) * 64;
        *(LAS u32x2*)(p) = u32x2{P[c >> 1][c & 1].x, P[c >> 1][c & 1].y}; *(LAS u32x2*)(p + 32) = u32x2{P[c >> 1][c & 1].z, P[c >> 1][c & 1].w};
      }
      lds_signal(L, S_PHIK, w2);
      STAMP(7);
      u32 pr = L_PKV + (4 * g + q4) * 224 + 8 * p4;
      asm volatile("" : "+v"(pr));
      lds_wait(L, S_V);
      u32x4 pb[8];
#pragma unroll
      for (int m = 0; m < 8; ++m) pb[m] = trfrag(L + pr + (m >> 1) * 32 * 224 + 128 + (m & 1) * 32, 16 * 224);
      lds_wait(L, S_PHIK);
      u32x4 pa[4];
#pragma unroll
      for (int m = 0; m < 4; ++m) pa[m] = trfrag(L + pr + w2 * 32 + m * 32 * 224, 16 * 224);
      f32x4 kv[2] = {};
#pragma unroll
      for (int m = 0; m < 8; ++m) kv[m & 1] = mfma16(pa[m >> 1], pb[m], kv[m & 1]);
      const u32x4 kvb = {pk(kv[0][0], kv[0][1]), pk(kv[0][2], kv[0][3]), pk(kv[1][0], kv[1][1]), pk(kv[1][2], kv[1][3])};
      ST16(rs, kvb, lane * 16, WS_KVP + ((head * 16 + tile) * 4 + w2) * 1024);
      STAMP(8);
      DRAIN();
      lds_signal(L, S_KVP, w2);
      __builtin_amdgcn_s_setprio(0);
      if (wave == 0) {
        lds_wait(L, S_KVP);
        if (lane == 0) __hip_atomic_store((GAS u64*)(ws + WS_FKV + (head * 16 + tile) * 128), tag + 1, RLX_AGENT);
        STAMP(9);
      } else {
        if (wave == 1) {
          const int f0 = (7 * r) >> 2, f1 = (7 * r + 6) >> 2;
          poll_flags(ws, WS_FG + (((f0 + hh) * 2 + (g & 1)) * 16 + min(l15, 13)) * 128, tag, 1);
          if (f1 > f0 + 1) poll_flags(ws, WS_FG + ((f1 * 2 + (g & 1)) * 16 + min(l15, 13)) * 128, tag, 1);
          *(volatile LAS u32x4*)(L + L_FLG + S_REPSEEN * 16) = u32x4{1u, 1u, 1u, 1u};
        } else {
          asm volatile("" : RP_OPS::"memory");
          if (has_unit) {
#pragma unroll
            for (int e = 0; e < 4; ++e) ST16(rs, piece(rp, e), (g >> 1) * 2048 + (g & 1) * 512 + l15 * 16 + (e >> 1) * 1024 + (e & 1) * 256, WS_WD + (uc * 112 + 2 * un) * 2048);
          }
        }
        lds_wait(L, S_KVP);
        lds_wait(L, S_REPSEEN);
        for (int i = wave - 1; i < PF1; i += 3) dma16c(gusrc + i * 1024, lane * 16, gu_dst(i));
      }
      lds_wait(L, S_WO);
      WOF_READ();
      if (wave == 0) {
        poll_flags(ws, WS_FKV + (head * 16 + l15) * 128, tag, 1);
        *(volatile LAS u32x4*)(L + L_FLG + S_KVSEEN * 16) = u32x4{1u, 1u, 1u, 1u};
      } else {
        lds_wait(L, S_KVSEEN);
      }
      STAMP(10);
      u32x4 tt[16];
#pragma unroll
      for (int i = 0; i < 16; ++i) tt[i] = __builtin_bit_cast(u32x4, __builtin_amdgcn_raw_buffer_load_b128(rs, lane * 16, WS_KVP + head * 65536 + w2 * 1024 + i * 4096, 16));
      SCHED();
      const u32 one = g == q4 ? ((p4 & 1) ? 0x3f800000u : 0x3f80u) : 0u, s01 = p4 < 2 ? one : 0u, s23 = p4 < 2 ? 0u : one;
      const u32x4 sel0 = {s01, s23, 0u, 0u}, sel1 = {0u, 0u, s01, s23};
      f32x4 s0 = {}, s1 = {};
#pragma unroll
      for (int i = 0; i < 16; ++i) { s0 = mfma16(sel0, tt[i], s0); s1 = mfma16(sel1, tt[i], s1); }
      LAS char* kw = L + L_KVA + (w2 >> 1) * 1024 + lane * 16 + (w2 & 1) * 8;
      *(LAS u32x2*)(kw) = u32x2{pk(s0[0], s0[1]), pk(s0[2], s0[3])};
      *(LAS u32x2*)(kw + 2048) = u32x2{pk(s1[0], s1[1]), pk(s1[2], s1[3])};
      lds_signal(L, S_KVA, w2);
      if (wave >= 2) { WAITVM(0); FLAG(has_unit && lane == 0 ? WS_FD + (uc * 64 + un) * 128 : NOWHERE, 1); }
      STAMP(11);
    } else {
      u32 wvo[7];
#pragma unroll
      for (int n = 0; n < 7; ++n) {
        const int rho = 8 * n + (lane >> 3), h1 = rho >= 28 ? 1 : 0, sg = rho - 28 * h1, e = (sg * 37) >> 8;
        wvo[n] = (28 * h1 + 4 * (sg - 7 * e) + e) * 896 + (((lane & 7) ^ ((lane >> 2) & 4)) * 16);
      }
      if (wave < 6) WAITVM(21); else WAITVM(7);
      asm volatile("" : RP_OPS::"memory");
      {
        const int so = has_unit ? 0 : NOWHERE;
        f32x4 c;
#pragma unroll
        for (int e = 0; e < 4; ++e) {
          const u32x4 f = piece(rp, e);
          ST16(rs, f, so | ((g >> 1) * 1024 + (g & 1) * 512 + l15 * 16), WS_WGU + (4 * ufl + e) * SLOT + 2 * ukp * 1024 + umat * 256);
          c[e] = mfma16(ones, f, z4)[0];
        }
        ST16(rs, c, g == 0 ? so | (l15 * 16) : NOWHERE, WS_CS + ukp * 14336 + umat * 7168 + ufl * 256);
      }
      WAITVM(5);
      if (wave < 6) {
        asm volatile("" : "+v"(csq[0]), "+v"(csq[1]), "+v"(csq[2]), "+v"(csq[3]), "+v"(csq[4]), "+v"(csq[5]), "+v"(csq[6]), "+v"(csq[7]), "+v"(csq[8]), "+v"(csq[9]), "+v"(csq[10]), "+v"(csq[11]), "+v"(csq[12]), "+v"(csq[13]));
        *(LAS float*)(L + L_CS1 + (wave == 4 ? 1 + hh : 0) * 128 + (lane & 31) * 4) =
            (((csq[0] + csq[1]) + (csq[2] + csq[3])) + ((csq[4] + csq[5]) + (csq[6] + csq[7]))) + (((csq[8] + csq[9]) + (csq[10] + csq[11])) + (csq[12] + csq[13]));
      }
      lds_signal(L, S_WV, w2);
      lds_wait(L, S_WV);
      STAMP(5);
      f32x4 t[2][2];
      const char* wqs = ws + WS_WQKV + head * 86016 + w2 * 1024;
      const char* wos = wo + (r2 < 14 ? cg : 0) * 128 + w2 * 112 * 896;
      project(L, L_WV + lane * 16, 2, xf, w2, lane, t, [&](int n) __attribute__((always_inline)) {
        if (n < 7) dma16c(wqs + n * 4096, lane * 16, L_WQ + (4 * n + w2) * 1024);
        if (n == 12) { WAITVM(7); FLAG(has_unit && lane == 0 ? WS_FG + ((ufl * 2 + umat) * 16 + ukp) * 128 : NOWHERE, 1); }
        if (n == 27) { lds_wait(L, S_WK); lds_wait(L, S_KDONE); }
      });
      STAMP(6);
#pragma unroll
      for (int c = 0; c < 2; ++c) {
        LAS char* p = pw + c * 16 * 224 + 128;
        *(LAS u32x2*)(p) = u32x2{pk(t[c][0][0], t[c][0][1]), pk(t[c][0][2], g == 1 ? 1.0f : t[c][0][3])};
        *(LAS u32x2*)(p + 32) = u32x2{pk(t[c][1][0], t[c][1][1]), pk(t[c][1][2], t[c][1][3])};
      }
      lds_signal(L, S_V, w2);
      STAMP(7);
      WAITVM(1);
      lds_signal(L, S_WQ, w2);
      lds_wait(L, S_WQ);
      STAMP(9);
      project(L, L_WQ + lane * 16, 0, xf, w2, lane, t, [&](int n) __attribute__((always_inline)) {
        if (!(n & 1)) dma16(wos + (n >= 14 ? 56 * 896 : 0), wvo[(n >> 1) % 7], L_WO + (w2 * 14 + (n >> 1)) * 1024);
      });
      STAMP(10);
      u32x4 pq[2][2];
      feature_map(t, wpf, g, pq);
      asm volatile("" : "+v"(pq[0][0]), "+v"(pq[0][1]), "+v"(pq[1][0]), "+v"(pq[1][1]));
      WAITVM(0);
      lds_signal(L, S_WO, w2);
      STAMP(11);
      lds_wait(L, S_WO);
      WOF_READ();
      STAMP(24);
      {
        lds_wait(L, S_REPSEEN);
        const int t2 = tid - 256, fb = 7 * r + min(t2 >> 5, 6), cvo = (g & 1) * 7168 + (64 * (fb >> 2) + 4 * l15 + (fb & 3)) * 4;
        float c[14];
#pragma unroll
        for (int i = 0; i < 14; ++i) c[i] = __builtin_bit_cast(float, __builtin_amdgcn_raw_buffer_load_b32(rs, cvo, WS_CS + i * 14336, 16));
        const float s = (((c[0] + c[1]) + (c[2] + c[3])) + ((c[4] + c[5]) + (c[6] + c[7]))) + (((c[8] + c[9]) + (c[10] + c[11])) + (c[12] + c[13]));
        if (t2 < 224) *(LAS float*)(L + L_CS + t2 * 4) = s;
      }
      lds_wait(L, S_KVA);
      STAMP(27);
      u32x4 ka[4];
#pragma unroll
      for (int i = 0; i < 4; ++i) ka[i] = *(LAS u32x4*)(L + L_KVA + i * 1024 + lane * 16);
      f32x4 o[2][2];
#pragma unroll
      for (int c = 0; c < 4; ++c) o[c >> 1][c & 1] = mfma16(ka[2 * (c & 1) + 1], pq[c >> 1][1], mfma16(ka[2 * (c & 1)], pq[c >> 1][0], z4));
      const f32x2 da = swp16(o[0][0][3], o[0][0][3]), db = swp16(o[1][0][3], o[1][0][3]);
      const float den[2] = {swp32(da.y, da.y).x, swp32(db.y, db.y).x};
#pragma unroll
      for (int c = 0; c < 2; ++c) {
        const float rd = frcp(den[c] + 1e-6f), r3 = (g & 1) ? 0.f : rd;
        const u32x4 ob = {pk(o[c][0][0] * rd, o[c][0][1] * rd), pk(o[c][0][2] * rd, o[c][0][3] * r3), pk(o[c][1][0] * rd, o[c][1][1] * rd), pk(o[c][1][2] * rd, o[c][1][3] * r3)};
        ST16(rs, ob, lane * 16, WS_O + ((tile * 8 + 2 * w2 + c) * 16 + head) * 1024);
      }
      STAMP(12);
    }
  }
  DRAIN();
  BAR();
  if (!A && tid == 448) __hip_atomic_store((GAS u64*)(ws + WS_FO + (tile * 16 + head) * 128), tag + 1, RLX_AGENT);
  STAMP(13);
  u32x4 hres = {0u, 0u, 0u, 0u};
  const int tok = tile2 * 128 + tg * 64 + tb * 16 + l15;
  const int xo = tok * 896 + cg * 128 + kh * 64 + g * 16;
  if (wave > 0) for (int i = PF1 + wave - 1; i < PF2; i += 7) dma16c(gusrc + i * 1024, lane * 16, gu_dst(i));
  STAMP(23);
  if (r2 < 14) {
    const u32x4 xr = *(const u32x4*)(x + xo);
    if (A && wave == 0) { __builtin_amdgcn_s_sleep(DLY2); poll_flags(ws, WS_FO + (tile2 * 16 + l15) * 128, tag, 1); }
    BAR();
    STAMP(14);
    u32x4 of[8];
#pragma unroll
    for (int i = 0; i < 8; ++i) of[i] = __builtin_bit_cast(u32x4, __builtin_amdgcn_raw_buffer_load_b128(rs, lane * 16 + (i & 3) * 1024, WS_O + ((tile2 * 8 + tg * 4 + tb) * 16 + kh * 8 + (i & 4)) * 1024, 16));
    SCHED();
    f32x4 a[4] = {};
#pragma unroll
    for (int i = 0; i < 8; ++i)
#pragma unroll
      for (int k = 0; k < 4; ++k) a[k] = mfma16(wof[i][k], of[i], a[k]);
    *(LAS f32x4*)(L + L_EX + wave * 2048 + lane * 16) = kh ? a[0] : a[2];
    *(LAS f32x4*)(L + L_EX + wave * 2048 + 1024 + lane * 16) = kh ? a[1] : a[3];
    BAR();
    STAMP(15);
    const f32x4 c0 = (kh ? a[2] : a[0]) + *(LAS f32x4*)(L + L_EX + (wave ^ 4) * 2048 + lane * 16);
    const f32x4 c1 = (kh ? a[3] : a[1]) + *(LAS f32x4*)(L + L_EX + (wave ^ 4) * 2048 + 1024 + lane * 16);
    const f32x4 h0 = c0 + f32x4{blo(xr.x), bhi(xr.x), blo(xr.y), bhi(xr.y)}, h1 = c1 + f32x4{blo(xr.z), bhi(xr.z), blo(xr.w), bhi(xr.w)};
    hres = u32x4{pk(h0[0], h0[1]), pk(h0[2], h0[3]), pk(h1[0], h1[1]), pk(h1[2], h1[3])};
    ST16(rs, hres, WS_H + ((tok >> 5) * 28 + 4 * cg + 2 * kh + (g >> 1)) * 1024 + ((g & 1) * 32 + (tok & 31)) * 16, 0);
    const f32x4 r0 = {blo(hres.x), bhi(hres.x), blo(hres.y), bhi(hres.y)}, r1 = {blo(hres.z), bhi(hres.z), blo(hres.w), bhi(hres.w)};
    const f32x4 a1 = r0 + r1, a2q = r0 * r0 + r1 * r1;
    float s1 = (a1[0] + a1[1]) + (a1[2] + a1[3]), s2 = (a2q[0] + a2q[1]) + (a2q[2] + a2q[3]);
    { const float t = swadd16(s1, s2); const f32x2 a2 = sw32(t); s1 = a2.x + a2.y; }
    if (g < 2) __builtin_amdgcn_raw_buffer_store_b32(__builtin_bit_cast(u32, s1), rs, WS_ST + ((2 * cg + kh) * 2048 + tok) * 8 + g * 4, 0, 16);
    DRAIN();
    BAR();
    if (A && tid == 192) __hip_atomic_store((GAS u64*)(ws + WS_FH + (tile2 * 16 + r2) * 128), tag + 1, RLX_AGENT);
  } else {
    DRAIN();
    BAR();
  }
  STAMP(16);
  u32x4 wdf[2][8];
  const int wdo = WS_WD + (cg * 112 + 28 * w2) * 2048 + lane * 16;
  if (A) {
    if (wave == 0) {
      __builtin_amdgcn_s_sleep(DLY3);
      poll_flags(ws, WS_FH + (tile * 16 + (l15 < 14 ? l15 : 0)) * 128, tag, 1);
      *(volatile LAS u32x4*)(L + L_FLG + S_HSEEN * 16) = u32x4{1u, 1u, 1u, 1u};
    } else {
      lds_wait(L, S_HSEEN);
    }
  }
  STAMP(17);
  if constexpr (A) {
    const int T32 = tile * 4 + wave;
    f32x2 st[7];
#pragma unroll
    for (int i = 0; i < 7; ++i) st[i] = __builtin_bit_cast(f32x2, __builtin_amdgcn_raw_buffer_load_b64(rs, WS_ST + (7 * hh * 2048 + (lane & 31)) * 8, (i * 2048 + T32 * 32) * 8, 16));
    u32x4 hb[28];
#pragma unroll
    for (int s = 0; s < 28; ++s) hb[s] = __builtin_bit_cast(u32x4, __builtin_amdgcn_raw_buffer_load_b128(rs, lane * 16 + (s & 3) * 1024, WS_H + (T32 * 28 + (s & ~3)) * 1024, 16));
    u32 wb_[3] = {(u32)lane * 16, (u32)lane * 16 + 2 * SLOT, (u32)lane * 16 + 4 * SLOT};
    asm volatile("" : "+v"(wb_[1]), "+v"(wb_[2]));
#define WFRAG(sl, s) (*(LAS u32x4*)(L + wb_[(sl) >> 1] + ((sl) & 1) * SLOT + (s) * 1024))
    LAS char* csl = L + L_CS + hh * 16;
    u32x4 ring[7];
    f32x4 cq[4];
#pragma unroll
    for (int k = 0; k < 4; ++k) cq[k] = *(LAS f32x4*)(csl + k * 32);
#pragma unroll
    for (int s = 0; s < 7; ++s) ring[s] = WFRAG(0, s);
    SCHED();
    float s1 = ((st[0].x + st[1].x) + (st[2].x + st[3].x)) + ((st[4].x + st[5].x) + st[6].x);
    float s2 = ((st[0].y + st[1].y) + (st[2].y + st[3].y)) + ((st[4].y + st[5].y) + st[6].y);
    { f32x2 a = sw32(s1); s1 = a.x + a.y; a = sw32(s2); s2 = a.x + a.y; }
    const float nmu = s1 * (-1.f / 448.f), ve = fmaxf(s2 * (1.f / 448.f) - nmu * nmu, 0.f) + 1e-5f;
    const float rstd = __builtin_amdgcn_rsqf(ve);
    const float k0 = -2.30220819f * rstd, k1 = -0.10294324f * rstd * rstd * rstd;
    const int pvo = lane == 0 ? WS_FHH + ((tile * 16 + r) * 4 + wave) * 128 : 0x7ffffff0;
#define PROG(n) FLAG(pvo, n)
    const int hho = WS_HH + (T32 * 112 + 7 * r) * 1024;
    f32x16 acc[2];
    float y[8], ta = 0.f;
#pragma unroll
    for (int i = 0; i < 16; ++i) acc[0][i] = nmu * cq[i >> 2][i & 3];
    __builtin_amdgcn_s_setprio(GUPRIO);
    SCHED();
    auto gu_block = [&](auto JC) __attribute__((always_inline)) {
      constexpr int j = JC.value;
#pragma unroll
      for (int s = 0; s < 28; ++s) {
        acc[j & 1] = mfma32(ring[s % 7], hb[s], acc[j & 1]);
        if (s == 20 && (j + 1) * 28 <= PF2 && (j + 2) * 28 > PF2 && j < 4) lds_wait(L, S_WIN3);
        if (s == 20 && j == 4) lds_wait(L, S_REFILL);
        if (s + 7 < 28) ring[s % 7] = WFRAG(gu_slot(j), s + 7);
        else if (j < 6) ring[s % 7] = WFRAG(gu_slot(j + 1), s - 21);
        if (s == 0 && (j == 2 || j == 5)) lds_signal(L, j == 2 ? S_FREE01 : S_DONE4, wave);
        if (j > 0 && s >= 1 && s < 17) {
          const int i = (s - 1) >> 1;
          const float ga = acc[(j - 1) & 1][i];
          if ((s - 1) & 1) y[i] = ga * acc[(j - 1) & 1][i + 8] * frcp(fmaf(ta, ve, ve));
          else ta = fexp2(ga * fmaf(ga * ga, k1, k0));
        }
        if (j > 1 && s == 14) {
 WAITVM(0);
 PROG(j - 1); }
        if (j > 0 && s == 17) { const u32x4 yb = {pk(y[0], y[1]), pk(y[2], y[3]), pk(y[4], y[5]), pk(y[6], y[7])}; ST16(rs, yb, lane * 16, hho + (j - 1) * 1024); }
        if (j < 6 && s >= 16 && s < 20) cq[s - 16] = *(LAS f32x4*)(csl + (j + 1) * 128 + (s - 16) * 32);
        if (j < 6 && s >= 22 && s < 26) {
#pragma unroll
          for (int e = 0; e < 4; ++e) acc[(j + 1) & 1][4 * (s - 22) + e] = nmu * cq[s - 22][e];
        }
        SCHED();
      }
      if (j == 0) STAMP(18);
      if (j == 3) STAMP(26);
      if (j == 1) STAMP(28);
      if (j == 2) STAMP(29);
      if (j == 4) STAMP(30);
      if (j == 5) STAMP(31);
    };
    gu_block(IC<0>{}); gu_block(IC<1>{}); gu_block(IC<2>{}); gu_block(IC<3>{}); gu_block(IC<4>{}); gu_block(IC<5>{}); gu_block(IC<6>{});
#pragma unroll
    for (int i = 0; i < 8; ++i) { const float ga = acc[0][i]; y[i] = ga * acc[0][i + 8] * frcp(fmaf(fexp2(ga * fmaf(ga * ga, k1, k0)), ve, ve)); }
    { const u32x4 yb = {pk(y[0], y[1]), pk(y[2], y[3]), pk(y[4], y[5]), pk(y[6], y[7])}; ST16(rs, yb, lane * 16, hho + 6 * 1024); }
    WAITVM(1);
    PROG(6);
    WAITVM(1);
    PROG(7);
    __builtin_amdgcn_s_setprio(0);
    STAMP(19);
  } else {
    for (int i = PF2 + w2; i < 140; i += 4) dma16c(gusrc + i * 1024, lane * 16, gu_dst(i));
    if (r2 < 14) {
      poll_flags(ws, WS_FD + (cg * 64 + 14 * w2 + min(l15, 13)) * 128, tag, 1);
      lds_signal(L, S_WIN3, w2);
#pragma unroll
      for (int i = 0; i < 8; ++i) wdf[0][i] = LD16(rs, wdo + (7 * (i >> 1)) * 2048 + (i & 1) * 1024);
      SCHED();
      STAMP(25);
      lds_wait(L, S_HSEEN);
      __builtin_amdgcn_s_sleep(8);
#pragma unroll
      for (int i = 0; i < 8; ++i) wdf[1][i] = LD16(rs, wdo + (7 * (i >> 1) + 1) * 2048 + (i & 1) * 1024);
      SCHED();
    } else { WAITVM(0); lds_signal(L, S_WIN3, w2); }
    lds_wait(L, S_FREE01);
    for (int i = w2; i < 56; i += 4) dma16c(gusrc + 5 * SLOT + i * 1024, lane * 16, gu_dst(i));
    if (r2 >= 14) { WAITVM(0); lds_signal(L, S_REFILL, w2); }
    else {
      const u32 po = WS_FHH + ((tile2 * 16 + 4 * w2 + ((lane >> 1) & 3)) * 4 + 2 * tg + (lane & 1)) * 128;
      const int ho = WS_HH + ((tile2 * 4 + 2 * tg) * 112 + 28 * w2) * 1024 + lane * 16;
      f32x16 acc[2][2] = {};
      u32x4 hf[2][8];
      u32 have = 0;
#define NEED(n) do { if (have < (n)) { _Pragma("nounroll") for (u32 sp = 0; sp < SPIN_CAP; ++sp) { const u64 d = __hip_atomic_load((const GAS u64*)(ws + po), RLX_AGENT) - tag; \
        u32 c = d <= 8 ? (u32)d : 0u; c = min(c, (u32)__builtin_amdgcn_update_dpp(0, (int)c, 0x121, 0xf, 0xf, false)); c = min(c, (u32)__builtin_amdgcn_update_dpp(0, (int)c, 0x122, 0xf, 0xf, false)); \
        c = min(c, (u32)__builtin_amdgcn_update_dpp(0, (int)c, 0x124, 0xf, 0xf, false)); have = __builtin_amdgcn_readfirstlane(c); if ((n) == 1) lds_signal(L, S_REFILL, w2);   if (have >= (n)) break; } \
        __builtin_amdgcn_fence(__ATOMIC_ACQUIRE, "wavefront"); } } while (0)
      NEED(1);
#pragma unroll
      for (int i = 0; i < 8; ++i) hf[0][i] = LD16(rs, ho + (i & 1) * 114688 + (7 * (i >> 1)) * 1024);
      SCHED();
#pragma unroll
      for (int j = 0; j < 7; ++j) {
        if (j < 6) {
          NEED(j + 2);
#pragma unroll
          for (int i = 0; i < 8; ++i) hf[(j + 1) & 1][i] = LD16(rs, ho + (i & 1) * 114688 + (7 * (i >> 1) + j + 1) * 1024);
          SCHED();
        }
#pragma unroll
        for (int i = 0; i < 4; ++i)
#pragma unroll
          for (int T = 0; T < 2; ++T)
#pragma unroll
            for (int k = 0; k < 2; ++k) acc[T][k] = mfma32(wdf[j & 1][2 * i + k], hf[j & 1][2 * i + T], acc[T][k]);
        SCHED();
        if (j < 5) {
#pragma unroll
          for (int i = 0; i < 8; ++i) wdf[j & 1][i] = LD16(rs, wdo + (7 * (i >> 1) + j + 2) * 2048 + (i & 1) * 1024);
          SCHED();
        }
        if (j == 0) STAMP(18);
        if (j == 3) STAMP(26);
        if (j == 5) STAMP(19);
      }
      lds_wait(L, S_DONE4);
#pragma unroll
      for (int T = 0; T < 2; ++T)
#pragma unroll
        for (int k = 0; k < 2; ++k)
#pragma unroll
          for (int q = 0; q < 4; ++q)
            *(LAS f32x4*)(L + L_RED + (((w2 * 2 + T) * 2 + k) * 4 + q) * 1024 + lane * 16) = f32x4{acc[T][k][4 * q], acc[T][k][4 * q + 1], acc[T][k][4 * q + 2], acc[T][k][4 * q + 3]};
      STAMP(20);
    }
  }
  if (r2 < 14) {
    BAR();
    float c[8] = {blo(hres.x), bhi(hres.x), blo(hres.y), bhi(hres.y), blo(hres.z), bhi(hres.z), blo(hres.w), bhi(hres.w)};
    LAS char* rq = L + L_RED + ((tb >> 1) * 8 + kh) * 1024 + ((g >> 1) * 32 + (tb & 1) * 16 + l15) * 16 + (g & 1) * 8;
#pragma unroll
    for (int w = 0; w < 4; ++w)
#pragma unroll
      for (int e = 0; e < 4; ++e) { const f32x2 v = *(LAS f32x2*)(rq + w * 16384 + (e >> 1) * 4096 + (e & 1) * 2048); c[e] += v.x; c[4 + e] += v.y; }
    *(u32x4*)(out + xo) = u32x4{pk(c[0], c[1]), pk(c[2], c[3]), pk(c[4], c[5]), pk(c[6], c[7])};
    if (A && b == 0 && tid == 0) *(u64*)(ws + WS_E) = tag;
  }
  STAMP(21);
  if (A && b == 8 && wave == 0) { const u64 e_ = __builtin_amdgcn_s_memrealtime() + 4 * (pt_ - p0_); while (__builtin_amdgcn_s_memrealtime() < e_) __builtin_amdgcn_s_sleep(1); }
  };
  if (wave < 4) role(IC<1>{}); else role(IC<0>{});
}

extern "C" void kernel_launch(void* const* d_in, const int* in_sizes, int n_in, void* d_out, int out_size, void* d_ws, size_t ws_size, hipStream_t stream) {
  (void)hipFuncSetAttribute((const void*)fused_block, hipFuncAttributeMaxDynamicSharedMemorySize, LDS_BYTES);
  fused_block<<<256, 512, LDS_BYTES, stream>>>((const char*)d_in[0], (const char*)d_in[1], (const char*)d_in[2], (const char*)d_in[3], (const char*)d_in[5], (const char*)d_in[6],
                                               (const char*)d_in[7], (char*)d_out, (char*)d_ws);
}
```

```cpp
#include <hip/hip_runtime.h>
#include <hip/hip_bf16.h>
#include <stdint.h>

typedef unsigned u32;
typedef unsigned long long u64;
typedef u32 u32x4 __attribute__((ext_vector_type(4)));
typedef u32 u32x2 __attribute__((ext_vector_type(2)));
typedef float f32x2 __attribute__((ext_vector_type(2)));
typedef float f32x4 __attribute__((ext_vector_type(4)));
typedef float f32x16 __attribute__((ext_vector_type(16)));
typedef short s16x4 __attribute__((ext_vector_type(4)));
typedef __bf16 bf16x2 __attribute__((ext_vector_type(2)));
typedef __bf16 bf16x8 __attribute__((ext_vector_type(8)));
#define LAS __attribute__((address_space(3)))
#define GAS __attribute__((address_space(1)))
#define DI __device__ __forceinline__
#define RLX_AGENT __ATOMIC_RELAXED, __HIP_MEMORY_SCOPE_AGENT

constexpr int WS_E = 0;
constexpr int WS_FKV = 8192;
constexpr int WS_FO = WS_FKV + 131072;
constexpr int WS_FH = WS_FO + 131072;
constexpr int WS_FG = WS_FH + 262144;
constexpr int WS_FD = WS_FG + 114688;
constexpr int WS_FHH = WS_FD + 57344;
constexpr int WS_CS = WS_FHH + 131072;
constexpr int WS_ST = WS_CS + 200704;
constexpr int WS_WGU = WS_ST + 229376;
constexpr int WS_WD = WS_WGU + 3211264;
constexpr int WS_KVP = WS_WD + 1605632;
constexpr int WS_O = WS_KVP + 1048576;
constexpr int WS_H = WS_O + 2097152;
constexpr int WS_HH = WS_H + 1835008;
constexpr int WS_STAMP = WS_HH + 7340032;
constexpr int WS_LOG = WS_STAMP + 256 * 8 * 32 * 8;
constexpr int WS_USED = WS_LOG + 4096;
constexpr int SLOT = 28672;
constexpr int L_WK = 4 * SLOT, L_WV = 0, L_WQ = SLOT;
constexpr int L_PKV = 4 * SLOT;
constexpr int L_WO = 2 * SLOT;
constexpr int L_EX = 5 * SLOT;
constexpr int L_KVA = 5 * SLOT;
constexpr int L_ST1 = 5 * SLOT + 4096;
constexpr int L_WPHI = 5 * SLOT + 5120;
constexpr int L_CS = 159744;
constexpr int L_FLG = 160768;
constexpr int L_JUNK = 161280;
constexpr int L_STAMP = 161536;
constexpr int L_RED = SLOT;
constexpr int LDS_BYTES = 163840;
constexpr int gu_slot(int j) { return j == 0 || j == 5 || j > 6 ? 0 : (j == 1 || j == 6 ? 4 : j - 1); }
constexpr int CODE_PF = 34816;
constexpr u32 SPIN_CAP = 1u << 16;

#define STAMP(i) do {} while (0)
#define STAMP2(i) do {} while (0)

DI f32x4 mfma16(u32x4 a, u32x4 b, f32x4 c) { return __builtin_amdgcn_mfma_f32_16x16x32_bf16(__builtin_bit_cast(bf16x8, a), __builtin_bit_cast(bf16x8, b), c, 0, 0, 0); }
DI f32x16 mfma32(u32x4 a, u32x4 b, f32x16 c) { return __builtin_amdgcn_mfma_f32_32x32x16_bf16(__builtin_bit_cast(bf16x8, a), __builtin_bit_cast(bf16x8, b), c, 0, 0, 0); }
DI u32 pk(float lo, float hi) { f32x2 p = {lo, hi}; return __builtin_bit_cast(u32, __builtin_convertvector(p, bf16x2)); }
DI float blo(u32 v) { return __builtin_bit_cast(float, v << 16); }
DI float bhi(u32 v) { return __builtin_bit_cast(float, v & 0xffff0000u); }
DI float dot2(u32 a, u32 b, float c) { return __builtin_amdgcn_fdot2_f32_bf16(__builtin_bit_cast(bf16x2, a), __builtin_bit_cast(bf16x2, b), c, false); }
DI u32x2 trread(LAS char* p) { return __builtin_bit_cast(u32x2, __builtin_amdgcn_ds_read_tr16_b64_v4i16((LAS s16x4*)p)); }
DI u32x4 trfrag(LAS char* p, int step) { u32x2 a = trread(p), b = trread(p + step); return u32x4{a.x, a.y, b.x, b.y}; }
DI float fexp2(float x) { return __builtin_amdgcn_exp2f(x); }
DI float frcp(float x) { return __builtin_amdgcn_rcpf(x); }
template <int C> DI float dppror(float x) { return __builtin_bit_cast(float, __builtin_amdgcn_update_dpp(0, __builtin_bit_cast(int, x), 0x120 + C, 0xf, 0xf, false)); }
DI float row_max(float x) { x = fmaxf(x, dppror<8>(x)); x = fmaxf(x, dppror<4>(x)); x = fmaxf(x, dppror<2>(x)); return fmaxf(x, dppror<1>(x)); }
DI float row_min(float x) { x = fminf(x, dppror<8>(x)); x = fminf(x, dppror<4>(x)); x = fminf(x, dppror<2>(x)); return fminf(x, dppror<1>(x)); }
DI float row_sum(float x) { x += dppror<8>(x); x += dppror<4>(x); x += dppror<2>(x); return x + dppror<1>(x); }
DI f32x2 sw16(float x) { u32 u = __builtin_bit_cast(u32, x); auto r = __builtin_amdgcn_permlane16_swap(u, u, false, false); return f32x2{__builtin_bit_cast(float, (u32)r[0]), __builtin_bit_cast(float, (u32)r[1])}; }
DI f32x2 sw32(float x) { u32 u = __builtin_bit_cast(u32, x); auto r = __builtin_amdgcn_permlane32_swap(u, u, false, false); return f32x2{__builtin_bit_cast(float, (u32)r[0]), __builtin_bit_cast(float, (u32)r[1])}; }
DI float grp_sum(float x) { f32x2 a = sw16(x); x = a.x + a.y; a = sw32(x); return a.x + a.y; }
DI float grp_max(float x) { f32x2 a = sw16(x); x = fmaxf(a.x, a.y); a = sw32(x); return fmaxf(a.x, a.y); }
DI float grp_min(float x) { f32x2 a = sw16(x); x = fminf(a.x, a.y); a = sw32(x); return fminf(a.x, a.y); }

#define BAR() asm volatile("s_waitcnt lgkmcnt(0)\n\ts_barrier" ::: "memory")
#define WAITVM(n) asm volatile("s_waitcnt vmcnt(" #n ")" ::: "memory")
#define DRAIN() do { asm volatile("s_waitcnt vmcnt(0)" ::: "memory"); __builtin_amdgcn_s_waitcnt(0x0F70); } while (0)
#define SCHED() __builtin_amdgcn_sched_barrier(0)
DI void dma16(const void* base, u32 voff, u32 lds) {
  u32 keep;
  asm volatile("s_mov_b32 %0, m0\n\ts_mov_b32 m0, %3\n\ts_nop 0\n\tglobal_load_lds_dwordx4 %1, %2\n\ts_mov_b32 m0, %0" : "=&s"(keep) : "v"(voff), "s"(base), "s"(lds) : "memory");
}
DI void dma16c(const void* base, u32 voff, u32 lds) {
  u32 keep;
  asm volatile("s_mov_b32 %0, m0\n\ts_mov_b32 m0, %3\n\ts_nop 0\n\tglobal_load_lds_dwordx4 %1, %2 sc1\n\ts_mov_b32 m0, %0" : "=&s"(keep) : "v"(voff), "s"(base), "s"(lds) : "memory");
}
DI void dma4(const void* base, u32 voff, u32 lds) {
  u32 keep;
  asm volatile("s_mov_b32 %0, m0\n\ts_mov_b32 m0, %3\n\ts_nop 0\n\tglobal_load_lds_dword %1, %2\n\ts_mov_b32 m0, %0" : "=&s"(keep) : "v"(voff), "s"(base), "s"(lds) : "memory");
}
#define LD16(rs, off) __builtin_bit_cast(u32x4, __builtin_amdgcn_raw_buffer_load_b128(rs, off, 0, 16))
#define ST16(rs, v, off, soff) do { const u32x4 d_ = __builtin_bit_cast(u32x4, v); __builtin_amdgcn_raw_buffer_store_b128(d_, rs, off, soff, 16); asm volatile("s_nop 1" :: "v"(d_) : "memory"); } while (0)

#define FLAG(off, n) __builtin_amdgcn_raw_buffer_store_b64(__builtin_bit_cast(u32x2, tag + (n)), rs, fvo, off, 16)
constexpr int NOWHERE = 0x7ffffff0;
DI void poll_flags(const char* ws, u32 off, u64 tag, u32 need) {
#pragma nounroll
  for (u32 sp = 0; sp < SPIN_CAP; ++sp) {
    u64 d = __hip_atomic_load((const GAS u64*)(ws + off), RLX_AGENT) - tag;
    if (__all(d >= need && d <= 8)) break;
  }
  __builtin_amdgcn_fence(__ATOMIC_ACQUIRE, "wavefront");
}
DI void lds_wait(LAS char* L, int sig) {
#pragma nounroll
  for (u32 sp = 0; sp < (SPIN_CAP << 4); ++sp) {
    const u32x4 v = *(volatile LAS u32x4*)(L + L_FLG + sig * 16);
    if (__builtin_amdgcn_readfirstlane(v.x & v.y & v.z & v.w)) break;
    __builtin_amdgcn_s_sleep(1);
  }
  asm volatile("" ::: "memory");
}
DI void lds_wait2(LAS char* L, int sig, int sig2) {
#pragma nounroll
  for (u32 sp = 0; sp < (SPIN_CAP << 4); ++sp) {
    const u32x4 v = *(volatile LAS u32x4*)(L + L_FLG + sig * 16) & *(volatile LAS u32x4*)(L + L_FLG + sig2 * 16);
    if (__builtin_amdgcn_readfirstlane(v.x & v.y & v.z & v.w)) break;
    __builtin_amdgcn_s_sleep(1);
  }
  asm volatile("" ::: "memory");
}
DI void lds_wait1(LAS char* L, int sig, int w) {
#pragma nounroll
  for (u32 sp = 0; sp < (SPIN_CAP << 4); ++sp) {
    if (__builtin_amdgcn_readfirstlane(*(volatile LAS u32*)(L + L_FLG + sig * 16 + w * 4))) break;
    __builtin_amdgcn_s_sleep(1);
  }
  asm volatile("" ::: "memory");
}
DI void lds_signal(LAS char* L, int sig, int w) {
  asm volatile("" ::: "memory");
  *(volatile LAS u32*)(L + L_FLG + sig * 16 + w * 4) = 1u;
}

template <int N> struct IC { static constexpr int value = N; };
constexpr float LOG2E = 1.4426950408889634f;
template <class F> DI void project(LAS char* L, u32 w0, u32 w1, const u32x4 (&xf)[2][14], int tp, int lane, f32x4 (&t)[2][2], F&& mid) {
  asm volatile("" : "+v"(w0), "+v"(w1));
  u32x4 ring[6];
  f32x4 cs[2] = {};
  f32x2 st[2];
  const u32x4 ones = {0x3f803f80u, 0x3f803f80u, 0x3f803f80u, 0x3f803f80u};
#pragma unroll
  for (int n = 0; n < 6; ++n) ring[n] = trfrag(L + (n & 1 ? w1 : w0) + (n >> 1) * 2048, 256);
#pragma unroll
  for (int i = 0; i < 4; ++i) t[i >> 1][i & 1] = f32x4{0.f, 0.f, 0.f, 0.f};
  SCHED();
#pragma unroll
  for (int n = 0; n < 28; ++n) {
    const u32x4 f = ring[n % 6];
    t[0][n & 1] = mfma16(f, xf[0][n >> 1], t[0][n & 1]);
    t[1][n & 1] = mfma16(f, xf[1][n >> 1], t[1][n & 1]);
    cs[n & 1] = mfma16(f, ones, cs[n & 1]);
    if (n + 6 < 28) ring[n % 6] = trfrag(L + (n & 1 ? w1 : w0) + ((n + 6) >> 1) * 2048, 256);
    if (n == 22 || n == 23) st[n & 1] = *(LAS f32x2*)(L + L_ST1 + (tp * 32 + (n & 1) * 16 + (lane & 15)) * 8);
    mid(n);
    SCHED();
  }
#pragma unroll
  for (int tb = 0; tb < 2; ++tb)
#pragma unroll
    for (int k = 0; k < 2; ++k) t[tb][k] = (t[tb][k] + cs[k] * st[tb].x) * st[tb].y;
}
DI f32x2 swp16(float a, float b) { auto r = __builtin_amdgcn_permlane16_swap(__builtin_bit_cast(u32, a), __builtin_bit_cast(u32, b), false, false); return f32x2{__builtin_bit_cast(float, (u32)r[0]), __builtin_bit_cast(float, (u32)r[1])}; }
DI f32x2 swp32(float a, float b) { auto r = __builtin_amdgcn_permlane32_swap(__builtin_bit_cast(u32, a), __builtin_bit_cast(u32, b), false, false); return f32x2{__builtin_bit_cast(float, (u32)r[0]), __builtin_bit_cast(float, (u32)r[1])}; }
DI float swadd32(float a, float b) { const f32x2 s = swp32(a, b); return s.x + s.y; }
DI float swadd16(float a, float b) { const f32x2 s = swp16(a, b); return s.x + s.y; }
DI float mx2(float a, float b) { float r; asm("v_max_f32 %0, %1, %2" : "=v"(r) : "v"(a), "v"(b)); return r; }
DI float mx3(float a, float b, float c) { return fmaxf(fmaxf(a, b), c); }
DI float mn3(float a, float b, float c) { return fminf(fminf(a, b), c); }
DI void feature_map(const f32x4 (&t)[2][2], const u32x4 (&wpf)[2], int g, u32x4 (&P)[2][2]) {
  const f32x4 z4 = {0.f, 0.f, 0.f, 0.f};
  const bool v23 = g < 3;
  float p[2][8], m[4];
#pragma unroll
  for (int tb = 0; tb < 2; ++tb) {
    const u32x4 tq = {pk(t[tb][0][0], t[tb][0][1]), pk(t[tb][0][2], t[tb][0][3]), pk(t[tb][1][0], t[tb][1][1]), pk(t[tb][1][2], t[tb][1][3])};
    const f32x4 p0 = mfma16(wpf[0], tq, z4), p1 = mfma16(wpf[1], tq, z4);
#pragma unroll
    for (int i = 0; i < 8; ++i) { const float v = i < 4 ? p0[i] : p1[i - 4]; p[tb][i] = ((i & 3) < 2 || v23) ? v : p0[0]; }
    m[2 * tb] = mx3(mx3(p[tb][0], p[tb][1], p[tb][2]), mx3(p[tb][3], p[tb][4], p[tb][5]), mx3(p[tb][6], p[tb][7], p[tb][0]));
    m[2 * tb + 1] = -mn3(mn3(p[tb][0], p[tb][1], p[tb][2]), mn3(p[tb][3], p[tb][4], p[tb][5]), mn3(p[tb][6], p[tb][7], p[tb][0]));
  }
  {
    const f32x2 a = swp16(m[0], m[1]), b = swp16(m[2], m[3]);
    const f32x2 c = swp32(mx2(a.x, a.y), mx2(b.x, b.y));
    const float d = mx2(c.x, c.y) * -LOG2E;
    const f32x2 e = swp32(d, d), f0 = swp16(e.x, e.x), f1 = swp16(e.y, e.y);
    m[0] = f0.x; m[1] = f0.y; m[2] = f1.x; m[3] = f1.y;
  }
  float ex[4][8], sm[4];
#pragma unroll
  for (int c = 0; c < 4; ++c) {
#pragma unroll
    for (int i = 0; i < 8; ++i) {
      ex[c][i] = fexp2(fmaf(p[c >> 1][i], (c & 1) ? -LOG2E : LOG2E, m[c]));
      if ((i & 3) >= 2) ex[c][i] = v23 ? ex[c][i] : 0.f;
    }
    sm[c] = ((ex[c][0] + ex[c][1]) + (ex[c][2] + ex[c][3])) + ((ex[c][4] + ex[c][5]) + (ex[c][6] + ex[c][7]));
  }
  {
    const float d = frcp(swadd32(swadd16(sm[0], sm[1]), swadd16(sm[2], sm[3])));
    const f32x2 e = swp32(d, d), f0 = swp16(e.x, e.x), f1 = swp16(e.y, e.y);
    sm[0] = f0.x; sm[1] = f0.y; sm[2] = f1.x; sm[3] = f1.y;
  }
#pragma unroll
  for (int c = 0; c < 4; ++c)
#pragma unroll
    for (int e = 0; e < 4; ++e) P[c >> 1][c & 1][e] = pk(ex[c][2 * e] * sm[c], ex[c][2 * e + 1] * sm[c]);
}
DI u32x4 piece(const u32x2 (&rp)[8], int e) {
  u32x4 f;
#pragma unroll
  for (int m = 0; m < 4; ++m) f[m] = __builtin_amdgcn_perm(rp[2 * m + 1][e >> 1], rp[2 * m][e >> 1], (e & 1) ? 0x07060302u : 0x05040100u);
  return f;
}
#define RP_OPS "+v"(rp[0]), "+v"(rp[1]), "+v"(rp[2]), "+v"(rp[3]), "+v"(rp[4]), "+v"(rp[5]), "+v"(rp[6]), "+v"(rp[7])
#define RP_LOAD(lvo, addr) _Pragma("unroll") for (int j = 0; j < 8; ++j) asm volatile("global_load_dwordx2 %0, %1, %2" : "=v"(rp[j]) : "v"(lvo), "s"(addr) : "memory")
#define PF1 0
#define PF2 64
#define PF3 120
#define DLY4 16
#define DLY2 8
#define DLY3 8
#define GUPRIO 2
enum { S_FREE01 = 0, S_REFILL, S_DONE4, S_WIN3, S_WK, S_WV, S_WQ, S_WSEEN, S_KDONE, S_V, S_PHIK, S_KVP, S_KVA, S_WO, S_REPSEEN, S_HSEEN, S_X0, S_X1, S_XA, S_XB, S_CS, S_WOFA, S_WOFB, S_EXA, S_EXB, S_W2A, S_W2B };
DI u32 gu_dst(int i) { return i < 28 ? i * 1024 : (i < 56 ? 3 * SLOT + i * 1024 : i * 1024 - SLOT); }

__global__ void __launch_bounds__(512, 2) fused_block(const char* __restrict__ x, const char* __restrict__ wqkv, const char* __restrict__ wphi, const char* __restrict__ wo,
                                                      const char* __restrict__ wg, const char* __restrict__ wu, const char* __restrict__ wd, char* __restrict__ out, char* ws) {
  extern __shared__ __attribute__((aligned(16))) char smem[];
  LAS char* L = (LAS char*)smem;
  const int tid = threadIdx.x, lane = tid & 63, wave = __builtin_amdgcn_readfirstlane(tid >> 6), w2 = wave & 3;
  const int b = blockIdx.x, li = b >> 3, xp = (b & 7) >> 1, xh = b & 1;
  const int tile = 4 * xp + (li >> 3), r = 8 * xh + (li & 7);
  const int tile2 = 4 * xp + 2 * xh + (li >> 4), r2 = li & 15;
  const int tg = r2 >= 7 ? 1 : 0, cg = r2 - 7 * tg;
  const int l15 = lane & 15, g = lane >> 4, q4 = l15 >> 2, p4 = lane & 3, hh = lane >> 5;
  const int head = r;
  asm volatile("" :: "s"(x), "s"(wqkv), "s"(wphi), "s"(wo), "s"(wg), "s"(wu), "s"(wd), "s"(out), "s"(ws));
  STAMP(0);
  u64 pc_; asm volatile("s_getpc_b64 %0" : "=s"(pc_));
  const char* pc0 = (const char*)(pc_ & ~127ull);
  const __amdgpu_buffer_rsrc_t rs = __builtin_amdgcn_make_buffer_rsrc((void*)ws, 0, WS_USED, 0x00020000);
  const u32x4 ones = {0x3f803f80u, 0x3f803f80u, 0x3f803f80u, 0x3f803f80u};
  const f32x4 z4 = {0.f, 0.f, 0.f, 0.f};
  auto role = [&](auto AC) __attribute__((always_inline)) {
  constexpr bool A = AC.value;
  if (A) asm volatile("; A" ::: "memory"); else asm volatile("; B" ::: "memory");
  __builtin_amdgcn_s_waitcnt(0x0F70);
  u64 epoch;
  u32x2 rp[8];
  const int fvo = lane < 16 ? lane * 8 : NOWHERE;
  const int uidx = b * 4 + w2, unit = uidx * 49 >> 6;
  const bool has_unit = (((uidx + 1) * 49) >> 6) != unit;
  const int umat = unit & 1, ukp = (unit >> 1) / 28, ufl = (unit >> 1) - 28 * ukp, unit2 = uidx * 49 >> 7, un = unit2 / 7, uc = unit2 - 7 * un;
  const bool has_unit2 = (((uidx + 1) * 49) >> 7) != unit2;
  const char* gusrc = ws + WS_WGU + 7 * r * SLOT;
  const u32 wvo = (lane >> 2) * 2688 + (((lane & 3) ^ (2 * (lane >> 5))) * 16);
  const char* wbase = wqkv + head * 56 - (head & 1) * 8 + 16 * w2 * 2688;
  const u32 wr0 = (8 * g + q4) * 64 + (((p4 + (head & 1)) ^ (4 * (g & 1))) * 8), wr1 = (8 * g + q4) * 64 + ((((4 + p4 + (head & 1)) & 7) ^ (4 * (g & 1))) * 8);
  const int tb = w2; constexpr int kh = A ? 0 : 1;
  u32x4 wof[8][4];
  u64 tag;
#define WOF_READ() do { u32 wa_ = L_WO + (kh * 224 + 4 * g + q4) * 128 + 16 * p4 + 64 * (q4 >> 1), wb_ = wa_ ^ 64, wc_ = wa_ + (g == 3 ? 0 : 2048), wd_ = wc_ ^ 64; \
    asm volatile("" : "+v"(wa_), "+v"(wb_), "+v"(wc_), "+v"(wd_)); \
    _Pragma("unroll") for (int i = 0; i < 8; ++i) _Pragma("unroll") for (int k = 0; k < 4; ++k) { \
      const u32x2 a_ = trread(L + (k >> 1 ? wb_ : wa_) + 28 * i * 128 + (k & 1) * 8), c_ = trread(L + (k >> 1 ? wd_ : wc_) + 28 * i * 128 + (k & 1) * 8); \
      wof[i][k] = u32x4{a_.x, a_.y, c_.x, c_.y}; } } while (0)
#define XF_READ(i) _Pragma("unroll") for (int s = 0; s < 14; ++s) xf[i][s] = *(LAS u32x4*)((s & 1 ? xr1 : xr0) + (i) * 14336 + (s >> 1) * 2048)
#define LN_STATS() do { f32x4 g1 = z4, g2 = z4; \
    _Pragma("unroll") for (int s = 0; s < 14; ++s) { g1 = mfma16(ones, xf[kh][s], g1); g2 = mfma16(xf[kh][s], xf[kh][s], g2); } \
    const float d01 = (p4 & 1) ? g2[1] : g2[0], d23 = (p4 & 1) ? g2[3] : g2[2], s2 = (p4 & 2) ? d23 : d01; \
    const float nmu = g1[0] * (-1.f / 448.f), var = fmaxf(s2 * (1.f / 448.f) - nmu * nmu, 0.f); \
    if (g == q4) *(LAS f32x2*)(L + L_ST1 + (w2 * 32 + kh * 16 + l15) * 8) = f32x2{nmu, __builtin_amdgcn_rsqf(var + 1e-5f)}; } while (0)
#define WPF_READ() do { u32 wp[8]; \
    _Pragma("unroll") for (int j = 0; j < 8; ++j) { \
      wp[j] = *(LAS u32*)(L + L_WPHI + 4 * g * 56 + l15 * 4 + ((j & 3) + 16 * (j >> 2)) * 56); \
      if (l15 >= 14 || (j >= 4 && g == 3)) wp[j] = 0u; } \
    _Pragma("unroll") for (int e = 0; e < 4; ++e) { \
      wpf[0][e] = __builtin_amdgcn_perm(wp[2 * e + 1], wp[2 * e], 0x05040100u); wpf[1][e] = __builtin_amdgcn_perm(wp[2 * e + 1], wp[2 * e], 0x07060302u); } \
    asm volatile("" : "+v"(wpf[0]), "+v"(wpf[1])); } while (0)
  {
    u32x4 xf[2][14], wpf[2];
    LAS char* xr0 = L + w2 * 28672 + l15 * 128 + ((g ^ (l15 & 7)) * 16);
    LAS char* xr1 = L + w2 * 28672 + l15 * 128 + (((4 + g) ^ (l15 & 7)) * 16);
    LAS char* pw = L + L_PKV + (w2 * 32 + l15) * 224 + g * 8;
    if constexpr (A) {
      if (wave < 2) dma4(x + (xp * 512 + li * 16) * 896 + wave * 7168, min(lane, 55) * 128, L_JUNK);
      if (wave == 3) { *(LAS u32*)(L + L_FLG + lane * 4) = 0u; *(LAS u32*)(L + L_FLG + 256 + lane * 4) = 0u; dma4(pc0, min((li % 5) * 8192 + lane * 128, CODE_PF), L_JUNK); }
      if (wave == 2) { dma16(wphi + head * 1568, lane * 16, L_WPHI); dma16(wphi + head * 1568, min(lane + 64, 97) * 16, L_WPHI + 1024); }
      BAR();
      asm volatile("s_load_dwordx2 %0, %1, 0x0 ; role A\n\ts_waitcnt lgkmcnt(0)" : "=s"(epoch) : "s"(ws) : "memory");
      tag = epoch * 6364136223846793005ull + 1442695040888963407ull;
      STAMP(1);
      lds_wait1(L, S_X0, w2);
      XF_READ(0);
#pragma unroll
      for (int i = 0; i < 7; ++i) dma16(wbase + 896 + 64 * i * 2688, wvo, L_WK + (4 * i + w2) * 1024);
      STAMP(27);
      lds_wait1(L, S_X1, w2);
      STAMP(2);
      XF_READ(1);
      lds_signal(L, S_XA, w2);
      STAMP(3);
      LN_STATS();
      WAITVM(0);
      STAMP(4);
      lds_signal(L, S_WK, w2);
      lds_wait2(L, S_WK, S_CS);
      STAMP(5);
      __builtin_amdgcn_s_setprio(2);
      f32x4 t[2][2];
      project(L, L_WK + wr0, L_WK + wr1, xf, w2, lane, t, [&](int n) __attribute__((always_inline)) { if (n == 22) lds_signal(L, S_KDONE, w2); if (n == 24) WPF_READ(); });
      STAMP(6);
      u32x4 P[2][2];
      feature_map(t, wpf, g, P);
      asm volatile("" : "+v"(P[0][0]), "+v"(P[0][1]), "+v"(P[1][0]), "+v"(P[1][1]));
      lds_wait(L, S_KDONE);
#pragma unroll
      for (int c = 0; c < 4; ++c) {
        LAS char* p = pw + (c >> 1) * 16 * 224 + (c & 1) * 64;
        *(LAS u32x2*)(p) = u32x2{P[c >> 1][c & 1].x, P[c >> 1][c & 1].y}; *(LAS u32x2*)(p + 32) = u32x2{P[c >> 1][c & 1].z, P[c >> 1][c & 1].w};
      }
      lds_signal(L, S_PHIK, w2);
      STAMP(7);
      u32 pr = L_PKV + (4 * g + q4) * 224 + 8 * p4;
      asm volatile("" : "+v"(pr));
      lds_wait2(L, S_V, S_PHIK);
      u32x4 pa[4], pb[8];
#pragma unroll
      for (int m = 0; m < 4; ++m) {
        pa[m] = trfrag(L + pr + w2 * 32 + m * 32 * 224, 16 * 224);
        pb[2 * m] = trfrag(L + pr + m * 32 * 224 + 128, 16 * 224); pb[2 * m + 1] = trfrag(L + pr + m * 32 * 224 + 160, 16 * 224);
      }
      f32x4 kv[2] = {};
#pragma unroll
      for (int m = 0; m < 8; ++m) kv[m & 1] = mfma16(pa[m >> 1], pb[m], kv[m & 1]);
      const u32x4 kvb = {pk(kv[0][0], kv[0][1]), pk(kv[0][2], kv[0][3]), pk(kv[1][0], kv[1][1]), pk(kv[1][2], kv[1][3])};
      ST16(rs, kvb, lane * 16, WS_KVP + ((head * 16 + tile) * 4 + w2) * 1024);
      STAMP(8);
      DRAIN();
      FLAG(WS_FKV + ((head * 4 + w2) * 16 + tile) * 128, 1);
      __builtin_amdgcn_s_setprio(0);
      STAMP(9);
      {
        const char* wos = wo + (r2 < 14 ? cg : 0) * 128 + w2 * 112 * 896;
#pragma unroll
        for (int n = 0; n < 14; ++n) dma16(wos + n * 8 * 896, (lane >> 3) * 896 + (((lane & 7) ^ ((lane >> 2) & 4)) * 16), L_WO + (w2 * 14 + n) * 1024);
      }
      poll_flags(ws, WS_FKV + ((head * 4 + w2) * 16 + l15) * 128, tag, 1);
      lds_signal(L, S_WO, w2);
      STAMP(10);
      u32x4 tt[16];
#pragma unroll
      for (int i = 0; i < 16; ++i) tt[i] = __builtin_bit_cast(u32x4, __builtin_amdgcn_raw_buffer_load_b128(rs, lane * 16, WS_KVP + head * 65536 + w2 * 1024 + i * 4096, 16));
      SCHED();
      const u32 one = g == q4 ? ((p4 & 1) ? 0x3f800000u : 0x3f80u) : 0u, s01 = p4 < 2 ? one : 0u, s23 = p4 < 2 ? 0u : one;
      const u32x4 sel0 = {s01, s23, 0u, 0u}, sel1 = {0u, 0u, s01, s23};
      f32x4 s0 = {}, s1 = {};
#pragma unroll
      for (int i = 0; i < 16; ++i) { s0 = mfma16(sel0, tt[i], s0); s1 = mfma16(sel1, tt[i], s1); }
      LAS char* kw = L + L_KVA + (w2 >> 1) * 1024 + lane * 16 + (w2 & 1) * 8;
      *(LAS u32x2*)(kw) = u32x2{pk(s0[0], s0[1]), pk(s0[2], s0[3])};
      *(LAS u32x2*)(kw + 2048) = u32x2{pk(s1[0], s1[1]), pk(s1[2], s1[3])};
      lds_signal(L, S_KVA, w2);
      STAMP(11);
      u64 fg0, fg1;
      const u32 fgo0 = WS_FG + (((((7 * r) >> 2) + hh) * 2 + (g & 1)) * 16 + min(l15, 13)) * 128, fgo1 = WS_FG + ((((7 * r + 6) >> 2) * 2 + (g & 1)) * 16 + min(l15, 13)) * 128;
      if (wave == 3) asm volatile("global_load_dwordx2 %0, %2, %4 sc1\n\tglobal_load_dwordx2 %1, %3, %4 sc1" : "=&v"(fg0), "=&v"(fg1) : "v"(fgo0), "v"(fgo1), "s"(ws) : "memory");
      lds_wait(L, S_WO);
      WOF_READ();
      STAMP(24);
      if (wave == 3) {
        asm volatile("s_waitcnt vmcnt(0)" : "+v"(fg0), "+v"(fg1)::"memory");
        if (!__all(fg0 == tag + 1 && fg1 == tag + 1)) { poll_flags(ws, fgo0, tag, 1); poll_flags(ws, fgo1, tag, 1); }
        __builtin_amdgcn_fence(__ATOMIC_ACQUIRE, "wavefront");
      }
      lds_signal(L, S_WOFA, w2);
    } else {
      const u32 vo = (lane >> 3) * 896 + (((lane & 7) ^ (lane >> 3)) * 16);
      const char* xb = x + (tile * 128 + w2 * 32) * 896;
#pragma unroll
      for (int n = 0; n < 28; ++n) {
        dma16(xb + ((n / 14) * 16 + (n & 1) * 8) * 896 + ((n % 14) >> 1) * 128, vo, w2 * 28672 + n * 1024);
        if (n == 3) { BAR(); asm volatile("s_load_dwordx2 %0, %1, 0x0 ; role B" : "=s"(epoch) : "s"(ws) : "memory"); }
        if (n == 21) { WAITVM(8); lds_signal(L, S_X0, w2); }
      }
      asm volatile("s_waitcnt lgkmcnt(0)" : "+s"(epoch)::"memory");
      tag = epoch * 6364136223846793005ull + 1442695040888963407ull;
      STAMP(1);
      XF_READ(0);
      WAITVM(0);
      lds_signal(L, S_X1, w2);
      STAMP(2);
      XF_READ(1);
      lds_signal(L, S_XB, w2);
      STAMP(3);
      LN_STATS();
      lds_signal(L, S_CS, w2);
      STAMP(4);
      lds_wait2(L, S_XA, S_XB);
      STAMP(8);
#pragma unroll
      for (int i = 0; i < 7; ++i) dma16(wbase + 1792 + 64 * i * 2688, wvo, L_WV + (4 * i + w2) * 1024);
      WAITVM(0);
      lds_signal(L, S_WV, w2);
      lds_wait2(L, S_WV, S_WK);
      STAMP(5);
      f32x4 t[2][2];
      u32x2 rq[8];
      const u32 lvo = has_unit ? g * 8 * 3584 + l15 * 8 : 0, lvo2 = has_unit2 ? (16 * (g & 1) + (g >> 1)) * 896 + l15 * 8 : 0;
      const char* us = (umat ? wu : wg) + ukp * 32 * 3584 + ufl * 128;
      const char* us2 = wd + (64 * (un >> 1) + 2 * (un & 1)) * 896 + uc * 128;
      project(L, L_WV + wr0, L_WV + wr1, xf, w2, lane, t, [&](int n) __attribute__((always_inline)) {
        if ((n & 3) == 0) dma16(wbase + 16 * n * 2688, wvo, L_WQ + (n + w2) * 1024);
        if (n == 27) lds_wait(L, S_KDONE);
      });
      STAMP(6);
#pragma unroll
      for (int c = 0; c < 2; ++c) {
        LAS char* p = pw + c * 16 * 224 + 128;
        *(LAS u32x2*)(p) = u32x2{pk(t[c][0][0], t[c][0][1]), pk(t[c][0][2], t[c][0][3])};
        *(LAS u32x2*)(p + 32) = g == 3 ? u32x2{0x3f80u, 0u} : u32x2{pk(t[c][1][0], t[c][1][1]), pk(t[c][1][2], t[c][1][3])};
      }
      lds_signal(L, S_V, w2);
      STAMP(7);
#pragma unroll
      for (int j = 0; j < 8; ++j) asm volatile("global_load_dwordx2 %0, %1, %2" : "=v"(rp[j]) : "v"(lvo), "s"(us + j * 3584) : "memory");
#pragma unroll
      for (int j = 0; j < 8; ++j) asm volatile("global_load_dwordx2 %0, %1, %2" : "=v"(rq[j]) : "v"(lvo2), "s"(us2 + ((j & 3) * 4 + (j >> 2) * 32) * 896) : "memory");
      WAITVM(16);
      lds_signal(L, S_WQ, w2);
      lds_wait(L, S_WQ);
      STAMP(9);
      project(L, L_WQ + wr0, L_WQ + wr1, xf, w2, lane, t, [&](int n) __attribute__((always_inline)) { if (n == 24) WPF_READ(); });
      STAMP(10);
      asm volatile("s_waitcnt vmcnt(0)" : RP_OPS, "+v"(rq[0]), "+v"(rq[1]), "+v"(rq[2]), "+v"(rq[3]), "+v"(rq[4]), "+v"(rq[5]), "+v"(rq[6]), "+v"(rq[7])::"memory");
      if (has_unit) {
        f32x4 c;
#pragma unroll
        for (int e = 0; e < 4; ++e) {
          const u32x4 f = piece(rp, e);
          ST16(rs, f, (g >> 1) * 1024 + (g & 1) * 512 + l15 * 16, WS_WGU + (4 * ufl + e) * SLOT + 2 * ukp * 1024 + umat * 256);
          c[e] = mfma16(ones, f, z4)[0];
        }
        ST16(rs, c, g == 0 ? l15 * 16 : NOWHERE, WS_CS + ufl * 7168 + ukp * 512 + umat * 256);
      }
      if (has_unit2) {
#pragma unroll
        for (int e = 0; e < 4; ++e) ST16(rs, piece(rq, e), (g >> 1) * 2048 + (g & 1) * 512 + l15 * 16 + (e >> 1) * 1024 + (e & 1) * 256, WS_WD + (uc * 112 + 2 * un) * 2048);
      }
      u32x4 pq[2][2];
      feature_map(t, wpf, g, pq);
      asm volatile("" : "+v"(pq[0][0]), "+v"(pq[0][1]), "+v"(pq[1][0]), "+v"(pq[1][1]));
      DRAIN();
      if (has_unit) FLAG(WS_FG + ((ufl * 2 + umat) * 16 + ukp) * 128, 1);
      if (has_unit2) FLAG(WS_FD + (uc * 64 + un) * 128, 1);
      STAMP(11);
      lds_wait(L, S_WO);
      WOF_READ();
      STAMP(24);
      lds_wait(L, S_KVA);
      STAMP(27);
      u32x4 ka[4];
#pragma unroll
      for (int i = 0; i < 4; ++i) ka[i] = *(LAS u32x4*)(L + L_KVA + i * 1024 + lane * 16);
      f32x4 o[2][2];
#pragma unroll
      for (int c = 0; c < 4; ++c) o[c >> 1][c & 1] = mfma16(ka[2 * (c & 1) + 1], pq[c >> 1][1], mfma16(ka[2 * (c & 1)], pq[c >> 1][0], z4));
      const f32x2 da = swp16(o[0][1][0], o[0][1][0]), db = swp16(o[1][1][0], o[1][1][0]);
      const float den[2] = {swp32(da.y, da.y).y, swp32(db.y, db.y).y};
#pragma unroll
      for (int c = 0; c < 2; ++c) {
        const float rd = frcp(den[c] + 1e-6f), r3 = g == 3 ? 0.f : rd;
        const u32x4 ob = {pk(o[c][0][0] * rd, o[c][0][1] * rd), pk(o[c][0][2] * rd, o[c][0][3] * rd), pk(o[c][1][0] * r3, o[c][1][1] * r3), pk(o[c][1][2] * r3, o[c][1][3] * r3)};
        ST16(rs, ob, lane * 16, WS_O + ((tile * 8 + 2 * w2 + c) * 16 + head) * 1024);
      }
      STAMP(12);
      DRAIN();
      FLAG(WS_FO + ((tile * 4 + w2) * 16 + head) * 128, 1);
      lds_signal(L, S_W2B, w2);
    }
  }
  STAMP(13);
  u32x4 hres = {0u, 0u, 0u, 0u};
  const int tok = tile2 * 128 + tg * 64 + tb * 16 + l15;
  const int xo = tok * 896 + cg * 128 + kh * 64 + g * 16;
  lds_wait2(L, S_W2B, S_WOFA);
  for (int i = PF1 + wave; i < PF2; i += 8) dma16c(gusrc + i * 1024, lane * 16, gu_dst(i));
  STAMP(23);
  if (r2 < 14) {
    const u32x4 xr = *(const u32x4*)(x + xo);
    poll_flags(ws, WS_FO + ((tile2 * 4 + 2 * tg + (tb >> 1)) * 16 + kh * 8 + (lane & 7)) * 128, tag, 1);
    STAMP(14);
    u32x4 of[8];
#pragma unroll
    for (int i = 0; i < 8; ++i) of[i] = __builtin_bit_cast(u32x4, __builtin_amdgcn_raw_buffer_load_b128(rs, lane * 16 + (i & 3) * 1024, WS_O + ((tile2 * 8 + tg * 4 + tb) * 16 + kh * 8 + (i & 4)) * 1024, 16));
    SCHED();
    f32x4 a[4] = {};
#pragma unroll
    for (int i = 0; i < 8; ++i)
#pragma unroll
      for (int k = 0; k < 4; ++k) a[k] = mfma16(wof[i][k], of[i], a[k]);
    *(LAS f32x4*)(L + L_EX + wave * 2048 + lane * 16) = kh ? a[0] : a[2];
    *(LAS f32x4*)(L + L_EX + wave * 2048 + 1024 + lane * 16) = kh ? a[1] : a[3];
    lds_signal(L, A ? S_EXA : S_EXB, w2);
    lds_wait1(L, A ? S_EXB : S_EXA, w2);
    STAMP(15);
    const f32x4 c0 = (kh ? a[2] : a[0]) + *(LAS f32x4*)(L + L_EX + (wave ^ 4) * 2048 + lane * 16);
    const f32x4 c1 = (kh ? a[3] : a[1]) + *(LAS f32x4*)(L + L_EX + (wave ^ 4) * 2048 + 1024 + lane * 16);
    const f32x4 h0 = c0 + f32x4{blo(xr.x), bhi(xr.x), blo(xr.y), bhi(xr.y)}, h1 = c1 + f32x4{blo(xr.z), bhi(xr.z), blo(xr.w), bhi(xr.w)};
    hres = u32x4{pk(h0[0], h0[1]), pk(h0[2], h0[3]), pk(h1[0], h1[1]), pk(h1[2], h1[3])};
    ST16(rs, hres, WS_H + ((tok >> 5) * 28 + 4 * cg + 2 * kh + (g >> 1)) * 1024 + ((g & 1) * 32 + (tok & 31)) * 16, 0);
    const f32x4 r0 = {blo(hres.x), bhi(hres.x), blo(hres.y), bhi(hres.y)}, r1 = {blo(hres.z), bhi(hres.z), blo(hres.w), bhi(hres.w)};
    const f32x4 a1 = r0 + r1, a2q = r0 * r0 + r1 * r1;
    float s1 = (a1[0] + a1[1]) + (a1[2] + a1[3]), s2 = (a2q[0] + a2q[1]) + (a2q[2] + a2q[3]);
    { const float t = swadd16(s1, s2); const f32x2 a2 = sw32(t); s1 = a2.x + a2.y; }
    __builtin_amdgcn_raw_buffer_store_b32(__builtin_bit_cast(u32, s1), rs, g < 2 ? WS_ST + ((2 * cg + kh) * 2048 + tok) * 8 + g * 4 : NOWHERE, 0, 16);
    DRAIN();
    FLAG(WS_FH + ((tile2 * 4 + 2 * tg + (tb >> 1)) * 32 + cg * 4 + (tb & 1) * 2 + kh) * 128, 1);
  } else {
    DRAIN();
    lds_signal(L, A ? S_EXA : S_EXB, w2);
  }
  STAMP(16);
  u32x4 wdf[2][8];
  const int wdo = WS_WD + (cg * 112 + 28 * w2) * 2048 + lane * 16;
  if (A) {
    if (((7 * r) >> 2) + w2 <= ((7 * r + 6) >> 2)) {
      const int fl = ((7 * r) >> 2) + w2;
      f32x4 c[7];
#pragma unroll
      for (int i = 0; i < 7; ++i) c[i] = __builtin_bit_cast(f32x4, __builtin_amdgcn_raw_buffer_load_b128(rs, lane * 16 + (i & 3) * 1024, WS_CS + fl * 7168 + (i & 4) * 1024, 16));
      const f32x4 sm = ((c[0] + c[1]) + (c[2] + c[3])) + ((c[4] + c[5]) + c[6]);
      const float e0 = swadd32(sm[0], sm[1]), e1 = swadd32(sm[2], sm[3]);
      const int j0 = 4 * fl + hh - 7 * r;
      if (j0 >= 0 && j0 < 7) *(LAS float*)(L + L_CS + j0 * 128 + (lane & 31) * 4) = e0;
      if (j0 + 2 >= 0 && j0 + 2 < 7) *(LAS float*)(L + L_CS + (j0 + 2) * 128 + (lane & 31) * 4) = e1;
    } else {
      __builtin_amdgcn_s_sleep(DLY3);
    }
    lds_signal(L, S_W2A, w2);
    poll_flags(ws, WS_FH + ((tile * 4 + w2) * 32 + min(lane & 31, 27)) * 128, tag, 1);
    lds_signal(L, S_HSEEN, w2);
  }
  STAMP(17);
  if constexpr (A) {
    const int T32 = tile * 4 + wave;
    f32x2 st[7];
#pragma unroll
    for (int i = 0; i < 7; ++i) st[i] = __builtin_bit_cast(f32x2, __builtin_amdgcn_raw_buffer_load_b64(rs, WS_ST + (7 * hh * 2048 + (lane & 31)) * 8, (i * 2048 + T32 * 32) * 8, 16));
    u32x4 hb[28];
#pragma unroll
    for (int s = 0; s < 28; ++s) hb[s] = __builtin_bit_cast(u32x4, __builtin_amdgcn_raw_buffer_load_b128(rs, lane * 16 + (s & 3) * 1024, WS_H + (T32 * 28 + (s & ~3)) * 1024, 16));
    lds_wait2(L, S_W2A, S_EXB);
    u32 wb_[3] = {(u32)lane * 16, (u32)lane * 16 + 2 * SLOT, (u32)lane * 16 + 4 * SLOT};
    asm volatile("" : "+v"(wb_[1]), "+v"(wb_[2]));
#define WFRAG(sl, s) (*(LAS u32x4*)(L + wb_[(sl) >> 1] + ((sl) & 1) * SLOT + (s) * 1024))
    LAS char* csl = L + L_CS + hh * 16;
    u32x4 ring[7];
    f32x4 cq[4];
#pragma unroll
    for (int k = 0; k < 4; ++k) cq[k] = *(LAS f32x4*)(csl + k * 32);
#pragma unroll
    for (int s = 0; s < 7; ++s) ring[s] = WFRAG(0, s);
    SCHED();
    float s1 = ((st[0].x + st[1].x) + (st[2].x + st[3].x)) + ((st[4].x + st[5].x) + st[6].x);
    float s2 = ((st[0].y + st[1].y) + (st[2].y + st[3].y)) + ((st[4].y + st[5].y) + st[6].y);
    { f32x2 a = sw32(s1); s1 = a.x + a.y; a = sw32(s2); s2 = a.x + a.y; }
    const float nmu = s1 * (-1.f / 448.f), ve = fmaxf(s2 * (1.f / 448.f) - nmu * nmu, 0.f) + 1e-5f;
    const float rstd = __builtin_amdgcn_rsqf(ve);
    const float k0 = -2.30220819f * rstd, k1 = -0.10294324f * rstd * rstd * rstd;
    const int pvo = WS_FHH + ((tile * 16 + r) * 4 + wave) * 128;
#define PROG(n) FLAG(pvo, n)
    const int hho = WS_HH + (T32 * 112 + 7 * r) * 1024;
    f32x16 acc[2];
    float y[8], ta = 0.f;
#pragma unroll
    for (int i = 0; i < 16; ++i) acc[0][i] = nmu * cq[i >> 2][i & 3];
    __builtin_amdgcn_s_setprio(GUPRIO);
    SCHED();
    auto gu_block = [&](auto JC) __attribute__((always_inline)) {
      constexpr int j = JC.value;
#pragma unroll
      for (int s = 0; s < 28; ++s) {
        acc[j & 1] = mfma32(ring[s % 7], hb[s], acc[j & 1]);
        if (s == 20 && (j + 1) * 28 <= PF2 && (j + 2) * 28 > PF2 && j < 4) lds_wait(L, S_WIN3);
        if (s == 20 && j == 4) lds_wait(L, S_REFILL);
        if (s + 7 < 28) ring[s % 7] = WFRAG(gu_slot(j), s + 7);
        else if (j < 6) ring[s % 7] = WFRAG(gu_slot(j + 1), s - 21);
        if (s == 0 && (j == 2 || j == 5)) lds_signal(L, j == 2 ? S_FREE01 : S_DONE4, wave);
        if (j > 0 && s >= 1 && s < 17) {
          const int i = (s - 1) >> 1;
          const float ga = acc[(j - 1) & 1][i];
          if ((s - 1) & 1) y[i] = ga * acc[(j - 1) & 1][i + 8] * frcp(fmaf(ta, ve, ve));
          else ta = fexp2(ga * fmaf(ga * ga, k1, k0));
        }
        if (j > 1 && s == 14) {
 WAITVM(0);
 PROG(j - 1); }
        if (j > 0 && s == 17) { const u32x4 yb = {pk(y[0], y[1]), pk(y[2], y[3]), pk(y[4], y[5]), pk(y[6], y[7])}; ST16(rs, yb, lane * 16, hho + (j - 1) * 1024); }
        if (j < 6 && s >= 16 && s < 20) cq[s - 16] = *(LAS f32x4*)(csl + (j + 1) * 128 + (s - 16) * 32);
        if (j < 6 && s >= 22 && s < 26) {
#pragma unroll
          for (int e = 0; e < 4; ++e) acc[(j + 1) & 1][4 * (s - 22) + e] = nmu * cq[s - 22][e];
        }
        SCHED();
      }
      if (j == 0) STAMP(18);
      if (j == 3) STAMP(26);
      if (j == 1) STAMP(28);
      if (j == 2) STAMP(29);
      if (j == 4) STAMP(30);
      if (j == 5) STAMP(31);
    };
    gu_block(IC<0>{}); STAMP2(1); gu_block(IC<1>{}); gu_block(IC<2>{}); gu_block(IC<3>{}); gu_block(IC<4>{}); gu_block(IC<5>{}); gu_block(IC<6>{}); STAMP2(2);
#pragma unroll
    for (int i = 0; i < 8; ++i) { const float ga = acc[0][i]; y[i] = ga * acc[0][i + 8] * frcp(fmaf(fexp2(ga * fmaf(ga * ga, k1, k0)), ve, ve)); }
    { const u32x4 yb = {pk(y[0], y[1]), pk(y[2], y[3]), pk(y[4], y[5]), pk(y[6], y[7])}; ST16(rs, yb, lane * 16, hho + 6 * 1024); }
    STAMP2(3);
    WAITVM(1);
    STAMP2(4);
    PROG(6);
    WAITVM(1);
    PROG(7);
    __builtin_amdgcn_s_setprio(0);
    STAMP(19);
  } else {
    for (int i = PF2 + w2; i < PF3; i += 4) dma16c(gusrc + i * 1024, lane * 16, gu_dst(i));
    STAMP(25);
    lds_wait(L, S_HSEEN);
    __builtin_amdgcn_s_sleep(DLY4);
    for (int i = PF3 + w2; i < 140; i += 4) dma16c(gusrc + i * 1024, lane * 16, gu_dst(i));
    if (r2 < 14) {
      poll_flags(ws, WS_FD + (cg * 64 + 14 * w2 + min(l15, 13)) * 128, tag, 1);
      lds_signal(L, S_WIN3, w2);
#pragma unroll
      for (int j = 0; j < 2; ++j)
#pragma unroll
        for (int i = 0; i < 8; ++i) wdf[j][i] = LD16(rs, wdo + (7 * (i >> 1) + j) * 2048 + (i & 1) * 1024);
      SCHED();
    } else { WAITVM(0); lds_signal(L, S_WIN3, w2); }
    STAMP2(23);
    lds_wait(L, S_FREE01);
    STAMP2(24);
    for (int i = w2; i < 56; i += 4) dma16c(gusrc + 5 * SLOT + i * 1024, lane * 16, gu_dst(i));
    if (r2 >= 14) { WAITVM(0); lds_signal(L, S_REFILL, w2); }
    else {
      const u32 po = WS_FHH + ((tile2 * 16 + 4 * w2 + ((lane >> 1) & 3)) * 4 + 2 * tg + (lane & 1)) * 128;
      const int ho = WS_HH + ((tile2 * 4 + 2 * tg) * 112 + 28 * w2) * 1024 + lane * 16;
      f32x16 acc[2][2] = {};
      u32x4 hf[2][8];
      u32 have = 0;
#define NEED(n) do { if (have < (n)) { _Pragma("nounroll") for (u32 sp = 0; sp < SPIN_CAP; ++sp) { const u64 d = __hip_atomic_load((const GAS u64*)(ws + po), RLX_AGENT) - tag; \
        u32 c = d <= 8 ? (u32)d : 0u; c = min(c, (u32)__builtin_amdgcn_update_dpp(0, (int)c, 0x121, 0xf, 0xf, false)); c = min(c, (u32)__builtin_amdgcn_update_dpp(0, (int)c, 0x122, 0xf, 0xf, false)); \
        c = min(c, (u32)__builtin_amdgcn_update_dpp(0, (int)c, 0x124, 0xf, 0xf, false)); have = __builtin_amdgcn_readfirstlane(c); if ((n) == 1) lds_signal(L, S_REFILL, w2);   if (have >= (n)) break; } \
        __builtin_amdgcn_fence(__ATOMIC_ACQUIRE, "wavefront"); } } while (0)
      STAMP2(16);
      NEED(1);
      STAMP2(1);
#pragma unroll
      for (int i = 0; i < 8; ++i) hf[0][i] = LD16(rs, ho + (i & 1) * 114688 + (7 * (i >> 1)) * 1024);
      SCHED();
#pragma unroll
      for (int j = 0; j < 7; ++j) {
        if (j < 6) {
          NEED(j + 2);
          STAMP2(j + 2);
#pragma unroll
          for (int i = 0; i < 8; ++i) hf[(j + 1) & 1][i] = LD16(rs, ho + (i & 1) * 114688 + (7 * (i >> 1) + j + 1) * 1024);
          SCHED();
        }
#pragma unroll
        for (int i = 0; i < 4; ++i)
#pragma unroll
          for (int T = 0; T < 2; ++T)
#pragma unroll
            for (int k = 0; k < 2; ++k) acc[T][k] = mfma32(wdf[j & 1][2 * i + k], hf[j & 1][2 * i + T], acc[T][k]);
        SCHED();
        if (j < 5) {
#pragma unroll
          for (int i = 0; i < 8; ++i) wdf[j & 1][i] = LD16(rs, wdo + (7 * (i >> 1) + j + 2) * 2048 + (i & 1) * 1024);
          SCHED();
        }
        if (j == 0) STAMP(18);
        if (j == 3) STAMP(26);
        if (j == 5) STAMP(19);
        STAMP2(8 + j);
      }
      lds_wait(L, S_DONE4);
#pragma unroll
      for (int T = 0; T < 2; ++T)
#pragma unroll
        for (int k = 0; k < 2; ++k)
#pragma unroll
          for (int q = 0; q < 4; ++q)
            *(LAS f32x4*)(L + L_RED + (((w2 * 2 + T) * 2 + k) * 4 + q) * 1024 + lane * 16) = f32x4{acc[T][k][4 * q], acc[T][k][4 * q + 1], acc[T][k][4 * q + 2], acc[T][k][4 * q + 3]};
      STAMP(20);
    }
  }
  if (r2 < 14) {
    BAR();
    float c[8] = {blo(hres.x), bhi(hres.x), blo(hres.y), bhi(hres.y), blo(hres.z), bhi(hres.z), blo(hres.w), bhi(hres.w)};
    LAS char* rq = L + L_RED + ((tb >> 1) * 8 + kh) * 1024 + ((g >> 1) * 32 + (tb & 1) * 16 + l15) * 16 + (g & 1) * 8;
#pragma unroll
    for (int w = 0; w < 4; ++w)
#pragma unroll
      for (int e = 0; e < 4; ++e) { const f32x2 v = *(LAS f32x2*)(rq + w * 16384 + (e >> 1) * 4096 + (e & 1) * 2048); c[e] += v.x; c[4 + e] += v.y; }
    *(u32x4*)(out + xo) = u32x4{pk(c[0], c[1]), pk(c[2], c[3]), pk(c[4], c[5]), pk(c[6], c[7])};
    if (A && b == 0 && tid == 0) *(u64*)(ws + WS_E) = tag;
  }
  STAMP(21);
  };
  if (wave < 4) role(IC<1>{}); else role(IC<0>{});
}

extern "C" void kernel_launch(void* const* d_in, const int* in_sizes, int n_in, void* d_out, int out_size, void* d_ws, size_t ws_size, hipStream_t stream) {
  (void)hipFuncSetAttribute((const void*)fused_block, hipFuncAttributeMaxDynamicSharedMemorySize, LDS_BYTES);
  fused_block<<<256, 512, LDS_BYTES, stream>>>((const char*)d_in[0], (const char*)d_in[1], (const char*)d_in[2], (const char*)d_in[3], (const char*)d_in[5], (const char*)d_in[6],
                                               (const char*)d_in[7], (char*)d_out, (char*)d_ws);
}
```

```cpp
#include <hip/hip_runtime.h>
#include <hip/hip_bf16.h>
#include <stdint.h>

typedef unsigned u32;
typedef unsigned long long u64;
typedef u32 u32x4 __attribute__((ext_vector_type(4)));
typedef u32 u32x2 __attribute__((ext_vector_type(2)));
typedef float f32x2 __attribute__((ext_vector_type(2)));
typedef float f32x4 __attribute__((ext_vector_type(4)));
typedef float f32x16 __attribute__((ext_vector_type(16)));
typedef short s16x4 __attribute__((ext_vector_type(4)));
typedef __bf16 bf16x2 __attribute__((ext_vector_type(2)));
typedef __bf16 bf16x8 __attribute__((ext_vector_type(8)));
#define LAS __attribute__((address_space(3)))
#define GAS __attribute__((address_space(1)))
#define DI __device__ __forceinline__
#define RLX_AGENT __ATOMIC_RELAXED, __HIP_MEMORY_SCOPE_AGENT

constexpr int WS_E = 0;
constexpr int WS_FKV = 8192;
constexpr int WS_FO = WS_FKV + 131072;
constexpr int WS_FH = WS_FO + 131072;
constexpr int WS_FG = WS_FH + 262144;
constexpr int WS_FD = WS_FG + 114688;
constexpr int WS_FHH = WS_FD + 57344;
constexpr int WS_CS = WS_FHH + 131072;
constexpr int WS_ST = WS_CS + 200704;
constexpr int WS_WGU = WS_ST + 229376;
constexpr int WS_WD = WS_WGU + 3211264;
constexpr int WS_KVP = WS_WD + 1605632;
constexpr int WS_O = WS_KVP + 1048576;
constexpr int WS_H = WS_O + 2097152;
constexpr int WS_HH = WS_H + 1835008;
#define UNITX4
#define HHOFF(T32, fb) (((((T32) >> 1) * 4 + (fb) / 28) * 7 + (fb) % 7) * 8 + ((fb) / 7 & 3) * 2 + ((T32) & 1))
constexpr int WS_STAMP = WS_HH + 7340032;
constexpr int WS_LOG = WS_STAMP + 256 * 8 * 32 * 8;
constexpr int WS_USED = WS_LOG + 4096;
constexpr int SLOT = 28672;
constexpr int L_WK = 4 * SLOT, L_WV = 0, L_WQ = 14336;
constexpr int L_PKV = 2 * SLOT;
constexpr int L_WO = 2 * SLOT;
constexpr int L_EX = 5 * SLOT;
constexpr int L_KVA = 5 * SLOT;
constexpr int L_ST1 = 5 * SLOT + 4096;
constexpr int L_WPHI = 5 * SLOT + 5120;
constexpr int L_CS = 159744;
constexpr int L_FLG = 160768;
constexpr int L_JUNK = 161280;
constexpr int L_STAMP = 161536;
constexpr int L_RED = SLOT;
constexpr int LDS_BYTES = 163840;
constexpr int gu_slot(int j) { return j == 0 || j == 5 || j > 6 ? 0 : (j == 1 || j == 6 ? 4 : j - 1); }
constexpr int CODE_PF = 39936;
constexpr u32 SPIN_CAP = 1u << 16;

#define STAMP(i) do {} while (0)
#define STAMP2(i) do {} while (0)
#define STAMP2X(i) do {} while (0)

DI f32x4 mfma16(u32x4 a, u32x4 b, f32x4 c) { return __builtin_amdgcn_mfma_f32_16x16x32_bf16(__builtin_bit_cast(bf16x8, a), __builtin_bit_cast(bf16x8, b), c, 0, 0, 0); }
DI f32x16 mfma32(u32x4 a, u32x4 b, f32x16 c) { return __builtin_amdgcn_mfma_f32_32x32x16_bf16(__builtin_bit_cast(bf16x8, a), __builtin_bit_cast(bf16x8, b), c, 0, 0, 0); }
DI u32 pk(float lo, float hi) { f32x2 p = {lo, hi}; return __builtin_bit_cast(u32, __builtin_convertvector(p, bf16x2)); }
DI float blo(u32 v) { return __builtin_bit_cast(float, v << 16); }
DI float bhi(u32 v) { return __builtin_bit_cast(float, v & 0xffff0000u); }
DI float dot2(u32 a, u32 b, float c) { return __builtin_amdgcn_fdot2_f32_bf16(__builtin_bit_cast(bf16x2, a), __builtin_bit_cast(bf16x2, b), c, false); }
DI u32x2 trread(LAS char* p) { return __builtin_bit_cast(u32x2, __builtin_amdgcn_ds_read_tr16_b64_v4i16((LAS s16x4*)p)); }
DI u32x4 trfrag(LAS char* p, int step) { u32x2 a = trread(p), b = trread(p + step); return u32x4{a.x, a.y, b.x, b.y}; }
DI float fexp2(float x) { return __builtin_amdgcn_exp2f(x); }
DI float frcp(float x) { return __builtin_amdgcn_rcpf(x); }
template <int C> DI float dppror(float x) { return __builtin_bit_cast(float, __builtin_amdgcn_update_dpp(0, __builtin_bit_cast(int, x), 0x120 + C, 0xf, 0xf, false)); }
DI float row_max(float x) { x = fmaxf(x, dppror<8>(x)); x = fmaxf(x, dppror<4>(x)); x = fmaxf(x, dppror<2>(x)); return fmaxf(x, dppror<1>(x)); }
DI float row_min(float x) { x = fminf(x, dppror<8>(x)); x = fminf(x, dppror<4>(x)); x = fminf(x, dppror<2>(x)); return fminf(x, dppror<1>(x)); }
DI float row_sum(float x) { x += dppror<8>(x); x += dppror<4>(x); x += dppror<2>(x); return x + dppror<1>(x); }
DI f32x2 sw16(float x) { u32 u = __builtin_bit_cast(u32, x); auto r = __builtin_amdgcn_permlane16_swap(u, u, false, false); return f32x2{__builtin_bit_cast(float, (u32)r[0]), __builtin_bit_cast(float, (u32)r[1])}; }
DI f32x2 sw32(float x) { u32 u = __builtin_bit_cast(u32, x); auto r = __builtin_amdgcn_permlane32_swap(u, u, false, false); return f32x2{__builtin_bit_cast(float, (u32)r[0]), __builtin_bit_cast(float, (u32)r[1])}; }
DI float grp_sum(float x) { f32x2 a = sw16(x); x = a.x + a.y; a = sw32(x); return a.x + a.y; }
DI float grp_max(float x) { f32x2 a = sw16(x); x = fmaxf(a.x, a.y); a = sw32(x); return fmaxf(a.x, a.y); }
DI float grp_min(float x) { f32x2 a = sw16(x); x = fminf(a.x, a.y); a = sw32(x); return fminf(a.x, a.y); }

#define BAR() asm volatile("s_waitcnt lgkmcnt(0)\n\ts_barrier" ::: "memory")
#define WAITVM(n) asm volatile("s_waitcnt vmcnt(" #n ")" ::: "memory")
#define DRAIN() do { asm volatile("s_waitcnt vmcnt(0)" ::: "memory"); __builtin_amdgcn_s_waitcnt(0x0F70); } while (0)
#define SCHED() __builtin_amdgcn_sched_barrier(0)
DI void dma16(const void* base, u32 voff, u32 lds) {
  u32 keep;
  asm volatile("s_mov_b32 %0, m0\n\ts_mov_b32 m0, %3\n\ts_nop 0\n\tglobal_load_lds_dwordx4 %1, %2\n\ts_mov_b32 m0, %0" : "=&s"(keep) : "v"(voff), "s"(base), "s"(lds) : "memory");
}
DI void dma16c(const void* base, u32 voff, u32 lds) {
  u32 keep;
  asm volatile("s_mov_b32 %0, m0\n\ts_mov_b32 m0, %3\n\ts_nop 0\n\tglobal_load_lds_dwordx4 %1, %2 sc1\n\ts_mov_b32 m0, %0" : "=&s"(keep) : "v"(voff), "s"(base), "s"(lds) : "memory");
}
DI void dma4(const void* base, u32 voff, u32 lds) {
  u32 keep;
  asm volatile("s_mov_b32 %0, m0\n\ts_mov_b32 m0, %3\n\ts_nop 0\n\tglobal_load_lds_dword %1, %2\n\ts_mov_b32 m0, %0" : "=&s"(keep) : "v"(voff), "s"(base), "s"(lds) : "memory");
}
DI void dma16c_if(u32 go, const void* base, u32 voff, u32 lds) {
  u32 keep;
  asm volatile("s_cmp_eq_u32 %4, 0\n\ts_cbranch_scc1 1f\n\ts_mov_b32 %0, m0\n\ts_mov_b32 m0, %3\n\ts_nop 0\n\tglobal_load_lds_dwordx4 %1, %2 sc1\n\ts_mov_b32 m0, %0\n1:"
               : "=&s"(keep) : "v"(voff), "s"(base), "s"(lds), "s"(go) : "memory", "scc");
}
template <int SI, int LI> DI void dma7(__amdgpu_buffer_rsrc_t rs, u32 voff, u32 so, u32 lds) {
  u32 t;
  asm volatile("s_add_u32 m0, %4, %6\n\ts_add_u32 %0, %3, %5\n\tbuffer_load_dwordx4 %1, %2, %0 offen sc1 lds\n\tbuffer_load_dwordx4 %1, %2, %0 offen offset:1024 sc1 lds\n\t"
               "buffer_load_dwordx4 %1, %2, %0 offen offset:2048 sc1 lds\n\tbuffer_load_dwordx4 %1, %2, %0 offen offset:3072 sc1 lds\n\t"
               "s_add_u32 m0, m0, 4096\n\ts_add_u32 %0, %0, 4096\n\tbuffer_load_dwordx4 %1, %2, %0 offen sc1 lds\n\tbuffer_load_dwordx4 %1, %2, %0 offen offset:1024 sc1 lds\n\t"
               "buffer_load_dwordx4 %1, %2, %0 offen offset:2048 sc1 lds"
               : "=&s"(t) : "v"(voff), "s"(rs), "s"(so), "s"(lds), "n"(SI), "n"(LI) : "memory", "scc");
}
DI void dma8(__amdgpu_buffer_rsrc_t rs, u32 voff, u32 so, u32 lds, u32 lds7) {
  dma7<0, 0>(rs, voff, so, lds);
  u32 t;
  asm volatile("s_mov_b32 m0, %4\n\ts_add_u32 %0, %3, 7168\n\tbuffer_load_dwordx4 %1, %2, %0 offen sc1 lds" : "=&s"(t) : "v"(voff), "s"(rs), "s"(so), "s"(lds7) : "memory", "scc");
}
DI u32 vm_count() {
  u32 t;
  asm volatile("s_getreg_b32 %0, hwreg(HW_REG_IB_STS)" : "=s"(t) :: "memory");
  return (t & 15u) | ((t >> 18) & 48u);
}
DI u32 min8(u32 c) {
  c = min(c, (u32)__builtin_amdgcn_update_dpp(0, (int)c, 0x121, 0xf, 0xf, false)); c = min(c, (u32)__builtin_amdgcn_update_dpp(0, (int)c, 0x122, 0xf, 0xf, false));
  return __builtin_amdgcn_readfirstlane(min(c, (u32)__builtin_amdgcn_update_dpp(0, (int)c, 0x124, 0xf, 0xf, false)));
}
DI void dma4c(const void* base, u32 voff, u32 lds) {
  u32 keep;
  asm volatile("s_mov_b32 %0, m0\n\ts_mov_b32 m0, %3\n\ts_nop 0\n\tglobal_load_lds_dword %1, %2 sc1\n\ts_mov_b32 m0, %0" : "=&s"(keep) : "v"(voff), "s"(base), "s"(lds) : "memory");
}
#define LD16(rs, off) __builtin_bit_cast(u32x4, __builtin_amdgcn_raw_buffer_load_b128(rs, off, 0, 16))
#define ST16(rs, v, off, soff) do { const u32x4 d_ = __builtin_bit_cast(u32x4, v); __builtin_amdgcn_raw_buffer_store_b128(d_, rs, off, soff, 16); asm volatile("s_nop 1" :: "v"(d_) : "memory"); } while (0)

#define FLAG(off, n) __builtin_amdgcn_raw_buffer_store_b64(__builtin_bit_cast(u32x2, tag + (n)), rs, fvo, off, 16)
#define FLAGC(c, off, n) __builtin_amdgcn_raw_buffer_store_b64(__builtin_bit_cast(u32x2, tag + (n)), rs, (c) ? fvo : NOWHERE, off, 16)
constexpr int NOWHERE = 0x7ffffff0;
DI void poll_flags(const char* ws, u32 off, u64 tag, u32 need) {
#pragma nounroll
  for (u32 sp = 0; sp < SPIN_CAP; ++sp) {
    u64 d = __hip_atomic_load((const GAS u64*)(ws + off), RLX_AGENT) - tag;
    if (__all(d >= need && d <= 8)) break;
  }
  __builtin_amdgcn_fence(__ATOMIC_ACQUIRE, "wavefront");
}
DI void lds_wait(LAS char* L, int sig) {
#pragma nounroll
  for (u32 sp = 0; sp < (SPIN_CAP << 4); ++sp) {
    const u32x4 v = *(volatile LAS u32x4*)(L + L_FLG + sig * 16);
    if (__builtin_amdgcn_readfirstlane(v.x & v.y & v.z & v.w)) break;
    __builtin_amdgcn_s_sleep(1);
  }
  asm volatile("" ::: "memory");
}
DI void lds_wait2(LAS char* L, int sig, int sig2) {
#pragma nounroll
  for (u32 sp = 0; sp < (SPIN_CAP << 4); ++sp) {
    const u32x4 v = *(volatile LAS u32x4*)(L + L_FLG + sig * 16) & *(volatile LAS u32x4*)(L + L_FLG + sig2 * 16);
    if (__builtin_amdgcn_readfirstlane(v.x & v.y & v.z & v.w)) break;
    __builtin_amdgcn_s_sleep(1);
  }
  asm volatile("" ::: "memory");
}
DI void lds_wait1(LAS char* L, int sig, int w) {
#pragma nounroll
  for (u32 sp = 0; sp < (SPIN_CAP << 4); ++sp) {
    if (__builtin_amdgcn_readfirstlane(*(volatile LAS u32*)(L + L_FLG + sig * 16 + w * 4))) break;
    __builtin_amdgcn_s_sleep(1);
  }
  asm volatile("" ::: "memory");
}
DI void lds_signal(LAS char* L, int sig, int w) {
  asm volatile("" ::: "memory");
  *(volatile LAS u32*)(L + L_FLG + sig * 16 + w * 4) = 1u;
}

template <int N> struct IC { static constexpr int value = N; };
template <int I, int N, class F> DI void sfor(F&& f) { if constexpr (I < N) { f(IC<I>{}); sfor<I + 1, N>(f); } }
#define LAM __attribute__((always_inline))
#define PIN(v) asm volatile("" : "+v"(v))
constexpr float LOG2E = 1.4426950408889634f;
template <int GAP, class F> DI void project(LAS char* L, u32 w0, u32 w1, const u32x4 (&xf)[2][14], int tp, int lane, f32x4 (&t)[2][2], F&& mid) {
  asm volatile("" : "+v"(w0), "+v"(w1));
  u32x4 ring[6];
  f32x4 cs[2] = {};
  f32x2 st[2];
  const u32x4 ones = {0x3f803f80u, 0x3f803f80u, 0x3f803f80u, 0x3f803f80u};
#pragma unroll
  for (int n = 0; n < 6; ++n) ring[n] = trfrag(L + (n & 1 ? w1 : w0) + (n >> 1) * 2048, 256);
#pragma unroll
  for (int i = 0; i < 4; ++i) t[i >> 1][i & 1] = f32x4{0.f, 0.f, 0.f, 0.f};
  SCHED();
#pragma unroll
  for (int n = 0; n < 28; ++n) {
    const u32x4 f = ring[n % 6];
    t[0][n & 1] = mfma16(f, xf[0][n >> 1], t[0][n & 1]);
    t[1][n & 1] = mfma16(f, xf[1][n >> 1], t[1][n & 1]);
    cs[n & 1] = mfma16(f, ones, cs[n & 1]);
    if (n + 6 < 28) ring[n % 6] = trfrag(L + (n & 1 ? w1 : w0) + ((n + 6) >> 1) * 2048 + (n + 6 >= 14 ? GAP : 0), 256);
    if (n == 22 || n == 23) st[n & 1] = *(LAS f32x2*)(L + L_ST1 + (tp * 32 + (n & 1) * 16 + (lane & 15)) * 8);
    mid(n);
    SCHED();
  }
#pragma unroll
  for (int tb = 0; tb < 2; ++tb)
#pragma unroll
    for (int k = 0; k < 2; ++k) t[tb][k] = (t[tb][k] + cs[k] * st[tb].x) * st[tb].y;
}
DI f32x2 swp16(float a, float b) { auto r = __builtin_amdgcn_permlane16_swap(__builtin_bit_cast(u32, a), __builtin_bit_cast(u32, b), false, false); return f32x2{__builtin_bit_cast(float, (u32)r[0]), __builtin_bit_cast(float, (u32)r[1])}; }
DI f32x2 swp32(float a, float b) { auto r = __builtin_amdgcn_permlane32_swap(__builtin_bit_cast(u32, a), __builtin_bit_cast(u32, b), false, false); return f32x2{__builtin_bit_cast(float, (u32)r[0]), __builtin_bit_cast(float, (u32)r[1])}; }
DI float swadd32(float a, float b) { const f32x2 s = swp32(a, b); return s.x + s.y; }
DI float swadd16(float a, float b) { const f32x2 s = swp16(a, b); return s.x + s.y; }
DI float mx2(float a, float b) { float r; asm("v_max_f32 %0, %1, %2" : "=v"(r) : "v"(a), "v"(b)); return r; }
DI float mx3(float a, float b, float c) { return fmaxf(fmaxf(a, b), c); }
DI float mn3(float a, float b, float c) { return fminf(fminf(a, b), c); }
DI void feature_map(const f32x4 (&t)[2][2], const u32x4 (&wpf)[2], int g, u32x4 (&P)[2][2]) {
  const f32x4 z4 = {0.f, 0.f, 0.f, 0.f};
  const bool v23 = g < 3;
  float p[2][8], m[4];
#pragma unroll
  for (int tb = 0; tb < 2; ++tb) {
    const u32x4 tq = {pk(t[tb][0][0], t[tb][0][1]), pk(t[tb][0][2], t[tb][0][3]), pk(t[tb][1][0], t[tb][1][1]), pk(t[tb][1][2], t[tb][1][3])};
    const f32x4 p0 = mfma16(wpf[0], tq, z4), p1 = mfma16(wpf[1], tq, z4);
#pragma unroll
    for (int i = 0; i < 8; ++i) { const float v = i < 4 ? p0[i] : p1[i - 4]; p[tb][i] = ((i & 3) < 2 || v23) ? v : p0[0]; }
    m[2 * tb] = mx3(mx3(p[tb][0], p[tb][1], p[tb][2]), mx3(p[tb][3], p[tb][4], p[tb][5]), mx3(p[tb][6], p[tb][7], p[tb][0]));
    m[2 * tb + 1] = -mn3(mn3(p[tb][0], p[tb][1], p[tb][2]), mn3(p[tb][3], p[tb][4], p[tb][5]), mn3(p[tb][6], p[tb][7], p[tb][0]));
  }
  {
    const f32x2 a = swp16(m[0], m[1]), b = swp16(m[2], m[3]);
    const f32x2 c = swp32(mx2(a.x, a.y), mx2(b.x, b.y));
    const float d = mx2(c.x, c.y) * -LOG2E;
    const f32x2 e = swp32(d, d), f0 = swp16(e.x, e.x), f1 = swp16(e.y, e.y);
    m[0] = f0.x; m[1] = f0.y; m[2] = f1.x; m[3] = f1.y;
  }
  float ex[4][8], sm[4];
#pragma unroll
  for (int c = 0; c < 4; ++c) {
#pragma unroll
    for (int i = 0; i < 8; ++i) {
      ex[c][i] = fexp2(fmaf(p[c >> 1][i], (c & 1) ? -LOG2E : LOG2E, m[c]));
      if ((i & 3) >= 2) ex[c][i] = v23 ? ex[c][i] : 0.f;
    }
    sm[c] = ((ex[c][0] + ex[c][1]) + (ex[c][2] + ex[c][3])) + ((ex[c][4] + ex[c][5]) + (ex[c][6] + ex[c][7]));
  }
  {
    const float d = frcp(swadd32(swadd16(sm[0], sm[1]), swadd16(sm[2], sm[3])));
    const f32x2 e = swp32(d, d), f0 = swp16(e.x, e.x), f1 = swp16(e.y, e.y);
    sm[0] = f0.x; sm[1] = f0.y; sm[2] = f1.x; sm[3] = f1.y;
  }
#pragma unroll
  for (int c = 0; c < 4; ++c)
#pragma unroll
    for (int e = 0; e < 4; ++e) P[c >> 1][c & 1][e] = pk(ex[c][2 * e] * sm[c], ex[c][2 * e + 1] * sm[c]);
}
DI u32x4 piece(const u32x4 (&rp)[8], int e) {
  u32x4 f;
#pragma unroll
  for (int m = 0; m < 4; ++m) f[m] = __builtin_amdgcn_perm(rp[2 * m + 1][e >> 1], rp[2 * m][e >> 1], (e & 1) ? 0x07060302u : 0x05040100u);
  return f;
}
#define RP_OPS "+v"(rp[0]), "+v"(rp[1]), "+v"(rp[2]), "+v"(rp[3]), "+v"(rp[4]), "+v"(rp[5]), "+v"(rp[6]), "+v"(rp[7])
#define PF1 0
#define PF2 56
#define PF3 120
#define TP0 -1
#define TP1 13
#define TP2 -1
#define TOUCH 5
#define NW3 4
#define THRN0 8
#define DNPRIO 0
#define NWD 4
#define MIDCHK 0
#define HELP 0
#define NHW 7
#define W3LATE 0
#define W3WAIT 1
#define ROT 1
#define EXPT 0
#define PFM 12
#define PSLP 4
#define PLAG 0
#define WTOUCH 0
#define REPLATE 0
#define WB0 0
#define WB1 4
#define ULATE 0
#define VPRIO 3
#define KGO 20
#define QEARLY 1
#define DLY4 16
#define PUBS 5
#define THRN 5
#define DLY2 8
#define DLY3 8
#define GUPRIO 2
enum { S_FREE01 = 0, S_REFILL, S_DONE4, S_WIN3, S_WK, S_WV, S_WQ, S_WSEEN, S_KDONE, S_V, S_PHIK, S_KVP, S_KVA, S_WO, S_REPSEEN, S_HSEEN, S_X0, S_X1, S_XA, S_XB, S_CS, S_WOFA, S_WOFB, S_EXA, S_EXB, S_W2A, S_W2B, S_HFA, S_HFB, S_CONS, S_LAND, S_XF0 };
DI u32 gu_dst(int i) { return i < 28 ? i * 1024 : (i < 56 ? 3 * SLOT + i * 1024 : i * 1024 - SLOT); }

__global__ void __launch_bounds__(512, 2) fused_block(const char* __restrict__ x, const char* __restrict__ wqkv, const char* __restrict__ wphi, const char* __restrict__ wo,
                                                      const char* __restrict__ wg, const char* __restrict__ wu, const char* __restrict__ wd, char* __restrict__ out, char* ws) {
  extern __shared__ __attribute__((aligned(16))) char smem[];
  LAS char* L = (LAS char*)smem;
  const int tid = threadIdx.x, lane = tid & 63, wave = __builtin_amdgcn_readfirstlane(tid >> 6), w2 = wave & 3;
  const int b = blockIdx.x, li = b >> 3, xp = (b & 7) >> 1, xh = b & 1;
  const int tile = 4 * xp + (li >> 3), r = 8 * xh + (li & 7);
  const int tile2 = 4 * xp + 2 * xh + (li >> 4), r2 = li & 15;
  const int tg = r2 >= 7 ? 1 : 0, cg = r2 - 7 * tg;
  const int l15 = lane & 15, g = lane >> 4, q4 = l15 >> 2, p4 = lane & 3, hh = lane >> 5;
  const int head = r;
  asm volatile("" :: "s"(x), "s"(wqkv), "s"(wphi), "s"(wo), "s"(wg), "s"(wu), "s"(wd), "s"(out), "s"(ws));
  STAMP(0);
  u64 pc_; asm volatile("s_getpc_b64 %0" : "=s"(pc_));
  const char* pc0 = (const char*)(pc_ & ~127ull);
  const __amdgpu_buffer_rsrc_t rs = __builtin_amdgcn_make_buffer_rsrc((void*)ws, 0, WS_USED, 0x00020000);
  const u32x4 ones = {0x3f803f80u, 0x3f803f80u, 0x3f803f80u, 0x3f803f80u};
  const f32x4 z4 = {0.f, 0.f, 0.f, 0.f};
  auto role = [&](auto AC) __attribute__((always_inline)) {
  constexpr bool A = AC.value;
  if (A) asm volatile("; A" ::: "memory"); else asm volatile("; B" ::: "memory");
  __builtin_amdgcn_s_waitcnt(0x0F70);
  u64 epoch;
  u32x4 rp[8];
  const int fvo = lane < 16 ? lane * 8 : NOWHERE;
  const int uidx = (tile * 10 + __builtin_popcount(~0x2A54u & ((1u << head) - 1u))) * 4 + w2, unit = uidx * 147 / 160;
  const bool has_unit = !((0x2A54 >> head) & 1) && (uidx + 1) * 147 / 160 != unit;
  const int u3 = unit / 3, umat = unit - 3 * u3, ukq = u3 / 28, ufl = u3 - 28 * ukq;
  const bool ud = umat == 2;
  const char* gusrc = ws + WS_WGU + 7 * r * SLOT;
  const u32 guo = WS_WGU + 7 * r * SLOT + w2 * 7168;
#define GUBLK(j) dma7<(j) * SLOT, gu_slot(j) * SLOT>(rs, lane * 16, guo, w2 * 7168)
  const u32 wvo = (lane >> 2) * 2688 + (((lane & 3) ^ (2 * (lane >> 5))) * 16);
  const char* wbase = wqkv + head * 56 - (head & 1) * 8 + 16 * w2 * 2688;
  const u32 wr0 = (8 * g + q4) * 64 + (((p4 + (head & 1)) ^ (4 * (g & 1))) * 8), wr1 = (8 * g + q4) * 64 + ((((4 + p4 + (head & 1)) & 7) ^ (4 * (g & 1))) * 8);
  const int tb = w2; constexpr int kh = A ? 0 : 1;
  u32x4 wof[8][4];
  u64 tag;
#define WOF_READ(i0, i1) do { u32 wa_ = L_WO + (kh * 224 + 4 * g + q4) * 128 + 16 * p4 + 64 * (q4 >> 1), wb_ = wa_ ^ 64, wc_ = wa_ + (g == 3 ? 0 : 2048), wd_ = wc_ ^ 64; \
    asm volatile("" : "+v"(wa_), "+v"(wb_), "+v"(wc_), "+v"(wd_)); \
    _Pragma("unroll") for (int i = i0; i < i1; ++i) _Pragma("unroll") for (int k = 0; k < 4; ++k) { \
      const u32x2 a_ = trread(L + (k >> 1 ? wb_ : wa_) + 28 * i * 128 + (k & 1) * 8), c_ = trread(L + (k >> 1 ? wd_ : wc_) + 28 * i * 128 + (k & 1) * 8); \
      wof[i][k] = u32x4{a_.x, a_.y, c_.x, c_.y}; } } while (0)
#define XF_READ(i) _Pragma("unroll") for (int s = 0; s < 14; ++s) xf[i][s] = *(LAS u32x4*)((s & 1 ? xr1 : xr0) + (i) * 14336 + (s >> 1) * 2048)
#define LN_STATS() do { f32x4 g1 = z4, g2 = z4; \
    _Pragma("unroll") for (int s = 0; s < 14; ++s) { g1 = mfma16(ones, xf[kh][s], g1); g2 = mfma16(xf[kh][s], xf[kh][s], g2); } \
    const float d01 = (p4 & 1) ? g2[1] : g2[0], d23 = (p4 & 1) ? g2[3] : g2[2], s2 = (p4 & 2) ? d23 : d01; \
    const float nmu = g1[0] * (-1.f / 448.f), var = fmaxf(s2 * (1.f / 448.f) - nmu * nmu, 0.f); \
    if (g == q4) *(LAS f32x2*)(L + L_ST1 + (w2 * 32 + kh * 16 + l15) * 8) = f32x2{nmu, __builtin_amdgcn_rsqf(var + 1e-5f)}; } while (0)
#define WPF_STEP(n) do { \
    if ((n) == 0) { _Pragma("unroll") for (int j = 0; j < 8; ++j) wp[j] = *(LAS u32*)(L + L_WPHI + 4 * g * 56 + l15 * 4 + ((j & 3) + 16 * (j >> 2)) * 56); } \
    if ((n) >= 8 && (n) < 12) { const int e = (n) - 8; const bool z = l15 >= 14 || (e >= 2 && g == 3); \
      wpf[0][e] = z ? 0u : __builtin_amdgcn_perm(wp[2 * e + 1], wp[2 * e], 0x05040100u); wpf[1][e] = z ? 0u : __builtin_amdgcn_perm(wp[2 * e + 1], wp[2 * e], 0x07060302u); } \
    if ((n) == 12) asm volatile("" : "+v"(wpf[0]), "+v"(wpf[1])); } while (0)
  {
    u32x4 xf[2][14], wpf[2];
    u32 wp[8];
    LAS char* xr0 = L + w2 * 28672 + l15 * 128 + ((g ^ (l15 & 7)) * 16);
    LAS char* xr1 = L + w2 * 28672 + l15 * 128 + (((4 + g) ^ (l15 & 7)) * 16);
    LAS char* pw = L + L_PKV + (w2 * 32 + l15) * 224 + g * 8;
    if constexpr (A) {
      if (wave < 2) dma4(x + (xp * 512 + li * 16) * 896 + wave * 7168, min(lane, 55) * 128, L_JUNK);
      if (wave == 3) { *(LAS u32*)(L + L_FLG + lane * 4) = 0u; *(LAS u32*)(L + L_FLG + 256 + lane * 4) = 0u; dma4(pc0, min((li % 5) * 8192 + lane * 128, CODE_PF), L_JUNK); }
      if (wave == 2) { dma16(wphi + head * 1568, lane * 16, L_WPHI); dma16(wphi + head * 1568, min(lane + 64, 97) * 16, L_WPHI + 1024); }
      if (WTOUCH && wave < WTOUCH) dma4(wqkv + li * 14 * 2688 + (wave == 2 ? 0 : (wave + 1) * 896) + xh * 384, min(lane >> 2, 13) * 2688 + (lane & 3) * 128, L_JUNK);
      BAR();
      asm volatile("s_load_dwordx2 %0, %1, 0x0 ; role A\n\ts_waitcnt lgkmcnt(0)" : "=s"(epoch) : "s"(ws) : "memory");
      tag = epoch * 6364136223846793005ull + 1442695040888963407ull;
      STAMP(1);
      lds_wait1(L, S_X0, w2);
      XF_READ(0);
      if (w2 < 2) lds_signal(L, S_XF0, w2);
#pragma unroll
      for (int i = 0; i < 7; ++i) dma16(wbase + 896 + 64 * i * 2688, wvo, L_WK + (4 * i + w2) * 1024);
      STAMP(27);
      lds_wait1(L, S_X1, w2);
      STAMP(2);
      XF_READ(1);
      STAMP(3);
      LN_STATS();
      WAITVM(0);
      STAMP(4);
      lds_signal(L, S_WK, w2);
      lds_wait(L, S_WK);
      STAMP(5);
      __builtin_amdgcn_s_setprio(2);
      f32x4 t[2][2];
      u32x4 cw;
      project<0>(L, L_WK + wr0, L_WK + wr1, xf, w2, lane, t, [&](int n) LAM {
        WPF_STEP(n);
        if (n == KGO) lds_signal(L, S_KDONE, w2);
        if (n == 15) cw = *(volatile LAS u32x4*)(L + L_FLG + S_CS * 16);
        if (n == 21 && __builtin_expect(!__builtin_amdgcn_readfirstlane(cw.x & cw.y & cw.z & cw.w), 0)) lds_wait(L, S_CS);
      });
      STAMP(6);
      u32x4 P[2][2];
      feature_map(t, wpf, g, P);
#pragma unroll
      for (int c = 0; c < 4; ++c) {
        LAS char* p = pw + (c >> 1) * 16 * 224 + (c & 1) * 64;
        *(LAS u32x2*)(p) = u32x2{P[c >> 1][c & 1].x, P[c >> 1][c & 1].y}; *(LAS u32x2*)(p + 32) = u32x2{P[c >> 1][c & 1].z, P[c >> 1][c & 1].w};
      }
      lds_signal(L, S_PHIK, w2);
      STAMP(7);
      u32 pr = L_PKV + (4 * g + q4) * 224 + 8 * p4;
      asm volatile("" : "+v"(pr));
      lds_wait2(L, S_V, S_PHIK);
      u32x4 pa[4], pb[8];
#pragma unroll
      for (int m = 0; m < 4; ++m) {
        pa[m] = trfrag(L + pr + w2 * 32 + m * 32 * 224, 16 * 224);
        pb[2 * m] = trfrag(L + pr + m * 32 * 224 + 128, 16 * 224); pb[2 * m + 1] = trfrag(L + pr + m * 32 * 224 + 160, 16 * 224);
      }
      lds_signal(L, S_KVP, w2);
      f32x4 kv[2] = {};
#pragma unroll
      for (int m = 0; m < 8; ++m) kv[m & 1] = mfma16(pa[m >> 1], pb[m], kv[m & 1]);
      const u32x4 kvb = {pk(kv[0][0], kv[0][1]), pk(kv[0][2], kv[0][3]), pk(kv[1][0], kv[1][1]), pk(kv[1][2], kv[1][3])};
      ST16(rs, kvb, lane * 16, WS_KVP + ((head * 16 + tile) * 4 + w2) * 1024);
      STAMP(8);
      lds_wait(L, S_KVP);
      DRAIN();
      FLAG(WS_FKV + ((head * 4 + w2) * 16 + tile) * 128, 1);
      __builtin_amdgcn_s_setprio(0);
      STAMP(9);
      {
        const char* wos = wo + (r2 < 14 ? cg : 0) * 128 + w2 * 112 * 896;
#pragma unroll
        for (int n = 0; n < 14; ++n) dma16(wos + n * 8 * 896, (lane >> 3) * 896 + (((lane & 7) ^ ((lane >> 2) & 4)) * 16), L_WO + (w2 * 14 + n) * 1024);
      }
      poll_flags(ws, WS_FKV + ((head * 4 + w2) * 16 + l15) * 128, tag, 1);
      lds_signal(L, S_WO, w2);
      STAMP(10);
      u32x4 tt[16];
#pragma unroll
      for (int i = 0; i < 16; ++i) tt[i] = __builtin_bit_cast(u32x4, __builtin_amdgcn_raw_buffer_load_b128(rs, lane * 16, WS_KVP + head * 65536 + w2 * 1024 + i * 4096, 16));
      SCHED();
      const u32 one = g == q4 ? ((p4 & 1) ? 0x3f800000u : 0x3f80u) : 0u, s01 = p4 < 2 ? one : 0u, s23 = p4 < 2 ? 0u : one;
      const u32x4 sel0 = {s01, s23, 0u, 0u}, sel1 = {0u, 0u, s01, s23};
      f32x4 s0 = {}, s1 = {};
#pragma unroll
      for (int i = 0; i < 16; ++i) { s0 = mfma16(sel0, tt[i], s0); s1 = mfma16(sel1, tt[i], s1); }
      LAS char* kw = L + L_KVA + (w2 >> 1) * 1024 + lane * 16 + (w2 & 1) * 8;
      *(LAS u32x2*)(kw) = u32x2{pk(s0[0], s0[1]), pk(s0[2], s0[3])};
      *(LAS u32x2*)(kw + 2048) = u32x2{pk(s1[0], s1[1]), pk(s1[2], s1[3])};
      lds_signal(L, S_KVA, w2);
      STAMP(11);
      u64 fg0;
      const u32 fgo0 = WS_FG + (min(((7 * r) >> 2) + g, (7 * r + 6) >> 2) * 16 + (l15 & 8) + min(lane & 7, 6)) * 128;
      if (wave == 3) asm volatile("global_load_dwordx2 %0, %1, %2 sc1" : "=v"(fg0) : "v"(fgo0), "s"(ws) : "memory");
      lds_wait(L, S_WO);
      WOF_READ(0, 8);
      STAMP(24);
      if (wave == 3) {
        asm volatile("s_waitcnt vmcnt(0)" : "+v"(fg0)::"memory");
        if (!__all(fg0 == tag + 1)) poll_flags(ws, fgo0, tag, 1);
        __builtin_amdgcn_fence(__ATOMIC_ACQUIRE, "wavefront");
      }
      lds_signal(L, S_WOFA, w2);
    } else {
      const u32 vo = (lane >> 3) * 896 + (((lane & 7) ^ (lane >> 3)) * 16);
      const char* xb = x + (tile * 128 + w2 * 32) * 896;
#pragma unroll
      for (int n = 0; n < 28; ++n) {
        dma16(xb + ((n / 14) * 16 + (n & 1) * 8) * 896 + ((n % 14) >> 1) * 128, vo, w2 * 28672 + n * 1024);
        if (n == 3) { BAR(); asm volatile("s_load_dwordx2 %0, %1, 0x0 ; role B" : "=s"(epoch) : "s"(ws) : "memory"); }
        if (n == 21) { WAITVM(8); lds_signal(L, S_X0, w2); }
      }
      asm volatile("s_waitcnt lgkmcnt(0)" : "+s"(epoch)::"memory");
      tag = epoch * 6364136223846793005ull + 1442695040888963407ull;
      STAMP(1);
      XF_READ(0);
      if (w2 < 2) lds_signal(L, S_XF0, 2 + w2);
      WAITVM(0);
      lds_signal(L, S_X1, w2);
      STAMP(2);
      lds_wait(L, S_XF0);
      XF_READ(1);
      SCHED();
#define WDST(c) ((c) * 1024 + ((c) >= 14 ? SLOT - 14336 : 0))
#pragma unroll
      for (int i = 0; i < 7; ++i) dma16(wbase + 1792 + 64 * i * 2688, wvo, L_WV + WDST(4 * i + w2));
      STAMP(3);
      LN_STATS();
      lds_signal(L, S_CS, w2);
      STAMP(4);
      if (ULATE) { WAITVM(0); lds_signal(L, S_WV, w2); }
      const int rg = lane >> 3;
      const u32 lvo = (ud ? (32 * (rg & 1) + (rg & 6)) * 896 : rg * 8 * 3584) + (lane & 7) * 16;
      const char* us = ud ? wd + ufl * 64 * 896 + ukq * 128 : (umat ? wu : wg) + ukq * 64 * 3584 + ufl * 128;
      int ust0 = ud ? 8 * 896 : 3584, ust1 = ud ? 896 : 4 * 3584;
      asm volatile("" : "+s"(us), "+s"(ust0), "+s"(ust1));
      if (has_unit) {
#pragma unroll
        for (int j = 0; j < 8; ++j) asm volatile("global_load_dwordx4 %0, %1, %2" : "=v"(rp[j]) : "v"(lvo), "s"(us + (j & 3) * ust0 + (j >> 2) * ust1) : "memory");
      } else WAITVM(0);
      if (!ULATE) { WAITVM(8); lds_signal(L, S_WV, w2); }
      lds_wait2(L, S_WV, S_WK);
      if (QEARLY) {
#pragma unroll
        for (int i = 0; i < 7; ++i) dma16(wbase + 64 * i * 2688, wvo, L_WQ + WDST(4 * i + w2));
      }
      if (KGO >= 0) lds_wait1(L, S_KDONE, w2);
      STAMP(5);
      f32x4 t[2][2];
      project<SLOT - 14336>(L, L_WV + wr0, L_WV + wr1, xf, w2, lane, t, [&](int n) LAM { if (n == 27) __builtin_amdgcn_s_setprio(VPRIO); });
      STAMP(6);
#pragma unroll
      for (int c = 0; c < 2; ++c) {
        LAS char* p = pw + c * 16 * 224 + 128;
        *(LAS u32x2*)(p) = u32x2{pk(t[c][0][0], t[c][0][1]), pk(t[c][0][2], t[c][0][3])};
        *(LAS u32x2*)(p + 32) = g == 3 ? u32x2{0x3f80u, 0u} : u32x2{pk(t[c][1][0], t[c][1][1]), pk(t[c][1][2], t[c][1][3])};
      }
      lds_signal(L, S_V, w2);
      __builtin_amdgcn_s_setprio(0);
      STAMP(7);
      if (!QEARLY) {
#pragma unroll
        for (int i = 0; i < 7; ++i) dma16(wbase + 64 * i * 2688, wvo, L_WQ + WDST(4 * i + w2));
      }
      asm volatile("s_waitcnt vmcnt(7)" : RP_OPS :: "memory");
      auto repack = [&]() LAM {
        if (has_unit) {
          const int so = (ud ? (rg >> 1) * 2048 : (rg >> 1) * 1024) + (rg & 1) * 512 + (lane & 7) * 16;
          const int sb = ud ? WS_WD + (ukq * 112 + 4 * ufl) * 2048 : WS_WGU + 4 * ufl * SLOT + ukq * 4096 + umat * 256;
          const int s1 = ud ? 256 : SLOT, s2 = ud ? 1024 : 2 * SLOT;
          float c[8];
#pragma unroll
          for (int e = 0; e < 8; ++e) {
            const u32x4 f = piece(rp, e);
            ST16(rs, f, so + (e & 1) * 128, sb + ((e >> 1) & 1) * s1 + (e >> 2) * s2);
            c[e] = mfma16(ones, f, z4)[0];
          }
          const f32x4 cv = g & 1 ? f32x4{c[4], c[5], c[6], c[7]} : f32x4{c[0], c[1], c[2], c[3]};
          ST16(rs, cv, g < 2 && !ud ? (l15 >> 3) * 512 + (lane & 7) * 32 + g * 16 : NOWHERE, WS_CS + ufl * 7168 + ukq * 1024 + (umat & 1) * 256);
        }
      };
#define UFLAG() FLAGC(has_unit, ud ? WS_FD + (ukq * 32 + ufl) * 128 : WS_FG + ((ufl * 2 + umat) * 8 + ukq) * 128, 1)
      if (!REPLATE) repack();
      STAMP(8);
      if (REPLATE || !has_unit) WAITVM(0); else WAITVM(9);
      lds_signal(L, S_WQ, w2);
      lds_wait(L, S_WQ);
      STAMP(9);
      project<SLOT - 14336>(L, L_WQ + wr0, L_WQ + wr1, xf, w2, lane, t, [&](int n) LAM {
        WPF_STEP(n);
        if (n == 14 && !REPLATE) { WAITVM(0); UFLAG(); }
      });
      STAMP(10);
      if (REPLATE == 2) repack();
      u32x4 pq[2][2];
      feature_map(t, wpf, g, pq);
      asm volatile("" : "+v"(pq[0][0]), "+v"(pq[0][1]), "+v"(pq[1][0]), "+v"(pq[1][1]));
      STAMP(11);
      if (REPLATE == 1) repack();
      if (WB0 > 0) { lds_wait(L, S_WO); WOF_READ(0, WB0); }
      STAMP(24);
      if (REPLATE == 2) { WAITVM(0); UFLAG(); }
      lds_wait(L, S_KVA);
      if (REPLATE == 1) { WAITVM(0); UFLAG(); }
      STAMP(27);
      u32x4 ka[4];
#pragma unroll
      for (int i = 0; i < 4; ++i) ka[i] = *(LAS u32x4*)(L + L_KVA + i * 1024 + lane * 16);
      f32x4 o[2][2];
#pragma unroll
      for (int c = 0; c < 4; ++c) o[c >> 1][c & 1] = mfma16(ka[2 * (c & 1) + 1], pq[c >> 1][1], mfma16(ka[2 * (c & 1)], pq[c >> 1][0], z4));
      const f32x2 da = swp16(o[0][1][0], o[0][1][0]), db = swp16(o[1][1][0], o[1][1][0]);
      const float den[2] = {swp32(da.y, da.y).y, swp32(db.y, db.y).y};
#pragma unroll
      for (int c = 0; c < 2; ++c) {
        const float rd = frcp(den[c] + 1e-6f), r3 = g == 3 ? 0.f : rd;
        const u32x4 ob = {pk(o[c][0][0] * rd, o[c][0][1] * rd), pk(o[c][0][2] * rd, o[c][0][3] * rd), pk(o[c][1][0] * r3, o[c][1][1] * r3), pk(o[c][1][2] * r3, o[c][1][3] * r3)};
        ST16(rs, ob, lane * 16, WS_O + ((tile * 8 + 2 * w2 + c) * 16 + head) * 1024);
      }
      STAMP(12);
      WOF_READ(WB0, WB1);
      DRAIN();
      FLAG(WS_FO + ((tile * 4 + w2) * 16 + head) * 128, 1);
      lds_signal(L, S_W2B, w2);
      WOF_READ(WB1, 8);
    }
  }
  STAMP(13);
  u32x4 hres = {0u, 0u, 0u, 0u};
  const int tok = tile2 * 128 + tg * 64 + tb * 16 + l15;
  const int xo = tok * 896 + cg * 128 + kh * 64 + g * 16;
  lds_wait2(L, S_W2B, S_WOFA);
  GUBLK(A ? 0 : 1);
  STAMP(23);
  bool fd_ok = false;
  if (r2 < 14) {
    const u32x4 xr = *(const u32x4*)(x + xo);
    poll_flags(ws, WS_FO + ((tile2 * 4 + 2 * tg + (tb >> 1)) * 16 + kh * 8 + (lane & 7)) * 128, tag, 1);
    STAMP(14);
    u32x4 of[8];
#pragma unroll
    for (int i = 0; i < 8; ++i) of[i] = __builtin_bit_cast(u32x4, __builtin_amdgcn_raw_buffer_load_b128(rs, lane * 16 + (i & 3) * 1024, WS_O + ((tile2 * 8 + tg * 4 + tb) * 16 + kh * 8 + (i & 4)) * 1024, 16));
    u64 fdv = 0;
    if (!A) fdv = __hip_atomic_load((const GAS u64*)(ws + WS_FD + (cg * 32 + 7 * w2 + min(lane & 7, 6)) * 128), RLX_AGENT);
    SCHED();
    f32x4 a[4] = {};
#pragma unroll
    for (int i = 0; i < 8; ++i)
#pragma unroll
      for (int k = 0; k < 4; ++k) a[k] = mfma16(wof[i][k], of[i], a[k]);
    fd_ok = __all(fdv == tag + 1);
    *(LAS f32x4*)(L + L_EX + wave * 2048 + lane * 16) = kh ? a[0] : a[2];
    *(LAS f32x4*)(L + L_EX + wave * 2048 + 1024 + lane * 16) = kh ? a[1] : a[3];
    lds_signal(L, A ? S_EXA : S_EXB, w2);
    lds_wait1(L, A ? S_EXB : S_EXA, w2);
    STAMP(15);
    const f32x4 c0 = (kh ? a[2] : a[0]) + *(LAS f32x4*)(L + L_EX + (wave ^ 4) * 2048 + lane * 16);
    const f32x4 c1 = (kh ? a[3] : a[1]) + *(LAS f32x4*)(L + L_EX + (wave ^ 4) * 2048 + 1024 + lane * 16);
    const f32x4 h0 = c0 + f32x4{blo(xr.x), bhi(xr.x), blo(xr.y), bhi(xr.y)}, h1 = c1 + f32x4{blo(xr.z), bhi(xr.z), blo(xr.w), bhi(xr.w)};
    hres = u32x4{pk(h0[0], h0[1]), pk(h0[2], h0[3]), pk(h1[0], h1[1]), pk(h1[2], h1[3])};
    ST16(rs, hres, WS_H + ((tok >> 5) * 28 + 4 * cg + 2 * kh + (g >> 1)) * 1024 + ((g & 1) * 32 + (tok & 31)) * 16, 0);
    const f32x4 r0 = {blo(hres.x), bhi(hres.x), blo(hres.y), bhi(hres.y)}, r1 = {blo(hres.z), bhi(hres.z), blo(hres.w), bhi(hres.w)};
    const f32x4 a1 = r0 + r1, a2q = r0 * r0 + r1 * r1;
    float s1 = (a1[0] + a1[1]) + (a1[2] + a1[3]), s2 = (a2q[0] + a2q[1]) + (a2q[2] + a2q[3]);
    { const float t = swadd16(s1, s2); const f32x2 a2 = sw32(t); s1 = a2.x + a2.y; }
    __builtin_amdgcn_raw_buffer_store_b32(__builtin_bit_cast(u32, s1), rs, g < 2 ? WS_ST + ((2 * cg + kh) * 2048 + tok) * 8 + g * 4 : NOWHERE, 0, 16);
    DRAIN();
    FLAG(WS_FH + ((tile2 * 4 + 2 * tg + (tb >> 1)) * 32 + cg * 4 + (tb & 1) * 2 + kh) * 128, 1);
  } else {
    DRAIN();
    lds_signal(L, A ? S_EXA : S_EXB, w2);
  }
  lds_signal(L, A ? S_HFA : S_HFB, w2);
  STAMP(16);
  u32x4 wdf[NWD][8];
  const int wdo = WS_WD + (cg * 112 + 28 * w2) * 2048;
  const u32 po = WS_FHH + ((tile2 * 16 + 4 * w2 + ((lane >> 1) & 3)) * 4 + 2 * tg + (lane & 1)) * 128;
  const u32 ho = WS_HH + ((tile2 * 2 + tg) * 4 + w2) * 57344;
#define HHLOAD(k, bf) dma8(rs, lane * 16, ho + (k) * 8192, ((bf) + 1) * SLOT + w2 * 7168, L_EX + (bf) * 4096 + w2 * 1024)
#define THR() asm volatile("s_waitcnt vmcnt(%0)" :: "n"(THRN) : "memory")
#define WLOAD1(j, i) wdf[(j) % NWD][i] = __builtin_bit_cast(u32x4, __builtin_amdgcn_raw_buffer_load_b128(rs, lane * 16 + ((i) & 1) * 1024, wdo + (7 * ((i) >> 1) + (j)) * 2048, 16))
#define THR0() asm volatile("s_waitcnt vmcnt(%0)" :: "n"(THRN0) : "memory")
#define WLOAD(j) _Pragma("unroll") for (int i = 0; i < 8; ++i) { THR0(); WLOAD1(j, i); }
  if (A) {
    if (((7 * r) >> 2) + w2 <= ((7 * r + 6) >> 2)) {
      const int fl = ((7 * r) >> 2) + w2;
      f32x4 c[7];
#pragma unroll
      for (int i = 0; i < 7; ++i) c[i] = __builtin_bit_cast(f32x4, __builtin_amdgcn_raw_buffer_load_b128(rs, lane * 16 + (i & 3) * 1024, WS_CS + fl * 7168 + (i & 4) * 1024, 16));
      const f32x4 sm = ((c[0] + c[1]) + (c[2] + c[3])) + ((c[4] + c[5]) + c[6]);
      const float e0 = swadd32(sm[0], sm[1]), e1 = swadd32(sm[2], sm[3]);
      const int j0 = 4 * fl + 2 * (lane & 1) - 7 * r, cr = ((g & 1) * 16 + hh * 8 + (l15 >> 1)) * 4;
      if (j0 >= 0 && j0 < 7) *(LAS float*)(L + L_CS + j0 * 128 + cr) = e0;
      if (j0 + 1 >= 0 && j0 + 1 < 7) *(LAS float*)(L + L_CS + (j0 + 1) * 128 + cr) = e1;
    } else {
      __builtin_amdgcn_s_sleep(DLY3);
    }
    lds_signal(L, S_W2A, w2);
    poll_flags(ws, WS_FH + ((tile * 4 + w2) * 32 + min(lane & 31, 27)) * 128, tag, 1);
    lds_signal(L, S_HSEEN, w2);
  }
  STAMP(17);
  if constexpr (A) {
    const int T32 = tile * 4 + wave;
    f32x2 st[7];
#pragma unroll
    for (int i = 0; i < 7; ++i) st[i] = __builtin_bit_cast(f32x2, __builtin_amdgcn_raw_buffer_load_b64(rs, WS_ST + (7 * hh * 2048 + (lane & 31)) * 8, (i * 2048 + T32 * 32) * 8, 16));
    u32x4 hb[28];
#pragma unroll
    for (int s = 0; s < 28; ++s) hb[s] = __builtin_bit_cast(u32x4, __builtin_amdgcn_raw_buffer_load_b128(rs, lane * 16 + (s & 3) * 1024, WS_H + (T32 * 28 + (s & ~3)) * 1024, 16));
    lds_wait2(L, S_W2A, S_EXB);
    u32 wb_[3] = {(u32)lane * 16, (u32)lane * 16 + 2 * SLOT, (u32)lane * 16 + 4 * SLOT};
    asm volatile("" : "+v"(wb_[1]), "+v"(wb_[2]));
#define WFRAG(sl, s) (*(LAS u32x4*)(L + wb_[(sl) >> 1] + ((sl) & 1) * SLOT + (s) * 1024))
    LAS char* csl = L + L_CS + hh * 16;
    u32x4 ring[7];
    f32x4 cq[4];
#pragma unroll
    for (int k = 0; k < 4; ++k) cq[k] = *(LAS f32x4*)(csl + k * 32);
#pragma unroll
    for (int s = 0; s < 7; ++s) ring[s] = WFRAG(0, s);
    SCHED();
    float s1 = ((st[0].x + st[1].x) + (st[2].x + st[3].x)) + ((st[4].x + st[5].x) + st[6].x);
    float s2 = ((st[0].y + st[1].y) + (st[2].y + st[3].y)) + ((st[4].y + st[5].y) + st[6].y);
    { f32x2 a = sw32(s1); s1 = a.x + a.y; a = sw32(s2); s2 = a.x + a.y; }
    const float nmu = s1 * (-1.f / 448.f), ve = fmaxf(s2 * (1.f / 448.f) - nmu * nmu, 0.f) + 1e-5f;
    const float rstd = __builtin_amdgcn_rsqf(ve);
    const float k0 = -2.30220819f * rstd, k1 = -0.10294324f * rstd * rstd * rstd;
    const int pvo = WS_FHH + ((tile * 16 + r) * 4 + wave) * 128;
#define PROG(n) FLAG(pvo, n)
    u32 pub = 0, ptmp; const u32 pav = pvo + l15 * 8;
    u64 pubv, tagv = tag;
    asm volatile("" : "+v"(tagv));
#define TRYPUB(cnt) asm volatile("s_getreg_b32 %1, hwreg(HW_REG_IB_STS, 0, 4)\n\ts_sub_i32 %1, %6, %1\n\ts_cmp_gt_i32 %1, %0\n\ts_cbranch_scc0 1f\n\ts_mov_b32 %0, %1\n\t" \
                               "v_mad_u64_u32 %2, vcc, %1, 1, %5\n\tglobal_store_dwordx2 %3, %2, %4 sc1\n1:" \
                               : "+s"(pub), "=&s"(ptmp), "=&v"(pubv) : "v"(pav), "s"(ws), "v"(tagv), "n"(cnt) : "memory", "scc", "vcc")
    const int hho = WS_HH + (((T32 >> 1) * 4 + (r >> 2)) * 56 + (r & 3) * 2 + (T32 & 1)) * 1024;
    f32x16 acc[2];
    float y[8], gu[8];
    u32x4 yb;
#pragma unroll
    for (int i = 0; i < 16; ++i) acc[0][i] = nmu * cq[i >> 2][i & 3];
    __builtin_amdgcn_s_setprio(GUPRIO);
    SCHED();
#define LT(i)
    auto gu_block = [&](auto JC) __attribute__((always_inline)) {
      constexpr int j = JC.value;
      sfor<0, 28>([&](auto SC) LAM {
        constexpr int s = SC.value;
        acc[j & 1] = mfma32(ring[s % 7], hb[s], acc[j & 1]);
        if constexpr (s == 20 && (j + 1) * 28 <= PF2 && (j + 2) * 28 > PF2 && j < 4) lds_wait(L, S_WIN3);
        if constexpr (s == 20 && j == 4) lds_wait(L, S_REFILL);
        if constexpr (s + 7 < 28) ring[s % 7] = WFRAG(gu_slot(j), s + 7);
        else if constexpr (j < 6) ring[s % 7] = WFRAG(gu_slot(j + 1), s - 21);
        if constexpr (s == 0 && (j == 1 || j == 2)) lds_signal(L, j == 1 ? S_WSEEN : S_FREE01, wave);
        if constexpr (s == 0 && j >= 3 && j <= 5) *(volatile LAS u32*)(L + L_FLG + S_DONE4 * 16 + wave * 4) = j == 5 ? 7u : j - 2u;
        if constexpr (j > 0) {
          sfor<0, 8>([&](auto EC) LAM {
            constexpr int i = EC.value, k = s - 1 - 2 * i;
            const float ga = acc[(j - 1) & 1][i];
            if constexpr (k == 0) y[i] = ga * ga;
            if constexpr (k == 1) y[i] = fmaf(y[i], k1, k0);
            if constexpr (k == 2) y[i] = ga * y[i];
            if constexpr (k == 3) y[i] = fexp2(y[i]);
            if constexpr (k == 4) y[i] = fmaf(y[i], ve, ve);
            if constexpr (k == 5) { gu[i] = ga * acc[(j - 1) & 1][i + 8]; PIN(gu[i]); }
            if constexpr (k == 6) y[i] = frcp(y[i]);
            if constexpr (k == 7) y[i] = y[i] * gu[i];
            if constexpr (k >= 0 && k < 8 && k != 5) PIN(y[i]);
          });
          if constexpr (s == 21) { yb[0] = pk(y[0], y[1]); yb[1] = pk(y[2], y[3]); }
          if constexpr (s == 22) yb[2] = pk(y[4], y[5]);
          if constexpr (s == 23) { yb[3] = pk(y[6], y[7]); ST16(rs, yb, lane * 16, hho + (j - 1) * 8192); }
          if constexpr (j > 1 && (s == TP0 || s == TP1 || s == TP2)) TRYPUB(j - 1);
          if constexpr (s == 27) TRYPUB(j);
        }
        if constexpr (j < 6 && s >= 17 && s < 21) cq[s - 17] = *(LAS f32x4*)(csl + (j + 1) * 128 + (s - 17) * 32);
        if constexpr (j < 6 && s >= 22 && s < 27) {
          sfor<0, 16>([&](auto EC) LAM {
            constexpr int e = EC.value;
            if constexpr ((e < 2 ? 22 : e < 4 ? 23 : 23 + (e >> 2)) == s) { float v = nmu * cq[e >> 2][e & 3]; PIN(v); acc[(j + 1) & 1][e] = v; }
          });
        }
        SCHED();
      });
      LT(j);
      if (j == 0) STAMP(18);
      if (j == 3) STAMP(26);
      if (j == 1) STAMP(28);
      if (j == 2) STAMP(29);
      if (j == 4) STAMP(30);
      if (j == 5) STAMP(31);
    };
    gu_block(IC<0>{}); gu_block(IC<1>{}); gu_block(IC<2>{}); gu_block(IC<3>{}); gu_block(IC<4>{}); gu_block(IC<5>{}); gu_block(IC<6>{});
#pragma unroll
    for (int i = 0; i < 8; ++i) { const float ga = acc[0][i]; y[i] = ga * acc[0][i + 8] * frcp(fmaf(fexp2(ga * fmaf(ga * ga, k1, k0)), ve, ve)); }
    TRYPUB(6);
    yb = u32x4{pk(y[0], y[1]), pk(y[2], y[3]), pk(y[4], y[5]), pk(y[6], y[7])};
    ST16(rs, yb, lane * 16, hho + 6 * 8192);
    __builtin_amdgcn_s_setprio(0);
    const u32 pz = L_EX + 12288 + w2 * 1024;
    dma16c(ws, po, pz);
    if (pub < 6) { WAITVM(2); PROG(6); WAITVM(2); } else { WAITVM(1); }
    PROG(7);
    STAMP(19);
    if (r2 < 14) {
      u32 have = 0, iss = 3, land = 3, cnt = 0, ep = 0, bf = 0;
      u64 ends = 0;
      WAITVM(2);
#pragma nounroll
      for (u32 sp = 0; land < 7; ++sp) {
        const u64 d = *(volatile LAS u64*)(L + pz + lane * 16) - tag;
        const u32 cons = __builtin_amdgcn_readfirstlane(*(volatile LAS u32*)(L + L_FLG + S_CONS * 16 + w2 * 4));
        have = max(have, min8(d <= 8 ? (u32)d : 0u));
        if (iss < min(have, cons + 3)) { HHLOAD(iss, bf); cnt += 8; ends |= (u64)(cnt & 0xffffu) << (16 * (iss - 3)); ++iss; bf = bf == 2 ? 0 : bf + 1; }
        const u32 v = vm_count();
        if (sp >= (SPIN_CAP << 2)) land = 6, iss = 7, ends = 0, cnt = 64;
        if (land < iss && v <= ((cnt - (u32)(ends >> (16 * (land - 3)))) & 0xffffu)) { ++land; *(volatile LAS u32*)(L + L_FLG + S_LAND * 16 + w2 * 4) = land; }
        if (have < 7 && v <= cnt - ep && (!PSLP || iss >= have)) { dma16c(ws, po, pz); ep = ++cnt; }
      }
    }
  } else {
    lds_wait2(L, S_HFA, S_HFB);
    GUBLK(2); GUBLK(3); GUBLK(4);
    if (r2 < 14) {
      if (!fd_ok) poll_flags(ws, WS_FD + (cg * 32 + 7 * w2 + min(lane & 7, 6)) * 128, tag, 1);
      __builtin_amdgcn_fence(__ATOMIC_ACQUIRE, "wavefront");
      sfor<0, 4>([&](auto KC) LAM {
        if constexpr (KC.value == NW3) { STAMP2(25); lds_wait(L, S_HSEEN); __builtin_amdgcn_s_sleep(DLY4); }
#pragma unroll
        for (int i = 0; i < 2 * NWD; ++i) { THR0(); WLOAD1(i >> 1, 2 * KC.value + (i & 1)); }
        SCHED();
        if constexpr (KC.value == 0) { asm volatile("s_waitcnt vmcnt(%0)" :: "n"(2 * NWD) : "memory"); lds_signal(L, S_WIN3, w2); }
      });
    } else { WAITVM(0); lds_signal(L, S_WIN3, w2); }
    STAMP2(23);
    lds_wait(L, S_WSEEN);
    GUBLK(5);
    lds_wait(L, S_FREE01);
    GUBLK(6);
    if (r2 >= 14) {
      WAITVM(0); lds_signal(L, S_REFILL, w2);
    } else {
      f32x16 acc[2][2] = {};
      u32 have = 0;
#define PLOAD() __hip_atomic_load((const GAS u64*)(ws + po), RLX_AGENT)
#define PMIN(pv) do { const u64 d = (pv) - tag; have = max(have, min8(d <= 8 ? (u32)d : 0u)); } while (0)
#define NEED(n) do { _Pragma("nounroll") for (u32 sp = 0; sp < (SPIN_CAP << 4); ++sp) { const u32x4 fr = *(volatile LAS u32x4*)(L + L_FLG + S_DONE4 * 16); \
          if (__builtin_amdgcn_readfirstlane(min(min(fr.x, fr.y), min(fr.z, fr.w))) >= (n) + PLAG) break; __builtin_amdgcn_s_sleep(1); } \
        _Pragma("nounroll") for (u32 sp = 0; have < (n) && sp < SPIN_CAP; ++sp) { PMIN(PLOAD()); if (PSLP && have < (n)) __builtin_amdgcn_s_sleep(PSLP); } } while (0)
      STAMP2(16);
      __builtin_amdgcn_s_setprio(DNPRIO);
      dma4c(ws + wdo + (7 * ((li >> 4) * 2 + tg) + NWD) * 2048, min(lane, (7 - NWD) * 16 - 1) * 128, L_JUNK);
      WAITVM(1);
      __builtin_amdgcn_s_waitcnt(0x0F70 | 1);
      lds_signal(L, S_REFILL, w2);
      u32 hb0 = SLOT + w2 * 7168 + lane * 16, hb2 = hb0 + 2 * SLOT, hb7 = L_EX + w2 * 1024 + lane * 16;
      asm volatile("" : "+v"(hb0), "+v"(hb2), "+v"(hb7));
#define HFRAG(bf, i) (*(LAS u32x4*)(L + ((i) < 7 ? ((bf) == 2 ? hb2 : hb0 + (bf) * SLOT) + (i) * 1024 : hb7 + (bf) * 4096)))
      NEED(1);
      STAMP2(1);
      HHLOAD(0, 0);
      NEED(2);
      STAMP2(2);
      HHLOAD(1, 1);
      WAITVM(8);
      u32x4 hf[8];
      u64 pv;
      u32 lv;
#pragma unroll
      for (int i = 0; i < 4; ++i) hf[i] = HFRAG(0, i);
      sfor<0, 7>([&](auto JC) LAM {
        constexpr int j = JC.value;
        if constexpr (j == 1) { PMIN(pv); NEED(3); STAMP2(3); HHLOAD(2, 2); }
        if constexpr (j >= 3) {
          if (__builtin_expect(__builtin_amdgcn_readfirstlane(lv) <= j, 0)) {
#pragma nounroll
            for (u32 sp = 0; sp < (SPIN_CAP << 4); ++sp)
              if (__builtin_amdgcn_readfirstlane(*(volatile LAS u32*)(L + L_FLG + S_LAND * 16 + w2 * 4)) > j) break;
            asm volatile("" ::: "memory");
#pragma unroll
            for (int i = 0; i < 4; ++i) hf[i] = HFRAG(j % 3, i);
          }
        }
        SCHED();
        sfor<0, 16>([&](auto MC) LAM {
          constexpr int m = MC.value;
          acc[(m >> 1) & 1][m & 1] = mfma32(wdf[j % NWD][2 * (m >> 2) + (m & 1)], hf[2 * (m >> 2) + ((m >> 1) & 1)], acc[(m >> 1) & 1][m & 1]);
          if constexpr (m < 4) hf[4 + m] = HFRAG(j % 3, 4 + m);
          if constexpr (m == 4 && j < 4) { asm volatile("" ::: "memory"); *(volatile LAS u32*)(L + L_FLG + S_CONS * 16 + w2 * 4) = j + 1; }
          if constexpr (j + NWD < 7 && (m & 3) >= 2) WLOAD1(j + NWD, 2 * (m >> 2) + (m & 1));
          if constexpr (j == 0 && m == 8) pv = PLOAD();
          if constexpr (j < 2 && m == PFM) asm volatile("s_waitcnt vmcnt(%0)" :: "n"((PFM >> 2) * 2 + (j == 0 ? 1 : 0)) : "memory");
          if constexpr (j >= 2 && j < 6 && m == PFM) { lv = *(volatile LAS u32*)(L + L_FLG + S_LAND * 16 + w2 * 4); asm volatile("" ::: "memory"); }
          if constexpr (j < 6 && m >= PFM && m < PFM + 2) { hf[2 * (m - PFM)] = HFRAG((j + 1) % 3, 2 * (m - PFM)); hf[2 * (m - PFM) + 1] = HFRAG((j + 1) % 3, 2 * (m - PFM) + 1); }
          SCHED();
        });
        if (j == 0) STAMP(18);
        if (j == 3) STAMP(26);
        if (j == 5) STAMP(19);
        STAMP2(8 + j);
      });
#pragma unroll
      for (int n = 0; n < 16; ++n) {
        const int T = n >> 3, k = (n >> 2) & 1, q = n & 3;
        *(LAS f32x4*)&HFRAG(1 + T, n & 7) = f32x4{acc[T][k][4 * q], acc[T][k][4 * q + 1], acc[T][k][4 * q + 2], acc[T][k][4 * q + 3]};
      }
      STAMP(20);
    }
  }
  if (r2 < 14) {
    BAR();
    float c[8] = {blo(hres.x), bhi(hres.x), blo(hres.y), bhi(hres.y), blo(hres.z), bhi(hres.z), blo(hres.w), bhi(hres.w)};
    const int rl = (kh * 32 + (tb & 1) * 16 + l15) * 16 + g * 4;
    LAS char* rq = L + ((tb >> 1) + 2) * SLOT + rl;
    LAS char* rq7 = L + L_EX + ((tb >> 1) + 1) * 4096 + rl;
#pragma unroll
    for (int w = 0; w < 4; ++w)
#pragma unroll
      for (int e = 0; e < 8; ++e) c[e] += *(LAS float*)(e == 7 ? rq7 + w * 1024 : rq + (7 * w + e) * 1024);
    { const u32x4 ov = {pk(c[0], c[1]), pk(c[2], c[3]), pk(c[4], c[5]), pk(c[6], c[7])}; asm volatile("global_store_dwordx4 %0, %1, %2 sc1\n\ts_nop 1" :: "v"(xo), "v"(ov), "s"(out) : "memory"); }
    if (A && b == 0 && tid == 0) __hip_atomic_store((GAS u64*)(ws + WS_E), tag, RLX_AGENT);
  }
  STAMP(21);
  };
  if (wave < 4) role(IC<1>{}); else role(IC<0>{});
}

extern "C" void kernel_launch(void* const* d_in, const int* in_sizes, int n_in, void* d_out, int out_size, void* d_ws, size_t ws_size, hipStream_t stream) {
  (void)hipFuncSetAttribute((const void*)fused_block, hipFuncAttributeMaxDynamicSharedMemorySize, LDS_BYTES);
  fused_block<<<256, 512, LDS_BYTES, stream>>>((const char*)d_in[0], (const char*)d_in[1], (const char*)d_in[2], (const char*)d_in[3], (const char*)d_in[5], (const char*)d_in[6],
                                               (const char*)d_in[7], (char*)d_out, (char*)d_ws);
}
```

```cpp
#include <hip/hip_runtime.h>
#include <hip/hip_bf16.h>
#include <stdint.h>

typedef unsigned u32;
typedef unsigned long long u64;
typedef u32 u32x4 __attribute__((ext_vector_type(4)));
typedef u32 u32x2 __attribute__((ext_vector_type(2)));
typedef float f32x2 __attribute__((ext_vector_type(2)));
typedef float f32x4 __attribute__((ext_vector_type(4)));
typedef float f32x16 __attribute__((ext_vector_type(16)));
typedef short s16x4 __attribute__((ext_vector_type(4)));
typedef __bf16 bf16x2 __attribute__((ext_vector_type(2)));
typedef __bf16 bf16x8 __attribute__((ext_vector_type(8)));
#define LAS __attribute__((address_space(3)))
#define GAS __attribute__((address_space(1)))
#define DI __device__ __forceinline__
#define RLX_AGENT __ATOMIC_RELAXED, __HIP_MEMORY_SCOPE_AGENT

constexpr int WS_E = 0;
constexpr int WS_FKV = 8192;
constexpr int WS_FO = WS_FKV + 131072;
constexpr int WS_FH = WS_FO + 131072;
constexpr int WS_FG = WS_FH + 262144;
constexpr int WS_FD = WS_FG + 114688;
constexpr int WS_FHH = WS_FD + 57344;
constexpr int WS_CS = WS_FHH + 262144;
constexpr int WS_ST = WS_CS + 200704;
constexpr int WS_WGU = WS_ST + 229376;
constexpr int WS_WD = WS_WGU + 3211264;
constexpr int WS_KVP = WS_WD + 1605632;
constexpr int WS_O = WS_KVP + 1048576;
constexpr int WS_H = WS_O + 2097152;
constexpr int WS_HH = WS_H + 1835008;
#define UNITX4
#define WDOFF(cg, fb, bl) (((((cg) * 4 + (fb) / 28) * 7 + (fb) % 7) * 4 + (fb) / 7 % 4) * 2 + (bl))
#define HHOFF(T32, fb) (((((T32) >> 1) * 4 + (fb) / 28) * 7 + (fb) % 7) * 8 + ((fb) / 7 & 3) * 2 + ((T32) & 1))
constexpr int WS_STAMP = WS_HH + 7340032;
constexpr int WS_LOG = WS_STAMP + 256 * 8 * 32 * 8;
constexpr int WS_USED = WS_LOG + 4096;
constexpr int SLOT = 28672;
constexpr int L_WK = 4 * SLOT, L_WV = 0, L_WQ = 14336;
constexpr int L_PKV = 2 * SLOT;
constexpr int L_WO = 3 * SLOT;
constexpr int L_EX = 5 * SLOT;
constexpr int L_KVA = 5 * SLOT;
constexpr int L_ST1 = 5 * SLOT + 4096;
constexpr int L_WPHI = 5 * SLOT + 5120;
constexpr int L_CS = 159744;
constexpr int L_FLG = 160768;
constexpr int L_JUNK = 161280;
constexpr int L_STAMP = 161536;
constexpr int L_RED = SLOT;
constexpr int LDS_BYTES = 163840;
constexpr int gu_slot(int j) { return j % 5; }
constexpr int CODE_PF = 41984;
constexpr u32 SPIN_CAP = 1u << 16;
#define LL 1
#define AP 1

#define STAMP(i) do {} while (0)
#define STAMP2(i) do {} while (0)
#define STAMP2X(i) do {} while (0)

DI f32x4 mfma16(u32x4 a, u32x4 b, f32x4 c) { return __builtin_amdgcn_mfma_f32_16x16x32_bf16(__builtin_bit_cast(bf16x8, a), __builtin_bit_cast(bf16x8, b), c, 0, 0, 0); }
DI f32x16 mfma32(u32x4 a, u32x4 b, f32x16 c) { return __builtin_amdgcn_mfma_f32_32x32x16_bf16(__builtin_bit_cast(bf16x8, a), __builtin_bit_cast(bf16x8, b), c, 0, 0, 0); }
DI u32 pk(float lo, float hi) { f32x2 p = {lo, hi}; return __builtin_bit_cast(u32, __builtin_convertvector(p, bf16x2)); }
DI float blo(u32 v) { return __builtin_bit_cast(float, v << 16); }
DI float bhi(u32 v) { return __builtin_bit_cast(float, v & 0xffff0000u); }
DI float dot2(u32 a, u32 b, float c) { return __builtin_amdgcn_fdot2_f32_bf16(__builtin_bit_cast(bf16x2, a), __builtin_bit_cast(bf16x2, b), c, false); }
DI u32x2 trread(LAS char* p) { return __builtin_bit_cast(u32x2, __builtin_amdgcn_ds_read_tr16_b64_v4i16((LAS s16x4*)p)); }
DI u32x4 trfrag(LAS char* p, int step) { u32x2 a = trread(p), b = trread(p + step); return u32x4{a.x, a.y, b.x, b.y}; }
DI float fexp2(float x) { return __builtin_amdgcn_exp2f(x); }
DI float frcp(float x) { return __builtin_amdgcn_rcpf(x); }
template <int C> DI float dppror(float x) { return __builtin_bit_cast(float, __builtin_amdgcn_update_dpp(0, __builtin_bit_cast(int, x), 0x120 + C, 0xf, 0xf, false)); }
DI float row_max(float x) { x = fmaxf(x, dppror<8>(x)); x = fmaxf(x, dppror<4>(x)); x = fmaxf(x, dppror<2>(x)); return fmaxf(x, dppror<1>(x)); }
DI float row_min(float x) { x = fminf(x, dppror<8>(x)); x = fminf(x, dppror<4>(x)); x = fminf(x, dppror<2>(x)); return fminf(x, dppror<1>(x)); }
DI float row_sum(float x) { x += dppror<8>(x); x += dppror<4>(x); x += dppror<2>(x); return x + dppror<1>(x); }
DI f32x2 sw16(float x) { u32 u = __builtin_bit_cast(u32, x); auto r = __builtin_amdgcn_permlane16_swap(u, u, false, false); return f32x2{__builtin_bit_cast(float, (u32)r[0]), __builtin_bit_cast(float, (u32)r[1])}; }
DI f32x2 sw32(float x) { u32 u = __builtin_bit_cast(u32, x); auto r = __builtin_amdgcn_permlane32_swap(u, u, false, false); return f32x2{__builtin_bit_cast(float, (u32)r[0]), __builtin_bit_cast(float, (u32)r[1])}; }
DI float grp_sum(float x) { f32x2 a = sw16(x); x = a.x + a.y; a = sw32(x); return a.x + a.y; }
DI float grp_max(float x) { f32x2 a = sw16(x); x = fmaxf(a.x, a.y); a = sw32(x); return fmaxf(a.x, a.y); }
DI float grp_min(float x) { f32x2 a = sw16(x); x = fminf(a.x, a.y); a = sw32(x); return fminf(a.x, a.y); }

#define BAR() asm volatile("s_waitcnt lgkmcnt(0)\n\ts_barrier" ::: "memory")
#define WAITVM(n) asm volatile("s_waitcnt vmcnt(" #n ")" ::: "memory")
#define DRAIN() do { asm volatile("s_waitcnt vmcnt(0)" ::: "memory"); __builtin_amdgcn_s_waitcnt(0x0F70); } while (0)
#define SCHED() __builtin_amdgcn_sched_barrier(0)
DI void dma16(const void* base, u32 voff, u32 lds) {
  u32 keep;
  asm volatile("s_mov_b32 %0, m0\n\ts_mov_b32 m0, %3\n\ts_nop 0\n\tglobal_load_lds_dwordx4 %1, %2\n\ts_mov_b32 m0, %0" : "=&s"(keep) : "v"(voff), "s"(base), "s"(lds) : "memory");
}
DI void dma16c(const void* base, u32 voff, u32 lds) {
  u32 keep;
  asm volatile("s_mov_b32 %0, m0\n\ts_mov_b32 m0, %3\n\ts_nop 0\n\tglobal_load_lds_dwordx4 %1, %2 sc1\n\ts_mov_b32 m0, %0" : "=&s"(keep) : "v"(voff), "s"(base), "s"(lds) : "memory");
}
DI void dma4(const void* base, u32 voff, u32 lds) {
  u32 keep;
  asm volatile("s_mov_b32 %0, m0\n\ts_mov_b32 m0, %3\n\ts_nop 0\n\tglobal_load_lds_dword %1, %2\n\ts_mov_b32 m0, %0" : "=&s"(keep) : "v"(voff), "s"(base), "s"(lds) : "memory");
}
DI void dma16c_if(u32 go, const void* base, u32 voff, u32 lds) {
  u32 keep;
  asm volatile("s_cmp_eq_u32 %4, 0\n\ts_cbranch_scc1 1f\n\ts_mov_b32 %0, m0\n\ts_mov_b32 m0, %3\n\ts_nop 0\n\tglobal_load_lds_dwordx4 %1, %2 sc1\n\ts_mov_b32 m0, %0\n1:"
               : "=&s"(keep) : "v"(voff), "s"(base), "s"(lds), "s"(go) : "memory", "scc");
}
template <int SI, int LI> DI void dma7(__amdgpu_buffer_rsrc_t rs, u32 voff, u32 so, u32 lds) {
  u32 t;
  asm volatile("s_add_u32 m0, %4, %6\n\ts_add_u32 %0, %3, %5\n\tbuffer_load_dwordx4 %1, %2, %0 offen sc1 lds\n\tbuffer_load_dwordx4 %1, %2, %0 offen offset:1024 sc1 lds\n\t"
               "buffer_load_dwordx4 %1, %2, %0 offen offset:2048 sc1 lds\n\tbuffer_load_dwordx4 %1, %2, %0 offen offset:3072 sc1 lds\n\t"
               "s_add_u32 m0, m0, 4096\n\ts_add_u32 %0, %0, 4096\n\tbuffer_load_dwordx4 %1, %2, %0 offen sc1 lds\n\tbuffer_load_dwordx4 %1, %2, %0 offen offset:1024 sc1 lds\n\t"
               "buffer_load_dwordx4 %1, %2, %0 offen offset:2048 sc1 lds"
               : "=&s"(t) : "v"(voff), "s"(rs), "s"(so), "s"(lds), "n"(SI), "n"(LI) : "memory", "scc");
}
DI void dma8(__amdgpu_buffer_rsrc_t rs, u32 voff, u32 so, u32 lds, u32 lds7) {
  dma7<0, 0>(rs, voff, so, lds);
  u32 t;
  asm volatile("s_mov_b32 m0, %4\n\ts_add_u32 %0, %3, 7168\n\tbuffer_load_dwordx4 %1, %2, %0 offen sc1 lds" : "=&s"(t) : "v"(voff), "s"(rs), "s"(so), "s"(lds7) : "memory", "scc");
}
DI u32 vm_count() {
  u32 t;
  asm volatile("s_getreg_b32 %0, hwreg(HW_REG_IB_STS)" : "=s"(t) :: "memory");
  return (t & 15u) | ((t >> 18) & 48u);
}
DI u32 min8(u32 c) {
  c = min(c, (u32)__builtin_amdgcn_update_dpp(0, (int)c, 0x121, 0xf, 0xf, false)); c = min(c, (u32)__builtin_amdgcn_update_dpp(0, (int)c, 0x122, 0xf, 0xf, false));
  c = min(c, (u32)__builtin_amdgcn_update_dpp(0, (int)c, 0x124, 0xf, 0xf, false));
  return __builtin_amdgcn_readfirstlane(min(c, (u32)__builtin_amdgcn_update_dpp(0, (int)c, 0x128, 0xf, 0xf, false)));
}
DI void dma4c(const void* base, u32 voff, u32 lds) {
  u32 keep;
  asm volatile("s_mov_b32 %0, m0\n\ts_mov_b32 m0, %3\n\ts_nop 0\n\tglobal_load_lds_dword %1, %2 sc1\n\ts_mov_b32 m0, %0" : "=&s"(keep) : "v"(voff), "s"(base), "s"(lds) : "memory");
}
#define LD16(rs, off) __builtin_bit_cast(u32x4, __builtin_amdgcn_raw_buffer_load_b128(rs, off, 0, 16))
#define ST16(rs, v, off, soff) do { const u32x4 d_ = __builtin_bit_cast(u32x4, v); __builtin_amdgcn_raw_buffer_store_b128(d_, rs, off, soff, 16); asm volatile("s_nop 1" :: "v"(d_) : "memory"); } while (0)

#define FLAG(off, n) __builtin_amdgcn_raw_buffer_store_b64(__builtin_bit_cast(u32x2, tag + (n)), rs, fvo, off, 16)
#define FLAGC(c, off, n) __builtin_amdgcn_raw_buffer_store_b64(__builtin_bit_cast(u32x2, tag + (n)), rs, (c) ? fvo : NOWHERE, off, 16)
constexpr int NOWHERE = 0x7ffffff0;
constexpr u32 EMPTY = 0xffffffffu;
DI u32 filled(u32 w) { return min(w, EMPTY - 1u); }
DI u64 peek(const char* ws, u32 off, bool on, u64 dflt) {
  u64 z = 0, v = dflt;
  asm volatile("" : "+v"(z));
  if (on) v = __hip_atomic_fetch_add((GAS u64*)(ws + off), z, __ATOMIC_RELAXED, AP == 2 ? __HIP_MEMORY_SCOPE_SYSTEM : __HIP_MEMORY_SCOPE_AGENT);
  return v;
}
DI void poll_flags(const char* ws, u32 off, bool on, u64 tag) {
#pragma nounroll
  for (u32 sp = 0; sp < SPIN_CAP; ++sp) {
    u64 v = tag + 1;
    if (on || !LL) v = __hip_atomic_load((const GAS u64*)(ws + off), RLX_AGENT);
    if (__all(v == tag + 1)) break;
  }
  __builtin_amdgcn_fence(__ATOMIC_ACQUIRE, "wavefront");
}
template <int SL = 1> DI void lds_wait(LAS char* L, int sig) {
#pragma nounroll
  for (u32 sp = 0; sp < (SPIN_CAP << 4); ++sp) {
    const u32x4 v = *(volatile LAS u32x4*)(L + L_FLG + sig * 16);
    if (__builtin_amdgcn_readfirstlane(v.x & v.y & v.z & v.w)) break;
    __builtin_amdgcn_s_sleep(SL);
  }
  asm volatile("" ::: "memory");
}
DI void lds_wait2(LAS char* L, int sig, int sig2) {
#pragma nounroll
  for (u32 sp = 0; sp < (SPIN_CAP << 4); ++sp) {
    const u32x4 v = *(volatile LAS u32x4*)(L + L_FLG + sig * 16) & *(volatile LAS u32x4*)(L + L_FLG + sig2 * 16);
    if (__builtin_amdgcn_readfirstlane(v.x & v.y & v.z & v.w)) break;
    __builtin_amdgcn_s_sleep(1);
  }
  asm volatile("" ::: "memory");
}
DI void lds_wait1(LAS char* L, int sig, int w) {
#pragma nounroll
  for (u32 sp = 0; sp < (SPIN_CAP << 4); ++sp) {
    if (__builtin_amdgcn_readfirstlane(*(volatile LAS u32*)(L + L_FLG + sig * 16 + w * 4))) break;
    __builtin_amdgcn_s_sleep(1);
  }
  asm volatile("" ::: "memory");
}
DI void lds_signal(LAS char* L, int sig, int w) {
  asm volatile("" ::: "memory");
  *(volatile LAS u32*)(L + L_FLG + sig * 16 + w * 4) = 1u;
}

template <int N> struct IC { static constexpr int value = N; };
template <int I, int N, class F> DI void sfor(F&& f) { if constexpr (I < N) { f(IC<I>{}); sfor<I + 1, N>(f); } }
#define LAM __attribute__((always_inline))
#define PIN(v) asm volatile("" : "+v"(v))
constexpr float LOG2E = 1.4426950408889634f;
template <int GAP, class F> DI void project(LAS char* L, u32 w0, u32 w1, const u32x4 (&xf)[2][14], int tp, int lane, f32x4 (&t)[2][2], F&& mid) {
  asm volatile("" : "+v"(w0), "+v"(w1));
  u32x4 ring[6];
  f32x4 cs[2] = {};
  f32x2 st[2];
  const u32x4 ones = {0x3f803f80u, 0x3f803f80u, 0x3f803f80u, 0x3f803f80u};
#pragma unroll
  for (int n = 0; n < 6; ++n) ring[n] = trfrag(L + (n & 1 ? w1 : w0) + (n >> 1) * 2048, 256);
#pragma unroll
  for (int i = 0; i < 4; ++i) t[i >> 1][i & 1] = f32x4{0.f, 0.f, 0.f, 0.f};
  SCHED();
#pragma unroll
  for (int n = 0; n < 28; ++n) {
    const u32x4 f = ring[n % 6];
    t[0][n & 1] = mfma16(f, xf[0][n >> 1], t[0][n & 1]);
    t[1][n & 1] = mfma16(f, xf[1][n >> 1], t[1][n & 1]);
    cs[n & 1] = mfma16(f, ones, cs[n & 1]);
    if (n + 6 < 28) ring[n % 6] = trfrag(L + (n & 1 ? w1 : w0) + ((n + 6) >> 1) * 2048 + (n + 6 >= 14 ? GAP : 0), 256);
    if (n == 22 || n == 23) st[n & 1] = *(LAS f32x2*)(L + L_ST1 + (tp * 32 + (n & 1) * 16 + (lane & 15)) * 8);
    mid(n);
    SCHED();
  }
#pragma unroll
  for (int tb = 0; tb < 2; ++tb)
#pragma unroll
    for (int k = 0; k < 2; ++k) t[tb][k] = (t[tb][k] + cs[k] * st[tb].x) * st[tb].y;
}
DI f32x2 swp16(float a, float b) { auto r = __builtin_amdgcn_permlane16_swap(__builtin_bit_cast(u32, a), __builtin_bit_cast(u32, b), false, false); return f32x2{__builtin_bit_cast(float, (u32)r[0]), __builtin_bit_cast(float, (u32)r[1])}; }
DI f32x2 swp32(float a, float b) { auto r = __builtin_amdgcn_permlane32_swap(__builtin_bit_cast(u32, a), __builtin_bit_cast(u32, b), false, false); return f32x2{__builtin_bit_cast(float, (u32)r[0]), __builtin_bit_cast(float, (u32)r[1])}; }
DI float swadd32(float a, float b) { const f32x2 s = swp32(a, b); return s.x + s.y; }
DI float swadd16(float a, float b) { const f32x2 s = swp16(a, b); return s.x + s.y; }
DI float mx2(float a, float b) { float r; asm("v_max_f32 %0, %1, %2" : "=v"(r) : "v"(a), "v"(b)); return r; }
DI float mx3(float a, float b, float c) { return fmaxf(fmaxf(a, b), c); }
DI float mn3(float a, float b, float c) { return fminf(fminf(a, b), c); }
DI void feature_map(const f32x4 (&t)[2][2], const u32x4 (&wpf)[2], int g, u32x4 (&P)[2][2]) {
  const f32x4 z4 = {0.f, 0.f, 0.f, 0.f};
  const bool v23 = g < 3;
  float p[2][8], m[4];
#pragma unroll
  for (int tb = 0; tb < 2; ++tb) {
    const u32x4 tq = {pk(t[tb][0][0], t[tb][0][1]), pk(t[tb][0][2], t[tb][0][3]), pk(t[tb][1][0], t[tb][1][1]), pk(t[tb][1][2], t[tb][1][3])};
    const f32x4 p0 = mfma16(wpf[0], tq, z4), p1 = mfma16(wpf[1], tq, z4);
#pragma unroll
    for (int i = 0; i < 8; ++i) { const float v = i < 4 ? p0[i] : p1[i - 4]; p[tb][i] = ((i & 3) < 2 || v23) ? v : p0[0]; }
    m[2 * tb] = mx3(mx3(p[tb][0], p[tb][1], p[tb][2]), mx3(p[tb][3], p[tb][4], p[tb][5]), mx3(p[tb][6], p[tb][7], p[tb][0]));
    m[2 * tb + 1] = -mn3(mn3(p[tb][0], p[tb][1], p[tb][2]), mn3(p[tb][3], p[tb][4], p[tb][5]), mn3(p[tb][6], p[tb][7], p[tb][0]));
  }
  {
    const f32x2 a = swp16(m[0], m[1]), b = swp16(m[2], m[3]);
    const f32x2 c = swp32(mx2(a.x, a.y), mx2(b.x, b.y));
    const float d = mx2(c.x, c.y) * -LOG2E;
    const f32x2 e = swp32(d, d), f0 = swp16(e.x, e.x), f1 = swp16(e.y, e.y);
    m[0] = f0.x; m[1] = f0.y; m[2] = f1.x; m[3] = f1.y;
  }
  float ex[4][8], sm[4];
#pragma unroll
  for (int c = 0; c < 4; ++c) {
#pragma unroll
    for (int i = 0; i < 8; ++i) {
      ex[c][i] = fexp2(fmaf(p[c >> 1][i], (c & 1) ? -LOG2E : LOG2E, m[c]));
      if ((i & 3) >= 2) ex[c][i] = v23 ? ex[c][i] : 0.f;
    }
    sm[c] = ((ex[c][0] + ex[c][1]) + (ex[c][2] + ex[c][3])) + ((ex[c][4] + ex[c][5]) + (ex[c][6] + ex[c][7]));
  }
  {
    const float d = frcp(swadd32(swadd16(sm[0], sm[1]), swadd16(sm[2], sm[3])));
    const f32x2 e = swp32(d, d), f0 = swp16(e.x, e.x), f1 = swp16(e.y, e.y);
    sm[0] = f0.x; sm[1] = f0.y; sm[2] = f1.x; sm[3] = f1.y;
  }
#pragma unroll
  for (int c = 0; c < 4; ++c)
#pragma unroll
    for (int e = 0; e < 4; ++e) P[c >> 1][c & 1][e] = pk(ex[c][2 * e] * sm[c], ex[c][2 * e + 1] * sm[c]);
}
DI u32x4 piece(const u32x4 (&rp)[8], int e) {
  u32x4 f;
#pragma unroll
  for (int m = 0; m < 4; ++m) f[m] = __builtin_amdgcn_perm(rp[2 * m + 1][e >> 1], rp[2 * m][e >> 1], (e & 1) ? 0x07060302u : 0x05040100u);
  return f;
}
#define RP_OPS "+v"(rp[0]), "+v"(rp[1]), "+v"(rp[2]), "+v"(rp[3]), "+v"(rp[4]), "+v"(rp[5]), "+v"(rp[6]), "+v"(rp[7])
#define PF1 0
#define PF2 84
#define PF3 120
#define TP0 -1
#define TP1 13
#define TP2 -1
#define TOUCH 5
#define NW3 4
#define THRN0 4
#define DNPRIO 3
#define NWD 3
#define MIDCHK 0
#define HELP 0
#define NHW 7
#define W3LATE 0
#define W3WAIT 1
#define ROT 1
#define EXPT 0
#define PFM 12
#define PSLP 4
#define PLAG 0
#define WTOUCH 0
#define REPLATE 0
#define WB0 0
#define WB1 4
#define ULATE 0
#define VPRIO 3
#define KGO 20
#define QEARLY 1
#define DLY1 4
#define W1 1
#define W2C 0
#define WOE 2
#define W3THR 1
#define HW3 1
#define HW1 1
#define SN 4
#define BATCH 1
#define B23 1
#define WD5L 0
#define A56 1
#define BGO -1
#define BALL 0
#define BSL 4
#define RSL 4
#define H5 1
#define H6 1
#define BORD 1
#define DP0 3
#define AIL 1
#define SEEN 3
#define ADLY 0
#define W6E 0
#define DLY4 16
#define PUBS 5
#define THRN 40
#define DLY2 8
#define DLY3 12
#define GUPRIO 2
enum { S_FREE01 = 0, S_REFILL, S_DONE4, S_WIN3, S_WK, S_WV, S_WQ, S_WSEEN, S_KDONE, S_V, S_PHIK, S_KVP, S_KVA, S_WO, S_REPSEEN, S_HSEEN, S_X0, S_X1, S_XA, S_XB, S_CS, S_WOFA, S_WOFB, S_EXA, S_EXB, S_W2A, S_W2B, S_HFA, S_HFB, S_CONS, S_LAND, S_XF0 };

__global__ void __launch_bounds__(512, 2) fused_block(const char* __restrict__ x, const char* __restrict__ wqkv, const char* __restrict__ wphi, const char* __restrict__ wo,
                                                      const char* __restrict__ wg, const char* __restrict__ wu, const char* __restrict__ wd, char* __restrict__ out, char* ws) {
  extern __shared__ __attribute__((aligned(16))) char smem[];
  LAS char* L = (LAS char*)smem;
  const int tid = threadIdx.x, lane = tid & 63, wave = __builtin_amdgcn_readfirstlane(tid >> 6), w2 = wave & 3;
  const int b = blockIdx.x, li = b >> 3, xp = (b & 7) >> 1, xh = b & 1;
  const int tile = 4 * xp + (li >> 3), r = 8 * xh + (li & 7);
  const int tile2 = 4 * xp + 2 * xh + (li >> 4), r2 = li & 15;
  const int tg = r2 >= 7 ? 1 : 0, cg = r2 - 7 * tg;
  const int l15 = lane & 15, g = lane >> 4, q4 = l15 >> 2, p4 = lane & 3, hh = lane >> 5;
  const int head = r;
  asm volatile("" :: "s"(x), "s"(wqkv), "s"(wphi), "s"(wo), "s"(wg), "s"(wu), "s"(wd), "s"(out), "s"(ws));
  STAMP(0);
  u64 pc_; asm volatile("s_getpc_b64 %0" : "=s"(pc_));
  const char* pc0 = (const char*)(pc_ & ~127ull);
  const __amdgpu_buffer_rsrc_t rs = __builtin_amdgcn_make_buffer_rsrc((void*)ws, 0, WS_USED, 0x00020000);
  const u32x4 ones = {0x3f803f80u, 0x3f803f80u, 0x3f803f80u, 0x3f803f80u};
  const f32x4 z4 = {0.f, 0.f, 0.f, 0.f};
  auto role = [&](auto AC) __attribute__((always_inline)) {
  constexpr bool A = AC.value;
  if (A) asm volatile("; A" ::: "memory"); else asm volatile("; B" ::: "memory");
  __builtin_amdgcn_s_waitcnt(0x0F70);
  u64 epoch;
  u32x4 rp[8];
  const int fvo = lane < 16 ? lane * 8 : NOWHERE;
  const int uidx = (tile * 10 + __builtin_popcount(~0x2A54u & ((1u << head) - 1u))) * 4 + w2, unit = uidx * 147 / 160;
  const bool has_unit = !((0x2A54 >> head) & 1) && (uidx + 1) * 147 / 160 != unit;
  const int u3 = unit / 3, umat = unit - 3 * u3, ukq = u3 / 28, ufl = u3 - 28 * ukq;
  const bool ud = umat == 2;
  const char* gusrc = ws + WS_WGU + 7 * r * SLOT;
  const u32 guo = WS_WGU + 7 * r * SLOT + w2 * 7168;
#define GUBLK(j) dma7<(j) * SLOT, gu_slot(j) * SLOT>(rs, lane * 16, guo, w2 * 7168)
  const u32 wvo = (lane >> 2) * 2688 + (((lane & 3) ^ (2 * (lane >> 5))) * 16);
  const char* wbase = wqkv + head * 56 - (head & 1) * 8 + 16 * w2 * 2688;
  const u32 wr0 = (8 * g + q4) * 64 + (((p4 + (head & 1)) ^ (4 * (g & 1))) * 8), wr1 = (8 * g + q4) * 64 + ((((4 + p4 + (head & 1)) & 7) ^ (4 * (g & 1))) * 8);
  const int tb = w2; constexpr int kh = A ? 0 : 1;
  const int hho = WS_HH + (((tile * 2 + (w2 >> 1)) * 4 + (r >> 2)) * 56 + (r & 3) * 2 + (w2 & 1)) * 1024;
  u32x4 wof[8][4];
  u64 tag;
#define WOF_READ(i0, i1) do { u32 wa_ = L_WO + (kh * 224 + 4 * g + q4) * 128 + 16 * p4 + 64 * (q4 >> 1), wb_ = wa_ ^ 64, wc_ = wa_ + (g == 3 ? 0 : 2048), wd_ = wc_ ^ 64; \
    asm volatile("" : "+v"(wa_), "+v"(wb_), "+v"(wc_), "+v"(wd_)); \
    _Pragma("unroll") for (int i = i0; i < i1; ++i) _Pragma("unroll") for (int k = 0; k < 4; ++k) { \
      const u32x2 a_ = trread(L + (k >> 1 ? wb_ : wa_) + 28 * i * 128 + (k & 1) * 8), c_ = trread(L + (k >> 1 ? wd_ : wc_) + 28 * i * 128 + (k & 1) * 8); \
      wof[i][k] = u32x4{a_.x, a_.y, c_.x, c_.y}; } } while (0)
#define XF_READ(i) _Pragma("unroll") for (int s = 0; s < 14; ++s) xf[i][s] = *(LAS u32x4*)((s & 1 ? xr1 : xr0) + (i) * 14336 + (s >> 1) * 2048)
#define LN_STATS() do { f32x4 g1 = z4, g2 = z4; \
    _Pragma("unroll") for (int s = 0; s < 14; ++s) { g1 = mfma16(ones, xf[kh][s], g1); g2 = mfma16(xf[kh][s], xf[kh][s], g2); } \
    const float d01 = (p4 & 1) ? g2[1] : g2[0], d23 = (p4 & 1) ? g2[3] : g2[2], s2 = (p4 & 2) ? d23 : d01; \
    const float nmu = g1[0] * (-1.f / 448.f), var = fmaxf(s2 * (1.f / 448.f) - nmu * nmu, 0.f); \
    if (g == q4) *(LAS f32x2*)(L + L_ST1 + (w2 * 32 + kh * 16 + l15) * 8) = f32x2{nmu, __builtin_amdgcn_rsqf(var + 1e-5f)}; } while (0)
#define WPF_STEP(n) do { \
    if ((n) == 0) { _Pragma("unroll") for (int j = 0; j < 8; ++j) wp[j] = *(LAS u32*)(L + L_WPHI + 4 * g * 56 + l15 * 4 + ((j & 3) + 16 * (j >> 2)) * 56); } \
    if ((n) >= 8 && (n) < 12) { const int e = (n) - 8; const bool z = l15 >= 14 || (e >= 2 && g == 3); \
      wpf[0][e] = z ? 0u : __builtin_amdgcn_perm(wp[2 * e + 1], wp[2 * e], 0x05040100u); wpf[1][e] = z ? 0u : __builtin_amdgcn_perm(wp[2 * e + 1], wp[2 * e], 0x07060302u); } \
    if ((n) == 12) asm volatile("" : "+v"(wpf[0]), "+v"(wpf[1])); } while (0)
  {
    u32x4 xf[2][14], wpf[2];
    u32 wp[8];
    LAS char* xr0 = L + w2 * 28672 + l15 * 128 + ((g ^ (l15 & 7)) * 16);
    LAS char* xr1 = L + w2 * 28672 + l15 * 128 + (((4 + g) ^ (l15 & 7)) * 16);
    LAS char* pw = L + L_PKV + (w2 * 32 + l15) * 224 + g * 8;
    const char* wos = wo + (r2 < 14 ? cg : 0) * 128 + w2 * 56 * 896;
    const u32 wovo = (lane >> 3) * 896 + (((lane & 7) ^ ((lane >> 2) & 4)) * 16);
#define WO_HALF(hf_) _Pragma("unroll") for (int n = 0; n < 7; ++n) dma16(wos + ((hf_) * 224 + n * 8) * 896, wovo, L_WO + ((hf_) * 28 + w2 * 7 + n) * 1024)
    if constexpr (A) {
      if (wave < 2) dma4(x + (xp * 512 + li * 16) * 896 + wave * 7168, min(lane, 55) * 128, L_JUNK);
      if (wave == 3) { *(LAS u32*)(L + L_FLG + lane * 4) = 0u; *(LAS u32*)(L + L_FLG + 256 + lane * 4) = 0u; dma4(pc0, min((li % 6) * 8192 + lane * 128, CODE_PF), L_JUNK); }
      if (wave == 2) { dma16(wphi + head * 1568, lane * 16, L_WPHI); dma16(wphi + head * 1568, min(lane + 64, 97) * 16, L_WPHI + 1024); }
      if (WTOUCH && wave < WTOUCH) dma4(wqkv + li * 14 * 2688 + (wave == 2 ? 0 : (wave + 1) * 896) + xh * 384, min(lane >> 2, 13) * 2688 + (lane & 3) * 128, L_JUNK);
      BAR();
      asm volatile("s_load_dwordx2 %0, %1, 0x0 ; role A\n\ts_waitcnt lgkmcnt(0)" : "=s"(epoch) : "s"(ws) : "memory");
      tag = epoch * 6364136223846793005ull + 1442695040888963407ull;
      STAMP(1);
      lds_wait1(L, S_X0, w2);
      XF_READ(0);
      if (w2 < 2) lds_signal(L, S_XF0, w2);
#pragma unroll
      for (int i = 0; i < 7; ++i) dma16(wbase + 896 + 64 * i * 2688, wvo, L_WK + (4 * i + w2) * 1024);
      STAMP(27);
      lds_wait1(L, S_X1, w2);
      STAMP(2);
      XF_READ(1);
      STAMP(3);
      LN_STATS();
      WAITVM(0);
      STAMP(4);
      lds_signal(L, S_WK, w2);
      lds_wait(L, S_WK);
      STAMP(5);
      __builtin_amdgcn_s_setprio(2);
      f32x4 t[2][2];
      u32x4 cw;
      project<0>(L, L_WK + wr0, L_WK + wr1, xf, w2, lane, t, [&](int n) LAM {
        WPF_STEP(n);
        if (n == KGO) lds_signal(L, S_KDONE, w2);
        if (n == 15) cw = *(volatile LAS u32x4*)(L + L_FLG + S_CS * 16);
        if (n == 21 && __builtin_expect(!__builtin_amdgcn_readfirstlane(cw.x & cw.y & cw.z & cw.w), 0)) lds_wait(L, S_CS);
      });
      STAMP(6);
      u32x4 P[2][2];
      feature_map(t, wpf, g, P);
#pragma unroll
      for (int c = 0; c < 4; ++c) {
        LAS char* p = pw + (c >> 1) * 16 * 224 + (c & 1) * 64;
        *(LAS u32x2*)(p) = u32x2{P[c >> 1][c & 1].x, P[c >> 1][c & 1].y}; *(LAS u32x2*)(p + 32) = u32x2{P[c >> 1][c & 1].z, P[c >> 1][c & 1].w};
      }
      lds_signal(L, S_PHIK, w2);
      STAMP(7);
      u32 pr = L_PKV + (4 * g + q4) * 224 + 8 * p4;
      asm volatile("" : "+v"(pr));
      lds_wait2(L, S_V, S_PHIK);
      u32x4 pa[4], pb[8];
#pragma unroll
      for (int m = 0; m < 4; ++m) {
        pa[m] = trfrag(L + pr + w2 * 32 + m * 32 * 224, 16 * 224);
        pb[2 * m] = trfrag(L + pr + m * 32 * 224 + 128, 16 * 224); pb[2 * m + 1] = trfrag(L + pr + m * 32 * 224 + 160, 16 * 224);
      }
      f32x4 kv[2] = {};
#pragma unroll
      for (int m = 0; m < 8; ++m) kv[m & 1] = mfma16(pa[m >> 1], pb[m], kv[m & 1]);
      const u32x4 kvb = {pk(kv[0][0], kv[0][1]), pk(kv[0][2], kv[0][3]), pk(kv[1][0], kv[1][1]), pk(kv[1][2], kv[1][3])};
      ST16(rs, kvb, lane * 16, WS_KVP + ((head * 16 + tile) * 4 + w2) * 1024);
      STAMP(8);
      DRAIN();
      FLAG(WS_FKV + ((head * 4 + w2) * 16 + tile) * 128, 1);
      __builtin_amdgcn_s_setprio(0);
      STAMP(9);
      if (HW1) { lds_signal(L, S_XA, w2); lds_wait(L, S_XA); }
      if (WOE == 2) { WO_HALF(0); WO_HALF(1); } else if (WOE == 3) { WO_HALF(0); } else __builtin_amdgcn_s_sleep(DLY1);
      poll_flags(ws, WS_FKV + ((head * 4 + w2) * 16 + l15) * 128, lane < 16, tag);
      STAMP(10);
      u32x4 tt[16];
#pragma unroll
      for (int i = 0; i < 16; ++i) tt[i] = __builtin_bit_cast(u32x4, __builtin_amdgcn_raw_buffer_load_b128(rs, lane * 16, WS_KVP + head * 65536 + w2 * 1024 + i * 4096, 16));
      const u32 fgo = WS_FG + (min(((7 * r) >> 2) + g, (7 * r + 6) >> 2) * 16 + (l15 & 8) + min(lane & 7, 6)) * 128;
      const u64 fg = __hip_atomic_load((const GAS u64*)(ws + (wave == 3 ? fgo : WS_FG)), RLX_AGENT);
      SCHED();
      const u32 one = g == q4 ? ((p4 & 1) ? 0x3f800000u : 0x3f80u) : 0u, s01 = p4 < 2 ? one : 0u, s23 = p4 < 2 ? 0u : one;
      const u32x4 sel0 = {s01, s23, 0u, 0u}, sel1 = {0u, 0u, s01, s23};
      f32x4 s0 = {}, s1 = {};
#pragma unroll
      for (int i = 0; i < 16; ++i) { s0 = mfma16(sel0, tt[i], s0); s1 = mfma16(sel1, tt[i], s1); }
      LAS char* kw = L + L_KVA + (w2 >> 1) * 1024 + lane * 16 + (w2 & 1) * 8;
      *(LAS u32x2*)(kw) = u32x2{pk(s0[0], s0[1]), pk(s0[2], s0[3])};
      *(LAS u32x2*)(kw + 2048) = u32x2{pk(s1[0], s1[1]), pk(s1[2], s1[3])};
      lds_signal(L, S_KVA, w2);
      STAMP(11);
      if (WOE == 3) { WO_HALF(1); WAITVM(0); lds_signal(L, S_WO, w2); }
      if (wave == 3 && !__all(fg == tag + 1)) poll_flags(ws, fgo, true, tag);
      __builtin_amdgcn_fence(__ATOMIC_ACQUIRE, "wavefront");
      lds_signal(L, S_WOFA, w2);
      lds_wait1(L, S_WOFA, 3);
      const int fl = min(((7 * r) >> 2) + w2, (7 * r + 6) >> 2);
      f32x4 c[7];
#pragma unroll
      for (int i = 0; i < 7; ++i) c[i] = __builtin_bit_cast(f32x4, __builtin_amdgcn_raw_buffer_load_b128(rs, ((7 * r) >> 2) + w2 == fl ? lane * 16 + (i & 3) * 1024 : NOWHERE, WS_CS + fl * 7168 + (i & 4) * 1024, 16));
      if (W1) GUBLK(2);
      lds_wait(L, WOE < 2 ? S_WQ : S_KVA);
      WOF_READ(0, 8);
      SCHED();
      if (((7 * r) >> 2) + w2 == fl) {
        const f32x4 sm = ((c[0] + c[1]) + (c[2] + c[3])) + ((c[4] + c[5]) + c[6]);
        const float e0 = swadd32(sm[0], sm[1]), e1 = swadd32(sm[2], sm[3]);
        const int j0 = 4 * fl + 2 * (lane & 1) - 7 * r, cr = ((g & 1) * 16 + hh * 8 + (l15 >> 1)) * 4;
        if (j0 >= 0 && j0 < 7) *(LAS float*)(L + L_CS + j0 * 128 + cr) = e0;
        if (j0 + 1 >= 0 && j0 + 1 < 7) *(LAS float*)(L + L_CS + (j0 + 1) * 128 + cr) = e1;
      }
      STAMP(24);
    } else {
      const u32 vo = (lane >> 3) * 896 + (((lane & 7) ^ (lane >> 3)) * 16);
      const char* xb = x + (tile * 128 + w2 * 32) * 896;
#pragma unroll
      for (int n = 0; n < 28; ++n) {
        dma16(xb + ((n / 14) * 16 + (n & 1) * 8) * 896 + ((n % 14) >> 1) * 128, vo, w2 * 28672 + n * 1024);
        if (n == 3) { BAR(); asm volatile("s_load_dwordx2 %0, %1, 0x0 ; role B" : "=s"(epoch) : "s"(ws) : "memory"); }
        if (n == 21) { WAITVM(8); lds_signal(L, S_X0, w2); }
      }
      asm volatile("s_waitcnt lgkmcnt(0)" : "+s"(epoch)::"memory");
      tag = epoch * 6364136223846793005ull + 1442695040888963407ull;
      STAMP(1);
      XF_READ(0);
      if (w2 < 2) lds_signal(L, S_XF0, 2 + w2);
      WAITVM(0);
      lds_signal(L, S_X1, w2);
      STAMP(2);
      lds_wait(L, S_XF0);
      XF_READ(1);
      SCHED();
#define WDST(c) ((c) * 1024 + ((c) >= 14 ? SLOT - 14336 : 0))
#pragma unroll
      for (int i = 0; i < 7; ++i) dma16(wbase + 1792 + 64 * i * 2688, wvo, L_WV + WDST(4 * i + w2));
      STAMP(3);
      LN_STATS();
      lds_signal(L, S_CS, w2);
      STAMP(4);
      if (ULATE) { WAITVM(0); lds_signal(L, S_WV, w2); }
      const int rg = lane >> 3;
      const u32 lvo = (ud ? (32 * (rg & 1) + (rg & 6)) * 896 : rg * 8 * 3584) + (lane & 7) * 16;
      const char* us = ud ? wd + ufl * 64 * 896 + ukq * 128 : (umat ? wu : wg) + ukq * 64 * 3584 + ufl * 128;
      int ust0 = ud ? 8 * 896 : 3584, ust1 = ud ? 896 : 4 * 3584;
      asm volatile("" : "+s"(us), "+s"(ust0), "+s"(ust1));
      if (has_unit) {
#pragma unroll
        for (int j = 0; j < 8; ++j) asm volatile("global_load_dwordx4 %0, %1, %2" : "=v"(rp[j]) : "v"(lvo), "s"(us + (j & 3) * ust0 + (j >> 2) * ust1) : "memory");
      } else WAITVM(0);
      if (!ULATE) { WAITVM(8); lds_signal(L, S_WV, w2); }
      lds_wait2(L, S_WV, S_WK);
      if (QEARLY) {
#pragma unroll
        for (int i = 0; i < 7; ++i) dma16(wbase + 64 * i * 2688, wvo, L_WQ + WDST(4 * i + w2));
      }
      if (WOE == 1) WO_HALF(0);
      lds_wait1(L, S_KDONE, w2);
      STAMP(5);
      f32x4 t[2][2];
      project<SLOT - 14336>(L, L_WV + wr0, L_WV + wr1, xf, w2, lane, t, [&](int n) LAM { if (n == 27) __builtin_amdgcn_s_setprio(VPRIO); });
      STAMP(6);
#pragma unroll
      for (int c = 0; c < 2; ++c) {
        LAS char* p = pw + c * 16 * 224 + 128;
        *(LAS u32x2*)(p) = u32x2{pk(t[c][0][0], t[c][0][1]), pk(t[c][0][2], t[c][0][3])};
        *(LAS u32x2*)(p + 32) = g == 3 ? u32x2{0x3f80u, 0u} : u32x2{pk(t[c][1][0], t[c][1][1]), pk(t[c][1][2], t[c][1][3])};
      }
      lds_signal(L, S_V, w2);
      __builtin_amdgcn_s_setprio(0);
      STAMP(7);
      if (!QEARLY) {
#pragma unroll
        for (int i = 0; i < 7; ++i) dma16(wbase + 64 * i * 2688, wvo, L_WQ + WDST(4 * i + w2));
      }
      if (WOE == 0) WO_HALF(0);
      if (WOE < 2) { lds_wait(L, S_KDONE); WO_HALF(1); }
      asm volatile("s_waitcnt vmcnt(%8)" : RP_OPS : "n"(WOE < 2 ? 21 : 7) : "memory");
      auto repack = [&]() LAM {
        if (has_unit) {
          const int fb = 4 * ufl + (rg >> 1), pr = fb * 147 >> 10;
          const int so = (ud ? (((pr >> 2) * 7 + fb - 7 * pr) * 4 + (pr & 3)) * 2048 : (rg >> 1) * 1024) + (rg & 1) * 512 + (lane & 7) * 16;
          const int sb = ud ? WS_WD + ukq * 229376 : WS_WGU + 4 * ufl * SLOT + ukq * 4096 + umat * 256;
          const int s1 = ud ? 256 : SLOT, s2 = ud ? 1024 : 2 * SLOT;
          float c[8];
#pragma unroll
          for (int e = 0; e < 8; ++e) {
            const u32x4 f = piece(rp, e);
            ST16(rs, f, so + (e & 1) * 128, sb + ((e >> 1) & 1) * s1 + (e >> 2) * s2);
            c[e] = mfma16(ones, f, z4)[0];
          }
          const f32x4 cv = g & 1 ? f32x4{c[4], c[5], c[6], c[7]} : f32x4{c[0], c[1], c[2], c[3]};
          ST16(rs, cv, g < 2 && !ud ? (l15 >> 3) * 512 + (lane & 7) * 32 + g * 16 : NOWHERE, WS_CS + ufl * 7168 + ukq * 1024 + (umat & 1) * 256);
        }
      };
#define UFLAG() FLAGC(has_unit, ud ? WS_FD + (ukq * 32 + ufl) * 128 : WS_FG + ((ufl * 2 + umat) * 8 + ukq) * 128, 1)
      if (!REPLATE) repack();
      STAMP(8);
      if (REPLATE || !has_unit) WAITVM(0); else WAITVM(9);
      lds_signal(L, S_WQ, w2);
      lds_wait(L, S_WQ);
      STAMP(9);
      project<SLOT - 14336>(L, L_WQ + wr0, L_WQ + wr1, xf, w2, lane, t, [&](int n) LAM {
        WPF_STEP(n);
        if (n == 14 && !REPLATE) { WAITVM(0); UFLAG(); }
      });
      STAMP(10);
      if (REPLATE == 2) repack();
      u32x4 pq[2][2];
      feature_map(t, wpf, g, pq);
      asm volatile("" : "+v"(pq[0][0]), "+v"(pq[0][1]), "+v"(pq[1][0]), "+v"(pq[1][1]));
      STAMP(11);
      if (REPLATE == 1) repack();
      WOF_READ(0, WB0);
      STAMP(24);
      if (REPLATE == 2) { WAITVM(0); UFLAG(); }
      lds_wait(L, S_KVA);
      if (REPLATE == 1) { WAITVM(0); UFLAG(); }
      STAMP(27);
      u32x4 ka[4];
#pragma unroll
      for (int i = 0; i < 4; ++i) ka[i] = *(LAS u32x4*)(L + L_KVA + i * 1024 + lane * 16);
      f32x4 o[2][2];
#pragma unroll
      for (int c = 0; c < 4; ++c) o[c >> 1][c & 1] = mfma16(ka[2 * (c & 1) + 1], pq[c >> 1][1], mfma16(ka[2 * (c & 1)], pq[c >> 1][0], z4));
      const f32x2 da = swp16(o[0][1][0], o[0][1][0]), db = swp16(o[1][1][0], o[1][1][0]);
      const float den[2] = {swp32(da.y, da.y).y, swp32(db.y, db.y).y};
#pragma unroll
      for (int c = 0; c < 2; ++c) {
        const float rd = frcp(den[c] + 1e-6f), r3 = g == 3 ? 0.f : rd;
        const u32x4 ob = {pk(o[c][0][0] * rd, o[c][0][1] * rd), pk(o[c][0][2] * rd, o[c][0][3] * rd), pk(o[c][1][0] * r3, o[c][1][1] * r3), pk(o[c][1][2] * r3, o[c][1][3] * r3)};
        ST16(rs, ob, lane * 16, WS_O + ((tile * 8 + 2 * w2 + c) * 16 + head) * 1024);
      }
      STAMP(12);
      if (WOE == 3) lds_wait(L, S_WO);
      WOF_READ(WB0, WB1);
      DRAIN();
      FLAG(WS_FO + ((tile * 4 + w2) * 16 + head) * 128, 1);
      lds_signal(L, S_W2B, w2);
      WOF_READ(WB1, 8);
    }
  }
  STAMP(13);
  u32x4 hres = {0u, 0u, 0u, 0u};
  const int tok = tile2 * 128 + tg * 64 + tb * 16 + l15;
  const int xo = tok * 896 + cg * 128 + kh * 64 + g * 16;
#define WIN2() do { lds_wait2(L, S_W2B, S_WOFA); GUBLK(A ? 0 : 1); if (!W1 && A) GUBLK(2); } while (0)
  STAMP(23);
  bool fd_ok = false;
  if (r2 < 14) {
    const u32x4 xr = *(const u32x4*)(x + xo);
    const u32 fo = WS_FO + ((tile2 * 4 + 2 * tg + (tb >> 1)) * 16 + kh * 8 + (lane & 7)) * 128;
    if (A) lds_wait(L, S_W2B);
    const bool late = W2C && __all(__hip_atomic_load((const GAS u64*)(ws + fo), RLX_AGENT) == tag + 1);
    if (!late) { WIN2(); poll_flags(ws, fo, lane < 8, tag); }
    __builtin_amdgcn_fence(__ATOMIC_ACQUIRE, "wavefront");
    STAMP(14);
    u32x4 of[8];
#pragma unroll
    for (int i = 0; i < 8; ++i)
      asm volatile("global_load_dwordx4 %0, %1, %2 offset:%3 sc1" : "=v"(of[i]) : "v"(lane * 16), "s"(ws + WS_O + ((tile2 * 8 + tg * 4 + tb) * 16 + kh * 8 + (i & 4)) * 1024), "n"((i & 3) * 1024) : "memory");
    u64 fdv;
    asm volatile("global_load_dwordx2 %0, %1, %2 sc1" : "=v"(fdv) : "v"(A ? 0 : min(lane & 7, 6) * 128), "s"(ws + WS_FD + (cg * 32 + 7 * w2) * 128) : "memory");
    if (late) { WIN2(); asm volatile("s_waitcnt vmcnt(%0)" :: "n"(!W1 && A ? 14 : 7) : "memory"); } else WAITVM(0);
    asm volatile("" : "+v"(of[0]), "+v"(of[1]), "+v"(of[2]), "+v"(of[3]), "+v"(of[4]), "+v"(of[5]), "+v"(of[6]), "+v"(of[7]), "+v"(fdv) :: "memory");
    f32x4 a[4] = {};
#pragma unroll
    for (int i = 0; i < 8; ++i)
#pragma unroll
      for (int k = 0; k < 4; ++k) a[k] = mfma16(wof[i][k], of[i], a[k]);
    fd_ok = __all(fdv == tag + 1);
    *(LAS f32x4*)(L + L_EX + wave * 2048 + lane * 16) = kh ? a[0] : a[2];
    *(LAS f32x4*)(L + L_EX + wave * 2048 + 1024 + lane * 16) = kh ? a[1] : a[3];
    lds_signal(L, A ? S_EXA : S_EXB, w2);
    lds_wait1(L, A ? S_EXB : S_EXA, w2);
    STAMP(15);
    const f32x4 c0 = (kh ? a[2] : a[0]) + *(LAS f32x4*)(L + L_EX + (wave ^ 4) * 2048 + lane * 16);
    const f32x4 c1 = (kh ? a[3] : a[1]) + *(LAS f32x4*)(L + L_EX + (wave ^ 4) * 2048 + 1024 + lane * 16);
    const f32x4 h0 = c0 + f32x4{blo(xr.x), bhi(xr.x), blo(xr.y), bhi(xr.y)}, h1 = c1 + f32x4{blo(xr.z), bhi(xr.z), blo(xr.w), bhi(xr.w)};
    hres = u32x4{pk(h0[0], h0[1]), pk(h0[2], h0[3]), pk(h1[0], h1[1]), pk(h1[2], h1[3])};
    ST16(rs, hres, WS_H + ((tok >> 5) * 28 + 4 * cg + 2 * kh + (g >> 1)) * 1024 + ((g & 1) * 32 + (tok & 31)) * 16, 0);
    const f32x4 r0 = {blo(hres.x), bhi(hres.x), blo(hres.y), bhi(hres.y)}, r1 = {blo(hres.z), bhi(hres.z), blo(hres.w), bhi(hres.w)};
    const f32x4 a1 = r0 + r1, a2q = r0 * r0 + r1 * r1;
    float s1 = (a1[0] + a1[1]) + (a1[2] + a1[3]), s2 = (a2q[0] + a2q[1]) + (a2q[2] + a2q[3]);
    { const float t = swadd16(s1, s2); const f32x2 a2 = sw32(t); s1 = a2.x + a2.y; }
    __builtin_amdgcn_raw_buffer_store_b64(__builtin_bit_cast(u32x2, sw16(s1)), rs, fvo, WS_ST + ((2 * cg + kh) * 2048 + tile2 * 128 + tg * 64 + tb * 16) * 8, 16);
    DRAIN();
    FLAG(WS_FH + ((tile2 * 4 + 2 * tg + (tb >> 1)) * 32 + cg * 4 + (tb & 1) * 2 + kh) * 128, 1);
  } else {
    WIN2();
    DRAIN();
  }
  lds_signal(L, A ? S_HFA : S_HFB, w2);
  STAMP(16);
  u32x4 wdf[NWD][8];
  const int wdo = WS_WD + (cg * 4 + w2) * 57344;
  const u32 po = WS_FHH + ((tile2 * 16 + 4 * w2 + ((lane >> 1) & 3)) * 4 + 2 * tg + (lane & 1)) * 128;
  const u32 ho = WS_HH + ((tile2 * 2 + tg) * 4 + w2) * 57344;
#define WDLOAD(k, bf) dma8(rs, lane * 16, wdo + (k) * 8192, ((bf) + 2) * SLOT + w2 * 7168, L_EX + (bf) * 4096 + w2 * 1024)
#define THR() asm volatile("s_waitcnt vmcnt(%0)" :: "n"(THRN) : "memory")
#define WLOAD1(j, i) wdf[(j) % NWD][i] = __builtin_bit_cast(u32x4, __builtin_amdgcn_raw_buffer_load_b128(rs, lane * 16 + ((i) & 3) * 1024, wdo + (j) * 8192 + ((i) & 4) * 1024, 16))
#define THR0() asm volatile("s_waitcnt vmcnt(%0)" :: "n"(THRN0) : "memory")
#define WLOAD(j) _Pragma("unroll") for (int i = 0; i < 8; ++i) { THR0(); WLOAD1(j, i); }
  u32 have = 0;
#define PLOAD() peek(ws, po, lane < 8, tag + 7)
#define PMIN(pv) do { const u64 d = (pv) - tag; have = max(have, min8(d <= 8 ? (u32)d : 0u)); } while (0)
#define NEEDP(n) do { _Pragma("nounroll") for (u32 sp = 0; have < (n) && sp < SPIN_CAP; ++sp) { PMIN(PLOAD()); if (PSLP && have < (n)) __builtin_amdgcn_s_sleep(PSLP); } \
    __builtin_amdgcn_fence(__ATOMIC_ACQUIRE, "wavefront"); } while (0)
#define HBASE() u32 hb3 = w2 * 7168 + lane * 16, hb0 = hb3 + 2 * SLOT, hb2 = hb0 + 2 * SLOT, hb7 = L_EX + w2 * 1024 + lane * 16; asm volatile("" : "+v"(hb0), "+v"(hb2), "+v"(hb7))
#define HFRAG(bf, i) (*(LAS u32x4*)(L + ((i) < 7 ? ((bf) == 3 ? hb3 : (bf) == 2 ? hb2 : hb0 + (bf) * SLOT) + (i) * 1024 : hb7 + (bf) * 4096)))
#define HQLOAD(q, j) _Pragma("unroll") for (int i = 0; i < 8; ++i) hq[q][i] = __builtin_bit_cast(u32x4, __builtin_amdgcn_raw_buffer_load_b128(rs, lane * 16 + (i & 3) * 1024, ho + (j) * 8192 + (i & 4) * 1024, 16))
#define HQLOADT(q, j) _Pragma("unroll") for (int i = 0; i < 8; ++i) { THR(); hq[q][i] = __builtin_bit_cast(u32x4, __builtin_amdgcn_raw_buffer_load_b128(rs, lane * 16 + (i & 3) * 1024, ho + (j) * 8192 + (i & 4) * 1024, 16)); }
#define DOWN16(ac, wf, hf_) _Pragma("unroll") for (int m = 0; m < 16; ++m) ac[(m >> 1) & 1][m & 1] = mfma32(wf[2 * (m >> 2) + (m & 1)], hf_[2 * (m >> 2) + ((m >> 1) & 1)], ac[(m >> 1) & 1][m & 1])
#define PARTIAL_OUT(ac, b0, b1) _Pragma("unroll") for (int n = 0; n < 16; ++n) { const int T = n >> 3, k = (n >> 2) & 1, q = n & 3; \
    *(LAS f32x4*)&HFRAG(T ? b1 : b0, n & 7) = f32x4{ac[T][k][4 * q], ac[T][k][4 * q + 1], ac[T][k][4 * q + 2], ac[T][k][4 * q + 3]}; }
  if (A) {
    if (HW3) lds_wait2(L, S_HFA, S_HFB);
    __builtin_amdgcn_s_sleep(DLY3);
    poll_flags(ws, WS_FH + ((tile * 4 + w2) * 32 + min(lane & 31, 27)) * 128, lane < 28, tag);
    lds_signal(L, S_HSEEN, w2);
  }
  STAMP(17);
  if constexpr (A) {
    const int T32 = tile * 4 + wave;
    f32x2 st[7];
#pragma unroll
    for (int i = 0; i < 7; ++i) st[i] = __builtin_bit_cast(f32x2, __builtin_amdgcn_raw_buffer_load_b64(rs, WS_ST + (7 * hh * 2048 + (lane & 31)) * 8, (i * 2048 + T32 * 32) * 8, 16));
    u32x4 hb[28];
#pragma unroll
    for (int s = 0; s < 28; ++s) hb[s] = __builtin_bit_cast(u32x4, __builtin_amdgcn_raw_buffer_load_b128(rs, lane * 16 + (s & 3) * 1024, WS_H + (T32 * 28 + (s & ~3)) * 1024, 16));
    lds_wait2(L, S_HFA, S_HFB);
    u32 wb_[3] = {(u32)lane * 16, (u32)lane * 16 + 2 * SLOT, (u32)lane * 16 + 4 * SLOT};
    asm volatile("" : "+v"(wb_[1]), "+v"(wb_[2]));
#define WFRAG(sl, s) (*(LAS u32x4*)(L + wb_[(sl) >> 1] + ((sl) & 1) * SLOT + (s) * 1024))
    LAS char* csl = L + L_CS + hh * 16;
    u32x4 ring[7];
    f32x4 cq[4];
#pragma unroll
    for (int k = 0; k < 4; ++k) cq[k] = *(LAS f32x4*)(csl + k * 32);
#pragma unroll
    for (int s = 0; s < 7; ++s) ring[s] = WFRAG(0, s);
    SCHED();
    float s1 = ((st[0].x + st[1].x) + (st[2].x + st[3].x)) + ((st[4].x + st[5].x) + st[6].x);
    float s2 = ((st[0].y + st[1].y) + (st[2].y + st[3].y)) + ((st[4].y + st[5].y) + st[6].y);
    { f32x2 a = sw32(s1); s1 = a.x + a.y; a = sw32(s2); s2 = a.x + a.y; }
    const float nmu = s1 * (-1.f / 448.f), ve = fmaxf(s2 * (1.f / 448.f) - nmu * nmu, 0.f) + 1e-5f;
    const float rstd = __builtin_amdgcn_rsqf(ve);
    const float k0 = -2.30220819f * rstd, k1 = -0.10294324f * rstd * rstd * rstd;
    const int pvo = WS_FHH + ((tile * 16 + r) * 4 + wave) * 128;
#define PROG(n) FLAG(pvo + 131072, n)
    u32 pub = 0, ptmp; const u32 pav = pvo + l15 * 8;
    u64 pubv, tagv = tag;
    asm volatile("" : "+v"(tagv));
#define TRYPUB(cnt) asm volatile("s_getreg_b32 %1, hwreg(HW_REG_IB_STS, 0, 4)\n\ts_sub_i32 %1, %6, %1\n\ts_min_i32 %1, %1, 4\n\ts_cmp_gt_i32 %1, %0\n\ts_cbranch_scc0 1f\n\ts_mov_b32 %0, %1\n\t" \
                               "v_mad_u64_u32 %2, vcc, %1, 1, %5\n\tglobal_store_dwordx2 %3, %2, %4 sc1\n1:" \
                               : "+s"(pub), "=&s"(ptmp), "=&v"(pubv) : "v"(pav), "s"(ws), "v"(tagv), "n"(cnt) : "memory", "scc", "vcc")
    f32x16 acc[2];
    float y[8], gu[8];
    u32x4 yb;
    u32x4 cw;
#pragma unroll
    for (int i = 0; i < 16; ++i) acc[0][i] = nmu * cq[i >> 2][i & 3];
    __builtin_amdgcn_s_setprio(GUPRIO);
    SCHED();
#define LT(i)
    auto gu_block = [&](auto JC) __attribute__((always_inline)) {
      constexpr int j = JC.value;
      sfor<0, 28>([&](auto SC) LAM {
        constexpr int s = SC.value;
        acc[j & 1] = mfma32(ring[s % 7], hb[s], acc[j & 1]);
        if constexpr ((j == 2 || j == 4) && s == 12) cw = *(volatile LAS u32x4*)(L + L_FLG + (j == 2 ? S_WIN3 : S_REFILL) * 16);
        if constexpr ((j == 2 || j == 4) && s == 20) { if (__builtin_expect(!__builtin_amdgcn_readfirstlane(cw.x & cw.y & cw.z & cw.w), 0)) lds_wait(L, j == 2 ? S_WIN3 : S_REFILL); asm volatile("" ::: "memory"); }
        if constexpr (j == 1 && s < 3) ST16(rs, (u32x4{EMPTY, EMPTY, EMPTY, EMPTY}), lane * 16, hho + (4 + s) * 8192);
        if constexpr (s + 7 < 28) ring[s % 7] = WFRAG(gu_slot(j), s + 7);
        else if constexpr (j < 6) ring[s % 7] = WFRAG(gu_slot(j + 1), s - 21);
        if constexpr (s == 0 && (j == 1 || j == 2)) lds_signal(L, j == 1 ? S_WSEEN : S_FREE01, wave);
        if constexpr (s == 0 && j >= 3) *(volatile LAS u32*)(L + L_FLG + S_DONE4 * 16 + wave * 4) = j - 2u;
        if constexpr (j * 28 + s == BGO) lds_signal(L, S_KVP, wave);
        if constexpr (j > 0) {
          sfor<0, 8>([&](auto EC) LAM {
            constexpr int i = EC.value, k = s - 1 - 2 * i;
            const float ga = acc[(j - 1) & 1][i];
            if constexpr (k == 0) y[i] = ga * ga;
            if constexpr (k == 1) y[i] = fmaf(y[i], k1, k0);
            if constexpr (k == 2) y[i] = ga * y[i];
            if constexpr (k == 3) y[i] = fexp2(y[i]);
            if constexpr (k == 4) y[i] = fmaf(y[i], ve, ve);
            if constexpr (k == 5) { gu[i] = ga * acc[(j - 1) & 1][i + 8]; PIN(gu[i]); }
            if constexpr (k == 6) y[i] = frcp(y[i]);
            if constexpr (k == 7) y[i] = y[i] * gu[i];
            if constexpr (k >= 0 && k < 8 && k != 5) PIN(y[i]);
          });
          if constexpr (s == 21) { yb[0] = pk(y[0], y[1]); yb[1] = pk(y[2], y[3]); }
          if constexpr (s == 22) yb[2] = pk(y[4], y[5]);
          if constexpr (j == 5 && s == 22) { if (pub == 0) WAITVM(0); }
          if constexpr (s == 23) { yb[3] = filled(pk(y[6], y[7])); ST16(rs, yb, lane * 16, hho + (j - 1) * 8192); }
          if constexpr (j > 1 && (s == TP0 || s == TP1 || s == TP2)) TRYPUB(j - 1);
          if constexpr (s == 27 && j < 6) TRYPUB(j);
        }
        if constexpr (j < 6 && s >= 17 && s < 21) cq[s - 17] = *(LAS f32x4*)(csl + (j + 1) * 128 + (s - 17) * 32);
        if constexpr (j < 6 && s >= 22 && s < 27) {
          sfor<0, 16>([&](auto EC) LAM {
            constexpr int e = EC.value;
            if constexpr ((e < 2 ? 22 : e < 4 ? 23 : 23 + (e >> 2)) == s) { float v = nmu * cq[e >> 2][e & 3]; PIN(v); acc[(j + 1) & 1][e] = v; }
          });
        }
        SCHED();
      });
      LT(j);
      if (j == 0) STAMP(18);
      if (j == 3) STAMP(26);
      if (j == 1) STAMP(28);
      if (j == 2) STAMP(29);
      if (j == 4) STAMP(30);
      if (j == 5) STAMP(31);
    };
    gu_block(IC<0>{}); gu_block(IC<1>{}); gu_block(IC<2>{}); gu_block(IC<3>{}); gu_block(IC<4>{}); gu_block(IC<5>{}); gu_block(IC<6>{});
#pragma unroll
    for (int i = 0; i < 8; ++i) { const float ga = acc[0][i]; y[i] = ga * acc[0][i + 8] * frcp(fmaf(fexp2(ga * fmaf(ga * ga, k1, k0)), ve, ve)); }
    yb = u32x4{pk(y[0], y[1]), pk(y[2], y[3]), pk(y[4], y[5]), filled(pk(y[6], y[7]))};
    ST16(rs, yb, lane * 16, hho + 6 * 8192);
    if (BGO == 196) lds_signal(L, S_KVP, wave);
    if (pub < 4) { WAITVM(3); FLAG(pvo, 4); }
    __builtin_amdgcn_s_setprio(0);
    STAMP(19);
    if (r2 < 14) {
      f32x16 da[2][2] = {};
      u32x4 hq[3][8], wl[2][8];
      HBASE();
      auto check = [&](auto JC) LAM {
        constexpr int j = JC.value;
        auto bad = [&]() LAM { u32 mx = 0;
#pragma unroll
          for (int i = 0; i < 8; ++i) mx = max(mx, hq[j - 4][i].w);
          return !__all(mx != EMPTY); };
        if (__builtin_expect(bad(), 0)) {
#pragma nounroll
          for (u32 sp = 0; sp < SPIN_CAP; ++sp) { __builtin_amdgcn_s_sleep(RSL); asm volatile("" ::: "memory"); HQLOAD(j - 4, j); if (!bad()) break; }
        }
        SCHED();
      };
      if (W6E) {
#pragma unroll
        for (int i = 0; i < 8; ++i) WLOAD1(6, i);
      }
      __builtin_amdgcn_s_sleep(ADLY);
      lds_wait1(L, S_REPSEEN, w2);
      STAMP2(15);
      HQLOAD(0, 4);
      HQLOAD(1, 5);
      lds_wait1(L, S_CONS, w2);
#pragma unroll
      for (int i = 0; i < 8; ++i) wl[0][i] = HFRAG(1, i);
      SCHED();
      STAMP2(16);
      check(IC<4>{});
#pragma unroll
      for (int m = 0; m < 16; ++m) {
        da[(m >> 1) & 1][m & 1] = mfma32(wl[0][2 * (m >> 2) + (m & 1)], hq[0][2 * (m >> 2) + ((m >> 1) & 1)], da[(m >> 1) & 1][m & 1]);
        if (m & 1) { if (!W6E) WLOAD1(6, m >> 1); else if (H6 == 0) hq[2][m >> 1] = __builtin_bit_cast(u32x4, __builtin_amdgcn_raw_buffer_load_b128(rs, lane * 16 + ((m >> 1) & 3) * 1024, ho + 6 * 8192 + ((m >> 1) & 4) * 1024, 16)); }
        else wl[1][m >> 1] = HFRAG(2, m >> 1);
        SCHED();
      }
      STAMP2(18);
      check(IC<5>{});
      if (H6 == 1) HQLOAD(2, 6);
      SCHED();
      DOWN16(da, wl[1], hq[1]);
      SCHED();
      STAMP2(23);
      lds_wait1(L, S_LAND, w2);
#pragma unroll
      for (int n = 0; n < 16; ++n) {
        const f32x4 v = *(LAS f32x4*)&HFRAG(n >> 3 ? 3 : 0, n & 7);
#pragma unroll
        for (int e = 0; e < 4; ++e) da[n >> 3][(n >> 2) & 1][4 * (n & 3) + e] += v[e];
      }
      STAMP2(31);
      check(IC<6>{});
      DOWN16(da, wdf[0], hq[2]);
      PARTIAL_OUT(da, 1, 2);
      STAMP(20);
    }
  } else {
    lds_wait2(L, S_HFA, S_HFB);
    GUBLK(3);
    if (W3THR) WAITVM(3);
    GUBLK(4);
    if (r2 < 14) {
      if (!fd_ok) poll_flags(ws, WS_FD + (cg * 32 + 7 * w2 + min(lane & 7, 6)) * 128, lane < 7, tag);
      __builtin_amdgcn_fence(__ATOMIC_ACQUIRE, "wavefront");
      sfor<0, 3>([&](auto KC) LAM {
        if constexpr (KC.value == NW3) { STAMP2(25); lds_wait(L, S_HSEEN); __builtin_amdgcn_s_sleep(DLY4); }
        WLOAD(KC.value);
        SCHED();
        if constexpr (KC.value == 0) { WAITVM(8); lds_signal(L, S_WIN3, w2); }
      });
    } else { WAITVM(0); lds_signal(L, S_WIN3, w2); }
    STAMP2(23);
    lds_wait<BSL>(L, S_WSEEN);
    GUBLK(5);
    lds_wait<BSL>(L, S_FREE01);
    GUBLK(6);
    if (r2 >= 14) {
      WAITVM(0); lds_signal(L, S_REFILL, w2);
    } else {
      f32x16 acc[2][2] = {};
      u32x4 hq[2][8];
#define FREE(f) do { _Pragma("nounroll") for (u32 sp = 0; sp < (SPIN_CAP << 4); ++sp) { const u32x4 fr = *(volatile LAS u32x4*)(L + L_FLG + S_DONE4 * 16); \
          if (__builtin_amdgcn_readfirstlane(min(min(fr.x, fr.y), min(fr.z, fr.w))) >= (f)) break; __builtin_amdgcn_s_sleep(BSL); } asm volatile("" ::: "memory"); } while (0)
      STAMP2(16);
      dma4c(ws + wdo + 6 * 8192 + ((li >> 4) * 2 + tg) * 2048, l15 * 128, L_JUNK);
      WAITVM(1);
      __builtin_amdgcn_s_waitcnt(0x0F70 | 1);
      lds_signal(L, S_REFILL, w2);
      HBASE();
      u64 pv;
#define PEEK() asm volatile("s_mov_b64 exec, 0xff\n\tglobal_atomic_add_x2 %0, %1, %2, %3 sc0\n\ts_mov_b64 exec, -1" : "+v"(pv) : "v"(po), "v"(0ull), "s"(ws) : "memory")
#define PEEKED(k_) do { asm volatile("s_waitcnt vmcnt(%1)" : "+v"(pv) : "n"(k_) : "memory"); PMIN(pv); } while (0)
      FREE(1); WDLOAD(3, 0);
      NEEDP(1);
      if (SEEN == 1) lds_signal(L, S_REPSEEN, w2);
      STAMP2(1);
      HQLOAD(0, 0);
      FREE(2); WDLOAD(4, 1);
      NEEDP(2);
      STAMP2(2);
      HQLOAD(1, 1);
      SCHED();
      __builtin_amdgcn_s_setprio(DP0);
      DOWN16(acc, wdf[0], hq[0]);
      __builtin_amdgcn_s_setprio(0);
      SCHED();
      STAMP2(8);
      FREE(3); WDLOAD(5, 2);
      NEEDP(3);
      STAMP2(3);
      HQLOAD(0, 2);
      pv = PLOAD();
      if (SEEN == 2) lds_signal(L, S_REPSEEN, w2);
      WAITVM(9);
      lds_signal(L, S_CONS, w2);
      if (SEEN == 3) lds_signal(L, S_REPSEEN, w2);
#pragma unroll
      for (int i = 0; i < 8; ++i) wdf[0][i] = HFRAG(0, i);
      SCHED();
      __builtin_amdgcn_s_setprio(DP0);
      DOWN16(acc, wdf[1], hq[1]);
      SCHED();
      STAMP2(9);
      if (have < 4) { PMIN(pv); NEEDP(4); }
      STAMP2(4);
      HQLOAD(1, 3);
      if (SEEN == 4) lds_signal(L, S_REPSEEN, w2);
      __builtin_amdgcn_s_setprio(DNPRIO);
      SCHED();
      DOWN16(acc, wdf[2], hq[0]);
      SCHED();
      STAMP2(10);
      DOWN16(acc, wdf[0], hq[1]);
      STAMP2(11);
      FREE(4);
      PARTIAL_OUT(acc, 0, 3);
      lds_signal(L, S_LAND, w2);
      STAMP(20);
    }
  }
  if (r2 < 14) {
    BAR();
    float c[8] = {blo(hres.x), bhi(hres.x), blo(hres.y), bhi(hres.y), blo(hres.z), bhi(hres.z), blo(hres.w), bhi(hres.w)};
    const int rl = (kh * 32 + (tb & 1) * 16 + l15) * 16 + g * 4;
    LAS char* rq = L + ((tb >> 1) + 3) * SLOT + rl;
    LAS char* rq7 = L + L_EX + ((tb >> 1) + 1) * 4096 + rl;
#pragma unroll
    for (int w = 0; w < 4; ++w)
#pragma unroll
      for (int e = 0; e < 8; ++e) c[e] += *(LAS float*)(e == 7 ? rq7 + w * 1024 : rq + (7 * w + e) * 1024);
    { const u32x4 ov = {pk(c[0], c[1]), pk(c[2], c[3]), pk(c[4], c[5]), pk(c[6], c[7])}; asm volatile("global_store_dwordx4 %0, %1, %2 sc1\n\ts_nop 1" :: "v"(xo), "v"(ov), "s"(out) : "memory"); }
    if (A && b == 0 && tid == 0) __hip_atomic_store((GAS u64*)(ws + WS_E), tag, RLX_AGENT);
  }
  STAMP(21);
  };
  if (wave < 4) role(IC<1>{}); else role(IC<0>{});
}

extern "C" void kernel_launch(void* const* d_in, const int* in_sizes, int n_in, void* d_out, int out_size, void* d_ws, size_t ws_size, hipStream_t stream) {
  (void)hipFuncSetAttribute((const void*)fused_block, hipFuncAttributeMaxDynamicSharedMemorySize, LDS_BYTES);
  fused_block<<<256, 512, LDS_BYTES, stream>>>((const char*)d_in[0], (const char*)d_in[1], (const char*)d_in[2], (const char*)d_in[3], (const char*)d_in[5], (const char*)d_in[6],
                                               (const char*)d_in[7], (char*)d_out, (char*)d_ws);
}
```

```cpp
#include <hip/hip_runtime.h>
#include <hip/hip_bf16.h>
#include <stdint.h>

typedef unsigned u32;
typedef unsigned long long u64;
typedef u32 u32x4 __attribute__((ext_vector_type(4)));
typedef u32 u32x2 __attribute__((ext_vector_type(2)));
typedef float f32x2 __attribute__((ext_vector_type(2)));
typedef float f32x4 __attribute__((ext_vector_type(4)));
typedef float f32x16 __attribute__((ext_vector_type(16)));
typedef short s16x4 __attribute__((ext_vector_type(4)));
typedef __bf16 bf16x2 __attribute__((ext_vector_type(2)));
typedef __bf16 bf16x8 __attribute__((ext_vector_type(8)));
#define LAS __attribute__((address_space(3)))
#define GAS __attribute__((address_space(1)))
#define DI __device__ __forceinline__
#define RLX_AGENT __ATOMIC_RELAXED, __HIP_MEMORY_SCOPE_AGENT

constexpr int WS_E = 0;
constexpr int WS_FKV = 8192;
constexpr int WS_FO = WS_FKV + 131072;
constexpr int WS_FH = WS_FO + 131072;
constexpr int WS_FG = WS_FH + 262144;
constexpr int WS_FD = WS_FG + 114688;
constexpr int WS_FHH = WS_FD + 57344;
constexpr int WS_CS = WS_FHH + 262144;
constexpr int WS_ST = WS_CS + 200704;
constexpr int WS_WGU = WS_ST + 229376;
constexpr int WS_WD = WS_WGU + 3211264;
constexpr int WS_KVP = WS_WD + 1605632;
constexpr int WS_O = WS_KVP + 1048576;
constexpr int WS_H = WS_O + 2097152;
constexpr int WS_HH = WS_H + 1835008;
#define UNITX4
#define WDOFF(cg, fb, bl) (((((cg) * 4 + (fb) / 28) * 7 + (fb) % 7) * 4 + (fb) / 7 % 4) * 2 + (bl))
#define HHOFF(T32, fb) (((((T32) >> 1) * 4 + (fb) / 28) * 7 + (fb) % 7) * 8 + ((fb) / 7 & 3) * 2 + ((T32) & 1))
constexpr int WS_STAMP = WS_HH + 7340032;
constexpr int WS_LOG = WS_STAMP + 256 * 8 * 32 * 8;
constexpr int WS_USED = WS_LOG + 4096;
constexpr int SLOT = 28672;
constexpr int L_WK = 4 * SLOT, L_WV = 0, L_WQ = 14336;
constexpr int L_PKV = 2 * SLOT;
constexpr int L_WO = 3 * SLOT;
constexpr int L_EX = 5 * SLOT;
constexpr int L_KVA = 5 * SLOT;
constexpr int L_ST1 = 5 * SLOT + 4096;
constexpr int L_WPHI = 5 * SLOT + 5120;
constexpr int L_CS = 159744;
constexpr int L_FLG = 160768;
constexpr int L_JUNK = 161280;
constexpr int L_STAMP = 161536;
constexpr int L_RED = SLOT;
constexpr int LDS_BYTES = 163840;
constexpr int gu_slot(int j) { return j % 5; }
constexpr int CODE_PF = 43904;
constexpr u32 SPIN_CAP = 1u << 16;
#define LL 1
#define AP 1

#define STAMP(i) do {} while (0)
#define STAMP2(i) do {} while (0)
#define STAMP2X(i) do {} while (0)

DI f32x4 mfma16(u32x4 a, u32x4 b, f32x4 c) { return __builtin_amdgcn_mfma_f32_16x16x32_bf16(__builtin_bit_cast(bf16x8, a), __builtin_bit_cast(bf16x8, b), c, 0, 0, 0); }
DI f32x16 mfma32(u32x4 a, u32x4 b, f32x16 c) { return __builtin_amdgcn_mfma_f32_32x32x16_bf16(__builtin_bit_cast(bf16x8, a), __builtin_bit_cast(bf16x8, b), c, 0, 0, 0); }
DI u32 pk(float lo, float hi) { f32x2 p = {lo, hi}; return __builtin_bit_cast(u32, __builtin_convertvector(p, bf16x2)); }
DI float blo(u32 v) { return __builtin_bit_cast(float, v << 16); }
DI float bhi(u32 v) { return __builtin_bit_cast(float, v & 0xffff0000u); }
DI float dot2(u32 a, u32 b, float c) { return __builtin_amdgcn_fdot2_f32_bf16(__builtin_bit_cast(bf16x2, a), __builtin_bit_cast(bf16x2, b), c, false); }
DI u32x2 trread(LAS char* p) { return __builtin_bit_cast(u32x2, __builtin_amdgcn_ds_read_tr16_b64_v4i16((LAS s16x4*)p)); }
DI u32x4 trfrag(LAS char* p, int step) { u32x2 a = trread(p), b = trread(p + step); return u32x4{a.x, a.y, b.x, b.y}; }
DI float fexp2(float x) { return __builtin_amdgcn_exp2f(x); }
DI float frcp(float x) { return __builtin_amdgcn_rcpf(x); }
template <int C> DI float dppror(float x) { return __builtin_bit_cast(float, __builtin_amdgcn_update_dpp(0, __builtin_bit_cast(int, x), 0x120 + C, 0xf, 0xf, false)); }
DI float row_max(float x) { x = fmaxf(x, dppror<8>(x)); x = fmaxf(x, dppror<4>(x)); x = fmaxf(x, dppror<2>(x)); return fmaxf(x, dppror<1>(x)); }
DI float row_min(float x) { x = fminf(x, dppror<8>(x)); x = fminf(x, dppror<4>(x)); x = fminf(x, dppror<2>(x)); return fminf(x, dppror<1>(x)); }
DI float row_sum(float x) { x += dppror<8>(x); x += dppror<4>(x); x += dppror<2>(x); return x + dppror<1>(x); }
DI f32x2 sw16(float x) { u32 u = __builtin_bit_cast(u32, x); auto r = __builtin_amdgcn_permlane16_swap(u, u, false, false); return f32x2{__builtin_bit_cast(float, (u32)r[0]), __builtin_bit_cast(float, (u32)r[1])}; }
DI f32x2 sw32(float x) { u32 u = __builtin_bit_cast(u32, x); auto r = __builtin_amdgcn_permlane32_swap(u, u, false, false); return f32x2{__builtin_bit_cast(float, (u32)r[0]), __builtin_bit_cast(float, (u32)r[1])}; }
DI float grp_sum(float x) { f32x2 a = sw16(x); x = a.x + a.y; a = sw32(x); return a.x + a.y; }
DI float grp_max(float x) { f32x2 a = sw16(x); x = fmaxf(a.x, a.y); a = sw32(x); return fmaxf(a.x, a.y); }
DI float grp_min(float x) { f32x2 a = sw16(x); x = fminf(a.x, a.y); a = sw32(x); return fminf(a.x, a.y); }

#define BAR() asm volatile("s_waitcnt lgkmcnt(0)\n\ts_barrier" ::: "memory")
#define WAITVM(n) asm volatile("s_waitcnt vmcnt(" #n ")" ::: "memory")
#define DRAIN() do { asm volatile("s_waitcnt vmcnt(0)" ::: "memory"); __builtin_amdgcn_s_waitcnt(0x0F70); } while (0)
#define SCHED() __builtin_amdgcn_sched_barrier(0)
DI void dma16(const void* base, u32 voff, u32 lds) {
  u32 keep;
  asm volatile("s_mov_b32 %0, m0\n\ts_mov_b32 m0, %3\n\ts_nop 0\n\tglobal_load_lds_dwordx4 %1, %2\n\ts_mov_b32 m0, %0" : "=&s"(keep) : "v"(voff), "s"(base), "s"(lds) : "memory");
}
DI void dma16c(const void* base, u32 voff, u32 lds) {
  u32 keep;
  asm volatile("s_mov_b32 %0, m0\n\ts_mov_b32 m0, %3\n\ts_nop 0\n\tglobal_load_lds_dwordx4 %1, %2 sc1\n\ts_mov_b32 m0, %0" : "=&s"(keep) : "v"(voff), "s"(base), "s"(lds) : "memory");
}
DI void dma4(const void* base, u32 voff, u32 lds) {
  u32 keep;
  asm volatile("s_mov_b32 %0, m0\n\ts_mov_b32 m0, %3\n\ts_nop 0\n\tglobal_load_lds_dword %1, %2\n\ts_mov_b32 m0, %0" : "=&s"(keep) : "v"(voff), "s"(base), "s"(lds) : "memory");
}
DI void dma16c_if(u32 go, const void* base, u32 voff, u32 lds) {
  u32 keep;
  asm volatile("s_cmp_eq_u32 %4, 0\n\ts_cbranch_scc1 1f\n\ts_mov_b32 %0, m0\n\ts_mov_b32 m0, %3\n\ts_nop 0\n\tglobal_load_lds_dwordx4 %1, %2 sc1\n\ts_mov_b32 m0, %0\n1:"
               : "=&s"(keep) : "v"(voff), "s"(base), "s"(lds), "s"(go) : "memory", "scc");
}
template <int SI, int LI> DI void dma7(__amdgpu_buffer_rsrc_t rs, u32 voff, u32 so, u32 lds) {
  u32 t;
  asm volatile("s_add_u32 m0, %4, %6\n\ts_add_u32 %0, %3, %5\n\tbuffer_load_dwordx4 %1, %2, %0 offen sc1 lds\n\tbuffer_load_dwordx4 %1, %2, %0 offen offset:1024 sc1 lds\n\t"
               "buffer_load_dwordx4 %1, %2, %0 offen offset:2048 sc1 lds\n\tbuffer_load_dwordx4 %1, %2, %0 offen offset:3072 sc1 lds\n\t"
               "s_add_u32 m0, m0, 4096\n\ts_add_u32 %0, %0, 4096\n\tbuffer_load_dwordx4 %1, %2, %0 offen sc1 lds\n\tbuffer_load_dwordx4 %1, %2, %0 offen offset:1024 sc1 lds\n\t"
               "buffer_load_dwordx4 %1, %2, %0 offen offset:2048 sc1 lds"
               : "=&s"(t) : "v"(voff), "s"(rs), "s"(so), "s"(lds), "n"(SI), "n"(LI) : "memory", "scc");
}
DI void dma8(__amdgpu_buffer_rsrc_t rs, u32 voff, u32 so, u32 lds, u32 lds7) {
  dma7<0, 0>(rs, voff, so, lds);
  u32 t;
  asm volatile("s_mov_b32 m0, %4\n\ts_add_u32 %0, %3, 7168\n\tbuffer_load_dwordx4 %1, %2, %0 offen sc1 lds" : "=&s"(t) : "v"(voff), "s"(rs), "s"(so), "s"(lds7) : "memory", "scc");
}
DI u32 vm_count() {
  u32 t;
  asm volatile("s_getreg_b32 %0, hwreg(HW_REG_IB_STS)" : "=s"(t) :: "memory");
  return (t & 15u) | ((t >> 18) & 48u);
}
DI u32 min8(u32 c) {
  c = min(c, (u32)__builtin_amdgcn_update_dpp(0, (int)c, 0x121, 0xf, 0xf, false)); c = min(c, (u32)__builtin_amdgcn_update_dpp(0, (int)c, 0x122, 0xf, 0xf, false));
  c = min(c, (u32)__builtin_amdgcn_update_dpp(0, (int)c, 0x124, 0xf, 0xf, false));
  return __builtin_amdgcn_readfirstlane(min(c, (u32)__builtin_amdgcn_update_dpp(0, (int)c, 0x128, 0xf, 0xf, false)));
}
DI void dma4c(const void* base, u32 voff, u32 lds) {
  u32 keep;
  asm volatile("s_mov_b32 %0, m0\n\ts_mov_b32 m0, %3\n\ts_nop 0\n\tglobal_load_lds_dword %1, %2 sc1\n\ts_mov_b32 m0, %0" : "=&s"(keep) : "v"(voff), "s"(base), "s"(lds) : "memory");
}
#define LD16(rs, off) __builtin_bit_cast(u32x4, __builtin_amdgcn_raw_buffer_load_b128(rs, off, 0, 16))
#define ST16(rs, v, off, soff) do { const u32x4 d_ = __builtin_bit_cast(u32x4, v); __builtin_amdgcn_raw_buffer_store_b128(d_, rs, off, soff, 16); asm volatile("s_nop 1" :: "v"(d_) : "memory"); } while (0)

#define FLAG(off, n) __builtin_amdgcn_raw_buffer_store_b64(__builtin_bit_cast(u32x2, tag + (n)), rs, fvo, off, 16)
#define FLAGC(c, off, n) __builtin_amdgcn_raw_buffer_store_b64(__builtin_bit_cast(u32x2, tag + (n)), rs, (c) ? fvo : NOWHERE, off, 16)
constexpr int NOWHERE = 0x7ffffff0;
constexpr u32 EMPTY = 0xffffffffu;
DI u32 filled(u32 w) { return min(w, EMPTY - 1u); }
DI u64 peek(const char* ws, u32 off, bool on, u64 dflt) {
  u64 z = 0, v = dflt;
  asm volatile("" : "+v"(z));
  if (on) v = __hip_atomic_fetch_add((GAS u64*)(ws + off), z, __ATOMIC_RELAXED, AP == 2 ? __HIP_MEMORY_SCOPE_SYSTEM : __HIP_MEMORY_SCOPE_AGENT);
  return v;
}
DI void poll_flags(const char* ws, u32 off, bool on, u64 tag) {
#pragma nounroll
  for (u32 sp = 0; sp < SPIN_CAP; ++sp) {
    u64 v = tag + 1;
    if (on || !LL) v = __hip_atomic_load((const GAS u64*)(ws + off), RLX_AGENT);
    if (__all(v == tag + 1)) break;
  }
  __builtin_amdgcn_fence(__ATOMIC_ACQUIRE, "wavefront");
}
template <int SL = 1> DI void lds_wait(LAS char* L, int sig) {
#pragma nounroll
  for (u32 sp = 0; sp < (SPIN_CAP << 4); ++sp) {
    const u32x4 v = *(volatile LAS u32x4*)(L + L_FLG + sig * 16);
    if (__builtin_amdgcn_readfirstlane(v.x & v.y & v.z & v.w)) break;
    __builtin_amdgcn_s_sleep(SL);
  }
  asm volatile("" ::: "memory");
}
DI void lds_wait2(LAS char* L, int sig, int sig2) {
#pragma nounroll
  for (u32 sp = 0; sp < (SPIN_CAP << 4); ++sp) {
    const u32x4 v = *(volatile LAS u32x4*)(L + L_FLG + sig * 16) & *(volatile LAS u32x4*)(L + L_FLG + sig2 * 16);
    if (__builtin_amdgcn_readfirstlane(v.x & v.y & v.z & v.w)) break;
    __builtin_amdgcn_s_sleep(1);
  }
  asm volatile("" ::: "memory");
}
DI void lds_wait1(LAS char* L, int sig, int w) {
#pragma nounroll
  for (u32 sp = 0; sp < (SPIN_CAP << 4); ++sp) {
    if (__builtin_amdgcn_readfirstlane(*(volatile LAS u32*)(L + L_FLG + sig * 16 + w * 4))) break;
    __builtin_amdgcn_s_sleep(1);
  }
  asm volatile("" ::: "memory");
}
DI void lds_signal(LAS char* L, int sig, int w) {
  asm volatile("" ::: "memory");
  *(volatile LAS u32*)(L + L_FLG + sig * 16 + w * 4) = 1u;
}

template <int N> struct IC { static constexpr int value = N; };
template <int I, int N, class F> DI void sfor(F&& f) { if constexpr (I < N) { f(IC<I>{}); sfor<I + 1, N>(f); } }
#define LAM __attribute__((always_inline))
#define PIN(v) asm volatile("" : "+v"(v))
constexpr float LOG2E = 1.4426950408889634f;
template <int GAP, class F> DI void project(LAS char* L, u32 w0, u32 w1, const u32x4 (&xf)[2][14], int tp, int lane, f32x4 (&t)[2][2], F&& mid) {
  asm volatile("" : "+v"(w0), "+v"(w1));
  u32x4 ring[6];
  f32x4 cs[2] = {};
  f32x2 st[2];
  const u32x4 ones = {0x3f803f80u, 0x3f803f80u, 0x3f803f80u, 0x3f803f80u};
#pragma unroll
  for (int n = 0; n < 6; ++n) ring[n] = trfrag(L + (n & 1 ? w1 : w0) + (n >> 1) * 2048, 256);
#pragma unroll
  for (int i = 0; i < 4; ++i) t[i >> 1][i & 1] = f32x4{0.f, 0.f, 0.f, 0.f};
  SCHED();
#pragma unroll
  for (int n = 0; n < 28; ++n) {
    const u32x4 f = ring[n % 6];
    t[0][n & 1] = mfma16(f, xf[0][n >> 1], t[0][n & 1]);
    t[1][n & 1] = mfma16(f, xf[1][n >> 1], t[1][n & 1]);
    cs[n & 1] = mfma16(f, ones, cs[n & 1]);
    if (n + 6 < 28) ring[n % 6] = trfrag(L + (n & 1 ? w1 : w0) + ((n + 6) >> 1) * 2048 + (n + 6 >= 14 ? GAP : 0), 256);
    if (n == 22 || n == 23) st[n & 1] = *(LAS f32x2*)(L + L_ST1 + (tp * 32 + (n & 1) * 16 + (lane & 15)) * 8);
    mid(n);
    SCHED();
  }
#pragma unroll
  for (int tb = 0; tb < 2; ++tb)
#pragma unroll
    for (int k = 0; k < 2; ++k) t[tb][k] = (t[tb][k] + cs[k] * st[tb].x) * st[tb].y;
}
DI f32x2 swp16(float a, float b) { auto r = __builtin_amdgcn_permlane16_swap(__builtin_bit_cast(u32, a), __builtin_bit_cast(u32, b), false, false); return f32x2{__builtin_bit_cast(float, (u32)r[0]), __builtin_bit_cast(float, (u32)r[1])}; }
DI f32x2 swp32(float a, float b) { auto r = __builtin_amdgcn_permlane32_swap(__builtin_bit_cast(u32, a), __builtin_bit_cast(u32, b), false, false); return f32x2{__builtin_bit_cast(float, (u32)r[0]), __builtin_bit_cast(float, (u32)r[1])}; }
DI float swadd32(float a, float b) { const f32x2 s = swp32(a, b); return s.x + s.y; }
DI float swadd16(float a, float b) { const f32x2 s = swp16(a, b); return s.x + s.y; }
DI float mx2(float a, float b) { float r; asm("v_max_f32 %0, %1, %2" : "=v"(r) : "v"(a), "v"(b)); return r; }
DI float mx3(float a, float b, float c) { return fmaxf(fmaxf(a, b), c); }
DI float mn3(float a, float b, float c) { return fminf(fminf(a, b), c); }
DI void feature_map(const f32x4 (&t)[2][2], const u32x4 (&wpf)[2], int g, u32x4 (&P)[2][2]) {
  const f32x4 z4 = {0.f, 0.f, 0.f, 0.f};
  const bool v23 = g < 3;
  float p[2][8], m[4];
#pragma unroll
  for (int tb = 0; tb < 2; ++tb) {
    const u32x4 tq = {pk(t[tb][0][0], t[tb][0][1]), pk(t[tb][0][2], t[tb][0][3]), pk(t[tb][1][0], t[tb][1][1]), pk(t[tb][1][2], t[tb][1][3])};
    const f32x4 p0 = mfma16(wpf[0], tq, z4), p1 = mfma16(wpf[1], tq, z4);
#pragma unroll
    for (int i = 0; i < 8; ++i) { const float v = i < 4 ? p0[i] : p1[i - 4]; p[tb][i] = ((i & 3) < 2 || v23) ? v : p0[0]; }
    m[2 * tb] = mx3(mx3(p[tb][0], p[tb][1], p[tb][2]), mx3(p[tb][3], p[tb][4], p[tb][5]), mx3(p[tb][6], p[tb][7], p[tb][0]));
    m[2 * tb + 1] = -mn3(mn3(p[tb][0], p[tb][1], p[tb][2]), mn3(p[tb][3], p[tb][4], p[tb][5]), mn3(p[tb][6], p[tb][7], p[tb][0]));
  }
  {
    const f32x2 a = swp16(m[0], m[1]), b = swp16(m[2], m[3]);
    const f32x2 c = swp32(mx2(a.x, a.y), mx2(b.x, b.y));
    const float d = mx2(c.x, c.y) * -LOG2E;
    const f32x2 e = swp32(d, d), f0 = swp16(e.x, e.x), f1 = swp16(e.y, e.y);
    m[0] = f0.x; m[1] = f0.y; m[2] = f1.x; m[3] = f1.y;
  }
  float ex[4][8], sm[4];
#pragma unroll
  for (int c = 0; c < 4; ++c) {
#pragma unroll
    for (int i = 0; i < 8; ++i) {
      ex[c][i] = fexp2(fmaf(p[c >> 1][i], (c & 1) ? -LOG2E : LOG2E, m[c]));
      if ((i & 3) >= 2) ex[c][i] = v23 ? ex[c][i] : 0.f;
    }
    sm[c] = ((ex[c][0] + ex[c][1]) + (ex[c][2] + ex[c][3])) + ((ex[c][4] + ex[c][5]) + (ex[c][6] + ex[c][7]));
  }
  {
    const float d = frcp(swadd32(swadd16(sm[0], sm[1]), swadd16(sm[2], sm[3])));
    const f32x2 e = swp32(d, d), f0 = swp16(e.x, e.x), f1 = swp16(e.y, e.y);
    sm[0] = f0.x; sm[1] = f0.y; sm[2] = f1.x; sm[3] = f1.y;
  }
#pragma unroll
  for (int c = 0; c < 4; ++c)
#pragma unroll
    for (int e = 0; e < 4; ++e) P[c >> 1][c & 1][e] = pk(ex[c][2 * e] * sm[c], ex[c][2 * e + 1] * sm[c]);
}
DI u32x4 piece(const u32x4 (&rp)[8], int e) {
  u32x4 f;
#pragma unroll
  for (int m = 0; m < 4; ++m) f[m] = __builtin_amdgcn_perm(rp[2 * m + 1][e >> 1], rp[2 * m][e >> 1], (e & 1) ? 0x07060302u : 0x05040100u);
  return f;
}
#define RP_OPS "+v"(rp[0]), "+v"(rp[1]), "+v"(rp[2]), "+v"(rp[3]), "+v"(rp[4]), "+v"(rp[5]), "+v"(rp[6]), "+v"(rp[7])
#define PF1 0
#define PF2 84
#define PF3 120
#define TP0 -1
#define TP1 13
#define TP2 -1
#define TOUCH 5
#define NW3 4
#define THRN0 4
#define DNPRIO 3
#define NWD 3
#define MIDCHK 0
#define HELP 0
#define NHW 7
#define W3LATE 0
#define W3WAIT 1
#define ROT 1
#define EXPT 0
#define PFM 12
#define PSLP 4
#define PLAG 0
#define WTOUCH 0
#define REPLATE 0
#define WB0 0
#define WB1 4
#define ULATE 0
#define VPRIO 3
#define KGO 20
#define QEARLY 1
#define DLY1 4
#define W1 1
#define W2C 0
#define WOE 2
#define W3THR 1
#define HW3 1
#define HW1 1
#define SN 4
#define BATCH 1
#define B23 1
#define WD5L 0
#define A56 1
#define BGO 196
#define BALL 0
#define BSL 4
#define RSL 4
#define H5 1
#define H6 1
#define BORD 1
#define DP0 3
#define AIL 1
#define SEEN 3
#define ADLY 16
#define W6E 0
#define SPLIT 1
#define NCL 4
#define B5L 1
#define A6L 1
#define BO2 2
#define TW 0
#define CL 0
#define CE 0
#define W5D 1
#define DLY4 16
#define PUBS 5
#define THRN 40
#define DLY2 8
#define DLY3 12
#define GUPRIO 2
enum { S_FREE01 = 0, S_REFILL, S_DONE4, S_WIN3, S_WK, S_WV, S_WQ, S_WSEEN, S_KDONE, S_V, S_PHIK, S_KVP, S_KVA, S_WO, S_REPSEEN, S_HSEEN, S_X0, S_X1, S_XA, S_XB, S_CS, S_WOFA, S_WOFB, S_EXA, S_EXB, S_W2A, S_W2B, S_HFA, S_HFB, S_CONS, S_LAND, S_XF0 };

__global__ void __launch_bounds__(512, 2) fused_block(const char* __restrict__ x, const char* __restrict__ wqkv, const char* __restrict__ wphi, const char* __restrict__ wo,
                                                      const char* __restrict__ wg, const char* __restrict__ wu, const char* __restrict__ wd, char* __restrict__ out, char* ws) {
  extern __shared__ __attribute__((aligned(16))) char smem[];
  LAS char* L = (LAS char*)smem;
  const int tid = threadIdx.x, lane = tid & 63, wave = __builtin_amdgcn_readfirstlane(tid >> 6), w2 = wave & 3;
  const int b = blockIdx.x, li = b >> 3, xp = (b & 7) >> 1, xh = b & 1;
  const int tile = 4 * xp + (li >> 3), r = 8 * xh + (li & 7);
  const int tile2 = 4 * xp + 2 * xh + (li >> 4), r2 = li & 15;
  const int tg = r2 >= 7 ? 1 : 0, cg = r2 - 7 * tg;
  const int l15 = lane & 15, g = lane >> 4, q4 = l15 >> 2, p4 = lane & 3, hh = lane >> 5;
  const int head = r;
  asm volatile("" :: "s"(x), "s"(wqkv), "s"(wphi), "s"(wo), "s"(wg), "s"(wu), "s"(wd), "s"(out), "s"(ws));
  STAMP(0);
  u64 pc_; asm volatile("s_getpc_b64 %0" : "=s"(pc_));
  const char* pc0 = (const char*)(pc_ & ~127ull);
  const __amdgpu_buffer_rsrc_t rs = __builtin_amdgcn_make_buffer_rsrc((void*)ws, 0, WS_USED, 0x00020000);
  const u32x4 ones = {0x3f803f80u, 0x3f803f80u, 0x3f803f80u, 0x3f803f80u};
  const f32x4 z4 = {0.f, 0.f, 0.f, 0.f};
  auto role = [&](auto AC) __attribute__((always_inline)) {
  constexpr bool A = AC.value;
  if (A) asm volatile("; A" ::: "memory"); else asm volatile("; B" ::: "memory");
  __builtin_amdgcn_s_waitcnt(0x0F70);
  u64 epoch;
  u32x4 rp[8];
  const int fvo = lane < 16 ? lane * 8 : NOWHERE;
  const int uidx = (tile * 10 + __builtin_popcount(~0x2A54u & ((1u << head) - 1u))) * 4 + w2, unit = uidx * 147 / 160;
  const bool has_unit = !((0x2A54 >> head) & 1) && (uidx + 1) * 147 / 160 != unit;
  const int u3 = unit / 3, umat = unit - 3 * u3, ukq = u3 / 28, ufl = u3 - 28 * ukq;
  const bool ud = umat == 2;
  const char* gusrc = ws + WS_WGU + 7 * r * SLOT;
  const u32 guo = WS_WGU + 7 * r * SLOT + w2 * 7168;
#define GUBLK(j) dma7<(j) * SLOT, gu_slot(j) * SLOT>(rs, lane * 16, guo, w2 * 7168)
  const u32 wvo = (lane >> 2) * 2688 + (((lane & 3) ^ (2 * (lane >> 5))) * 16);
  const char* wbase = wqkv + head * 56 - (head & 1) * 8 + 16 * w2 * 2688;
  const u32 wr0 = (8 * g + q4) * 64 + (((p4 + (head & 1)) ^ (4 * (g & 1))) * 8), wr1 = (8 * g + q4) * 64 + ((((4 + p4 + (head & 1)) & 7) ^ (4 * (g & 1))) * 8);
  const int tb = w2; constexpr int kh = A ? 0 : 1;
  const int hho = WS_HH + (((tile * 2 + (w2 >> 1)) * 4 + (r >> 2)) * 56 + (r & 3) * 2 + (w2 & 1)) * 1024;
  u32x4 wof[8][4];
  u64 tag;
#define WOF_READ(i0, i1) do { u32 wa_ = L_WO + (kh * 224 + 4 * g + q4) * 128 + 16 * p4 + 64 * (q4 >> 1), wb_ = wa_ ^ 64, wc_ = wa_ + (g == 3 ? 0 : 2048), wd_ = wc_ ^ 64; \
    asm volatile("" : "+v"(wa_), "+v"(wb_), "+v"(wc_), "+v"(wd_)); \
    _Pragma("unroll") for (int i = i0; i < i1; ++i) _Pragma("unroll") for (int k = 0; k < 4; ++k) { \
      const u32x2 a_ = trread(L + (k >> 1 ? wb_ : wa_) + 28 * i * 128 + (k & 1) * 8), c_ = trread(L + (k >> 1 ? wd_ : wc_) + 28 * i * 128 + (k & 1) * 8); \
      wof[i][k] = u32x4{a_.x, a_.y, c_.x, c_.y}; } } while (0)
#define XF_READ(i) _Pragma("unroll") for (int s = 0; s < 14; ++s) xf[i][s] = *(LAS u32x4*)((s & 1 ? xr1 : xr0) + (i) * 14336 + (s >> 1) * 2048)
#define LN_STATS() do { f32x4 g1 = z4, g2 = z4; \
    _Pragma("unroll") for (int s = 0; s < 14; ++s) { g1 = mfma16(ones, xf[kh][s], g1); g2 = mfma16(xf[kh][s], xf[kh][s], g2); } \
    const float d01 = (p4 & 1) ? g2[1] : g2[0], d23 = (p4 & 1) ? g2[3] : g2[2], s2 = (p4 & 2) ? d23 : d01; \
    const float nmu = g1[0] * (-1.f / 448.f), var = fmaxf(s2 * (1.f / 448.f) - nmu * nmu, 0.f); \
    if (g == q4) *(LAS f32x2*)(L + L_ST1 + (w2 * 32 + kh * 16 + l15) * 8) = f32x2{nmu, __builtin_amdgcn_rsqf(var + 1e-5f)}; } while (0)
#define WPF_STEP(n) do { \
    if ((n) == 0) { _Pragma("unroll") for (int j = 0; j < 8; ++j) wp[j] = *(LAS u32*)(L + L_WPHI + 4 * g * 56 + l15 * 4 + ((j & 3) + 16 * (j >> 2)) * 56); } \
    if ((n) >= 8 && (n) < 12) { const int e = (n) - 8; const bool z = l15 >= 14 || (e >= 2 && g == 3); \
      wpf[0][e] = z ? 0u : __builtin_amdgcn_perm(wp[2 * e + 1], wp[2 * e], 0x05040100u); wpf[1][e] = z ? 0u : __builtin_amdgcn_perm(wp[2 * e + 1], wp[2 * e], 0x07060302u); } \
    if ((n) == 12) asm volatile("" : "+v"(wpf[0]), "+v"(wpf[1])); } while (0)
  {
    u32x4 xf[2][14], wpf[2];
    u32 wp[8];
    LAS char* xr0 = L + w2 * 28672 + l15 * 128 + ((g ^ (l15 & 7)) * 16);
    LAS char* xr1 = L + w2 * 28672 + l15 * 128 + (((4 + g) ^ (l15 & 7)) * 16);
    LAS char* pw = L + L_PKV + (w2 * 32 + l15) * 224 + g * 8;
    const char* wos = wo + (r2 < 14 ? cg : 0) * 128 + w2 * 56 * 896;
    const u32 wovo = (lane >> 3) * 896 + (((lane & 7) ^ ((lane >> 2) & 4)) * 16);
#define WO_HALF(hf_) _Pragma("unroll") for (int n = 0; n < 7; ++n) dma16(wos + ((hf_) * 224 + n * 8) * 896, wovo, L_WO + ((hf_) * 28 + w2 * 7 + n) * 1024)
    if constexpr (A) {
      if (wave < 2) dma4(x + (xp * 512 + li * 16) * 896 + wave * 7168, min(lane, 55) * 128, L_JUNK);
      if (wave == 3) { *(LAS u32*)(L + L_FLG + lane * 4) = 0u; *(LAS u32*)(L + L_FLG + 256 + lane * 4) = 0u; dma4(pc0, min((li % 6) * 8192 + lane * 128, CODE_PF), L_JUNK); }
      if (wave == 2) { dma16(wphi + head * 1568, lane * 16, L_WPHI); dma16(wphi + head * 1568, min(lane + 64, 97) * 16, L_WPHI + 1024); }
      if (WTOUCH && wave < WTOUCH) dma4(wqkv + li * 14 * 2688 + (wave == 2 ? 0 : (wave + 1) * 896) + xh * 384, min(lane >> 2, 13) * 2688 + (lane & 3) * 128, L_JUNK);
      BAR();
      asm volatile("s_load_dwordx2 %0, %1, 0x0 ; role A\n\ts_waitcnt lgkmcnt(0)" : "=s"(epoch) : "s"(ws) : "memory");
      tag = epoch * 6364136223846793005ull + 1442695040888963407ull;
      STAMP(1);
      lds_wait1(L, S_X0, w2);
      XF_READ(0);
      if (w2 < 2) lds_signal(L, S_XF0, w2);
#pragma unroll
      for (int i = 0; i < 7; ++i) dma16(wbase + 896 + 64 * i * 2688, wvo, L_WK + (4 * i + w2) * 1024);
      STAMP(27);
      lds_wait1(L, S_X1, w2);
      STAMP(2);
      XF_READ(1);
      STAMP(3);
      LN_STATS();
      WAITVM(0);
      STAMP(4);
      lds_signal(L, S_WK, w2);
      lds_wait(L, S_WK);
      STAMP(5);
      __builtin_amdgcn_s_setprio(2);
      f32x4 t[2][2];
      u32x4 cw;
      project<0>(L, L_WK + wr0, L_WK + wr1, xf, w2, lane, t, [&](int n) LAM {
        WPF_STEP(n);
        if (n == KGO) lds_signal(L, S_KDONE, w2);
        if (n == 15) cw = *(volatile LAS u32x4*)(L + L_FLG + S_CS * 16);
        if (n == 21 && __builtin_expect(!__builtin_amdgcn_readfirstlane(cw.x & cw.y & cw.z & cw.w), 0)) lds_wait(L, S_CS);
      });
      STAMP(6);
      u32x4 P[2][2];
      feature_map(t, wpf, g, P);
#pragma unroll
      for (int c = 0; c < 4; ++c) {
        LAS char* p = pw + (c >> 1) * 16 * 224 + (c & 1) * 64;
        *(LAS u32x2*)(p) = u32x2{P[c >> 1][c & 1].x, P[c >> 1][c & 1].y}; *(LAS u32x2*)(p + 32) = u32x2{P[c >> 1][c & 1].z, P[c >> 1][c & 1].w};
      }
      lds_signal(L, S_PHIK, w2);
      STAMP(7);
      u32 pr = L_PKV + (4 * g + q4) * 224 + 8 * p4;
      asm volatile("" : "+v"(pr));
      lds_wait2(L, S_V, S_PHIK);
      u32x4 pa[4], pb[8];
#pragma unroll
      for (int m = 0; m < 4; ++m) {
        pa[m] = trfrag(L + pr + w2 * 32 + m * 32 * 224, 16 * 224);
        pb[2 * m] = trfrag(L + pr + m * 32 * 224 + 128, 16 * 224); pb[2 * m + 1] = trfrag(L + pr + m * 32 * 224 + 160, 16 * 224);
      }
      f32x4 kv[2] = {};
#pragma unroll
      for (int m = 0; m < 8; ++m) kv[m & 1] = mfma16(pa[m >> 1], pb[m], kv[m & 1]);
      const u32x4 kvb = {pk(kv[0][0], kv[0][1]), pk(kv[0][2], kv[0][3]), pk(kv[1][0], kv[1][1]), pk(kv[1][2], kv[1][3])};
      ST16(rs, kvb, lane * 16, WS_KVP + ((head * 16 + tile) * 4 + w2) * 1024);
      STAMP(8);
      DRAIN();
      FLAG(WS_FKV + ((head * 4 + w2) * 16 + tile) * 128, 1);
      __builtin_amdgcn_s_setprio(0);
      STAMP(9);
      if (HW1) { lds_signal(L, S_XA, w2); lds_wait(L, S_XA); }
      if (WOE == 2) { WO_HALF(0); WO_HALF(1); } else if (WOE == 3) { WO_HALF(0); } else __builtin_amdgcn_s_sleep(DLY1);
      poll_flags(ws, WS_FKV + ((head * 4 + w2) * 16 + l15) * 128, lane < 16, tag);
      STAMP(10);
      u32x4 tt[16];
#pragma unroll
      for (int i = 0; i < 16; ++i) tt[i] = __builtin_bit_cast(u32x4, __builtin_amdgcn_raw_buffer_load_b128(rs, lane * 16, WS_KVP + head * 65536 + w2 * 1024 + i * 4096, 16));
      const u32 fgo = WS_FG + (min(((7 * r) >> 2) + g, (7 * r + 6) >> 2) * 16 + (l15 & 8) + min(lane & 7, 6)) * 128;
      const u64 fg = __hip_atomic_load((const GAS u64*)(ws + (wave == 3 ? fgo : WS_FG)), RLX_AGENT);
      SCHED();
      const u32 one = g == q4 ? ((p4 & 1) ? 0x3f800000u : 0x3f80u) : 0u, s01 = p4 < 2 ? one : 0u, s23 = p4 < 2 ? 0u : one;
      const u32x4 sel0 = {s01, s23, 0u, 0u}, sel1 = {0u, 0u, s01, s23};
      f32x4 s0 = {}, s1 = {};
#pragma unroll
      for (int i = 0; i < 16; ++i) { s0 = mfma16(sel0, tt[i], s0); s1 = mfma16(sel1, tt[i], s1); }
      LAS char* kw = L + L_KVA + (w2 >> 1) * 1024 + lane * 16 + (w2 & 1) * 8;
      *(LAS u32x2*)(kw) = u32x2{pk(s0[0], s0[1]), pk(s0[2], s0[3])};
      *(LAS u32x2*)(kw + 2048) = u32x2{pk(s1[0], s1[1]), pk(s1[2], s1[3])};
      lds_signal(L, S_KVA, w2);
      STAMP(11);
      if (WOE == 3) { WO_HALF(1); WAITVM(0); lds_signal(L, S_WO, w2); }
      if (wave == 3 && !__all(fg == tag + 1)) poll_flags(ws, fgo, true, tag);
      __builtin_amdgcn_fence(__ATOMIC_ACQUIRE, "wavefront");
      lds_signal(L, S_WOFA, w2);
      lds_wait1(L, S_WOFA, 3);
      const int fl = min(((7 * r) >> 2) + w2, (7 * r + 6) >> 2);
      f32x4 c[7];
#pragma unroll
      for (int i = 0; i < 7; ++i) c[i] = __builtin_bit_cast(f32x4, __builtin_amdgcn_raw_buffer_load_b128(rs, ((7 * r) >> 2) + w2 == fl ? lane * 16 + (i & 3) * 1024 : NOWHERE, WS_CS + fl * 7168 + (i & 4) * 1024, 16));
      if (W1) GUBLK(2);
      lds_wait(L, WOE < 2 ? S_WQ : S_KVA);
      WOF_READ(0, 8);
      SCHED();
      if (((7 * r) >> 2) + w2 == fl) {
        const f32x4 sm = ((c[0] + c[1]) + (c[2] + c[3])) + ((c[4] + c[5]) + c[6]);
        const float e0 = swadd32(sm[0], sm[1]), e1 = swadd32(sm[2], sm[3]);
        const int j0 = 4 * fl + 2 * (lane & 1) - 7 * r, cr = ((g & 1) * 16 + hh * 8 + (l15 >> 1)) * 4;
        if (j0 >= 0 && j0 < 7) *(LAS float*)(L + L_CS + j0 * 128 + cr) = e0;
        if (j0 + 1 >= 0 && j0 + 1 < 7) *(LAS float*)(L + L_CS + (j0 + 1) * 128 + cr) = e1;
      }
      STAMP(24);
    } else {
      const u32 vo = (lane >> 3) * 896 + (((lane & 7) ^ (lane >> 3)) * 16);
      const char* xb = x + (tile * 128 + w2 * 32) * 896;
#pragma unroll
      for (int n = 0; n < 28; ++n) {
        dma16(xb + ((n / 14) * 16 + (n & 1) * 8) * 896 + ((n % 14) >> 1) * 128, vo, w2 * 28672 + n * 1024);
        if (n == 3) { BAR(); asm volatile("s_load_dwordx2 %0, %1, 0x0 ; role B" : "=s"(epoch) : "s"(ws) : "memory"); }
        if (n == 21) { WAITVM(8); lds_signal(L, S_X0, w2); }
      }
      asm volatile("s_waitcnt lgkmcnt(0)" : "+s"(epoch)::"memory");
      tag = epoch * 6364136223846793005ull + 1442695040888963407ull;
      STAMP(1);
      XF_READ(0);
      if (w2 < 2) lds_signal(L, S_XF0, 2 + w2);
      WAITVM(0);
      lds_signal(L, S_X1, w2);
      STAMP(2);
      lds_wait(L, S_XF0);
      XF_READ(1);
      SCHED();
#define WDST(c) ((c) * 1024 + ((c) >= 14 ? SLOT - 14336 : 0))
#pragma unroll
      for (int i = 0; i < 7; ++i) dma16(wbase + 1792 + 64 * i * 2688, wvo, L_WV + WDST(4 * i + w2));
      STAMP(3);
      LN_STATS();
      lds_signal(L, S_CS, w2);
      STAMP(4);
      if (ULATE) { WAITVM(0); lds_signal(L, S_WV, w2); }
      const int rg = lane >> 3;
      const u32 lvo = (ud ? (32 * (rg & 1) + (rg & 6)) * 896 : rg * 8 * 3584) + (lane & 7) * 16;
      const char* us = ud ? wd + ufl * 64 * 896 + ukq * 128 : (umat ? wu : wg) + ukq * 64 * 3584 + ufl * 128;
      int ust0 = ud ? 8 * 896 : 3584, ust1 = ud ? 896 : 4 * 3584;
      asm volatile("" : "+s"(us), "+s"(ust0), "+s"(ust1));
      if (has_unit) {
#pragma unroll
        for (int j = 0; j < 8; ++j) asm volatile("global_load_dwordx4 %0, %1, %2" : "=v"(rp[j]) : "v"(lvo), "s"(us + (j & 3) * ust0 + (j >> 2) * ust1) : "memory");
      } else WAITVM(0);
      if (!ULATE) { WAITVM(8); lds_signal(L, S_WV, w2); }
      lds_wait2(L, S_WV, S_WK);
      if (QEARLY) {
#pragma unroll
        for (int i = 0; i < 7; ++i) dma16(wbase + 64 * i * 2688, wvo, L_WQ + WDST(4 * i + w2));
      }
      if (WOE == 1) WO_HALF(0);
      lds_wait1(L, S_KDONE, w2);
      STAMP(5);
      f32x4 t[2][2];
      project<SLOT - 14336>(L, L_WV + wr0, L_WV + wr1, xf, w2, lane, t, [&](int n) LAM { if (n == 27) __builtin_amdgcn_s_setprio(VPRIO); });
      STAMP(6);
#pragma unroll
      for (int c = 0; c < 2; ++c) {
        LAS char* p = pw + c * 16 * 224 + 128;
        *(LAS u32x2*)(p) = u32x2{pk(t[c][0][0], t[c][0][1]), pk(t[c][0][2], t[c][0][3])};
        *(LAS u32x2*)(p + 32) = g == 3 ? u32x2{0x3f80u, 0u} : u32x2{pk(t[c][1][0], t[c][1][1]), pk(t[c][1][2], t[c][1][3])};
      }
      lds_signal(L, S_V, w2);
      __builtin_amdgcn_s_setprio(0);
      STAMP(7);
      if (!QEARLY) {
#pragma unroll
        for (int i = 0; i < 7; ++i) dma16(wbase + 64 * i * 2688, wvo, L_WQ + WDST(4 * i + w2));
      }
      if (WOE == 0) WO_HALF(0);
      if (WOE < 2) { lds_wait(L, S_KDONE); WO_HALF(1); }
      asm volatile("s_waitcnt vmcnt(%8)" : RP_OPS : "n"(WOE < 2 ? 21 : 7) : "memory");
      auto repack = [&]() LAM {
        if (has_unit) {
          const int fb = 4 * ufl + (rg >> 1), pr = fb * 147 >> 10;
          const int so = (ud ? (((pr >> 2) * 7 + fb - 7 * pr) * 4 + (pr & 3)) * 2048 : (rg >> 1) * 1024) + (rg & 1) * 512 + (lane & 7) * 16;
          const int sb = ud ? WS_WD + ukq * 229376 : WS_WGU + 4 * ufl * SLOT + ukq * 4096 + umat * 256;
          const int s1 = ud ? 256 : SLOT, s2 = ud ? 1024 : 2 * SLOT;
          float c[8];
#pragma unroll
          for (int e = 0; e < 8; ++e) {
            const u32x4 f = piece(rp, e);
            ST16(rs, f, so + (e & 1) * 128, sb + ((e >> 1) & 1) * s1 + (e >> 2) * s2);
            c[e] = mfma16(ones, f, z4)[0];
          }
          const f32x4 cv = g & 1 ? f32x4{c[4], c[5], c[6], c[7]} : f32x4{c[0], c[1], c[2], c[3]};
          ST16(rs, cv, g < 2 && !ud ? (l15 >> 3) * 512 + (lane & 7) * 32 + g * 16 : NOWHERE, WS_CS + ufl * 7168 + ukq * 1024 + (umat & 1) * 256);
        }
      };
#define UFLAG() FLAGC(has_unit, ud ? WS_FD + (ukq * 32 + ufl) * 128 : WS_FG + ((ufl * 2 + umat) * 8 + ukq) * 128, 1)
      if (!REPLATE) repack();
      STAMP(8);
      if (REPLATE || !has_unit) WAITVM(0); else WAITVM(9);
      lds_signal(L, S_WQ, w2);
      lds_wait(L, S_WQ);
      STAMP(9);
      project<SLOT - 14336>(L, L_WQ + wr0, L_WQ + wr1, xf, w2, lane, t, [&](int n) LAM {
        WPF_STEP(n);
        if (n == 14 && !REPLATE) { WAITVM(0); UFLAG(); }
      });
      STAMP(10);
      if (REPLATE == 2) repack();
      u32x4 pq[2][2];
      feature_map(t, wpf, g, pq);
      asm volatile("" : "+v"(pq[0][0]), "+v"(pq[0][1]), "+v"(pq[1][0]), "+v"(pq[1][1]));
      STAMP(11);
      if (REPLATE == 1) repack();
      WOF_READ(0, WB0);
      STAMP(24);
      if (REPLATE == 2) { WAITVM(0); UFLAG(); }
      lds_wait(L, S_KVA);
      if (REPLATE == 1) { WAITVM(0); UFLAG(); }
      STAMP(27);
      u32x4 ka[4];
#pragma unroll
      for (int i = 0; i < 4; ++i) ka[i] = *(LAS u32x4*)(L + L_KVA + i * 1024 + lane * 16);
      f32x4 o[2][2];
#pragma unroll
      for (int c = 0; c < 4; ++c) o[c >> 1][c & 1] = mfma16(ka[2 * (c & 1) + 1], pq[c >> 1][1], mfma16(ka[2 * (c & 1)], pq[c >> 1][0], z4));
      const f32x2 da = swp16(o[0][1][0], o[0][1][0]), db = swp16(o[1][1][0], o[1][1][0]);
      const float den[2] = {swp32(da.y, da.y).y, swp32(db.y, db.y).y};
#pragma unroll
      for (int c = 0; c < 2; ++c) {
        const float rd = frcp(den[c] + 1e-6f), r3 = g == 3 ? 0.f : rd;
        const u32x4 ob = {pk(o[c][0][0] * rd, o[c][0][1] * rd), pk(o[c][0][2] * rd, o[c][0][3] * rd), pk(o[c][1][0] * r3, o[c][1][1] * r3), pk(o[c][1][2] * r3, o[c][1][3] * r3)};
        ST16(rs, ob, lane * 16, WS_O + ((tile * 8 + 2 * w2 + c) * 16 + head) * 1024);
      }
      STAMP(12);
      if (WOE == 3) lds_wait(L, S_WO);
      WOF_READ(WB0, WB1);
      DRAIN();
      FLAG(WS_FO + ((tile * 4 + w2) * 16 + head) * 128, 1);
      lds_signal(L, S_W2B, w2);
      WOF_READ(WB1, 8);
    }
  }
  STAMP(13);
  u32x4 hres = {0u, 0u, 0u, 0u};
  const int tok = tile2 * 128 + tg * 64 + tb * 16 + l15;
  const int xo = tok * 896 + cg * 128 + kh * 64 + g * 16;
#define WIN2() do { lds_wait2(L, S_W2B, S_WOFA); GUBLK(A ? 0 : 1); if (!W1 && A) GUBLK(2); } while (0)
  STAMP(23);
  bool fd_ok = false;
  if (r2 < 14) {
    const u32x4 xr = *(const u32x4*)(x + xo);
    const u32 fo = WS_FO + ((tile2 * 4 + 2 * tg + (tb >> 1)) * 16 + kh * 8 + (lane & 7)) * 128;
    if (A) lds_wait(L, S_W2B);
    const bool late = W2C && __all(__hip_atomic_load((const GAS u64*)(ws + fo), RLX_AGENT) == tag + 1);
    if (!late) { WIN2(); poll_flags(ws, fo, lane < 8, tag); }
    __builtin_amdgcn_fence(__ATOMIC_ACQUIRE, "wavefront");
    STAMP(14);
    u32x4 of[8];
#pragma unroll
    for (int i = 0; i < 8; ++i)
      asm volatile("global_load_dwordx4 %0, %1, %2 offset:%3 sc1" : "=v"(of[i]) : "v"(lane * 16), "s"(ws + WS_O + ((tile2 * 8 + tg * 4 + tb) * 16 + kh * 8 + (i & 4)) * 1024), "n"((i & 3) * 1024) : "memory");
    u64 fdv;
    asm volatile("global_load_dwordx2 %0, %1, %2 sc1" : "=v"(fdv) : "v"(A ? 0 : min(lane & 7, 6) * 128), "s"(ws + WS_FD + (cg * 32 + 7 * w2) * 128) : "memory");
    if (late) { WIN2(); asm volatile("s_waitcnt vmcnt(%0)" :: "n"(!W1 && A ? 14 : 7) : "memory"); } else WAITVM(0);
    asm volatile("" : "+v"(of[0]), "+v"(of[1]), "+v"(of[2]), "+v"(of[3]), "+v"(of[4]), "+v"(of[5]), "+v"(of[6]), "+v"(of[7]), "+v"(fdv) :: "memory");
    f32x4 a[4] = {};
#pragma unroll
    for (int i = 0; i < 8; ++i)
#pragma unroll
      for (int k = 0; k < 4; ++k) a[k] = mfma16(wof[i][k], of[i], a[k]);
    fd_ok = __all(fdv == tag + 1);
    *(LAS f32x4*)(L + L_EX + wave * 2048 + lane * 16) = kh ? a[0] : a[2];
    *(LAS f32x4*)(L + L_EX + wave * 2048 + 1024 + lane * 16) = kh ? a[1] : a[3];
    lds_signal(L, A ? S_EXA : S_EXB, w2);
    lds_wait1(L, A ? S_EXB : S_EXA, w2);
    STAMP(15);
    const f32x4 c0 = (kh ? a[2] : a[0]) + *(LAS f32x4*)(L + L_EX + (wave ^ 4) * 2048 + lane * 16);
    const f32x4 c1 = (kh ? a[3] : a[1]) + *(LAS f32x4*)(L + L_EX + (wave ^ 4) * 2048 + 1024 + lane * 16);
    const f32x4 h0 = c0 + f32x4{blo(xr.x), bhi(xr.x), blo(xr.y), bhi(xr.y)}, h1 = c1 + f32x4{blo(xr.z), bhi(xr.z), blo(xr.w), bhi(xr.w)};
    hres = u32x4{pk(h0[0], h0[1]), pk(h0[2], h0[3]), pk(h1[0], h1[1]), pk(h1[2], h1[3])};
    ST16(rs, hres, WS_H + ((tok >> 5) * 28 + 4 * cg + 2 * kh + (g >> 1)) * 1024 + ((g & 1) * 32 + (tok & 31)) * 16, 0);
    const f32x4 r0 = {blo(hres.x), bhi(hres.x), blo(hres.y), bhi(hres.y)}, r1 = {blo(hres.z), bhi(hres.z), blo(hres.w), bhi(hres.w)};
    const f32x4 a1 = r0 + r1, a2q = r0 * r0 + r1 * r1;
    float s1 = (a1[0] + a1[1]) + (a1[2] + a1[3]), s2 = (a2q[0] + a2q[1]) + (a2q[2] + a2q[3]);
    { const float t = swadd16(s1, s2); const f32x2 a2 = sw32(t); s1 = a2.x + a2.y; }
    __builtin_amdgcn_raw_buffer_store_b64(__builtin_bit_cast(u32x2, sw16(s1)), rs, fvo, WS_ST + ((2 * cg + kh) * 2048 + tile2 * 128 + tg * 64 + tb * 16) * 8, 16);
    DRAIN();
    FLAG(WS_FH + ((tile2 * 4 + 2 * tg + (tb >> 1)) * 32 + cg * 4 + (tb & 1) * 2 + kh) * 128, 1);
  } else {
    WIN2();
#pragma unroll
    for (int i = 0; i < 4 * NCL; ++i) ST16(rs, (u32x4{EMPTY, EMPTY, EMPTY, EMPTY}), lane * 16 + (i & 1) * 2048, WS_HH + (((tile2 * 2 + r2 - 14) * 4 + w2) * 56 + kh + (i >> 1) * 4) * 1024);
    DRAIN();
    FLAG(WS_FH + ((tile2 * 4 + 2 * (r2 - 14) + kh) * 32 + 28 + w2) * 128, 1);
  }
  lds_signal(L, A ? S_HFA : S_HFB, w2);
  STAMP(16);
  u32x4 wdf[NWD][8];
  const int wdo = WS_WD + (cg * 4 + w2) * 57344;
  const u32 po = WS_FHH + ((tile2 * 16 + 4 * w2 + ((lane >> 1) & 3)) * 4 + 2 * tg + (lane & 1)) * 128;
  const u32 ho = WS_HH + ((tile2 * 2 + tg) * 4 + w2) * 57344;
#define WDLOAD(k, bf) dma8(rs, lane * 16, wdo + (k) * 8192, ((bf) + 2) * SLOT + w2 * 7168, L_EX + (bf) * 4096 + w2 * 1024)
#define THR() asm volatile("s_waitcnt vmcnt(%0)" :: "n"(THRN) : "memory")
#define WLOAD1(j, i) wdf[(j) % NWD][i] = __builtin_bit_cast(u32x4, __builtin_amdgcn_raw_buffer_load_b128(rs, lane * 16 + ((i) & 3) * 1024, wdo + (j) * 8192 + ((i) & 4) * 1024, 16))
#define THR0() asm volatile("s_waitcnt vmcnt(%0)" :: "n"(THRN0) : "memory")
#define WLOAD(j) _Pragma("unroll") for (int i = 0; i < 8; ++i) { THR0(); WLOAD1(j, i); }
  u32 have = 0;
#define PLOAD() peek(ws, po, lane < 8, tag + 7)
#define PMIN(pv) do { const u64 d = (pv) - tag; have = max(have, min8(d <= 8 ? (u32)d : 0u)); } while (0)
#define NEEDP(n) do { _Pragma("nounroll") for (u32 sp = 0; have < (n) && sp < SPIN_CAP; ++sp) { PMIN(PLOAD()); if (PSLP && have < (n)) __builtin_amdgcn_s_sleep(PSLP); } \
    __builtin_amdgcn_fence(__ATOMIC_ACQUIRE, "wavefront"); } while (0)
#define HBASE() u32 hb3 = w2 * 7168 + lane * 16, hb0 = hb3 + 2 * SLOT, hb2 = hb0 + 2 * SLOT, hb7 = L_EX + w2 * 1024 + lane * 16; asm volatile("" : "+v"(hb0), "+v"(hb2), "+v"(hb7))
#define HFRAG(bf, i) (*(LAS u32x4*)(L + ((i) < 7 ? ((bf) == 3 ? hb3 : (bf) == 2 ? hb2 : hb0 + (bf) * SLOT) + (i) * 1024 : hb7 + (bf) * 4096)))
#define HQLOAD(q, j) _Pragma("unroll") for (int i = 0; i < 8; ++i) hq[q][i] = __builtin_bit_cast(u32x4, __builtin_amdgcn_raw_buffer_load_b128(rs, lane * 16 + (i & 3) * 1024, ho + (j) * 8192 + (i & 4) * 1024, 16))
#define HQLOADT(q, j) _Pragma("unroll") for (int i = 0; i < 8; ++i) { THR(); hq[q][i] = __builtin_bit_cast(u32x4, __builtin_amdgcn_raw_buffer_load_b128(rs, lane * 16 + (i & 3) * 1024, ho + (j) * 8192 + (i & 4) * 1024, 16)); }
#define DOWN16(ac, wf, hf_) _Pragma("unroll") for (int m = 0; m < 16; ++m) ac[(m >> 1) & 1][m & 1] = mfma32(wf[2 * (m >> 2) + (m & 1)], hf_[2 * (m >> 2) + ((m >> 1) & 1)], ac[(m >> 1) & 1][m & 1])
#define PARTIAL_OUT(ac, b0, b1) _Pragma("unroll") for (int n = 0; n < 16; ++n) { const int T = n >> 3, k = (n >> 2) & 1, q = n & 3; \
    *(LAS f32x4*)&HFRAG(T ? b1 : b0, n & 7) = f32x4{ac[T][k][4 * q], ac[T][k][4 * q + 1], ac[T][k][4 * q + 2], ac[T][k][4 * q + 3]}; }
#define FREE(f) do { _Pragma("nounroll") for (u32 sp = 0; sp < (SPIN_CAP << 4); ++sp) { const u32x4 fr = *(volatile LAS u32x4*)(L + L_FLG + S_DONE4 * 16); \
          if (__builtin_amdgcn_readfirstlane(min(min(fr.x, fr.y), min(fr.z, fr.w))) >= (f)) break; __builtin_amdgcn_s_sleep(BSL); } asm volatile("" ::: "memory"); } while (0)
#define HQBAD(q) (!__all(max(max(max(hq[q][0].w, hq[q][1].w), max(hq[q][2].w, hq[q][3].w)), max(max(hq[q][4].w, hq[q][5].w), max(hq[q][6].w, hq[q][7].w))) != EMPTY))
#define HQCHECK(q, j) do { if (__builtin_expect(HQBAD(q), 0)) { _Pragma("nounroll") for (u32 sp = 0; sp < SPIN_CAP; ++sp) { \
      __builtin_amdgcn_s_sleep(RSL); asm volatile("" ::: "memory"); HQLOAD(q, j); if (!HQBAD(q)) break; } } SCHED(); } while (0)
  if (A) {
    if (HW3) lds_wait2(L, S_HFA, S_HFB);
    __builtin_amdgcn_s_sleep(DLY3);
    poll_flags(ws, WS_FH + ((tile * 4 + w2) * 32 + (lane & 31)) * 128, lane < 32, tag);
    lds_signal(L, S_HSEEN, w2);
  }
  STAMP(17);
  if constexpr (A) {
    const int T32 = tile * 4 + wave;
    f32x2 st[7];
#pragma unroll
    for (int i = 0; i < 7; ++i) st[i] = __builtin_bit_cast(f32x2, __builtin_amdgcn_raw_buffer_load_b64(rs, WS_ST + (7 * hh * 2048 + (lane & 31)) * 8, (i * 2048 + T32 * 32) * 8, 16));
    u32x4 hb[28];
#pragma unroll
    for (int s = 0; s < 28; ++s) hb[s] = __builtin_bit_cast(u32x4, __builtin_amdgcn_raw_buffer_load_b128(rs, lane * 16 + (s & 3) * 1024, WS_H + (T32 * 28 + (s & ~3)) * 1024, 16));
    lds_wait2(L, S_HFA, S_HFB);
    u32 wb_[3] = {(u32)lane * 16, (u32)lane * 16 + 2 * SLOT, (u32)lane * 16 + 4 * SLOT};
    asm volatile("" : "+v"(wb_[1]), "+v"(wb_[2]));
#define WFRAG(sl, s) (*(LAS u32x4*)(L + wb_[(sl) >> 1] + ((sl) & 1) * SLOT + (s) * 1024))
    LAS char* csl = L + L_CS + hh * 16;
    u32x4 ring[7];
    f32x4 cq[4];
#pragma unroll
    for (int k = 0; k < 4; ++k) cq[k] = *(LAS f32x4*)(csl + k * 32);
#pragma unroll
    for (int s = 0; s < 7; ++s) ring[s] = WFRAG(0, s);
    SCHED();
    float s1 = ((st[0].x + st[1].x) + (st[2].x + st[3].x)) + ((st[4].x + st[5].x) + st[6].x);
    float s2 = ((st[0].y + st[1].y) + (st[2].y + st[3].y)) + ((st[4].y + st[5].y) + st[6].y);
    { f32x2 a = sw32(s1); s1 = a.x + a.y; a = sw32(s2); s2 = a.x + a.y; }
    const float nmu = s1 * (-1.f / 448.f), ve = fmaxf(s2 * (1.f / 448.f) - nmu * nmu, 0.f) + 1e-5f;
    const float rstd = __builtin_amdgcn_rsqf(ve);
    const float k0 = -2.30220819f * rstd, k1 = -0.10294324f * rstd * rstd * rstd;
    const int pvo = WS_FHH + ((tile * 16 + r) * 4 + wave) * 128;
#define PROG(n) FLAG(pvo + 131072, n)
    f32x16 acc[2];
    float y[8], gu[8];
    u32x4 yb;
    u32x4 cw;
#pragma unroll
    for (int i = 0; i < 16; ++i) acc[0][i] = nmu * cq[i >> 2][i & 3];
    __builtin_amdgcn_s_setprio(GUPRIO);
    SCHED();
#define LT(i)
    auto gu_block = [&](auto JC) __attribute__((always_inline)) {
      constexpr int j = JC.value;
      sfor<0, 28>([&](auto SC) LAM {
        constexpr int s = SC.value;
        acc[j & 1] = mfma32(ring[s % 7], hb[s], acc[j & 1]);
        if constexpr ((j == 2 || j == 4) && s == 12) cw = *(volatile LAS u32x4*)(L + L_FLG + (j == 2 ? S_WIN3 : S_REFILL) * 16);
        if constexpr ((j == 2 || j == 4) && s == 20) { if (__builtin_expect(!__builtin_amdgcn_readfirstlane(cw.x & cw.y & cw.z & cw.w), 0)) lds_wait(L, j == 2 ? S_WIN3 : S_REFILL); asm volatile("" ::: "memory"); }
        if constexpr (j == 1 && s < 7 - NCL) ST16(rs, (u32x4{EMPTY, EMPTY, EMPTY, EMPTY}), lane * 16, hho + (NCL + s) * 8192);
        if constexpr (s + 7 < 28) ring[s % 7] = WFRAG(gu_slot(j), s + 7);
        else if constexpr (j < 6) ring[s % 7] = WFRAG(gu_slot(j + 1), s - 21);
        if constexpr (s == 0 && (j == 1 || j == 2)) lds_signal(L, j == 1 ? S_WSEEN : S_FREE01, wave);
        if constexpr (s == 0 && j >= 3) *(volatile LAS u32*)(L + L_FLG + S_DONE4 * 16 + wave * 4) = j - 2u;
        if constexpr (j * 28 + s == BGO) lds_signal(L, S_KVP, wave);
        if constexpr (j > 0) {
          sfor<0, 8>([&](auto EC) LAM {
            constexpr int i = EC.value, k = s - 1 - 2 * i;
            const float ga = acc[(j - 1) & 1][i];
            if constexpr (k == 0) y[i] = ga * ga;
            if constexpr (k == 1) y[i] = fmaf(y[i], k1, k0);
            if constexpr (k == 2) y[i] = ga * y[i];
            if constexpr (k == 3) y[i] = fexp2(y[i]);
            if constexpr (k == 4) y[i] = fmaf(y[i], ve, ve);
            if constexpr (k == 5) { gu[i] = ga * acc[(j - 1) & 1][i + 8]; PIN(gu[i]); }
            if constexpr (k == 6) y[i] = frcp(y[i]);
            if constexpr (k == 7) y[i] = y[i] * gu[i];
            if constexpr (k >= 0 && k < 8 && k != 5) PIN(y[i]);
          });
          if constexpr (s == 21) { yb[0] = pk(y[0], y[1]); yb[1] = pk(y[2], y[3]); }
          if constexpr (s == 22) yb[2] = pk(y[4], y[5]);
          if constexpr (j == 3 && s == 22 && NCL < 7) WAITVM(4);
          if constexpr (s == 23) { yb[3] = filled(pk(y[6], y[7])); ST16(rs, yb, lane * 16, hho + (j - 1) * 8192); }
          if constexpr (s == 24 && j <= (HELP > 4 ? HELP : 4)) FLAG(pvo, j);
        }
        if constexpr (j < 6 && s >= 17 && s < 21) cq[s - 17] = *(LAS f32x4*)(csl + (j + 1) * 128 + (s - 17) * 32);
        if constexpr (j < 6 && s >= 22 && s < 27) {
          sfor<0, 16>([&](auto EC) LAM {
            constexpr int e = EC.value;
            if constexpr ((e < 2 ? 22 : e < 4 ? 23 : 23 + (e >> 2)) == s) { float v = nmu * cq[e >> 2][e & 3]; PIN(v); acc[(j + 1) & 1][e] = v; }
          });
        }
        SCHED();
      });
      LT(j);
      if (j == 0) STAMP(18);
      if (j == 3) STAMP(26);
      if (j == 1) STAMP(28);
      if (j == 2) STAMP(29);
      if (j == 4) STAMP(30);
      if (j == 5) STAMP(31);
    };
    gu_block(IC<0>{}); gu_block(IC<1>{}); gu_block(IC<2>{}); gu_block(IC<3>{}); gu_block(IC<4>{}); gu_block(IC<5>{}); gu_block(IC<6>{});
#pragma unroll
    for (int i = 0; i < 8; ++i) { const float ga = acc[0][i]; y[i] = ga * acc[0][i + 8] * frcp(fmaf(fexp2(ga * fmaf(ga * ga, k1, k0)), ve, ve)); }
    yb = u32x4{pk(y[0], y[1]), pk(y[2], y[3]), pk(y[4], y[5]), filled(pk(y[6], y[7]))};
    ST16(rs, yb, lane * 16, hho + 6 * 8192);
    if (HELP == 7) FLAG(pvo, 7);
    if (BGO == 196) lds_signal(L, S_KVP, wave);
    __builtin_amdgcn_s_setprio(0);
    STAMP(19);
    if (r2 < 14) {
      f32x16 da[2][2] = {};
      u32x4 hq[2][8], wl[2][8];
      HBASE();
      __builtin_amdgcn_s_sleep(ADLY);
      lds_wait1(L, CE == 2 ? S_LAND : CE == 1 ? S_REPSEEN : S_CONS, w2);
      STAMP2(15);
      HQLOAD(0, 5);
      if (A6L == 0) HQLOAD(1, 6);
      if (CE == 2) lds_wait1(L, S_REPSEEN, w2);
#pragma unroll
      for (int i = 0; i < 8; ++i) wl[0][i] = HFRAG(2, i);
      SCHED();
      STAMP2(16);
      HQCHECK(0, 5);
      if (A6L == 1) HQLOAD(1, 6);
      if (CE) lds_wait1(L, S_CONS, w2);
#pragma unroll
      for (int i = 0; i < 8; ++i) wl[1][i] = HFRAG(0, i);
      SCHED();
      DOWN16(da, wl[0], hq[0]);
      SCHED();
      STAMP2(18);
      HQCHECK(1, 6);
      STAMP2(31);
      DOWN16(da, wl[1], hq[1]);
      PARTIAL_OUT(da, 0, 2);
      STAMP(20);
    }
  } else {
    lds_wait2(L, S_HFA, S_HFB);
    GUBLK(3);
    if (W3THR) WAITVM(3);
    GUBLK(4);
#define REFILL() do { lds_wait<BSL>(L, S_WSEEN); GUBLK(5); lds_wait<BSL>(L, S_FREE01); GUBLK(6); } while (0)
    if (r2 >= 14) {
      WAITVM(0); lds_signal(L, S_WIN3, w2);
      REFILL();
      WAITVM(0); lds_signal(L, S_REFILL, w2);
    } else {
      if (!fd_ok) poll_flags(ws, WS_FD + (cg * 32 + 7 * w2 + min(lane & 7, 6)) * 128, lane < 7, tag);
      __builtin_amdgcn_fence(__ATOMIC_ACQUIRE, "wavefront");
#pragma unroll
      for (int n = 0; n < 24; ++n) {
        if (n == 8 * NW3) { STAMP2(25); lds_wait(L, S_HSEEN); __builtin_amdgcn_s_sleep(DLY4); }
        asm volatile("s_waitcnt vmcnt(%4)\n\tglobal_load_dwordx4 %0, %1, %2 offset:%3 sc1" : "=v"(wdf[n >> 3][n & 7]) : "v"(lane * 16), "s"(ws + wdo + 4096 + (n >> 3) * 8192), "n"((n & 7) * 1024 - 4096), "n"(THRN0) : "memory");
        if (n == 7) { WAITVM(8); lds_signal(L, S_WIN3, w2); }
      }
      STAMP2(23);
      REFILL();
      f32x16 acc[2][2] = {};
      u32x4 hq[2][8];
      STAMP2(16);
      HBASE();
      u64 pv;
      const char* hs = ws + ho + 4096;
#define PEEK() do { pv = tag + 7; asm volatile("s_mov_b64 exec, 0xff\n\tglobal_atomic_add_x2 %0, %1, %2, %3 sc0\n\ts_mov_b64 exec, -1" : "+v"(pv) : "v"(po), "v"(0ull), "s"(ws) : "memory"); } while (0)
#define PEEKED(k_, n_) do { asm volatile("s_waitcnt vmcnt(%1)" : "+v"(pv) : "n"(k_) : "memory"); PMIN(pv); NEEDP(n_); } while (0)
#define HQA(c_, q, j) _Pragma("unroll") for (int i = 0; i < 8; ++i) asm volatile("global_load_dwordx4 %0, %1, %2 offset:%3 sc1" : c_(hq[q][i]) : "v"(lane * 16), "s"(hs + (j) * 8192), "n"(i * 1024 - 4096) : "memory")
#define HQW(q, k_) asm volatile("s_waitcnt vmcnt(%8)" : "+v"(hq[q][0]), "+v"(hq[q][1]), "+v"(hq[q][2]), "+v"(hq[q][3]), "+v"(hq[q][4]), "+v"(hq[q][5]), "+v"(hq[q][6]), "+v"(hq[q][7]) : "n"(k_) : "memory")
#define HQOK(q, j, k_) do { HQW(q, k_); if (__builtin_expect(HQBAD(q), 0)) { _Pragma("nounroll") for (u32 sp = 0; sp < SPIN_CAP; ++sp) { \
        __builtin_amdgcn_s_sleep(RSL); HQA("+v", q, j); HQW(q, 0); if (!HQBAD(q)) break; } } SCHED(); } while (0)
      PEEK();
      FREE(1); WDLOAD(3, 0);
      WAITVM(8);
#pragma unroll
      for (int k = 0; k < 3; ++k) asm volatile("" : "+v"(wdf[k][0]), "+v"(wdf[k][1]), "+v"(wdf[k][2]), "+v"(wdf[k][3]), "+v"(wdf[k][4]), "+v"(wdf[k][5]), "+v"(wdf[k][6]), "+v"(wdf[k][7]) :: "memory");
      lds_signal(L, S_REFILL, w2);
      PEEKED(8, 1);
      STAMP2(1);
      PEEK();
      HQA("=v", 0, 0);
      FREE(2); WDLOAD(4, 1);
      PEEKED(16, 2);
      STAMP2(2);
      HQA("=v", 1, 1);
      PEEK();
      u32 fr3;
      { const u32x4 fr = *(volatile LAS u32x4*)(L + L_FLG + S_DONE4 * 16); fr3 = !W5D || __builtin_amdgcn_readfirstlane(min(min(fr.x, fr.y), min(fr.z, fr.w))) >= 3; }
      if (fr3) { if (!W5D) FREE(3); WDLOAD(5, 2); }
      if (fr3) WAITVM(25); else WAITVM(17);
      HQOK(0, 0, 25);
      __builtin_amdgcn_s_setprio(DP0);
      DOWN16(acc, wdf[0], hq[0]);
      __builtin_amdgcn_s_setprio(0);
#pragma unroll
      for (int i = 0; i < 8; ++i) wdf[0][i] = HFRAG(0, i);
      SCHED();
      STAMP2(8);
      if (!fr3) { FREE(3); WDLOAD(5, 2); }
      PEEKED(8, 3);
      lds_signal(L, S_LAND, w2);
      STAMP2(3);
      HQA("=v", 0, 2);
      PEEK();
      asm volatile("s_waitcnt lgkmcnt(0)" : "+v"(wdf[0][7]) :: "memory");
      WDLOAD(6, 0);
      HQOK(1, 1, 25);
      __builtin_amdgcn_s_setprio(DP0);
      DOWN16(acc, wdf[1], hq[1]);
#pragma unroll
      for (int i = 0; i < 8; ++i) wdf[1][i] = HFRAG(1, i);
      SCHED();
      STAMP2(9);
      PEEKED(8, 4);
      lds_signal(L, S_REPSEEN, w2);
      STAMP2(4);
      HQA("=v", 1, 3);
      __builtin_amdgcn_s_setprio(DNPRIO);
      HQOK(0, 2, 8);
      lds_signal(L, S_CONS, w2);
      SCHED();
      DOWN16(acc, wdf[2], hq[0]);
      SCHED();
      STAMP2(10);
      FREE(4);
      HQA("=v", 0, 4);
      HQOK(1, 3, 8);
      DOWN16(acc, wdf[0], hq[1]);
      SCHED();
      STAMP2(11);
      HQOK(0, 4, 0);
      DOWN16(acc, wdf[1], hq[0]);
      PARTIAL_OUT(acc, 1, 3);
      STAMP(20);
    }
  }
  if (r2 < 14) {
    BAR();
    STAMP2(7);
    float c[8] = {blo(hres.x), bhi(hres.x), blo(hres.y), bhi(hres.y), blo(hres.z), bhi(hres.z), blo(hres.w), bhi(hres.w)};
    const int rl = (kh * 32 + (tb & 1) * 16 + l15) * 16;
    f32x4 v0 = z4, v1 = z4;
#pragma unroll
    for (int h = 0; h < 2; ++h) {
      const int bf = (tb >> 1) * 2 + h;
      u32 rq = (bf == 3 ? 0 : (bf + 2) * SLOT) + g * 1024 + rl;
      const u32 rq7 = g == 3 ? L_EX + bf * 4096 + rl : rq + 4096, st7 = g == 3 ? 1024 : 7168;
      asm volatile("" : "+v"(rq));
#pragma unroll
      for (int w = 0; w < 4; ++w) { v0 += *(LAS f32x4*)(L + rq + w * 7168); v1 += *(LAS f32x4*)(L + rq7 + w * st7); }
    }
#pragma unroll
    for (int h = 0; h < 2; ++h) {
      const f32x4 v = h ? v1 : v0;
      const f32x2 p = swp32(v[0], v[2]), q = swp32(v[1], v[3]), t0 = swp16(p.x, q.x), t1 = swp16(p.y, q.y);
      c[4 * h] += t0.x; c[4 * h + 1] += t0.y; c[4 * h + 2] += t1.x; c[4 * h + 3] += t1.y;
    }
    { const u32x4 ov = {pk(c[0], c[1]), pk(c[2], c[3]), pk(c[4], c[5]), pk(c[6], c[7])}; asm volatile("global_store_dwordx4 %0, %1, %2 sc1\n\ts_nop 1" :: "v"(xo), "v"(ov), "s"(out) : "memory"); }
    if (A && b == 0 && tid == 0) __hip_atomic_store((GAS u64*)(ws + WS_E), tag, RLX_AGENT);
  }
  STAMP(21);
  };
  if (wave < 4) role(IC<1>{}); else role(IC<0>{});
}

extern "C" void kernel_launch(void* const* d_in, const int* in_sizes, int n_in, void* d_out, int out_size, void* d_ws, size_t ws_size, hipStream_t stream) {
  (void)hipFuncSetAttribute((const void*)fused_block, hipFuncAttributeMaxDynamicSharedMemorySize, LDS_BYTES);
  fused_block<<<256, 512, LDS_BYTES, stream>>>((const char*)d_in[0], (const char*)d_in[1], (const char*)d_in[2], (const char*)d_in[3], (const char*)d_in[5], (const char*)d_in[6],
                                               (const char*)d_in[7], (char*)d_out, (char*)d_ws);
}
```

```cpp
#include <hip/hip_runtime.h>
#include <hip/hip_bf16.h>
#include <stdint.h>

typedef unsigned u32;
typedef unsigned long long u64;
typedef u32 u32x4 __attribute__((ext_vector_type(4)));
typedef u32 u32x2 __attribute__((ext_vector_type(2)));
typedef float f32x2 __attribute__((ext_vector_type(2)));
typedef float f32x4 __attribute__((ext_vector_type(4)));
typedef float f32x16 __attribute__((ext_vector_type(16)));
typedef short s16x4 __attribute__((ext_vector_type(4)));
typedef __bf16 bf16x2 __attribute__((ext_vector_type(2)));
typedef __bf16 bf16x8 __attribute__((ext_vector_type(8)));
#define LAS __attribute__((address_space(3)))
#define GAS __attribute__((address_space(1)))
#define DI __device__ __forceinline__
#define RLX_AGENT __ATOMIC_RELAXED, __HIP_MEMORY_SCOPE_AGENT

constexpr int WS_E = 0;
constexpr int WS_FKV = 8192;
constexpr int WS_FO = WS_FKV + 131072;
constexpr int WS_FH = WS_FO + 131072;
constexpr int WS_FG = WS_FH + 262144;
constexpr int WS_FD = WS_FG + 114688;
constexpr int WS_FHH = WS_FD + 57344;
constexpr int WS_CS = WS_FHH + 262144;
constexpr int WS_ST = WS_CS + 200704;
constexpr int WS_WGU = WS_ST + 229376;
constexpr int WS_WD = WS_WGU + 3211264;
constexpr int WS_KVP = WS_WD + 1605632;
constexpr int WS_O = WS_KVP + 1048576;
constexpr int WS_H = WS_O + 2097152;
constexpr int WS_HH = WS_H + 1835008;
#define UNITX4
#define WDOFF(cg, fb, bl) (((((cg) * 4 + (fb) / 28) * 7 + (fb) % 7) * 4 + (fb) / 7 % 4) * 2 + (bl))
#define HHOFF(T32, fb) (((((T32) >> 1) * 4 + (fb) / 28) * 7 + (fb) % 7) * 8 + ((fb) / 7 & 3) * 2 + ((T32) & 1))
constexpr int WS_STAMP = WS_HH + 7340032;
constexpr int WS_LOG = WS_STAMP + 256 * 8 * 32 * 8;
constexpr int WS_USED = WS_LOG + 4096;
constexpr int SLOT = 28672;
constexpr int L_WK = 4 * SLOT, L_WV = 0, L_WQ = 14336;
constexpr int L_PKV = 2 * SLOT;
constexpr int L_WO = 3 * SLOT;
constexpr int L_EX = 5 * SLOT;
constexpr int L_KVA = 5 * SLOT;
constexpr int L_ST1 = 5 * SLOT + 4096;
constexpr int L_WPHI = 5 * SLOT + 5120;
constexpr int L_CS = 159744;
constexpr int L_FLG = 160768;
constexpr int L_JUNK = 161280;
constexpr int L_STAMP = 161536;
constexpr int L_RED = SLOT;
constexpr int LDS_BYTES = 163840;
constexpr int gu_slot(int j) { return j % 5; }
constexpr int CODE_PF = 45056;
constexpr u32 SPIN_CAP = 1u << 16;
#define LL 1
#define AP 1

#define STAMP(i) do {} while (0)
#define STAMP2(i) do {} while (0)
#define STAMP2X(i) do {} while (0)

DI f32x4 mfma16(u32x4 a, u32x4 b, f32x4 c) { return __builtin_amdgcn_mfma_f32_16x16x32_bf16(__builtin_bit_cast(bf16x8, a), __builtin_bit_cast(bf16x8, b), c, 0, 0, 0); }
DI f32x16 mfma32(u32x4 a, u32x4 b, f32x16 c) { return __builtin_amdgcn_mfma_f32_32x32x16_bf16(__builtin_bit_cast(bf16x8, a), __builtin_bit_cast(bf16x8, b), c, 0, 0, 0); }
DI u32 pk(float lo, float hi) { f32x2 p = {lo, hi}; return __builtin_bit_cast(u32, __builtin_convertvector(p, bf16x2)); }
DI float blo(u32 v) { return __builtin_bit_cast(float, v << 16); }
DI float bhi(u32 v) { return __builtin_bit_cast(float, v & 0xffff0000u); }
DI float dot2(u32 a, u32 b, float c) { return __builtin_amdgcn_fdot2_f32_bf16(__builtin_bit_cast(bf16x2, a), __builtin_bit_cast(bf16x2, b), c, false); }
DI u32x2 trread(LAS char* p) { return __builtin_bit_cast(u32x2, __builtin_amdgcn_ds_read_tr16_b64_v4i16((LAS s16x4*)p)); }
DI u32x4 trfrag(LAS char* p, int step) { u32x2 a = trread(p), b = trread(p + step); return u32x4{a.x, a.y, b.x, b.y}; }
DI float fexp2(float x) { return __builtin_amdgcn_exp2f(x); }
DI float frcp(float x) { return __builtin_amdgcn_rcpf(x); }
template <int C> DI float dppror(float x) { return __builtin_bit_cast(float, __builtin_amdgcn_update_dpp(0, __builtin_bit_cast(int, x), 0x120 + C, 0xf, 0xf, false)); }
DI float row_max(float x) { x = fmaxf(x, dppror<8>(x)); x = fmaxf(x, dppror<4>(x)); x = fmaxf(x, dppror<2>(x)); return fmaxf(x, dppror<1>(x)); }
DI float row_min(float x) { x = fminf(x, dppror<8>(x)); x = fminf(x, dppror<4>(x)); x = fminf(x, dppror<2>(x)); return fminf(x, dppror<1>(x)); }
DI float row_sum(float x) { x += dppror<8>(x); x += dppror<4>(x); x += dppror<2>(x); return x + dppror<1>(x); }
DI f32x2 sw16(float x) { u32 u = __builtin_bit_cast(u32, x); auto r = __builtin_amdgcn_permlane16_swap(u, u, false, false); return f32x2{__builtin_bit_cast(float, (u32)r[0]), __builtin_bit_cast(float, (u32)r[1])}; }
DI f32x2 sw32(float x) { u32 u = __builtin_bit_cast(u32, x); auto r = __builtin_amdgcn_permlane32_swap(u, u, false, false); return f32x2{__builtin_bit_cast(float, (u32)r[0]), __builtin_bit_cast(float, (u32)r[1])}; }
DI float grp_sum(float x) { f32x2 a = sw16(x); x = a.x + a.y; a = sw32(x); return a.x + a.y; }
DI float grp_max(float x) { f32x2 a = sw16(x); x = fmaxf(a.x, a.y); a = sw32(x); return fmaxf(a.x, a.y); }
DI float grp_min(float x) { f32x2 a = sw16(x); x = fminf(a.x, a.y); a = sw32(x); return fminf(a.x, a.y); }

#define BAR() asm volatile("s_waitcnt lgkmcnt(0)\n\ts_barrier" ::: "memory")
#define WAITVM(n) asm volatile("s_waitcnt vmcnt(" #n ")" ::: "memory")
#define DRAIN() do { asm volatile("s_waitcnt vmcnt(0)" ::: "memory"); __builtin_amdgcn_s_waitcnt(0x0F70); } while (0)
#define SCHED() __builtin_amdgcn_sched_barrier(0)
DI void dma16(const void* base, u32 voff, u32 lds) {
  u32 keep;
  asm volatile("s_mov_b32 %0, m0\n\ts_mov_b32 m0, %3\n\ts_nop 0\n\tglobal_load_lds_dwordx4 %1, %2\n\ts_mov_b32 m0, %0" : "=&s"(keep) : "v"(voff), "s"(base), "s"(lds) : "memory");
}
DI void dma16c(const void* base, u32 voff, u32 lds) {
  u32 keep;
  asm volatile("s_mov_b32 %0, m0\n\ts_mov_b32 m0, %3\n\ts_nop 0\n\tglobal_load_lds_dwordx4 %1, %2 sc1\n\ts_mov_b32 m0, %0" : "=&s"(keep) : "v"(voff), "s"(base), "s"(lds) : "memory");
}
DI void dma4(const void* base, u32 voff, u32 lds) {
  u32 keep;
  asm volatile("s_mov_b32 %0, m0\n\ts_mov_b32 m0, %3\n\ts_nop 0\n\tglobal_load_lds_dword %1, %2\n\ts_mov_b32 m0, %0" : "=&s"(keep) : "v"(voff), "s"(base), "s"(lds) : "memory");
}
DI void dma16c_if(u32 go, const void* base, u32 voff, u32 lds) {
  u32 keep;
  asm volatile("s_cmp_eq_u32 %4, 0\n\ts_cbranch_scc1 1f\n\ts_mov_b32 %0, m0\n\ts_mov_b32 m0, %3\n\ts_nop 0\n\tglobal_load_lds_dwordx4 %1, %2 sc1\n\ts_mov_b32 m0, %0\n1:"
               : "=&s"(keep) : "v"(voff), "s"(base), "s"(lds), "s"(go) : "memory", "scc");
}
template <int SI, int LI> DI void dma7(__amdgpu_buffer_rsrc_t rs, u32 voff, u32 so, u32 lds) {
  u32 t;
  asm volatile("s_add_u32 m0, %4, %6\n\ts_add_u32 %0, %3, %5\n\tbuffer_load_dwordx4 %1, %2, %0 offen sc1 lds\n\tbuffer_load_dwordx4 %1, %2, %0 offen offset:1024 sc1 lds\n\t"
               "buffer_load_dwordx4 %1, %2, %0 offen offset:2048 sc1 lds\n\tbuffer_load_dwordx4 %1, %2, %0 offen offset:3072 sc1 lds\n\t"
               "s_add_u32 m0, m0, 4096\n\ts_add_u32 %0, %0, 4096\n\tbuffer_load_dwordx4 %1, %2, %0 offen sc1 lds\n\tbuffer_load_dwordx4 %1, %2, %0 offen offset:1024 sc1 lds\n\t"
               "buffer_load_dwordx4 %1, %2, %0 offen offset:2048 sc1 lds"
               : "=&s"(t) : "v"(voff), "s"(rs), "s"(so), "s"(lds), "n"(SI), "n"(LI) : "memory", "scc");
}
DI void dma8(__amdgpu_buffer_rsrc_t rs, u32 voff, u32 so, u32 lds, u32 lds7) {
  dma7<0, 0>(rs, voff, so, lds);
  u32 t;
  asm volatile("s_mov_b32 m0, %4\n\ts_add_u32 %0, %3, 7168\n\tbuffer_load_dwordx4 %1, %2, %0 offen sc1 lds" : "=&s"(t) : "v"(voff), "s"(rs), "s"(so), "s"(lds7) : "memory", "scc");
}
DI u32 vm_count() {
  u32 t;
  asm volatile("s_getreg_b32 %0, hwreg(HW_REG_IB_STS)" : "=s"(t) :: "memory");
  return (t & 15u) | ((t >> 18) & 48u);
}
DI u32 min8(u32 c) {
  c = min(c, (u32)__builtin_amdgcn_update_dpp(0, (int)c, 0x121, 0xf, 0xf, false)); c = min(c, (u32)__builtin_amdgcn_update_dpp(0, (int)c, 0x122, 0xf, 0xf, false));
  c = min(c, (u32)__builtin_amdgcn_update_dpp(0, (int)c, 0x124, 0xf, 0xf, false));
  return __builtin_amdgcn_readfirstlane(min(c, (u32)__builtin_amdgcn_update_dpp(0, (int)c, 0x128, 0xf, 0xf, false)));
}
DI void dma4c(const void* base, u32 voff, u32 lds) {
  u32 keep;
  asm volatile("s_mov_b32 %0, m0\n\ts_mov_b32 m0, %3\n\ts_nop 0\n\tglobal_load_lds_dword %1, %2 sc1\n\ts_mov_b32 m0, %0" : "=&s"(keep) : "v"(voff), "s"(base), "s"(lds) : "memory");
}
#define LD16(rs, off) __builtin_bit_cast(u32x4, __builtin_amdgcn_raw_buffer_load_b128(rs, off, 0, 16))
#define ST16(rs, v, off, soff) do { const u32x4 d_ = __builtin_bit_cast(u32x4, v); __builtin_amdgcn_raw_buffer_store_b128(d_, rs, off, soff, 16); asm volatile("s_nop 1" :: "v"(d_) : "memory"); } while (0)

#define FLAG(off, n) __builtin_amdgcn_raw_buffer_store_b64(__builtin_bit_cast(u32x2, tag + (n)), rs, fvo, off, 16)
#define FLAGC(c, off, n) __builtin_amdgcn_raw_buffer_store_b64(__builtin_bit_cast(u32x2, tag + (n)), rs, (c) ? fvo : NOWHERE, off, 16)
constexpr int NOWHERE = 0x7ffffff0;
constexpr u32 EMPTY = 0xffffffffu;
DI u32 filled(u32 w) { return min(w, EMPTY - 1u); }
DI u64 peek(const char* ws, u32 off, bool on, u64 dflt) {
  u64 z = 0, v = dflt;
  asm volatile("" : "+v"(z));
  if (on) v = __hip_atomic_fetch_add((GAS u64*)(ws + off), z, __ATOMIC_RELAXED, AP == 2 ? __HIP_MEMORY_SCOPE_SYSTEM : __HIP_MEMORY_SCOPE_AGENT);
  return v;
}
DI void poll_flags(const char* ws, u32 off, bool on, u64 tag) {
#pragma nounroll
  for (u32 sp = 0; sp < SPIN_CAP; ++sp) {
    u64 v = tag + 1;
    if (on || !LL) v = __hip_atomic_load((const GAS u64*)(ws + off), RLX_AGENT);
    if (__all(v == tag + 1)) break;
  }
  __builtin_amdgcn_fence(__ATOMIC_ACQUIRE, "wavefront");
}
template <int SL = 1> DI void lds_wait(LAS char* L, int sig) {
#pragma nounroll
  for (u32 sp = 0; sp < (SPIN_CAP << 4); ++sp) {
    const u32x4 v = *(volatile LAS u32x4*)(L + L_FLG + sig * 16);
    if (__builtin_amdgcn_readfirstlane(v.x & v.y & v.z & v.w)) break;
    __builtin_amdgcn_s_sleep(SL);
  }
  asm volatile("" ::: "memory");
}
DI void lds_wait2(LAS char* L, int sig, int sig2) {
#pragma nounroll
  for (u32 sp = 0; sp < (SPIN_CAP << 4); ++sp) {
    const u32x4 v = *(volatile LAS u32x4*)(L + L_FLG + sig * 16) & *(volatile LAS u32x4*)(L + L_FLG + sig2 * 16);
    if (__builtin_amdgcn_readfirstlane(v.x & v.y & v.z & v.w)) break;
    __builtin_amdgcn_s_sleep(1);
  }
  asm volatile("" ::: "memory");
}
DI void lds_wait1(LAS char* L, int sig, int w) {
#pragma nounroll
  for (u32 sp = 0; sp < (SPIN_CAP << 4); ++sp) {
    if (__builtin_amdgcn_readfirstlane(*(volatile LAS u32*)(L + L_FLG + sig * 16 + w * 4))) break;
    __builtin_amdgcn_s_sleep(1);
  }
  asm volatile("" ::: "memory");
}
DI void lds_signal(LAS char* L, int sig, int w) {
  asm volatile("" ::: "memory");
  *(volatile LAS u32*)(L + L_FLG + sig * 16 + w * 4) = 1u;
}

template <int N> struct IC { static constexpr int value = N; };
template <int I, int N, class F> DI void sfor(F&& f) { if constexpr (I < N) { f(IC<I>{}); sfor<I + 1, N>(f); } }
#define LAM __attribute__((always_inline))
#define PIN(v) asm volatile("" : "+v"(v))
constexpr float LOG2E = 1.4426950408889634f;
template <int GAP, class F> DI void project(LAS char* L, u32 w0, u32 w1, const u32x4 (&xf)[2][14], int tp, int lane, f32x4 (&t)[2][2], F&& mid) {
  asm volatile("" : "+v"(w0), "+v"(w1));
  u32x4 ring[6];
  f32x4 cs[2] = {};
  f32x2 st[2];
  const u32x4 ones = {0x3f803f80u, 0x3f803f80u, 0x3f803f80u, 0x3f803f80u};
#pragma unroll
  for (int n = 0; n < 6; ++n) ring[n] = trfrag(L + (n & 1 ? w1 : w0) + (n >> 1) * 2048, 256);
#pragma unroll
  for (int i = 0; i < 4; ++i) t[i >> 1][i & 1] = f32x4{0.f, 0.f, 0.f, 0.f};
  SCHED();
#pragma unroll
  for (int n = 0; n < 28; ++n) {
    const u32x4 f = ring[n % 6];
    t[0][n & 1] = mfma16(f, xf[0][n >> 1], t[0][n & 1]);
    t[1][n & 1] = mfma16(f, xf[1][n >> 1], t[1][n & 1]);
    cs[n & 1] = mfma16(f, ones, cs[n & 1]);
    if (n + 6 < 28) ring[n % 6] = trfrag(L + (n & 1 ? w1 : w0) + ((n + 6) >> 1) * 2048 + (n + 6 >= 14 ? GAP : 0), 256);
    if (n == 22 || n == 23) st[n & 1] = *(LAS f32x2*)(L + L_ST1 + (tp * 32 + (n & 1) * 16 + (lane & 15)) * 8);
    mid(n);
    SCHED();
  }
#pragma unroll
  for (int tb = 0; tb < 2; ++tb)
#pragma unroll
    for (int k = 0; k < 2; ++k) t[tb][k] = (t[tb][k] + cs[k] * st[tb].x) * st[tb].y;
}
DI f32x2 swp16(float a, float b) { auto r = __builtin_amdgcn_permlane16_swap(__builtin_bit_cast(u32, a), __builtin_bit_cast(u32, b), false, false); return f32x2{__builtin_bit_cast(float, (u32)r[0]), __builtin_bit_cast(float, (u32)r[1])}; }
DI f32x2 swp32(float a, float b) { auto r = __builtin_amdgcn_permlane32_swap(__builtin_bit_cast(u32, a), __builtin_bit_cast(u32, b), false, false); return f32x2{__builtin_bit_cast(float, (u32)r[0]), __builtin_bit_cast(float, (u32)r[1])}; }
DI float swadd32(float a, float b) { const f32x2 s = swp32(a, b); return s.x + s.y; }
DI float swadd16(float a, float b) { const f32x2 s = swp16(a, b); return s.x + s.y; }
DI float mx2(float a, float b) { float r; asm("v_max_f32 %0, %1, %2" : "=v"(r) : "v"(a), "v"(b)); return r; }
DI float mx3(float a, float b, float c) { return fmaxf(fmaxf(a, b), c); }
DI float mn3(float a, float b, float c) { return fminf(fminf(a, b), c); }
DI void feature_map(const f32x4 (&t)[2][2], const u32x4 (&wpf)[2], int g, u32x4 (&P)[2][2]) {
  const f32x4 z4 = {0.f, 0.f, 0.f, 0.f};
  const bool v23 = g < 3;
  float p[2][8], m[4];
#pragma unroll
  for (int tb = 0; tb < 2; ++tb) {
    const u32x4 tq = {pk(t[tb][0][0], t[tb][0][1]), pk(t[tb][0][2], t[tb][0][3]), pk(t[tb][1][0], t[tb][1][1]), pk(t[tb][1][2], t[tb][1][3])};
    const f32x4 p0 = mfma16(wpf[0], tq, z4), p1 = mfma16(wpf[1], tq, z4);
#pragma unroll
    for (int i = 0; i < 8; ++i) { const float v = i < 4 ? p0[i] : p1[i - 4]; p[tb][i] = ((i & 3) < 2 || v23) ? v : p0[0]; }
    m[2 * tb] = mx3(mx3(p[tb][0], p[tb][1], p[tb][2]), mx3(p[tb][3], p[tb][4], p[tb][5]), mx3(p[tb][6], p[tb][7], p[tb][0]));
    m[2 * tb + 1] = -mn3(mn3(p[tb][0], p[tb][1], p[tb][2]), mn3(p[tb][3], p[tb][4], p[tb][5]), mn3(p[tb][6], p[tb][7], p[tb][0]));
  }
  {
    const f32x2 a = swp16(m[0], m[1]), b = swp16(m[2], m[3]);
    const f32x2 c = swp32(mx2(a.x, a.y), mx2(b.x, b.y));
    const float d = mx2(c.x, c.y) * -LOG2E;
    const f32x2 e = swp32(d, d), f0 = swp16(e.x, e.x), f1 = swp16(e.y, e.y);
    m[0] = f0.x; m[1] = f0.y; m[2] = f1.x; m[3] = f1.y;
  }
  float ex[4][8], sm[4];
#pragma unroll
  for (int c = 0; c < 4; ++c) {
#pragma unroll
    for (int i = 0; i < 8; ++i) {
      ex[c][i] = fexp2(fmaf(p[c >> 1][i], (c & 1) ? -LOG2E : LOG2E, m[c]));
      if ((i & 3) >= 2) ex[c][i] = v23 ? ex[c][i] : 0.f;
    }
    sm[c] = ((ex[c][0] + ex[c][1]) + (ex[c][2] + ex[c][3])) + ((ex[c][4] + ex[c][5]) + (ex[c][6] + ex[c][7]));
  }
  {
    const float d = frcp(swadd32(swadd16(sm[0], sm[1]), swadd16(sm[2], sm[3])));
    const f32x2 e = swp32(d, d), f0 = swp16(e.x, e.x), f1 = swp16(e.y, e.y);
    sm[0] = f0.x; sm[1] = f0.y; sm[2] = f1.x; sm[3] = f1.y;
  }
#pragma unroll
  for (int c = 0; c < 4; ++c)
#pragma unroll
    for (int e = 0; e < 4; ++e) P[c >> 1][c & 1][e] = pk(ex[c][2 * e] * sm[c], ex[c][2 * e + 1] * sm[c]);
}
DI u32x4 piece(const u32x4 (&rp)[8], int e) {
  u32x4 f;
#pragma unroll
  for (int m = 0; m < 4; ++m) f[m] = __builtin_amdgcn_perm(rp[2 * m + 1][e >> 1], rp[2 * m][e >> 1], (e & 1) ? 0x07060302u : 0x05040100u);
  return f;
}
#define RP_OPS "+v"(rp[0]), "+v"(rp[1]), "+v"(rp[2]), "+v"(rp[3]), "+v"(rp[4]), "+v"(rp[5]), "+v"(rp[6]), "+v"(rp[7])
#define PF1 0
#define PF2 84
#define PF3 120
#define TP0 -1
#define TP1 13
#define TP2 -1
#define TOUCH 5
#define NW3 4
#define THRN0 4
#define DNPRIO 3
#define NWD 3
#define MIDCHK 0
#define HELP 0
#define NHW 7
#define W3LATE 0
#define W3WAIT 1
#define ROT 1
#define EXPT 0
#define PFM 12
#define PSLP 4
#define PLAG 0
#define WTOUCH 0
#define REPLATE 0
#define WB0 8
#define WB1 4
#define ULATE 0
#define VPRIO 3
#define KGO 20
#define QEARLY 1
#define DLY1 4
#define W1 1
#define W2C 0
#define WOE 2
#define W3THR 1
#define HW3 1
#define HW1 1
#define SN 4
#define BATCH 1
#define B23 1
#define WD5L 0
#define A56 1
#define BGO 196
#define BALL 0
#define BSL 4
#define RSL 4
#define H5 1
#define H6 1
#define BORD 1
#define DP0 3
#define AIL 1
#define SEEN 3
#define ADLY 16
#define W6E 0
#define SPLIT 1
#define NCL 4
#define B5L 1
#define A6L 1
#define BO2 2
#define TW 0
#define CL 0
#define CE 0
#define W5D 1
#define W2F 2
#define D2A 12
#define D2B 24
#define SPEC 1
#define CLP 28
#define CLS 3
#define CE2 0
#define DLY5 60
#define CSL 0
#define DLY4 16
#define PUBS 5
#define THRN 40
#define DLY2 8
#define DLY3 16
#define GUPRIO 2
enum { S_FREE01 = 0, S_REFILL, S_DONE4, S_WIN3, S_WK, S_WV, S_WQ, S_WSEEN, S_KDONE, S_V, S_PHIK, S_KVP, S_KVA, S_WO, S_REPSEEN, S_HSEEN, S_X0, S_X1, S_XA, S_XB, S_CS, S_WOFA, S_WOFB, S_EXA, S_EXB, S_W2A, S_W2B, S_HFA, S_HFB, S_CONS, S_LAND, S_XF0 };

__global__ void __launch_bounds__(512, 2) fused_block(const char* __restrict__ x, const char* __restrict__ wqkv, const char* __restrict__ wphi, const char* __restrict__ wo,
                                                      const char* __restrict__ wg, const char* __restrict__ wu, const char* __restrict__ wd, char* __restrict__ out, char* ws) {
  extern __shared__ __attribute__((aligned(16))) char smem[];
  LAS char* L = (LAS char*)smem;
  const int tid = threadIdx.x, lane = tid & 63, wave = __builtin_amdgcn_readfirstlane(tid >> 6), w2 = wave & 3;
  const int b = blockIdx.x, li = b >> 3, xp = (b & 7) >> 1, xh = b & 1;
  const int tile = 4 * xp + (li >> 3), r = 8 * xh + (li & 7);
  const int tile2 = 4 * xp + 2 * xh + (li >> 4), r2 = li & 15;
  const int tg = r2 >= 7 ? 1 : 0, cg = r2 - 7 * tg;
  const int l15 = lane & 15, g = lane >> 4, q4 = l15 >> 2, p4 = lane & 3, hh = lane >> 5;
  const int head = r;
  asm volatile("" :: "s"(x), "s"(wqkv), "s"(wphi), "s"(wo), "s"(wg), "s"(wu), "s"(wd), "s"(out), "s"(ws));
  STAMP(0);
  u64 pc_; asm volatile("s_getpc_b64 %0" : "=s"(pc_));
  const char* pc0 = (const char*)(pc_ & ~127ull);
  const __amdgpu_buffer_rsrc_t rs = __builtin_amdgcn_make_buffer_rsrc((void*)ws, 0, WS_USED, 0x00020000);
  const u32x4 ones = {0x3f803f80u, 0x3f803f80u, 0x3f803f80u, 0x3f803f80u};
  const f32x4 z4 = {0.f, 0.f, 0.f, 0.f};
  auto role = [&](auto AC) __attribute__((always_inline)) {
  constexpr bool A = AC.value;
  if (A) asm volatile("; A" ::: "memory"); else asm volatile("; B" ::: "memory");
  __builtin_amdgcn_s_waitcnt(0x0F70);
  u64 epoch;
  u32x4 rp[8];
  const int fvo = lane < 16 ? lane * 8 : NOWHERE;
  const int uidx = (tile * 10 + __builtin_popcount(~0x2A54u & ((1u << head) - 1u))) * 4 + w2, unit = uidx * 147 / 160;
  const bool has_unit = !((0x2A54 >> head) & 1) && (uidx + 1) * 147 / 160 != unit;
  const int u3 = unit / 3, umat = unit - 3 * u3, ukq = u3 / 28, ufl = u3 - 28 * ukq;
  const bool ud = umat == 2;
  const char* gusrc = ws + WS_WGU + 7 * r * SLOT;
  const u32 guo = WS_WGU + 7 * r * SLOT + w2 * 7168;
#define GUBLK(j) dma7<(j) * SLOT, gu_slot(j) * SLOT>(rs, lane * 16, guo, w2 * 7168)
  const u32 wvo = (lane >> 2) * 2688 + (((lane & 3) ^ (2 * (lane >> 5))) * 16);
  const char* wbase = wqkv + head * 56 - (head & 1) * 8 + 16 * w2 * 2688;
  const u32 wr0 = (8 * g + q4) * 64 + (((p4 + (head & 1)) ^ (4 * (g & 1))) * 8), wr1 = (8 * g + q4) * 64 + ((((4 + p4 + (head & 1)) & 7) ^ (4 * (g & 1))) * 8);
  const int tb = w2; constexpr int kh = A ? 0 : 1;
  const int hho = WS_HH + (((tile * 2 + (w2 >> 1)) * 4 + (r >> 2)) * 56 + (r & 3) * 2 + (w2 & 1)) * 1024;
  const int tok = tile2 * 128 + tg * 64 + tb * 16 + l15;
  const int hvo = WS_H + ((tok >> 5) * 28 + 4 * cg + (g >> 1)) * 1024 + ((g & 1) * 32 + (tok & 31)) * 16;
  const int sto = WS_ST + (2 * cg * 2048 + tile2 * 128 + tg * 64 + tb * 16) * 8;
  const u32x4 empty4 = {EMPTY, EMPTY, EMPTY, EMPTY};
#define HH_CLEAR() _Pragma("unroll") for (int i = 0; i < 4 * NCL; ++i) ST16(rs, empty4, lane * 16 + (i & 1) * 2048, WS_HH + (((tile2 * 2 + r2 - 14) * 4 + w2) * 56 + kh + (i >> 1) * 4) * 1024)
#define HH_FLAG() FLAG(WS_FH + ((tile2 * 4 + 2 * (r2 - 14) + kh) * 32 + 28 + w2) * 128, 1)
  u32x4 wof[8][4];
  f32x4 csp[7];
  u64 tag;
#define WOF_READ(i0, i1) do { u32 wa_ = L_WO + (kh * 224 + 4 * g + q4) * 128 + 16 * p4 + 64 * (q4 >> 1), wb_ = wa_ ^ 64, wc_ = wa_ + (g == 3 ? 0 : 2048), wd_ = wc_ ^ 64; \
    asm volatile("" : "+v"(wa_), "+v"(wb_), "+v"(wc_), "+v"(wd_)); \
    _Pragma("unroll") for (int i = i0; i < i1; ++i) _Pragma("unroll") for (int k = 0; k < 4; ++k) { \
      const u32x2 a_ = trread(L + (k >> 1 ? wb_ : wa_) + 28 * i * 128 + (k & 1) * 8), c_ = trread(L + (k >> 1 ? wd_ : wc_) + 28 * i * 128 + (k & 1) * 8); \
      wof[i][k] = u32x4{a_.x, a_.y, c_.x, c_.y}; } } while (0)
#define XF_READ(i) _Pragma("unroll") for (int s = 0; s < 14; ++s) xf[i][s] = *(LAS u32x4*)((s & 1 ? xr1 : xr0) + (i) * 14336 + (s >> 1) * 2048)
#define LN_STATS() do { f32x4 g1 = z4, g2 = z4; \
    _Pragma("unroll") for (int s = 0; s < 14; ++s) { g1 = mfma16(ones, xf[kh][s], g1); g2 = mfma16(xf[kh][s], xf[kh][s], g2); } \
    const float d01 = (p4 & 1) ? g2[1] : g2[0], d23 = (p4 & 1) ? g2[3] : g2[2], s2 = (p4 & 2) ? d23 : d01; \
    const float nmu = g1[0] * (-1.f / 448.f), var = fmaxf(s2 * (1.f / 448.f) - nmu * nmu, 0.f); \
    if (g == q4) *(LAS f32x2*)(L + L_ST1 + (w2 * 32 + kh * 16 + l15) * 8) = f32x2{nmu, __builtin_amdgcn_rsqf(var + 1e-5f)}; } while (0)
#define WPF_STEP(n) do { \
    if ((n) == 0) { _Pragma("unroll") for (int j = 0; j < 8; ++j) wp[j] = *(LAS u32*)(L + L_WPHI + 4 * g * 56 + l15 * 4 + ((j & 3) + 16 * (j >> 2)) * 56); } \
    if ((n) >= 8 && (n) < 12) { const int e = (n) - 8; const bool z = l15 >= 14 || (e >= 2 && g == 3); \
      wpf[0][e] = z ? 0u : __builtin_amdgcn_perm(wp[2 * e + 1], wp[2 * e], 0x05040100u); wpf[1][e] = z ? 0u : __builtin_amdgcn_perm(wp[2 * e + 1], wp[2 * e], 0x07060302u); } \
    if ((n) == 12) asm volatile("" : "+v"(wpf[0]), "+v"(wpf[1])); } while (0)
  {
    u32x4 xf[2][14], wpf[2];
    u32 wp[8];
    LAS char* xr0 = L + w2 * 28672 + l15 * 128 + ((g ^ (l15 & 7)) * 16);
    LAS char* xr1 = L + w2 * 28672 + l15 * 128 + (((4 + g) ^ (l15 & 7)) * 16);
    LAS char* pw = L + L_PKV + (w2 * 32 + l15) * 224 + g * 8;
    const char* wos = wo + (r2 < 14 ? cg : 0) * 128 + w2 * 56 * 896;
    const u32 wovo = (lane >> 3) * 896 + (((lane & 7) ^ ((lane >> 2) & 4)) * 16);
#define WO_HALF(hf_) _Pragma("unroll") for (int n = 0; n < 7; ++n) dma16(wos + ((hf_) * 224 + n * 8) * 896, wovo, L_WO + ((hf_) * 28 + w2 * 7 + n) * 1024)
    if constexpr (A) {
      if (wave < 2) dma4(x + (xp * 512 + li * 16) * 896 + wave * 7168, min(lane, 55) * 128, L_JUNK);
      if (wave == 3) { *(LAS u32*)(L + L_FLG + lane * 4) = 0u; *(LAS u32*)(L + L_FLG + 256 + lane * 4) = 0u; dma4(pc0, min((li % 6) * 8192 + lane * 128, CODE_PF), L_JUNK); }
      if (wave == 2) { dma16(wphi + head * 1568, lane * 16, L_WPHI); dma16(wphi + head * 1568, min(lane + 64, 97) * 16, L_WPHI + 1024); }
      if (WTOUCH && wave < WTOUCH) dma4(wqkv + li * 14 * 2688 + (wave == 2 ? 0 : (wave + 1) * 896) + xh * 384, min(lane >> 2, 13) * 2688 + (lane & 3) * 128, L_JUNK);
      BAR();
      asm volatile("s_load_dwordx2 %0, %1, 0x0 ; role A\n\ts_waitcnt lgkmcnt(0)" : "=s"(epoch) : "s"(ws) : "memory");
      tag = epoch * 6364136223846793005ull + 1442695040888963407ull;
      STAMP(1);
      lds_wait1(L, S_X0, w2);
      XF_READ(0);
      if (w2 < 2) lds_signal(L, S_XF0, w2);
#pragma unroll
      for (int i = 0; i < 7; ++i) dma16(wbase + 896 + 64 * i * 2688, wvo, L_WK + (4 * i + w2) * 1024);
      STAMP(27);
      lds_wait1(L, S_X1, w2);
      STAMP(2);
      XF_READ(1);
      STAMP(3);
      LN_STATS();
      WAITVM(0);
      STAMP(4);
      lds_signal(L, S_WK, w2);
      auto clear = [&](int c) LAM {
        if (c < 2) ST16(rs, empty4, lane * 16, WS_O + ((tile * 8 + 2 * w2 + c) * 16 + head) * 1024);
        else if (c < 4) ST16(rs, empty4, r2 < 14 ? hvo : NOWHERE, (c - 2) * 2048);
        else ST16(rs, empty4, r2 < 14 && lane < 16 ? (lane >> 3) * 16384 + (lane & 7) * 16 : NOWHERE, sto);
      };
      lds_wait(L, S_WK);
      STAMP(5);
      __builtin_amdgcn_s_setprio(2);
      f32x4 t[2][2];
      u32x4 cw;
      project<0>(L, L_WK + wr0, L_WK + wr1, xf, w2, lane, t, [&](int n) LAM {
        WPF_STEP(n);
        if (n == KGO) lds_signal(L, S_KDONE, w2);
        if (CLP < 28 && n >= CLP && n < CLP + 5 * CLS && (n - CLP) % CLS == 0) clear((n - CLP) / CLS);
        if (n == 15) cw = *(volatile LAS u32x4*)(L + L_FLG + S_CS * 16);
        if (n == 21 && __builtin_expect(!__builtin_amdgcn_readfirstlane(cw.x & cw.y & cw.z & cw.w), 0)) lds_wait(L, S_CS);
      });
      STAMP(6);
      u32x4 P[2][2];
      feature_map(t, wpf, g, P);
#pragma unroll
      for (int c = 0; c < 4; ++c) {
        LAS char* p = pw + (c >> 1) * 16 * 224 + (c & 1) * 64;
        *(LAS u32x2*)(p) = u32x2{P[c >> 1][c & 1].x, P[c >> 1][c & 1].y}; *(LAS u32x2*)(p + 32) = u32x2{P[c >> 1][c & 1].z, P[c >> 1][c & 1].w};
      }
      lds_signal(L, S_PHIK, w2);
      STAMP(7);
      u32 pr = L_PKV + (4 * g + q4) * 224 + 8 * p4;
      asm volatile("" : "+v"(pr));
      if (CLP == 28) { clear(0); clear(1); clear(2); clear(3); clear(4); }
      lds_wait2(L, S_V, S_PHIK);
      u32x4 pa[4], pb[8];
#pragma unroll
      for (int m = 0; m < 4; ++m) {
        pa[m] = trfrag(L + pr + w2 * 32 + m * 32 * 224, 16 * 224);
        pb[2 * m] = trfrag(L + pr + m * 32 * 224 + 128, 16 * 224); pb[2 * m + 1] = trfrag(L + pr + m * 32 * 224 + 160, 16 * 224);
      }
      f32x4 kv[2] = {};
#pragma unroll
      for (int m = 0; m < 8; ++m) kv[m & 1] = mfma16(pa[m >> 1], pb[m], kv[m & 1]);
      const u32x4 kvb = {pk(kv[0][0], kv[0][1]), pk(kv[0][2], kv[0][3]), pk(kv[1][0], kv[1][1]), pk(kv[1][2], kv[1][3])};
      ST16(rs, kvb, lane * 16, WS_KVP + ((head * 16 + tile) * 4 + w2) * 1024);
      STAMP(8);
      DRAIN();
      FLAG(WS_FKV + ((head * 4 + w2) * 16 + tile) * 128, 1);
      __builtin_amdgcn_s_setprio(0);
      STAMP(9);
      if (HW1) { lds_signal(L, S_XA, w2); lds_wait(L, S_XA); }
      if (r2 < 14 || !CE2) { WO_HALF(0); WO_HALF(1); } else { HH_CLEAR(); }
      poll_flags(ws, WS_FKV + ((head * 4 + w2) * 16 + l15) * 128, lane < 16, tag);
      lds_signal(L, S_WO, w2);
      if (CE2 && r2 >= 14) { DRAIN(); HH_FLAG(); }
      STAMP(10);
      u32x4 tt[16];
#pragma unroll
      for (int i = 0; i < 16; ++i) tt[i] = __builtin_bit_cast(u32x4, __builtin_amdgcn_raw_buffer_load_b128(rs, lane * 16, WS_KVP + head * 65536 + w2 * 1024 + i * 4096, 16));
      u32 fgo;
      {
        const int pl = (tile & 1) * 16 + min(lane >> 2, 13);
        fgo = wave == 0 ? WS_FKV + ((l15 * 4 + 2 * tg + (g & 1)) * 16 + tile2) * 128
            : wave == 1 ? WS_FKV + (((((tile >> 1) & 1) * 8 + (pl & 7)) * 4 + p4) * 16 + (tile & 12) + (pl >> 3)) * 128
            : wave == 2 ? WS_FD + (min(cg, 6) * 32 + min(lane & 31, 27)) * 128
                        : WS_FG + (min(((7 * r) >> 2) + g, (7 * r + 6) >> 2) * 16 + (l15 & 8) + min(lane & 7, 6)) * 128;
      }
      u64 fg = __hip_atomic_load((const GAS u64*)(ws + fgo), RLX_AGENT);
      SCHED();
      const u32 one = g == q4 ? ((p4 & 1) ? 0x3f800000u : 0x3f80u) : 0u, s01 = p4 < 2 ? one : 0u, s23 = p4 < 2 ? 0u : one;
      const u32x4 sel0 = {s01, s23, 0u, 0u}, sel1 = {0u, 0u, s01, s23};
      f32x4 s0 = {}, s1 = {};
#pragma unroll
      for (int i = 0; i < 16; ++i) { s0 = mfma16(sel0, tt[i], s0); s1 = mfma16(sel1, tt[i], s1); }
      LAS char* kw = L + L_KVA + (w2 >> 1) * 1024 + lane * 16 + (w2 & 1) * 8;
      *(LAS u32x2*)(kw) = u32x2{pk(s0[0], s0[1]), pk(s0[2], s0[3])};
      *(LAS u32x2*)(kw + 2048) = u32x2{pk(s1[0], s1[1]), pk(s1[2], s1[3])};
      lds_signal(L, S_KVA, w2);
      STAMP(11);
      if (WOE == 3) { WO_HALF(1); WAITVM(0); lds_signal(L, S_WO, w2); }
      asm volatile("" : "+v"(fg) :: "memory");
      if (!__all(fg == tag + 1)) poll_flags(ws, fgo, true, tag);
      __builtin_amdgcn_fence(__ATOMIC_ACQUIRE, "wavefront");
      lds_signal(L, S_WOFA, w2);
      lds_wait1(L, S_WOFA, 3);
      const int fl = min(((7 * r) >> 2) + w2, (7 * r + 6) >> 2);
#pragma unroll
      for (int i = 0; i < 7; ++i) csp[i] = __builtin_bit_cast(f32x4, __builtin_amdgcn_raw_buffer_load_b128(rs, ((7 * r) >> 2) + w2 == fl ? lane * 16 + (i & 3) * 1024 : NOWHERE, WS_CS + fl * 7168 + (i & 4) * 1024, 16));
      GUBLK(2);
      if (W1 > 1) { lds_wait(L, S_XB); GUBLK(0); }
      if (W1 > 2) GUBLK(1);
      lds_wait(L, WOE < 2 ? S_WQ : S_KVA);
      WOF_READ(0, 8);
      SCHED();
#define CS_OUT() do { const f32x4 sm = ((csp[0] + csp[1]) + (csp[2] + csp[3])) + ((csp[4] + csp[5]) + csp[6]); \
        const float e0 = swadd32(sm[0], sm[1]), e1 = swadd32(sm[2], sm[3]);     \
        const int j0 = 4 * (((7 * r) >> 2) + w2) + 2 * (lane & 1) - 7 * r, cr = ((g & 1) * 16 + hh * 8 + (l15 >> 1)) * 4; \
        if (j0 >= 0 && j0 < 7) *(LAS float*)(L + L_CS + j0 * 128 + cr) = e0; \
        if (j0 + 1 >= 0 && j0 + 1 < 7) *(LAS float*)(L + L_CS + (j0 + 1) * 128 + cr) = e1; } while (0)
      if (!CSL) CS_OUT();
      STAMP(24);
    } else {
      const u32 vo = (lane >> 3) * 896 + (((lane & 7) ^ (lane >> 3)) * 16);
      const char* xb = x + (tile * 128 + w2 * 32) * 896;
#pragma unroll
      for (int n = 0; n < 28; ++n) {
        dma16(xb + ((n / 14) * 16 + (n & 1) * 8) * 896 + ((n % 14) >> 1) * 128, vo, w2 * 28672 + n * 1024);
        if (n == 3) { BAR(); asm volatile("s_load_dwordx2 %0, %1, 0x0 ; role B" : "=s"(epoch) : "s"(ws) : "memory"); }
        if (n == 21) { WAITVM(8); lds_signal(L, S_X0, w2); }
      }
      asm volatile("s_waitcnt lgkmcnt(0)" : "+s"(epoch)::"memory");
      tag = epoch * 6364136223846793005ull + 1442695040888963407ull;
      STAMP(1);
      XF_READ(0);
      if (w2 < 2) lds_signal(L, S_XF0, 2 + w2);
      WAITVM(0);
      lds_signal(L, S_X1, w2);
      STAMP(2);
      lds_wait(L, S_XF0);
      XF_READ(1);
      SCHED();
#define WDST(c) ((c) * 1024 + ((c) >= 14 ? SLOT - 14336 : 0))
#pragma unroll
      for (int i = 0; i < 7; ++i) dma16(wbase + 1792 + 64 * i * 2688, wvo, L_WV + WDST(4 * i + w2));
      STAMP(3);
      LN_STATS();
      lds_signal(L, S_CS, w2);
      STAMP(4);
      if (ULATE) { WAITVM(0); lds_signal(L, S_WV, w2); }
      const int rg = lane >> 3;
      const u32 lvo = (ud ? (32 * (rg & 1) + (rg & 6)) * 896 : rg * 8 * 3584) + (lane & 7) * 16;
      const char* us = ud ? wd + ufl * 64 * 896 + ukq * 128 : (umat ? wu : wg) + ukq * 64 * 3584 + ufl * 128;
      int ust0 = ud ? 8 * 896 : 3584, ust1 = ud ? 896 : 4 * 3584;
      asm volatile("" : "+s"(us), "+s"(ust0), "+s"(ust1));
      if (has_unit) {
#pragma unroll
        for (int j = 0; j < 8; ++j) asm volatile("global_load_dwordx4 %0, %1, %2" : "=v"(rp[j]) : "v"(lvo), "s"(us + (j & 3) * ust0 + (j >> 2) * ust1) : "memory");
      } else WAITVM(0);
      if (!ULATE) { WAITVM(8); lds_signal(L, S_WV, w2); }
      lds_wait2(L, S_WV, S_WK);
      if (QEARLY) {
#pragma unroll
        for (int i = 0; i < 7; ++i) dma16(wbase + 64 * i * 2688, wvo, L_WQ + WDST(4 * i + w2));
      }
      if (WOE == 1) WO_HALF(0);
      lds_wait1(L, S_KDONE, w2);
      STAMP(5);
      f32x4 t[2][2];
      project<SLOT - 14336>(L, L_WV + wr0, L_WV + wr1, xf, w2, lane, t, [&](int n) LAM { if (n == 27) __builtin_amdgcn_s_setprio(VPRIO); });
      STAMP(6);
#pragma unroll
      for (int c = 0; c < 2; ++c) {
        LAS char* p = pw + c * 16 * 224 + 128;
        *(LAS u32x2*)(p) = u32x2{pk(t[c][0][0], t[c][0][1]), pk(t[c][0][2], t[c][0][3])};
        *(LAS u32x2*)(p + 32) = g == 3 ? u32x2{0x3f80u, 0u} : u32x2{pk(t[c][1][0], t[c][1][1]), pk(t[c][1][2], t[c][1][3])};
      }
      lds_signal(L, S_V, w2);
      __builtin_amdgcn_s_setprio(0);
      STAMP(7);
      if (!QEARLY) {
#pragma unroll
        for (int i = 0; i < 7; ++i) dma16(wbase + 64 * i * 2688, wvo, L_WQ + WDST(4 * i + w2));
      }
      if (WOE == 0) WO_HALF(0);
      if (WOE < 2) { lds_wait(L, S_KDONE); WO_HALF(1); }
      asm volatile("s_waitcnt vmcnt(%8)" : RP_OPS : "n"(WOE < 2 ? 21 : 7) : "memory");
      auto repack = [&]() LAM {
        if (has_unit) {
          const int fb = 4 * ufl + (rg >> 1), pr = fb * 147 >> 10;
          const int so = (ud ? (((pr >> 2) * 7 + fb - 7 * pr) * 4 + (pr & 3)) * 2048 : (rg >> 1) * 1024) + (rg & 1) * 512 + (lane & 7) * 16;
          const int sb = ud ? WS_WD + ukq * 229376 : WS_WGU + 4 * ufl * SLOT + ukq * 4096 + umat * 256;
          const int s1 = ud ? 256 : SLOT, s2 = ud ? 1024 : 2 * SLOT;
          float c[8];
#pragma unroll
          for (int e = 0; e < 8; ++e) {
            const u32x4 f = piece(rp, e);
            ST16(rs, f, so + (e & 1) * 128, sb + ((e >> 1) & 1) * s1 + (e >> 2) * s2);
            c[e] = mfma16(ones, f, z4)[0];
          }
          const f32x4 cv = g & 1 ? f32x4{c[4], c[5], c[6], c[7]} : f32x4{c[0], c[1], c[2], c[3]};
          ST16(rs, cv, g < 2 && !ud ? (l15 >> 3) * 512 + (lane & 7) * 32 + g * 16 : NOWHERE, WS_CS + ufl * 7168 + ukq * 1024 + (umat & 1) * 256);
        }
      };
#define UFLAG() FLAGC(has_unit, ud ? WS_FD + (ukq * 32 + ufl) * 128 : WS_FG + ((ufl * 2 + umat) * 8 + ukq) * 128, 1)
      if (!REPLATE) repack();
      STAMP(8);
      if (REPLATE || !has_unit) WAITVM(0); else WAITVM(9);
      lds_signal(L, S_WQ, w2);
      lds_wait(L, S_WQ);
      STAMP(9);
      project<SLOT - 14336>(L, L_WQ + wr0, L_WQ + wr1, xf, w2, lane, t, [&](int n) LAM {
        WPF_STEP(n);
        if (n == 14 && !REPLATE) { WAITVM(0); UFLAG(); }
        if (n == 22) lds_signal(L, S_XB, w2);
      });
      STAMP(10);
      if (REPLATE == 2) repack();
      u32x4 pq[2][2];
      feature_map(t, wpf, g, pq);
      asm volatile("" : "+v"(pq[0][0]), "+v"(pq[0][1]), "+v"(pq[1][0]), "+v"(pq[1][1]));
      STAMP(11);
      if (REPLATE == 1) repack();
      if (CE2 && r2 >= 14) { HH_CLEAR(); DRAIN(); HH_FLAG(); }
      if (WB0) { lds_wait(L, S_WO); WOF_READ(0, WB0); }
      STAMP(24);
      if (REPLATE == 2) { WAITVM(0); UFLAG(); }
      lds_wait(L, S_KVA);
      if (REPLATE == 1) { WAITVM(0); UFLAG(); }
      STAMP(27);
      u32x4 ka[4];
#pragma unroll
      for (int i = 0; i < 4; ++i) ka[i] = *(LAS u32x4*)(L + L_KVA + i * 1024 + lane * 16);
      f32x4 o[2][2];
#pragma unroll
      for (int c = 0; c < 4; ++c) o[c >> 1][c & 1] = mfma16(ka[2 * (c & 1) + 1], pq[c >> 1][1], mfma16(ka[2 * (c & 1)], pq[c >> 1][0], z4));
      const f32x2 da = swp16(o[0][1][0], o[0][1][0]), db = swp16(o[1][1][0], o[1][1][0]);
      const float den[2] = {swp32(da.y, da.y).y, swp32(db.y, db.y).y};
#pragma unroll
      for (int c = 0; c < 2; ++c) {
        const float rd = frcp(den[c] + 1e-6f), r3 = g == 3 ? 0.f : rd;
        const u32x4 ob = {pk(o[c][0][0] * rd, o[c][0][1] * rd), pk(o[c][0][2] * rd, o[c][0][3] * rd), pk(o[c][1][0] * r3, o[c][1][1] * r3), filled(pk(o[c][1][2] * r3, o[c][1][3] * r3))};
        ST16(rs, ob, lane * 16, WS_O + ((tile * 8 + 2 * w2 + c) * 16 + head) * 1024);
      }
      STAMP(12);
      if (WOE == 3) lds_wait(L, S_WO);
      lds_signal(L, S_W2B, w2);
      WOF_READ(WB0, 8);
    }
  }
  STAMP(13);
  u32x4 hres = {0u, 0u, 0u, 0u};
  const int xo = tok * 896 + cg * 128 + kh * 64 + g * 16;
  STAMP(23);
  if (r2 < 14) {
    u32x4 xr, of[8];
    asm volatile("global_load_dwordx4 %0, %1, %2" : "=v"(xr) : "v"(xo), "s"(x) : "memory");
    lds_wait2(L, S_W2B, S_WOFA);
#define WIN2() do { if (A ? W1 < 2 : W1 < 3) GUBLK(A ? 0 : 1); } while (0)
    if (W2F == 1) WIN2();
    __builtin_amdgcn_s_sleep(A ? D2A : D2B);
    STAMP(14);
#define OFA(c_) _Pragma("unroll") for (int i = 0; i < 8; ++i) \
      asm volatile("global_load_dwordx4 %0, %1, %2 offset:%3 sc1" : c_(of[i]) : "v"(lane * 16), "s"(ws + WS_O + ((tile2 * 8 + tg * 4 + tb) * 16 + kh * 8 + 4) * 1024), "n"(i * 1024 - 4096) : "memory")
#define OFW(k_) asm volatile("s_waitcnt vmcnt(%9)" : "+v"(of[0]), "+v"(of[1]), "+v"(of[2]), "+v"(of[3]), "+v"(of[4]), "+v"(of[5]), "+v"(of[6]), "+v"(of[7]), "+v"(xr) : "n"(k_) : "memory")
#define OFBAD() (!__all(max(max(max(of[0].w, of[1].w), max(of[2].w, of[3].w)), max(max(of[4].w, of[5].w), max(of[6].w, of[7].w))) != EMPTY))
    OFA("=v");
    if (W2F == 0) WIN2();
    OFW(W2F == 0 && (A ? W1 < 2 : W1 < 3) ? 7 : 0);
    if (A && CSL) { CS_OUT(); SCHED(); }
    if (W2F == 2) WIN2();
    if (__builtin_expect(OFBAD(), 0)) {
#pragma nounroll
      for (u32 sp = 0; sp < SPIN_CAP; ++sp) { __builtin_amdgcn_s_sleep(RSL); OFA("+v"); OFW(0); if (!OFBAD()) break; }
    }
    STAMP(25);
    f32x4 a[4] = {};
#pragma unroll
    for (int i = 0; i < 8; ++i)
#pragma unroll
      for (int k = 0; k < 4; ++k) a[k] = mfma16(wof[i][k], of[i], a[k]);
    *(LAS f32x4*)(L + L_EX + wave * 2048 + lane * 16) = kh ? a[0] : a[2];
    *(LAS f32x4*)(L + L_EX + wave * 2048 + 1024 + lane * 16) = kh ? a[1] : a[3];
    lds_signal(L, A ? S_EXA : S_EXB, w2);
    lds_wait1(L, A ? S_EXB : S_EXA, w2);
    STAMP(15);
    const f32x4 c0 = (kh ? a[2] : a[0]) + *(LAS f32x4*)(L + L_EX + (wave ^ 4) * 2048 + lane * 16);
    const f32x4 c1 = (kh ? a[3] : a[1]) + *(LAS f32x4*)(L + L_EX + (wave ^ 4) * 2048 + 1024 + lane * 16);
    const f32x4 h0 = c0 + f32x4{blo(xr.x), bhi(xr.x), blo(xr.y), bhi(xr.y)}, h1 = c1 + f32x4{blo(xr.z), bhi(xr.z), blo(xr.w), bhi(xr.w)};
    hres = u32x4{pk(h0[0], h0[1]), pk(h0[2], h0[3]), pk(h1[0], h1[1]), filled(pk(h1[2], h1[3]))};
    ST16(rs, hres, hvo, kh * 2048);
    const f32x4 r0 = {blo(hres.x), bhi(hres.x), blo(hres.y), bhi(hres.y)}, r1 = {blo(hres.z), bhi(hres.z), blo(hres.w), bhi(hres.w)};
    const f32x4 a1 = r0 + r1, a2q = r0 * r0 + r1 * r1;
    float s1 = (a1[0] + a1[1]) + (a1[2] + a1[3]), s2 = (a2q[0] + a2q[1]) + (a2q[2] + a2q[3]);
    { const float t = swadd16(s1, s2); const f32x2 a2 = sw32(t); s1 = a2.x + a2.y; }
    { const u32x2 sv = __builtin_bit_cast(u32x2, sw16(s1)); __builtin_amdgcn_raw_buffer_store_b64(u32x2{sv.x, filled(sv.y)}, rs, fvo, sto + kh * 16384, 16); }
    WAITVM(2);
  } else {
    lds_wait2(L, S_W2B, S_WOFA);
    WIN2();
    if (!CE2) { HH_CLEAR(); }
    DRAIN();
    if (!CE2) HH_FLAG();
    if (A && CSL) CS_OUT();
    if (CE2 && A) __builtin_amdgcn_s_sleep(DLY5);
  }
  lds_signal(L, A ? S_HFA : S_HFB, w2);
  STAMP(16);
  u32x4 wdf[NWD][8];
  const int wdo = WS_WD + (cg * 4 + w2) * 57344;
  const u32 po = WS_FHH + ((tile2 * 16 + 4 * w2 + ((lane >> 1) & 3)) * 4 + 2 * tg + (lane & 1)) * 128;
  const u32 ho = WS_HH + ((tile2 * 2 + tg) * 4 + w2) * 57344;
#define WDLOAD(k, bf) dma8(rs, lane * 16, wdo + (k) * 8192, ((bf) + 2) * SLOT + w2 * 7168, L_EX + (bf) * 4096 + w2 * 1024)
#define THR() asm volatile("s_waitcnt vmcnt(%0)" :: "n"(THRN) : "memory")
#define WLOAD1(j, i) wdf[(j) % NWD][i] = __builtin_bit_cast(u32x4, __builtin_amdgcn_raw_buffer_load_b128(rs, lane * 16 + ((i) & 3) * 1024, wdo + (j) * 8192 + ((i) & 4) * 1024, 16))
#define THR0() asm volatile("s_waitcnt vmcnt(%0)" :: "n"(THRN0) : "memory")
#define WLOAD(j) _Pragma("unroll") for (int i = 0; i < 8; ++i) { THR0(); WLOAD1(j, i); }
  u32 have = 0;
#define PLOAD() peek(ws, po, lane < 8, tag + 7)
#define PMIN(pv) do { const u64 d = (pv) - tag; have = max(have, min8(d <= 8 ? (u32)d : 0u)); } while (0)
#define NEEDP(n) do { _Pragma("nounroll") for (u32 sp = 0; have < (n) && sp < SPIN_CAP; ++sp) { PMIN(PLOAD()); if (PSLP && have < (n)) __builtin_amdgcn_s_sleep(PSLP); } \
    __builtin_amdgcn_fence(__ATOMIC_ACQUIRE, "wavefront"); } while (0)
#define HBASE() u32 hb3 = w2 * 7168 + lane * 16, hb0 = hb3 + 2 * SLOT, hb2 = hb0 + 2 * SLOT, hb7 = L_EX + w2 * 1024 + lane * 16; asm volatile("" : "+v"(hb0), "+v"(hb2), "+v"(hb7))
#define HFRAG(bf, i) (*(LAS u32x4*)(L + ((i) < 7 ? ((bf) == 3 ? hb3 : (bf) == 2 ? hb2 : hb0 + (bf) * SLOT) + (i) * 1024 : hb7 + (bf) * 4096)))
#define HQLOAD(q, j) _Pragma("unroll") for (int i = 0; i < 8; ++i) hq[q][i] = __builtin_bit_cast(u32x4, __builtin_amdgcn_raw_buffer_load_b128(rs, lane * 16 + (i & 3) * 1024, ho + (j) * 8192 + (i & 4) * 1024, 16))
#define HQLOADT(q, j) _Pragma("unroll") for (int i = 0; i < 8; ++i) { THR(); hq[q][i] = __builtin_bit_cast(u32x4, __builtin_amdgcn_raw_buffer_load_b128(rs, lane * 16 + (i & 3) * 1024, ho + (j) * 8192 + (i & 4) * 1024, 16)); }
#define DOWN16(ac, wf, hf_) _Pragma("unroll") for (int m = 0; m < 16; ++m) ac[(m >> 1) & 1][m & 1] = mfma32(wf[2 * (m >> 2) + (m & 1)], hf_[2 * (m >> 2) + ((m >> 1) & 1)], ac[(m >> 1) & 1][m & 1])
#define PARTIAL_OUT(ac, b0, b1) _Pragma("unroll") for (int n = 0; n < 16; ++n) { const int T = n >> 3, k = (n >> 2) & 1, q = n & 3; \
    *(LAS f32x4*)&HFRAG(T ? b1 : b0, n & 7) = f32x4{ac[T][k][4 * q], ac[T][k][4 * q + 1], ac[T][k][4 * q + 2], ac[T][k][4 * q + 3]}; }
#define FREE(f) do { _Pragma("nounroll") for (u32 sp = 0; sp < (SPIN_CAP << 4); ++sp) { const u32x4 fr = *(volatile LAS u32x4*)(L + L_FLG + S_DONE4 * 16); \
          if (__builtin_amdgcn_readfirstlane(min(min(fr.x, fr.y), min(fr.z, fr.w))) >= (f)) break; __builtin_amdgcn_s_sleep(BSL); } asm volatile("" ::: "memory"); } while (0)
#define HQBAD(q) (!__all(max(max(max(hq[q][0].w, hq[q][1].w), max(hq[q][2].w, hq[q][3].w)), max(max(hq[q][4].w, hq[q][5].w), max(hq[q][6].w, hq[q][7].w))) != EMPTY))
#define HQCHECK(q, j) do { if (__builtin_expect(HQBAD(q), 0)) { _Pragma("nounroll") for (u32 sp = 0; sp < SPIN_CAP; ++sp) { \
      __builtin_amdgcn_s_sleep(RSL); asm volatile("" ::: "memory"); HQLOAD(q, j); if (!HQBAD(q)) break; } } SCHED(); } while (0)
  if constexpr (A) {
    const int T32 = tile * 4 + wave;
    u32x2 st[7];
    u32x4 hb[28];
    u64 fcl;
    const u32 stv = (7 * hh * 2048 + (lane & 31)) * 8;
#define STA(c_) _Pragma("unroll") for (int i = 0; i < 7; ++i) asm volatile("global_load_dwordx2 %0, %1, %2 sc1" : c_(st[i]) : "v"(stv), "s"(ws + WS_ST + (i * 2048 + T32 * 32) * 8) : "memory")
#define STW(k_) asm volatile("s_waitcnt vmcnt(%7)" : "+v"(st[0]), "+v"(st[1]), "+v"(st[2]), "+v"(st[3]), "+v"(st[4]), "+v"(st[5]), "+v"(st[6]) : "n"(k_) : "memory")
#define STBAD() (!__all(max(max(max(st[0].y, st[1].y), max(st[2].y, st[3].y)), max(max(st[4].y, st[5].y), st[6].y)) != EMPTY))
#define HBA(c_, q) _Pragma("unroll") for (int i = 0; i < 4; ++i) asm volatile("global_load_dwordx4 %0, %1, %2 offset:%3 sc1" : c_(hb[4 * (q) + i]) : "v"(lane * 16), "s"(ws + WS_H + (T32 * 28 + 4 * (q)) * 1024), "n"(i * 1024) : "memory")
    lds_wait2(L, S_HFA, S_HFB);
    __builtin_amdgcn_s_sleep(DLY3);
    if (!SPEC || r2 >= 14) {
#pragma nounroll
      for (u32 sp = 0; sp < SPIN_CAP; ++sp) { STA("=v"); STW(0); if (!STBAD()) break; __builtin_amdgcn_s_sleep(RSL); }
    }
    STA("=v");
    lds_signal(L, S_HSEEN, w2);
    STAMP(17);
#pragma unroll
    for (int q = 0; q < 7; ++q) { HBA("=v", q); }
    asm volatile("global_load_dwordx2 %0, %1, %2 sc1" : "=v"(fcl) : "v"(p4 * 128), "s"(ws + WS_FH + (T32 * 32 + 28) * 128) : "memory");
    u32 wb_[3] = {(u32)lane * 16, (u32)lane * 16 + 2 * SLOT, (u32)lane * 16 + 4 * SLOT};
    asm volatile("" : "+v"(wb_[1]), "+v"(wb_[2]));
#define WFRAG(sl, s) (*(LAS u32x4*)(L + wb_[(sl) >> 1] + ((sl) & 1) * SLOT + (s) * 1024))
    LAS char* csl = L + L_CS + hh * 16;
    u32x4 ring[7];
    f32x4 cq[4];
    float nmu, ve, k0, k1;
    u32 mx;
    const int pvo = WS_FHH + ((tile * 16 + r) * 4 + wave) * 128;
#define PROG(n) FLAG(pvo + 131072, n)
    f32x16 acc[2];
    float y[8], gu[8];
    u32x4 yb;
    u32x4 cw;
#define LT(i)
    auto gu_block = [&](auto JC) __attribute__((always_inline)) {
      constexpr int j = JC.value;
      sfor<0, 28>([&](auto SC) LAM {
        constexpr int s = SC.value;
        if constexpr (j == 0) { u32x4 t_ = hb[s]; asm volatile("s_waitcnt vmcnt(%1)" : "+v"(t_) : "n"(28 - s) : "memory"); hb[s] = t_; }
        acc[j & 1] = mfma32(ring[s % 7], hb[s], acc[j & 1]);
        if constexpr (j == 0) mx = max(mx, hb[s].w);
        if constexpr ((j == 2 || j == 4) && s == 12) cw = *(volatile LAS u32x4*)(L + L_FLG + (j == 2 ? S_WIN3 : S_REFILL) * 16);
        if constexpr ((j == 2 || j == 4) && s == 20) { if (__builtin_expect(!__builtin_amdgcn_readfirstlane(cw.x & cw.y & cw.z & cw.w), 0)) lds_wait(L, j == 2 ? S_WIN3 : S_REFILL); asm volatile("" ::: "memory"); }
        if constexpr (j == 1 && s < 7 - NCL) ST16(rs, (u32x4{EMPTY, EMPTY, EMPTY, EMPTY}), lane * 16, hho + (NCL + s) * 8192);
        if constexpr (s + 7 < 28) ring[s % 7] = WFRAG(gu_slot(j), s + 7);
        else if constexpr (j < 6) ring[s % 7] = WFRAG(gu_slot(j + 1), s - 21);
        if constexpr (s == 0 && (j == 1 || j == 2)) lds_signal(L, j == 1 ? S_WSEEN : S_FREE01, wave);
        if constexpr (s == 0 && j >= 3) *(volatile LAS u32*)(L + L_FLG + S_DONE4 * 16 + wave * 4) = j - 2u;
        if constexpr (j * 28 + s == BGO) lds_signal(L, S_KVP, wave);
        if constexpr (j > 0) {
          sfor<0, 8>([&](auto EC) LAM {
            constexpr int i = EC.value, k = s - 1 - 2 * i;
            const float ga = acc[(j - 1) & 1][i];
            if constexpr (k == 0) y[i] = ga * ga;
            if constexpr (k == 1) y[i] = fmaf(y[i], k1, k0);
            if constexpr (k == 2) y[i] = ga * y[i];
            if constexpr (k == 3) y[i] = fexp2(y[i]);
            if constexpr (k == 4) y[i] = fmaf(y[i], ve, ve);
            if constexpr (k == 5) { gu[i] = ga * acc[(j - 1) & 1][i + 8]; PIN(gu[i]); }
            if constexpr (k == 6) y[i] = frcp(y[i]);
            if constexpr (k == 7) y[i] = y[i] * gu[i];
            if constexpr (k >= 0 && k < 8 && k != 5) PIN(y[i]);
          });
          if constexpr (s == 21) { yb[0] = pk(y[0], y[1]); yb[1] = pk(y[2], y[3]); }
          if constexpr (s == 22) yb[2] = pk(y[4], y[5]);
          if constexpr (j == 3 && s == 22 && NCL < 7) WAITVM(4);
          if constexpr (s == 23) { yb[3] = filled(pk(y[6], y[7])); ST16(rs, yb, lane * 16, hho + (j - 1) * 8192); }
          if constexpr (s == 24 && j <= (HELP > 4 ? HELP : 4)) FLAG(pvo, j);
        }
        if constexpr (j < 6 && s >= 17 && s < 21) cq[s - 17] = *(LAS f32x4*)(csl + (j + 1) * 128 + (s - 17) * 32);
        if constexpr (j < 6 && s >= 22 && s < 27) {
          sfor<0, 16>([&](auto EC) LAM {
            constexpr int e = EC.value;
            if constexpr ((e < 2 ? 22 : e < 4 ? 23 : 23 + (e >> 2)) == s) { float v = nmu * cq[e >> 2][e & 3]; PIN(v); acc[(j + 1) & 1][e] = v; }
          });
        }
        SCHED();
      });
      LT(j);
      if (j == 0) STAMP(18);
      if (j == 3) STAMP(26);
      if (j == 1) STAMP(28);
      if (j == 2) STAMP(29);
      if (j == 4) STAMP(30);
      if (j == 5) STAMP(31);
    };
#define HBW(q) asm volatile("s_waitcnt vmcnt(0)" : "+v"(hb[4 * (q)]), "+v"(hb[4 * (q) + 1]), "+v"(hb[4 * (q) + 2]), "+v"(hb[4 * (q) + 3]) :: "memory")
#define HBBAD(q) (!__all(max(max(hb[4 * (q)].w, hb[4 * (q) + 1].w), max(hb[4 * (q) + 2].w, hb[4 * (q) + 3].w)) != EMPTY))
#define FIX() do { _Pragma("unroll") for (int q = 0; q < 7; ++q) HBW(q); \
      _Pragma("nounroll") for (u32 sp = 0; sp < SPIN_CAP && STBAD(); ++sp) { __builtin_amdgcn_s_sleep(RSL); STA("+v"); STW(0); } \
      _Pragma("unroll") for (int q = 0; q < 7; ++q) { _Pragma("nounroll") for (u32 sp = 0; sp < SPIN_CAP && HBBAD(q); ++sp) { HBA("+v", q); HBW(q); } } } while (0)
    auto first = [&]() LAM {
#pragma unroll
      for (int k = 0; k < 4; ++k) cq[k] = *(LAS f32x4*)(csl + k * 32);
#pragma unroll
      for (int s = 0; s < 7; ++s) ring[s] = WFRAG(0, s);
      f32x2 sf[7];
#pragma unroll
      for (int i = 0; i < 7; ++i) sf[i] = __builtin_bit_cast(f32x2, st[i]);
      float s1 = ((sf[0].x + sf[1].x) + (sf[2].x + sf[3].x)) + ((sf[4].x + sf[5].x) + sf[6].x);
      float s2 = ((sf[0].y + sf[1].y) + (sf[2].y + sf[3].y)) + ((sf[4].y + sf[5].y) + sf[6].y);
      { f32x2 a = sw32(s1); s1 = a.x + a.y; a = sw32(s2); s2 = a.x + a.y; }
      nmu = s1 * (-1.f / 448.f); ve = fmaxf(s2 * (1.f / 448.f) - nmu * nmu, 0.f) + 1e-5f;
      const float rstd = __builtin_amdgcn_rsqf(ve);
      k0 = -2.30220819f * rstd; k1 = -0.10294324f * rstd * rstd * rstd;
#pragma unroll
      for (int i = 0; i < 16; ++i) acc[0][i] = nmu * cq[i >> 2][i & 3];
      mx = 0;
      __builtin_amdgcn_s_setprio(GUPRIO);
      SCHED();
      gu_block(IC<0>{});
    };
    STW(29);
    if (__builtin_expect(STBAD(), 0)) FIX();
    first();
    if (__builtin_expect(!__all(mx != EMPTY), 0)) { FIX(); first(); }
    asm volatile("s_waitcnt vmcnt(0)" : "+v"(fcl) :: "memory");
    if (__builtin_expect(!__all(fcl == tag + 1), 0)) poll_flags(ws, WS_FH + (T32 * 32 + 28 + p4) * 128, lane < 4, tag);
    gu_block(IC<1>{}); gu_block(IC<2>{}); gu_block(IC<3>{}); gu_block(IC<4>{}); gu_block(IC<5>{}); gu_block(IC<6>{});
#pragma unroll
    for (int i = 0; i < 8; ++i) { const float ga = acc[0][i]; y[i] = ga * acc[0][i + 8] * frcp(fmaf(fexp2(ga * fmaf(ga * ga, k1, k0)), ve, ve)); }
    yb = u32x4{pk(y[0], y[1]), pk(y[2], y[3]), pk(y[4], y[5]), filled(pk(y[6], y[7]))};
    ST16(rs, yb, lane * 16, hho + 6 * 8192);
    if (HELP == 7) FLAG(pvo, 7);
    if (BGO == 196) lds_signal(L, S_KVP, wave);
    __builtin_amdgcn_s_setprio(0);
    STAMP(19);
    if (r2 < 14) {
      f32x16 da[2][2] = {};
      u32x4 hq[2][8], wl[2][8];
      HBASE();
      __builtin_amdgcn_s_sleep(ADLY);
      lds_wait1(L, CE == 2 ? S_LAND : CE == 1 ? S_REPSEEN : S_CONS, w2);
      STAMP2(15);
      HQLOAD(0, 5);
      if (A6L == 0) HQLOAD(1, 6);
      if (CE == 2) lds_wait1(L, S_REPSEEN, w2);
#pragma unroll
      for (int i = 0; i < 8; ++i) wl[0][i] = HFRAG(2, i);
      SCHED();
      STAMP2(16);
      HQCHECK(0, 5);
      if (A6L == 1) HQLOAD(1, 6);
      if (CE) lds_wait1(L, S_CONS, w2);
#pragma unroll
      for (int i = 0; i < 8; ++i) wl[1][i] = HFRAG(0, i);
      SCHED();
      DOWN16(da, wl[0], hq[0]);
      SCHED();
      STAMP2(18);
      HQCHECK(1, 6);
      STAMP2(31);
      DOWN16(da, wl[1], hq[1]);
      PARTIAL_OUT(da, 0, 2);
      STAMP(20);
    }
  } else {
    lds_wait2(L, S_HFA, S_HFB);
    GUBLK(3);
    if (W3THR) WAITVM(3);
    GUBLK(4);
#define REFILL() do { lds_wait<BSL>(L, S_WSEEN); GUBLK(5); lds_wait<BSL>(L, S_FREE01); GUBLK(6); } while (0)
    if (r2 >= 14) {
      WAITVM(0); lds_signal(L, S_WIN3, w2);
      REFILL();
      WAITVM(0); lds_signal(L, S_REFILL, w2);
    } else {
#pragma unroll
      for (int n = 0; n < 24; ++n) {
        if (n == 8 * NW3) { STAMP2(25); lds_wait(L, S_HSEEN); __builtin_amdgcn_s_sleep(DLY4); }
        asm volatile("s_waitcnt vmcnt(%4)\n\tglobal_load_dwordx4 %0, %1, %2 offset:%3 sc1" : "=v"(wdf[n >> 3][n & 7]) : "v"(lane * 16), "s"(ws + wdo + 4096 + (n >> 3) * 8192), "n"((n & 7) * 1024 - 4096), "n"(THRN0) : "memory");
        if (n == 7) { WAITVM(8); lds_signal(L, S_WIN3, w2); }
      }
      STAMP2(23);
      REFILL();
      f32x16 acc[2][2] = {};
      u32x4 hq[2][8];
      STAMP2(16);
      HBASE();
      u64 pv;
      const char* hs = ws + ho + 4096;
#define PEEK() do { pv = tag + 7; asm volatile("s_mov_b64 exec, 0xff\n\tglobal_atomic_add_x2 %0, %1, %2, %3 sc0\n\ts_mov_b64 exec, -1" : "+v"(pv) : "v"(po), "v"(0ull), "s"(ws) : "memory"); } while (0)
#define PEEKED(k_, n_) do { asm volatile("s_waitcnt vmcnt(%1)" : "+v"(pv) : "n"(k_) : "memory"); PMIN(pv); NEEDP(n_); } while (0)
#define HQA(c_, q, j) _Pragma("unroll") for (int i = 0; i < 8; ++i) asm volatile("global_load_dwordx4 %0, %1, %2 offset:%3 sc1" : c_(hq[q][i]) : "v"(lane * 16), "s"(hs + (j) * 8192), "n"(i * 1024 - 4096) : "memory")
#define HQW(q, k_) asm volatile("s_waitcnt vmcnt(%8)" : "+v"(hq[q][0]), "+v"(hq[q][1]), "+v"(hq[q][2]), "+v"(hq[q][3]), "+v"(hq[q][4]), "+v"(hq[q][5]), "+v"(hq[q][6]), "+v"(hq[q][7]) : "n"(k_) : "memory")
#define HQOK(q, j, k_) do { HQW(q, k_); if (__builtin_expect(HQBAD(q), 0)) { _Pragma("nounroll") for (u32 sp = 0; sp < SPIN_CAP; ++sp) { \
        __builtin_amdgcn_s_sleep(RSL); HQA("+v", q, j); HQW(q, 0); if (!HQBAD(q)) break; } } SCHED(); } while (0)
      PEEK();
      FREE(1); WDLOAD(3, 0);
      WAITVM(8);
#pragma unroll
      for (int k = 0; k < 3; ++k) asm volatile("" : "+v"(wdf[k][0]), "+v"(wdf[k][1]), "+v"(wdf[k][2]), "+v"(wdf[k][3]), "+v"(wdf[k][4]), "+v"(wdf[k][5]), "+v"(wdf[k][6]), "+v"(wdf[k][7]) :: "memory");
      lds_signal(L, S_REFILL, w2);
      PEEKED(8, 1);
      STAMP2(1);
      PEEK();
      HQA("=v", 0, 0);
      FREE(2); WDLOAD(4, 1);
      PEEKED(16, 2);
      STAMP2(2);
      HQA("=v", 1, 1);
      PEEK();
      u32 fr3;
      { const u32x4 fr = *(volatile LAS u32x4*)(L + L_FLG + S_DONE4 * 16); fr3 = !W5D || __builtin_amdgcn_readfirstlane(min(min(fr.x, fr.y), min(fr.z, fr.w))) >= 3; }
      if (fr3) { if (!W5D) FREE(3); WDLOAD(5, 2); }
      if (fr3) WAITVM(25); else WAITVM(17);
      HQOK(0, 0, 25);
      __builtin_amdgcn_s_setprio(DP0);
      DOWN16(acc, wdf[0], hq[0]);
      __builtin_amdgcn_s_setprio(0);
#pragma unroll
      for (int i = 0; i < 8; ++i) wdf[0][i] = HFRAG(0, i);
      SCHED();
      STAMP2(8);
      if (!fr3) { FREE(3); WDLOAD(5, 2); }
      PEEKED(8, 3);
      lds_signal(L, S_LAND, w2);
      STAMP2(3);
      HQA("=v", 0, 2);
      PEEK();
      asm volatile("s_waitcnt lgkmcnt(0)" : "+v"(wdf[0][7]) :: "memory");
      WDLOAD(6, 0);
      HQOK(1, 1, 25);
      __builtin_amdgcn_s_setprio(DP0);
      DOWN16(acc, wdf[1], hq[1]);
#pragma unroll
      for (int i = 0; i < 8; ++i) wdf[1][i] = HFRAG(1, i);
      SCHED();
      STAMP2(9);
      PEEKED(8, 4);
      lds_signal(L, S_REPSEEN, w2);
      STAMP2(4);
      HQA("=v", 1, 3);
      __builtin_amdgcn_s_setprio(DNPRIO);
      HQOK(0, 2, 8);
      lds_signal(L, S_CONS, w2);
      SCHED();
      DOWN16(acc, wdf[2], hq[0]);
      SCHED();
      STAMP2(10);
      FREE(4);
      HQA("=v", 0, 4);
      HQOK(1, 3, 8);
      DOWN16(acc, wdf[0], hq[1]);
      SCHED();
      STAMP2(11);
      HQOK(0, 4, 0);
      DOWN16(acc, wdf[1], hq[0]);
      PARTIAL_OUT(acc, 1, 3);
      STAMP(20);
    }
  }
  if (r2 < 14) {
    BAR();
    STAMP2(7);
    float c[8] = {blo(hres.x), bhi(hres.x), blo(hres.y), bhi(hres.y), blo(hres.z), bhi(hres.z), blo(hres.w), bhi(hres.w)};
    const int rl = (kh * 32 + (tb & 1) * 16 + l15) * 16;
    f32x4 v0 = z4, v1 = z4;
#pragma unroll
    for (int h = 0; h < 2; ++h) {
      const int bf = (tb >> 1) * 2 + h;
      u32 rq = (bf == 3 ? 0 : (bf + 2) * SLOT) + g * 1024 + rl;
      const u32 rq7 = g == 3 ? L_EX + bf * 4096 + rl : rq + 4096, st7 = g == 3 ? 1024 : 7168;
      asm volatile("" : "+v"(rq));
#pragma unroll
      for (int w = 0; w < 4; ++w) { v0 += *(LAS f32x4*)(L + rq + w * 7168); v1 += *(LAS f32x4*)(L + rq7 + w * st7); }
    }
#pragma unroll
    for (int h = 0; h < 2; ++h) {
      const f32x4 v = h ? v1 : v0;
      const f32x2 p = swp32(v[0], v[2]), q = swp32(v[1], v[3]), t0 = swp16(p.x, q.x), t1 = swp16(p.y, q.y);
      c[4 * h] += t0.x; c[4 * h + 1] += t0.y; c[4 * h + 2] += t1.x; c[4 * h + 3] += t1.y;
    }
    { const u32x4 ov = {pk(c[0], c[1]), pk(c[2], c[3]), pk(c[4], c[5]), pk(c[6], c[7])}; asm volatile("global_store_dwordx4 %0, %1, %2 sc1\n\ts_nop 1" :: "v"(xo), "v"(ov), "s"(out) : "memory"); }
    if (A && b == 0 && tid == 0) __hip_atomic_store((GAS u64*)(ws + WS_E), tag, RLX_AGENT);
  }
  STAMP(21);
  };
  if (wave < 4) role(IC<1>{}); else role(IC<0>{});
}

extern "C" void kernel_launch(void* const* d_in, const int* in_sizes, int n_in, void* d_out, int out_size, void* d_ws, size_t ws_size, hipStream_t stream) {
  (void)hipFuncSetAttribute((const void*)fused_block, hipFuncAttributeMaxDynamicSharedMemorySize, LDS_BYTES);
  fused_block<<<256, 512, LDS_BYTES, stream>>>((const char*)d_in[0], (const char*)d_in[1], (const char*)d_in[2], (const char*)d_in[3], (const char*)d_in[5], (const char*)d_in[6],
                                               (const char*)d_in[7], (char*)d_out, (char*)d_ws);
}
```

```cpp
#include <hip/hip_runtime.h>
#include <hip/hip_bf16.h>
#include <stdint.h>

typedef unsigned u32;
typedef unsigned long long u64;
typedef u32 u32x4 __attribute__((ext_vector_type(4)));
typedef u32 u32x2 __attribute__((ext_vector_type(2)));
typedef float f32x2 __attribute__((ext_vector_type(2)));
typedef float f32x4 __attribute__((ext_vector_type(4)));
typedef float f32x16 __attribute__((ext_vector_type(16)));
typedef short s16x4 __attribute__((ext_vector_type(4)));
typedef __bf16 bf16x2 __attribute__((ext_vector_type(2)));
typedef __bf16 bf16x8 __attribute__((ext_vector_type(8)));
#define LAS __attribute__((address_space(3)))
#define GAS __attribute__((address_space(1)))
#define DI __device__ __forceinline__
#define RLX_AGENT __ATOMIC_RELAXED, __HIP_MEMORY_SCOPE_AGENT

constexpr int WS_E = 0;
constexpr int WS_FKV = 8192;
constexpr int WS_FO = WS_FKV + 131072;
constexpr int WS_FH = WS_FO + 131072;
constexpr int WS_FG = WS_FH + 262144;
constexpr int WS_FD = WS_FG + 114688;
constexpr int WS_FHH = WS_FD + 57344;
constexpr int WS_CS = WS_FHH + 262144;
constexpr int WS_ST = WS_CS + 200704;
constexpr int WS_WGU = WS_ST + 229376;
constexpr int WS_WD = WS_WGU + 3211264;
constexpr int WS_KVP = WS_WD + 1605632;
constexpr int WS_O = WS_KVP + 1048576;
constexpr int WS_H = WS_O + 2097152;
constexpr int WS_HH = WS_H + 1835008;
#define UNITX4
#define WDOFF(cg, fb, bl) (((((cg) * 4 + (fb) / 28) * 7 + (fb) % 7) * 4 + (fb) / 7 % 4) * 2 + (bl))
#define HHOFF(T32, fb) (((((T32) >> 1) * 4 + (fb) / 28) * 7 + (fb) % 7) * 8 + ((fb) / 7 & 3) * 2 + ((T32) & 1))
constexpr int WS_STAMP = WS_HH + 7340032;
constexpr int WS_LOG = WS_STAMP + 256 * 8 * 32 * 8;
constexpr int WS_USED = WS_LOG + 4096;
constexpr int SLOT = 28672;
constexpr int L_WK = 4 * SLOT, L_WV = 0, L_WQ = 14336;
constexpr int L_PKV = 2 * SLOT;
constexpr int L_WO = 3 * SLOT;
constexpr int L_EX = 5 * SLOT;
constexpr int L_KVA = 5 * SLOT;
constexpr int L_ST1 = 5 * SLOT + 4096;
constexpr int L_WPHI = 5 * SLOT + 5120;
constexpr int L_CS = 159744;
constexpr int L_FLG = 160768;
constexpr int L_JUNK = 161280;
constexpr int L_STAMP = 161536;
constexpr int L_RED = SLOT;
constexpr int LDS_BYTES = 163840;
constexpr int gu_slot(int j) { return j % 5; }
constexpr int CODE_PF = 45056;
constexpr u32 SPIN_CAP = 1u << 16;
#define LL 1
#define AP 1

#define STAMP(i) do {} while (0)
#define STAMP2(i) do {} while (0)
#define STAMP2X(i) do {} while (0)

DI f32x4 mfma16(u32x4 a, u32x4 b, f32x4 c) { return __builtin_amdgcn_mfma_f32_16x16x32_bf16(__builtin_bit_cast(bf16x8, a), __builtin_bit_cast(bf16x8, b), c, 0, 0, 0); }
DI f32x16 mfma32(u32x4 a, u32x4 b, f32x16 c) { return __builtin_amdgcn_mfma_f32_32x32x16_bf16(__builtin_bit_cast(bf16x8, a), __builtin_bit_cast(bf16x8, b), c, 0, 0, 0); }
DI u32 pk(float lo, float hi) { f32x2 p = {lo, hi}; return __builtin_bit_cast(u32, __builtin_convertvector(p, bf16x2)); }
DI float blo(u32 v) { return __builtin_bit_cast(float, v << 16); }
DI float bhi(u32 v) { return __builtin_bit_cast(float, v & 0xffff0000u); }
DI float dot2(u32 a, u32 b, float c) { return __builtin_amdgcn_fdot2_f32_bf16(__builtin_bit_cast(bf16x2, a), __builtin_bit_cast(bf16x2, b), c, false); }
DI u32x2 trread(LAS char* p) { return __builtin_bit_cast(u32x2, __builtin_amdgcn_ds_read_tr16_b64_v4i16((LAS s16x4*)p)); }
DI u32x4 trfrag(LAS char* p, int step) { u32x2 a = trread(p), b = trread(p + step); return u32x4{a.x, a.y, b.x, b.y}; }
DI float fexp2(float x) { return __builtin_amdgcn_exp2f(x); }
DI float frcp(float x) { return __builtin_amdgcn_rcpf(x); }
template <int C> DI float dppror(float x) { return __builtin_bit_cast(float, __builtin_amdgcn_update_dpp(0, __builtin_bit_cast(int, x), 0x120 + C, 0xf, 0xf, false)); }
DI float row_max(float x) { x = fmaxf(x, dppror<8>(x)); x = fmaxf(x, dppror<4>(x)); x = fmaxf(x, dppror<2>(x)); return fmaxf(x, dppror<1>(x)); }
DI float row_min(float x) { x = fminf(x, dppror<8>(x)); x = fminf(x, dppror<4>(x)); x = fminf(x, dppror<2>(x)); return fminf(x, dppror<1>(x)); }
DI float row_sum(float x) { x += dppror<8>(x); x += dppror<4>(x); x += dppror<2>(x); return x + dppror<1>(x); }
DI f32x2 sw16(float x) { u32 u = __builtin_bit_cast(u32, x); auto r = __builtin_amdgcn_permlane16_swap(u, u, false, false); return f32x2{__builtin_bit_cast(float, (u32)r[0]), __builtin_bit_cast(float, (u32)r[1])}; }
DI f32x2 sw32(float x) { u32 u = __builtin_bit_cast(u32, x); auto r = __builtin_amdgcn_permlane32_swap(u, u, false, false); return f32x2{__builtin_bit_cast(float, (u32)r[0]), __builtin_bit_cast(float, (u32)r[1])}; }
DI float grp_sum(float x) { f32x2 a = sw16(x); x = a.x + a.y; a = sw32(x); return a.x + a.y; }
DI float grp_max(float x) { f32x2 a = sw16(x); x = fmaxf(a.x, a.y); a = sw32(x); return fmaxf(a.x, a.y); }
DI float grp_min(float x) { f32x2 a = sw16(x); x = fminf(a.x, a.y); a = sw32(x); return fminf(a.x, a.y); }

#define BAR() asm volatile("s_waitcnt lgkmcnt(0)\n\ts_barrier" ::: "memory")
#define WAITVM(n) asm volatile("s_waitcnt vmcnt(" #n ")" ::: "memory")
#define DRAIN() do { asm volatile("s_waitcnt vmcnt(0)" ::: "memory"); __builtin_amdgcn_s_waitcnt(0x0F70); } while (0)
#define SCHED() __builtin_amdgcn_sched_barrier(0)
DI void dma16(const void* base, u32 voff, u32 lds) {
  u32 keep;
  asm volatile("s_mov_b32 %0, m0\n\ts_mov_b32 m0, %3\n\ts_nop 0\n\tglobal_load_lds_dwordx4 %1, %2\n\ts_mov_b32 m0, %0" : "=&s"(keep) : "v"(voff), "s"(base), "s"(lds) : "memory");
}
DI void dma16c(const void* base, u32 voff, u32 lds) {
  u32 keep;
  asm volatile("s_mov_b32 %0, m0\n\ts_mov_b32 m0, %3\n\ts_nop 0\n\tglobal_load_lds_dwordx4 %1, %2 sc1\n\ts_mov_b32 m0, %0" : "=&s"(keep) : "v"(voff), "s"(base), "s"(lds) : "memory");
}
DI void dma4(const void* base, u32 voff, u32 lds) {
  u32 keep;
  asm volatile("s_mov_b32 %0, m0\n\ts_mov_b32 m0, %3\n\ts_nop 0\n\tglobal_load_lds_dword %1, %2\n\ts_mov_b32 m0, %0" : "=&s"(keep) : "v"(voff), "s"(base), "s"(lds) : "memory");
}
DI void dma16c_if(u32 go, const void* base, u32 voff, u32 lds) {
  u32 keep;
  asm volatile("s_cmp_eq_u32 %4, 0\n\ts_cbranch_scc1 1f\n\ts_mov_b32 %0, m0\n\ts_mov_b32 m0, %3\n\ts_nop 0\n\tglobal_load_lds_dwordx4 %1, %2 sc1\n\ts_mov_b32 m0, %0\n1:"
               : "=&s"(keep) : "v"(voff), "s"(base), "s"(lds), "s"(go) : "memory", "scc");
}
template <int SI, int LI> DI void dma7(__amdgpu_buffer_rsrc_t rs, u32 voff, u32 so, u32 lds) {
  u32 t;
  asm volatile("s_add_u32 m0, %4, %6\n\ts_add_u32 %0, %3, %5\n\tbuffer_load_dwordx4 %1, %2, %0 offen sc1 lds\n\tbuffer_load_dwordx4 %1, %2, %0 offen offset:1024 sc1 lds\n\t"
               "buffer_load_dwordx4 %1, %2, %0 offen offset:2048 sc1 lds\n\tbuffer_load_dwordx4 %1, %2, %0 offen offset:3072 sc1 lds\n\t"
               "s_add_u32 m0, m0, 4096\n\ts_add_u32 %0, %0, 4096\n\tbuffer_load_dwordx4 %1, %2, %0 offen sc1 lds\n\tbuffer_load_dwordx4 %1, %2, %0 offen offset:1024 sc1 lds\n\t"
               "buffer_load_dwordx4 %1, %2, %0 offen offset:2048 sc1 lds"
               : "=&s"(t) : "v"(voff), "s"(rs), "s"(so), "s"(lds), "n"(SI), "n"(LI) : "memory", "scc");
}
DI void dma8(__amdgpu_buffer_rsrc_t rs, u32 voff, u32 so, u32 lds, u32 lds7) {
  dma7<0, 0>(rs, voff, so, lds);
  u32 t;
  asm volatile("s_mov_b32 m0, %4\n\ts_add_u32 %0, %3, 7168\n\tbuffer_load_dwordx4 %1, %2, %0 offen sc1 lds" : "=&s"(t) : "v"(voff), "s"(rs), "s"(so), "s"(lds7) : "memory", "scc");
}
DI u32 vm_count() {
  u32 t;
  asm volatile("s_getreg_b32 %0, hwreg(HW_REG_IB_STS)" : "=s"(t) :: "memory");
  return (t & 15u) | ((t >> 18) & 48u);
}
DI u32 min8(u32 c) {
  c = min(c, (u32)__builtin_amdgcn_update_dpp(0, (int)c, 0x121, 0xf, 0xf, false)); c = min(c, (u32)__builtin_amdgcn_update_dpp(0, (int)c, 0x122, 0xf, 0xf, false));
  c = min(c, (u32)__builtin_amdgcn_update_dpp(0, (int)c, 0x124, 0xf, 0xf, false));
  return __builtin_amdgcn_readfirstlane(min(c, (u32)__builtin_amdgcn_update_dpp(0, (int)c, 0x128, 0xf, 0xf, false)));
}
DI void dma4c(const void* base, u32 voff, u32 lds) {
  u32 keep;
  asm volatile("s_mov_b32 %0, m0\n\ts_mov_b32 m0, %3\n\ts_nop 0\n\tglobal_load_lds_dword %1, %2 sc1\n\ts_mov_b32 m0, %0" : "=&s"(keep) : "v"(voff), "s"(base), "s"(lds) : "memory");
}
#define LD16(rs, off) __builtin_bit_cast(u32x4, __builtin_amdgcn_raw_buffer_load_b128(rs, off, 0, 16))
#define ST16(rs, v, off, soff) do { const u32x4 d_ = __builtin_bit_cast(u32x4, v); __builtin_amdgcn_raw_buffer_store_b128(d_, rs, off, soff, 16); asm volatile("s_nop 1" :: "v"(d_) : "memory"); } while (0)

#define FLAG(off, n) __builtin_amdgcn_raw_buffer_store_b64(__builtin_bit_cast(u32x2, tag + (n)), rs, fvo, off, 16)
#define FLAGC(c, off, n) __builtin_amdgcn_raw_buffer_store_b64(__builtin_bit_cast(u32x2, tag + (n)), rs, (c) ? fvo : NOWHERE, off, 16)
constexpr int NOWHERE = 0x7ffffff0;
constexpr u32 EMPTY = 0xffffffffu;
DI u32 filled(u32 w) { return min(w, EMPTY - 1u); }
DI u64 peek(const char* ws, u32 off, bool on, u64 dflt) {
  u64 z = 0, v = dflt;
  asm volatile("" : "+v"(z));
  if (on) v = __hip_atomic_fetch_add((GAS u64*)(ws + off), z, __ATOMIC_RELAXED, AP == 2 ? __HIP_MEMORY_SCOPE_SYSTEM : __HIP_MEMORY_SCOPE_AGENT);
  return v;
}
DI void poll_flags(const char* ws, u32 off, bool on, u64 tag) {
#pragma nounroll
  for (u32 sp = 0; sp < SPIN_CAP; ++sp) {
    u64 v = tag + 1;
    if (on || !LL) v = __hip_atomic_load((const GAS u64*)(ws + off), RLX_AGENT);
    if (__all(v == tag + 1)) break;
  }
  __builtin_amdgcn_fence(__ATOMIC_ACQUIRE, "wavefront");
}
template <int SL = 1> DI void lds_wait(LAS char* L, int sig) {
#pragma nounroll
  for (u32 sp = 0; sp < (SPIN_CAP << 4); ++sp) {
    const u32x4 v = *(volatile LAS u32x4*)(L + L_FLG + sig * 16);
    if (__builtin_amdgcn_readfirstlane(v.x & v.y & v.z & v.w)) break;
    __builtin_amdgcn_s_sleep(SL);
  }
  asm volatile("" ::: "memory");
}
DI void lds_wait2(LAS char* L, int sig, int sig2) {
#pragma nounroll
  for (u32 sp = 0; sp < (SPIN_CAP << 4); ++sp) {
    const u32x4 v = *(volatile LAS u32x4*)(L + L_FLG + sig * 16) & *(volatile LAS u32x4*)(L + L_FLG + sig2 * 16);
    if (__builtin_amdgcn_readfirstlane(v.x & v.y & v.z & v.w)) break;
    __builtin_amdgcn_s_sleep(1);
  }
  asm volatile("" ::: "memory");
}
DI void lds_wait1(LAS char* L, int sig, int w) {
#pragma nounroll
  for (u32 sp = 0; sp < (SPIN_CAP << 4); ++sp) {
    if (__builtin_amdgcn_readfirstlane(*(volatile LAS u32*)(L + L_FLG + sig * 16 + w * 4))) break;
    __builtin_amdgcn_s_sleep(1);
  }
  asm volatile("" ::: "memory");
}
DI void lds_signal(LAS char* L, int sig, int w) {
  asm volatile("" ::: "memory");
  *(volatile LAS u32*)(L + L_FLG + sig * 16 + w * 4) = 1u;
}

template <int N> struct IC { static constexpr int value = N; };
template <int I, int N, class F> DI void sfor(F&& f) { if constexpr (I < N) { f(IC<I>{}); sfor<I + 1, N>(f); } }
#define LAM __attribute__((always_inline))
#define PIN(v) asm volatile("" : "+v"(v))
constexpr float LOG2E = 1.4426950408889634f;
template <int GAP, class F> DI void project(LAS char* L, u32 w0, u32 w1, const u32x4 (&xf)[2][14], int tp, int lane, f32x4 (&t)[2][2], F&& mid) {
  asm volatile("" : "+v"(w0), "+v"(w1));
  u32x4 ring[6];
  f32x4 cs[2] = {};
  f32x2 st[2];
  const u32x4 ones = {0x3f803f80u, 0x3f803f80u, 0x3f803f80u, 0x3f803f80u};
#pragma unroll
  for (int n = 0; n < 6; ++n) ring[n] = trfrag(L + (n & 1 ? w1 : w0) + (n >> 1) * 2048, 256);
#pragma unroll
  for (int i = 0; i < 4; ++i) t[i >> 1][i & 1] = f32x4{0.f, 0.f, 0.f, 0.f};
  SCHED();
#pragma unroll
  for (int n = 0; n < 28; ++n) {
    const u32x4 f = ring[n % 6];
    t[0][n & 1] = mfma16(f, xf[0][n >> 1], t[0][n & 1]);
    t[1][n & 1] = mfma16(f, xf[1][n >> 1], t[1][n & 1]);
    cs[n & 1] = mfma16(f, ones, cs[n & 1]);
    if (n + 6 < 28) ring[n % 6] = trfrag(L + (n & 1 ? w1 : w0) + ((n + 6) >> 1) * 2048 + (n + 6 >= 14 ? GAP : 0), 256);
    if (n == 22 || n == 23) st[n & 1] = *(LAS f32x2*)(L + L_ST1 + (tp * 32 + (n & 1) * 16 + (lane & 15)) * 8);
    mid(n);
    SCHED();
  }
#pragma unroll
  for (int tb = 0; tb < 2; ++tb)
#pragma unroll
    for (int k = 0; k < 2; ++k) t[tb][k] = (t[tb][k] + cs[k] * st[tb].x) * st[tb].y;
}
DI f32x2 swp16(float a, float b) { auto r = __builtin_amdgcn_permlane16_swap(__builtin_bit_cast(u32, a), __builtin_bit_cast(u32, b), false, false); return f32x2{__builtin_bit_cast(float, (u32)r[0]), __builtin_bit_cast(float, (u32)r[1])}; }
DI f32x2 swp32(float a, float b) { auto r = __builtin_amdgcn_permlane32_swap(__builtin_bit_cast(u32, a), __builtin_bit_cast(u32, b), false, false); return f32x2{__builtin_bit_cast(float, (u32)r[0]), __builtin_bit_cast(float, (u32)r[1])}; }
DI float swadd32(float a, float b) { const f32x2 s = swp32(a, b); return s.x + s.y; }
DI float swadd16(float a, float b) { const f32x2 s = swp16(a, b); return s.x + s.y; }
DI float mx2(float a, float b) { float r; asm("v_max_f32 %0, %1, %2" : "=v"(r) : "v"(a), "v"(b)); return r; }
DI float mx3(float a, float b, float c) { return fmaxf(fmaxf(a, b), c); }
DI float mn3(float a, float b, float c) { return fminf(fminf(a, b), c); }
DI void feature_map(const f32x4 (&t)[2][2], const u32x4 (&wpf)[2], int g, u32x4 (&P)[2][2]) {
  const float pad = g < 3 ? 0.f : -3.0e38f;
  const f32x4 z4 = {0.f, 0.f, pad, pad};
  float p[4][8], m[4];
#pragma unroll
  for (int c = 0; c < 4; ++c) {
    const int tb = c >> 1;
    u32x4 tq = {pk(t[tb][0][0], t[tb][0][1]), pk(t[tb][0][2], t[tb][0][3]), pk(t[tb][1][0], t[tb][1][1]), pk(t[tb][1][2], t[tb][1][3])};
    if (c & 1) tq ^= u32x4{0x80008000u, 0x80008000u, 0x80008000u, 0x80008000u};
    const f32x4 p0 = mfma16(wpf[0], tq, z4), p1 = mfma16(wpf[1], tq, z4);
#pragma unroll
    for (int i = 0; i < 8; ++i) p[c][i] = i < 4 ? p0[i] : p1[i - 4];
    m[c] = mx3(mx3(p[c][0], p[c][1], p[c][2]), mx3(p[c][3], p[c][4], p[c][5]), fmaxf(p[c][6], p[c][7]));
  }
  {
    const f32x2 a = swp16(m[0], m[1]), b = swp16(m[2], m[3]);
    const f32x2 c = swp32(mx2(a.x, a.y), mx2(b.x, b.y));
    const float d = mx2(c.x, c.y) * -LOG2E;
    const f32x2 e = swp32(d, d), f0 = swp16(e.x, e.x), f1 = swp16(e.y, e.y);
    m[0] = f0.x; m[1] = f0.y; m[2] = f1.x; m[3] = f1.y;
  }
  float ex[4][8], sm[4];
#pragma unroll
  for (int c = 0; c < 4; ++c) {
#pragma unroll
    for (int i = 0; i < 8; ++i) {
      ex[c][i] = fexp2(fmaf(p[c][i], LOG2E, m[c]));
    }
    sm[c] = ((ex[c][0] + ex[c][1]) + (ex[c][2] + ex[c][3])) + ((ex[c][4] + ex[c][5]) + (ex[c][6] + ex[c][7]));
  }
  {
    const float d = frcp(swadd32(swadd16(sm[0], sm[1]), swadd16(sm[2], sm[3])));
    const f32x2 e = swp32(d, d), f0 = swp16(e.x, e.x), f1 = swp16(e.y, e.y);
    sm[0] = f0.x; sm[1] = f0.y; sm[2] = f1.x; sm[3] = f1.y;
  }
#pragma unroll
  for (int c = 0; c < 4; ++c)
#pragma unroll
    for (int e = 0; e < 4; ++e) P[c >> 1][c & 1][e] = pk(ex[c][2 * e] * sm[c], ex[c][2 * e + 1] * sm[c]);
}
DI u32x4 piece(const u32x4 (&rp)[8], int e) {
  u32x4 f;
#pragma unroll
  for (int m = 0; m < 4; ++m) f[m] = __builtin_amdgcn_perm(rp[2 * m + 1][e >> 1], rp[2 * m][e >> 1], (e & 1) ? 0x07060302u : 0x05040100u);
  return f;
}
#define RP_OPS "+v"(rp[0]), "+v"(rp[1]), "+v"(rp[2]), "+v"(rp[3]), "+v"(rp[4]), "+v"(rp[5]), "+v"(rp[6]), "+v"(rp[7])
#define PF1 0
#define PF2 84
#define PF3 120
#define TP0 -1
#define TP1 13
#define TP2 -1
#define TOUCH 5
#define NW3 4
#define THRN0 4
#define DNPRIO 3
#define NWD 3
#define MIDCHK 0
#define HELP 0
#define NHW 7
#define W3LATE 0
#define W3WAIT 1
#define ROT 1
#define EXPT 0
#define PFM 12
#define PSLP 4
#define PLAG 0
#define WTOUCH 0
#define REPLATE 0
#define WB0 8
#define WB1 4
#define ULATE 0
#define VPRIO 3
#define KGO 20
#define QEARLY 1
#define DLY1 4
#define W1 1
#define W2C 0
#define WOE 2
#define W3THR 1
#define HW3 1
#define HW1 1
#define SN 4
#define BATCH 1
#define B23 1
#define WD5L 0
#define A56 1
#define BGO 196
#define BALL 0
#define BSL 4
#define RSL 4
#define H5 1
#define H6 1
#define BORD 1
#define DP0 3
#define AIL 1
#define SEEN 3
#define ADLY 16
#define W6E 0
#define SPLIT 1
#define NCL 4
#define B5L 1
#define A6L 1
#define BO2 2
#define TW 0
#define CL 0
#define CE 0
#define W5D 1
#define W2F 5
#define D2A 12
#define D2B 16
#define SPEC 1
#define CLP 28
#define CLS 3
#define CE2 0
#define DLY5 60
#define CSL 1
#define CF 0
#define OPRIO 3
#define PG 1
#define ISK 1
#define T2 70
#define FXW 8
#define DLY4 16
#define PUBS 5
#define THRN 40
#define DLY2 8
#define DLY3 20
#define GUPRIO 2
enum { S_FREE01 = 0, S_REFILL, S_DONE4, S_WIN3, S_WK, S_WV, S_WQ, S_WSEEN, S_KDONE, S_V, S_PHIK, S_KVP, S_KVA, S_WO, S_REPSEEN, S_HSEEN, S_X0, S_X1, S_XA, S_XB, S_CS, S_WOFA, S_WOFB, S_EXA, S_EXB, S_W2A, S_W2B, S_HFA, S_HFB, S_CONS, S_LAND, S_XF0 };

__global__ void __launch_bounds__(512, 2) fused_block(const char* __restrict__ x, const char* __restrict__ wqkv, const char* __restrict__ wphi, const char* __restrict__ wo,
                                                      const char* __restrict__ wg, const char* __restrict__ wu, const char* __restrict__ wd, char* __restrict__ out, char* ws) {
  extern __shared__ __attribute__((aligned(16))) char smem[];
  LAS char* L = (LAS char*)smem;
  const int tid = threadIdx.x, lane = tid & 63, wave = __builtin_amdgcn_readfirstlane(tid >> 6), w2 = wave & 3;
  const int b = blockIdx.x, li = b >> 3, xp = (b & 7) >> 1, xh = b & 1;
  const int tile = 4 * xp + (li >> 3), r = 8 * xh + (li & 7);
  const int tile2 = 4 * xp + 2 * xh + (li >> 4), r2 = li & 15;
  const int tg = r2 >= 7 ? 1 : 0, cg = r2 - 7 * tg;
  const int l15 = lane & 15, g = lane >> 4, q4 = l15 >> 2, p4 = lane & 3, hh = lane >> 5;
  const int head = r;
  asm volatile("" :: "s"(x), "s"(wqkv), "s"(wphi), "s"(wo), "s"(wg), "s"(wu), "s"(wd), "s"(out), "s"(ws));
  STAMP(0);
  u64 pc_; asm volatile("s_getpc_b64 %0" : "=s"(pc_));
  const char* pc0 = (const char*)(pc_ & ~127ull);
  const __amdgpu_buffer_rsrc_t rs = __builtin_amdgcn_make_buffer_rsrc((void*)ws, 0, WS_USED, 0x00020000);
  const u32x4 ones = {0x3f803f80u, 0x3f803f80u, 0x3f803f80u, 0x3f803f80u};
  const f32x4 z4 = {0.f, 0.f, 0.f, 0.f};
  auto role = [&](auto AC) __attribute__((always_inline)) {
  constexpr bool A = AC.value;
  if (A) asm volatile("; A" ::: "memory"); else asm volatile("; B" ::: "memory");
  __builtin_amdgcn_s_waitcnt(0x0F70);
  u64 epoch;
  u32x4 rp[8];
  const int fvo = lane < 16 ? lane * 8 : NOWHERE;
  const int uidx = (tile * 10 + __builtin_popcount(~0x2A54u & ((1u << head) - 1u))) * 4 + w2, unit = uidx * 147 / 160;
  const bool has_unit = !((0x2A54 >> head) & 1) && (uidx + 1) * 147 / 160 != unit;
  const int u3 = unit / 3, umat = unit - 3 * u3, ukq = u3 / 28, ufl = u3 - 28 * ukq;
  const bool ud = umat == 2;
  const char* gusrc = ws + WS_WGU + 7 * r * SLOT;
  const u32 guo = WS_WGU + 7 * r * SLOT + w2 * 7168;
#define GUBLK(j) dma7<(j) * SLOT, gu_slot(j) * SLOT>(rs, lane * 16, guo, w2 * 7168)
  const u32 wvo = (lane >> 2) * 2688 + (((lane & 3) ^ (2 * (lane >> 5))) * 16);
  const char* wbase = wqkv + head * 56 - (head & 1) * 8 + 16 * w2 * 2688;
  const u32 wr0 = (8 * g + q4) * 64 + (((p4 + (head & 1)) ^ (4 * (g & 1))) * 8), wr1 = (8 * g + q4) * 64 + ((((4 + p4 + (head & 1)) & 7) ^ (4 * (g & 1))) * 8);
  const int tb = w2; constexpr int kh = A ? 0 : 1;
  const int hho = WS_HH + (((tile * 2 + (w2 >> 1)) * 4 + (r >> 2)) * 56 + (r & 3) * 2 + (w2 & 1)) * 1024;
  const int tok = tile2 * 128 + tg * 64 + tb * 16 + l15;
  const int hvo = WS_H + ((tok >> 5) * 28 + 4 * cg + (g >> 1)) * 1024 + ((g & 1) * 32 + (tok & 31)) * 16;
  const int sto = WS_ST + (2 * cg * 2048 + tile2 * 128 + tg * 64 + tb * 16) * 8;
  const u32x4 empty4 = {EMPTY, EMPTY, EMPTY, EMPTY};
#define HH_CLEAR() _Pragma("unroll") for (int i = 0; i < 4 * NCL; ++i) ST16(rs, empty4, lane * 16 + (i & 1) * 2048, WS_HH + (((tile2 * 2 + r2 - 14) * 4 + w2) * 56 + kh + (i >> 1) * 4) * 1024)
#define HH_FLAG() FLAG(WS_FH + ((tile2 * 4 + 2 * (r2 - 14) + kh) * 32 + 28 + w2) * 128, 1)
  u32x4 wof[8][4];
  f32x4 csp[7];
  u64 tag;
#define WOF_READ(i0, i1) do { u32 wa_ = L_WO + (kh * 224 + 4 * g + q4) * 128 + 16 * p4 + 64 * (q4 >> 1), wb_ = wa_ ^ 64, wc_ = wa_ + (g == 3 ? 0 : 2048), wd_ = wc_ ^ 64; \
    asm volatile("" : "+v"(wa_), "+v"(wb_), "+v"(wc_), "+v"(wd_)); \
    _Pragma("unroll") for (int i = i0; i < i1; ++i) _Pragma("unroll") for (int k = 0; k < 4; ++k) { \
      const u32x2 a_ = trread(L + (k >> 1 ? wb_ : wa_) + 28 * i * 128 + (k & 1) * 8), c_ = trread(L + (k >> 1 ? wd_ : wc_) + 28 * i * 128 + (k & 1) * 8); \
      wof[i][k] = u32x4{a_.x, a_.y, c_.x, c_.y}; } } while (0)
#define XF_READ(i) _Pragma("unroll") for (int s = 0; s < 14; ++s) xf[i][s] = *(LAS u32x4*)((s & 1 ? xr1 : xr0) + (i) * 14336 + (s >> 1) * 2048)
#define LN_STATS() do { f32x4 g1 = z4, g2 = z4; \
    _Pragma("unroll") for (int s = 0; s < 14; ++s) { g1 = mfma16(ones, xf[kh][s], g1); g2 = mfma16(xf[kh][s], xf[kh][s], g2); } \
    const float d01 = (p4 & 1) ? g2[1] : g2[0], d23 = (p4 & 1) ? g2[3] : g2[2], s2 = (p4 & 2) ? d23 : d01; \
    const float nmu = g1[0] * (-1.f / 448.f), var = fmaxf(s2 * (1.f / 448.f) - nmu * nmu, 0.f); \
    if (g == q4) *(LAS f32x2*)(L + L_ST1 + (w2 * 32 + kh * 16 + l15) * 8) = f32x2{nmu, __builtin_amdgcn_rsqf(var + 1e-5f)}; } while (0)
#define WPF_STEP(n) do { \
    if ((n) == 0) { _Pragma("unroll") for (int j = 0; j < 8; ++j) wp[j] = *(LAS u32*)(L + L_WPHI + 4 * g * 56 + l15 * 4 + ((j & 3) + 16 * (j >> 2)) * 56); } \
    if ((n) >= 8 && (n) < 12) { const int e = (n) - 8; const bool z = l15 >= 14 || (e >= 2 && g == 3); \
      wpf[0][e] = z ? 0u : __builtin_amdgcn_perm(wp[2 * e + 1], wp[2 * e], 0x05040100u); wpf[1][e] = z ? 0u : __builtin_amdgcn_perm(wp[2 * e + 1], wp[2 * e], 0x07060302u); } \
    if ((n) == 12) asm volatile("" : "+v"(wpf[0]), "+v"(wpf[1])); } while (0)
  {
    u32x4 xf[2][14], wpf[2];
    u32 wp[8];
    LAS char* xr0 = L + w2 * 28672 + l15 * 128 + ((g ^ (l15 & 7)) * 16);
    LAS char* xr1 = L + w2 * 28672 + l15 * 128 + (((4 + g) ^ (l15 & 7)) * 16);
    LAS char* pw = L + L_PKV + (w2 * 32 + l15) * 224 + g * 8;
    const char* wos = wo + (r2 < 14 ? cg : 0) * 128 + w2 * 56 * 896;
    const u32 wovo = (lane >> 3) * 896 + (((lane & 7) ^ ((lane >> 2) & 4)) * 16);
#define WO_HALF(hf_) _Pragma("unroll") for (int n = 0; n < 7; ++n) dma16(wos + ((hf_) * 224 + n * 8) * 896, wovo, L_WO + ((hf_) * 28 + w2 * 7 + n) * 1024)
    if constexpr (A) {
      if (wave < 2) dma4(x + (xp * 512 + li * 16) * 896 + wave * 7168, min(lane, 55) * 128, L_JUNK);
      if (wave == 3) { *(LAS u32*)(L + L_FLG + lane * 4) = 0u; *(LAS u32*)(L + L_FLG + 256 + lane * 4) = 0u; dma4(pc0, min((li % 6) * 8192 + lane * 128, CODE_PF), L_JUNK); }
      if (wave == 2) { dma16(wphi + head * 1568, lane * 16, L_WPHI); dma16(wphi + head * 1568, min(lane + 64, 97) * 16, L_WPHI + 1024); }
      if (WTOUCH && wave < WTOUCH) dma4(wqkv + li * 14 * 2688 + (wave == 2 ? 0 : (wave + 1) * 896) + xh * 384, min(lane >> 2, 13) * 2688 + (lane & 3) * 128, L_JUNK);
      BAR();
      asm volatile("s_load_dwordx2 %0, %1, 0x0 ; role A\n\ts_waitcnt lgkmcnt(0)" : "=s"(epoch) : "s"(ws) : "memory");
      tag = epoch * 6364136223846793005ull + 1442695040888963407ull;
      STAMP(1);
      lds_wait1(L, S_X0, w2);
      XF_READ(0);
      if (w2 < 2) lds_signal(L, S_XF0, w2);
#pragma unroll
      for (int i = 0; i < 7; ++i) dma16(wbase + 896 + 64 * i * 2688, wvo, L_WK + (4 * i + w2) * 1024);
      STAMP(27);
      lds_wait1(L, S_X1, w2);
      STAMP(2);
      XF_READ(1);
      STAMP(3);
      LN_STATS();
      WAITVM(0);
      STAMP(4);
      lds_signal(L, S_WK, w2);
      auto clear = [&](int c) LAM {
        if (c < 2) ST16(rs, empty4, lane * 16, WS_O + ((tile * 8 + 2 * w2 + c) * 16 + head) * 1024);
        else if (c < 4) ST16(rs, empty4, r2 < 14 ? hvo : NOWHERE, (c - 2) * 2048);
        else ST16(rs, empty4, r2 < 14 && lane < 16 ? (lane >> 3) * 16384 + (lane & 7) * 16 : NOWHERE, sto);
      };
      lds_wait(L, S_WK);
      STAMP(5);
      __builtin_amdgcn_s_setprio(2);
      f32x4 t[2][2];
      u32x4 cw;
      project<0>(L, L_WK + wr0, L_WK + wr1, xf, w2, lane, t, [&](int n) LAM {
        WPF_STEP(n);
        if (n == KGO) lds_signal(L, S_KDONE, w2);
        if (CLP < 28 && n >= CLP && n < CLP + 5 * CLS && (n - CLP) % CLS == 0) clear((n - CLP) / CLS);
        if (n == 15) cw = *(volatile LAS u32x4*)(L + L_FLG + S_CS * 16);
        if (n == 21 && __builtin_expect(!__builtin_amdgcn_readfirstlane(cw.x & cw.y & cw.z & cw.w), 0)) lds_wait(L, S_CS);
      });
      STAMP(6);
      u32x4 P[2][2];
      feature_map(t, wpf, g, P);
#pragma unroll
      for (int c = 0; c < 4; ++c) {
        LAS char* p = pw + (c >> 1) * 16 * 224 + (c & 1) * 64;
        *(LAS u32x2*)(p) = u32x2{P[c >> 1][c & 1].x, P[c >> 1][c & 1].y}; *(LAS u32x2*)(p + 32) = u32x2{P[c >> 1][c & 1].z, P[c >> 1][c & 1].w};
      }
      lds_signal(L, S_PHIK, w2);
      STAMP(7);
      u32 pr = L_PKV + (4 * g + q4) * 224 + 8 * p4;
      asm volatile("" : "+v"(pr));
      if (CLP == 28) { clear(0); clear(1); clear(2); clear(3); clear(4); }
      lds_wait2(L, S_V, S_PHIK);
      u32x4 pa[4], pb[8];
#pragma unroll
      for (int m = 0; m < 4; ++m) {
        pa[m] = trfrag(L + pr + w2 * 32 + m * 32 * 224, 16 * 224);
        pb[2 * m] = trfrag(L + pr + m * 32 * 224 + 128, 16 * 224); pb[2 * m + 1] = trfrag(L + pr + m * 32 * 224 + 160, 16 * 224);
      }
      f32x4 kv[2] = {};
#pragma unroll
      for (int m = 0; m < 8; ++m) kv[m & 1] = mfma16(pa[m >> 1], pb[m], kv[m & 1]);
      const u32x4 kvb = {pk(kv[0][0], kv[0][1]), pk(kv[0][2], kv[0][3]), pk(kv[1][0], kv[1][1]), pk(kv[1][2], kv[1][3])};
      ST16(rs, kvb, lane * 16, WS_KVP + ((head * 16 + tile) * 4 + w2) * 1024);
      STAMP(8);
      DRAIN();
      FLAG(WS_FKV + ((head * 4 + w2) * 16 + tile) * 128, 1);
      __builtin_amdgcn_s_setprio(0);
      STAMP(9);
      if (HW1) { lds_signal(L, S_XA, w2); lds_wait(L, S_XA); }
      if (r2 < 14 || !ISK) { WO_HALF(0); WO_HALF(1); } else __builtin_amdgcn_s_sleep(20);
      poll_flags(ws, WS_FKV + ((head * 4 + w2) * 16 + l15) * 128, lane < 16, tag);
      lds_signal(L, S_WO, w2);
      if (CE2 && r2 >= 14) { DRAIN(); HH_FLAG(); }
      STAMP(10);
      u32x4 tt[16];
#pragma unroll
      for (int i = 0; i < 16; ++i) tt[i] = __builtin_bit_cast(u32x4, __builtin_amdgcn_raw_buffer_load_b128(rs, lane * 16, WS_KVP + head * 65536 + w2 * 1024 + i * 4096, 16));
      u32 fgo;
      {
        const int pl = (tile & 1) * 16 + min(lane >> 2, 13);
        fgo = wave == 0 ? WS_FKV + ((l15 * 4 + 2 * tg + (g & 1)) * 16 + tile2) * 128
            : wave == 1 ? WS_FKV + (((((tile >> 1) & 1) * 8 + (pl & 7)) * 4 + p4) * 16 + (tile & 12) + (pl >> 3)) * 128
            : wave == 2 ? WS_FD + (min(cg, 6) * 32 + min(lane & 31, 27)) * 128
                        : WS_FG + (min(((7 * r) >> 2) + g, (7 * r + 6) >> 2) * 16 + (l15 & 8) + min(lane & 7, 6)) * 128;
      }
      u64 fg = __hip_atomic_load((const GAS u64*)(ws + fgo), RLX_AGENT);
      SCHED();
      const u32 one = g == q4 ? ((p4 & 1) ? 0x3f800000u : 0x3f80u) : 0u, s01 = p4 < 2 ? one : 0u, s23 = p4 < 2 ? 0u : one;
      const u32x4 sel0 = {s01, s23, 0u, 0u}, sel1 = {0u, 0u, s01, s23};
      f32x4 s0 = {}, s1 = {};
#pragma unroll
      for (int i = 0; i < 16; ++i) { s0 = mfma16(sel0, tt[i], s0); s1 = mfma16(sel1, tt[i], s1); }
      LAS char* kw = L + L_KVA + (w2 >> 1) * 1024 + lane * 16 + (w2 & 1) * 8;
      *(LAS u32x2*)(kw) = u32x2{pk(s0[0], s0[1]), pk(s0[2], s0[3])};
      *(LAS u32x2*)(kw + 2048) = u32x2{pk(s1[0], s1[1]), pk(s1[2], s1[3])};
      lds_signal(L, S_KVA, w2);
      STAMP(11);
      if (WOE == 3) { WO_HALF(1); WAITVM(0); lds_signal(L, S_WO, w2); }
      asm volatile("" : "+v"(fg) :: "memory");
      if (!__all(fg == tag + 1)) poll_flags(ws, fgo, true, tag);
      __builtin_amdgcn_fence(__ATOMIC_ACQUIRE, "wavefront");
      lds_signal(L, S_WOFA, w2);
      lds_wait1(L, S_WOFA, 3);
      const int fl = min(((7 * r) >> 2) + w2, (7 * r + 6) >> 2);
#pragma unroll
      for (int i = 0; i < 7; ++i) asm volatile("global_load_dwordx4 %0, %1, %2 offset:%3 sc1" : "=v"(csp[i]) : "v"(((7 * r) >> 2) + w2 == fl ? lane * 16 : 0), "s"(ws + WS_CS + fl * 7168 + 4096), "n"(i * 1024 - 4096) : "memory");
      GUBLK(2);
      if (W1 > 1) { lds_wait(L, S_XB); GUBLK(0); }
      if (W1 > 2) GUBLK(1);
      if (r2 < 14 || !ISK) { lds_wait(L, S_WO); WOF_READ(0, 8); }
      SCHED();
#define CS_OUT(k_) do { asm volatile("s_waitcnt vmcnt(%7)" : "+v"(csp[0]), "+v"(csp[1]), "+v"(csp[2]), "+v"(csp[3]), "+v"(csp[4]), "+v"(csp[5]), "+v"(csp[6]) : "n"(k_) : "memory"); \
        const f32x4 sm = ((csp[0] + csp[1]) + (csp[2] + csp[3])) + ((csp[4] + csp[5]) + csp[6]); \
        const float e0 = swadd32(sm[0], sm[1]), e1 = swadd32(sm[2], sm[3]);     \
        const int j0 = 4 * (((7 * r) >> 2) + w2) + 2 * (lane & 1) - 7 * r, cr = ((g & 1) * 16 + hh * 8 + (l15 >> 1)) * 4; \
        if (j0 >= 0 && j0 < 7) *(LAS float*)(L + L_CS + j0 * 128 + cr) = e0; \
        if (j0 + 1 >= 0 && j0 + 1 < 7) *(LAS float*)(L + L_CS + (j0 + 1) * 128 + cr) = e1; } while (0)
      lds_signal(L, S_WOFB, w2);
      if (!CSL) CS_OUT(7);
      STAMP(24);
    } else {
      const u32 vo = (lane >> 3) * 896 + (((lane & 7) ^ (lane >> 3)) * 16);
      const char* xb = x + (tile * 128 + w2 * 32) * 896;
#pragma unroll
      for (int n = 0; n < 28; ++n) {
        dma16(xb + ((n / 14) * 16 + (n & 1) * 8) * 896 + ((n % 14) >> 1) * 128, vo, w2 * 28672 + n * 1024);
        if (n == 3) { BAR(); asm volatile("s_load_dwordx2 %0, %1, 0x0 ; role B" : "=s"(epoch) : "s"(ws) : "memory"); }
        if (n == 21) { WAITVM(8); lds_signal(L, S_X0, w2); }
      }
      asm volatile("s_waitcnt lgkmcnt(0)" : "+s"(epoch)::"memory");
      tag = epoch * 6364136223846793005ull + 1442695040888963407ull;
      STAMP(1);
      XF_READ(0);
      if (w2 < 2) lds_signal(L, S_XF0, 2 + w2);
      WAITVM(0);
      lds_signal(L, S_X1, w2);
      STAMP(2);
      lds_wait(L, S_XF0);
      XF_READ(1);
      SCHED();
#define WDST(c) ((c) * 1024 + ((c) >= 14 ? SLOT - 14336 : 0))
#pragma unroll
      for (int i = 0; i < 7; ++i) dma16(wbase + 1792 + 64 * i * 2688, wvo, L_WV + WDST(4 * i + w2));
      STAMP(3);
      LN_STATS();
      lds_signal(L, S_CS, w2);
      STAMP(4);
      if (ULATE) { WAITVM(0); lds_signal(L, S_WV, w2); }
      const int rg = lane >> 3;
      const u32 lvo = (ud ? (32 * (rg & 1) + (rg & 6)) * 896 : rg * 8 * 3584) + (lane & 7) * 16;
      const char* us = ud ? wd + ufl * 64 * 896 + ukq * 128 : (umat ? wu : wg) + ukq * 64 * 3584 + ufl * 128;
      int ust0 = ud ? 8 * 896 : 3584, ust1 = ud ? 896 : 4 * 3584;
      asm volatile("" : "+s"(us), "+s"(ust0), "+s"(ust1));
      if (has_unit) {
#pragma unroll
        for (int j = 0; j < 8; ++j) asm volatile("global_load_dwordx4 %0, %1, %2" : "=v"(rp[j]) : "v"(lvo), "s"(us + (j & 3) * ust0 + (j >> 2) * ust1) : "memory");
      } else WAITVM(0);
      if (!ULATE) { WAITVM(8); lds_signal(L, S_WV, w2); }
      lds_wait2(L, S_WV, S_WK);
      if (QEARLY) {
#pragma unroll
        for (int i = 0; i < 7; ++i) dma16(wbase + 64 * i * 2688, wvo, L_WQ + WDST(4 * i + w2));
      }
      if (WOE == 1) WO_HALF(0);
      lds_wait1(L, S_KDONE, w2);
      STAMP(5);
      f32x4 t[2][2];
      project<SLOT - 14336>(L, L_WV + wr0, L_WV + wr1, xf, w2, lane, t, [&](int n) LAM { if (n == 27) __builtin_amdgcn_s_setprio(VPRIO); });
      STAMP(6);
#pragma unroll
      for (int c = 0; c < 2; ++c) {
        LAS char* p = pw + c * 16 * 224 + 128;
        *(LAS u32x2*)(p) = u32x2{pk(t[c][0][0], t[c][0][1]), pk(t[c][0][2], t[c][0][3])};
        *(LAS u32x2*)(p + 32) = g == 3 ? u32x2{0x3f80u, 0u} : u32x2{pk(t[c][1][0], t[c][1][1]), pk(t[c][1][2], t[c][1][3])};
      }
      lds_signal(L, S_V, w2);
      __builtin_amdgcn_s_setprio(0);
      STAMP(7);
      if (!QEARLY) {
#pragma unroll
        for (int i = 0; i < 7; ++i) dma16(wbase + 64 * i * 2688, wvo, L_WQ + WDST(4 * i + w2));
      }
      if (WOE == 0) WO_HALF(0);
      if (WOE < 2) { lds_wait(L, S_KDONE); WO_HALF(1); }
      asm volatile("s_waitcnt vmcnt(%8)" : RP_OPS : "n"(WOE < 2 ? 21 : 7) : "memory");
      auto repack = [&]() LAM {
        if (has_unit) {
          const int fb = 4 * ufl + (rg >> 1), pr = fb * 147 >> 10;
          const int so = (ud ? (((pr >> 2) * 7 + fb - 7 * pr) * 4 + (pr & 3)) * 2048 : (rg >> 1) * 1024) + (rg & 1) * 512 + (lane & 7) * 16;
          const int sb = ud ? WS_WD + ukq * 229376 : WS_WGU + 4 * ufl * SLOT + ukq * 4096 + umat * 256;
          const int s1 = ud ? 256 : SLOT, s2 = ud ? 1024 : 2 * SLOT;
          float c[8];
#pragma unroll
          for (int e = 0; e < 8; ++e) {
            const u32x4 f = piece(rp, e);
            ST16(rs, f, so + (e & 1) * 128, sb + ((e >> 1) & 1) * s1 + (e >> 2) * s2);
            c[e] = mfma16(ones, f, z4)[0];
          }
          const f32x4 cv = g & 1 ? f32x4{c[4], c[5], c[6], c[7]} : f32x4{c[0], c[1], c[2], c[3]};
          ST16(rs, cv, g < 2 && !ud ? (l15 >> 3) * 512 + (lane & 7) * 32 + g * 16 : NOWHERE, WS_CS + ufl * 7168 + ukq * 1024 + (umat & 1) * 256);
        }
      };
#define UFLAG() FLAGC(has_unit, ud ? WS_FD + (ukq * 32 + ufl) * 128 : WS_FG + ((ufl * 2 + umat) * 8 + ukq) * 128, 1)
      if (!REPLATE) repack();
      STAMP(8);
      if (REPLATE || !has_unit) WAITVM(0); else WAITVM(9);
      lds_signal(L, S_WQ, w2);
      lds_wait(L, S_WQ);
      STAMP(9);
      project<SLOT - 14336>(L, L_WQ + wr0, L_WQ + wr1, xf, w2, lane, t, [&](int n) LAM {
        WPF_STEP(n);
        if (n == 14 && !REPLATE) { WAITVM(0); UFLAG(); }
        if (n == 22) lds_signal(L, S_XB, w2);
      });
      STAMP(10);
      if (REPLATE == 2) repack();
      u32x4 pq[2][2];
      feature_map(t, wpf, g, pq);
      asm volatile("" : "+v"(pq[0][0]), "+v"(pq[0][1]), "+v"(pq[1][0]), "+v"(pq[1][1]));
      STAMP(11);
      if (REPLATE == 1) repack();
      if (CE2 && r2 >= 14) { HH_CLEAR(); DRAIN(); HH_FLAG(); }
      if (r2 < 14 || !ISK) { lds_wait(L, S_WO); WOF_READ(0, 8); }
      STAMP(24);
      if (REPLATE == 2) { WAITVM(0); UFLAG(); }
      __builtin_amdgcn_s_setprio(OPRIO);
      lds_wait(L, S_KVA);
      if (REPLATE == 1) { WAITVM(0); UFLAG(); }
      STAMP(27);
      u32x4 ka[4];
#pragma unroll
      for (int i = 0; i < 4; ++i) ka[i] = *(LAS u32x4*)(L + L_KVA + i * 1024 + lane * 16);
      f32x4 o[2][2];
#pragma unroll
      for (int c = 0; c < 4; ++c) o[c >> 1][c & 1] = mfma16(ka[2 * (c & 1) + 1], pq[c >> 1][1], mfma16(ka[2 * (c & 1)], pq[c >> 1][0], z4));
      const f32x2 da = swp16(o[0][1][0], o[0][1][0]), db = swp16(o[1][1][0], o[1][1][0]);
      const float den[2] = {swp32(da.y, da.y).y, swp32(db.y, db.y).y};
#pragma unroll
      for (int c = 0; c < 2; ++c) {
        const float rd = frcp(den[c] + 1e-6f), r3 = g == 3 ? 0.f : rd;
        const u32x4 ob = {pk(o[c][0][0] * rd, o[c][0][1] * rd), pk(o[c][0][2] * rd, o[c][0][3] * rd), pk(o[c][1][0] * r3, o[c][1][1] * r3), filled(pk(o[c][1][2] * r3, o[c][1][3] * r3))};
        ST16(rs, ob, lane * 16, WS_O + ((tile * 8 + 2 * w2 + c) * 16 + head) * 1024);
      }
      STAMP(12);
      if (WOE == 3) lds_wait(L, S_WO);
      *(volatile LAS u32*)(L + L_FLG + S_W2A * 16 + w2 * 4) = (u32)__builtin_amdgcn_s_memrealtime();
      lds_signal(L, S_W2B, w2);
      __builtin_amdgcn_s_setprio(0);
    }
  }
  STAMP(13);
  u32x4 hres = {0u, 0u, 0u, 0u};
  const int xo = tok * 896 + cg * 128 + kh * 64 + g * 16;
  STAMP(23);
  if (r2 < 14) {
    u32x4 xr, of[8];
    asm volatile("global_load_dwordx4 %0, %1, %2" : "=v"(xr) : "v"(xo), "s"(x) : "memory");
    lds_wait2(L, S_W2B, S_WOFA);
#define WIN2() do { if (A ? W1 < 2 : W1 < 3) GUBLK(A ? 0 : 1); } while (0)
    if (W2F == 1 || W2F == 3) WIN2();
    if (W2F == 4 && !A) { GUBLK(0); GUBLK(1); }
    if (W2F == 5 && !A) GUBLK(1);
    if (T2) {
      const u32x4 ts = *(volatile LAS u32x4*)(L + L_FLG + S_W2A * 16);
      const u32 t0 = __builtin_amdgcn_readfirstlane(ts.x), due = t0 + T2 + __builtin_amdgcn_readfirstlane(max(max(0, (int)(ts.y - ts.x)), max((int)(ts.z - ts.x), (int)(ts.w - ts.x))));
#pragma nounroll
      for (u32 sp = 0; sp < 64 && (int)((u32)__builtin_amdgcn_s_memrealtime() - due) < 0; ++sp) __builtin_amdgcn_s_sleep(1);
    } else __builtin_amdgcn_s_sleep(A ? D2A : D2B);
    STAMP(14);
#define OFA(c_) _Pragma("unroll") for (int i = 0; i < 8; ++i) \
      asm volatile("global_load_dwordx4 %0, %1, %2 offset:%3 sc1" : c_(of[i]) : "v"(lane * 16), "s"(ws + WS_O + ((tile2 * 8 + tg * 4 + tb) * 16 + kh * 8 + 4) * 1024), "n"(i * 1024 - 4096) : "memory")
#define OFW(k_) asm volatile("s_waitcnt vmcnt(%9)" : "+v"(of[0]), "+v"(of[1]), "+v"(of[2]), "+v"(of[3]), "+v"(of[4]), "+v"(of[5]), "+v"(of[6]), "+v"(of[7]), "+v"(xr) : "n"(k_) : "memory")
#define OFBAD() (!__all(max(max(max(of[0].w, of[1].w), max(of[2].w, of[3].w)), max(max(of[4].w, of[5].w), max(of[6].w, of[7].w))) != EMPTY))
    OFA("=v");
    f32x4 a[4] = {};
    u32 mx = 0;
#pragma unroll
    for (int i = 0; i < 8; ++i) {
      asm volatile("s_waitcnt vmcnt(%1)" : "+v"(of[i]) : "n"(7 - i) : "memory");
      if (i == PG - 1) { lds_wait(L, S_WOFB); }
      if (i == 7) GUBLK(A ? 0 : 3);
#pragma unroll
      for (int k = 0; k < 4; ++k) a[k] = mfma16(wof[i][k], of[i], a[k]);
      mx = max(mx, of[i].w);
      SCHED();
    }
    asm volatile("" : "+v"(xr));
    if (__builtin_expect(!__all(mx != EMPTY), 0)) {
#pragma nounroll
      for (u32 sp = 0; sp < SPIN_CAP; ++sp) { __builtin_amdgcn_s_sleep(RSL); OFA("+v"); OFW(0); if (!OFBAD()) break; }
#pragma unroll
      for (int i = 0; i < 8; ++i)
#pragma unroll
        for (int k = 0; k < 4; ++k) a[k] = mfma16(wof[i][k], of[i], i ? a[k] : z4);
    }
    STAMP(25);
    *(LAS f32x4*)(L + L_EX + wave * 2048 + lane * 16) = kh ? a[0] : a[2];
    *(LAS f32x4*)(L + L_EX + wave * 2048 + 1024 + lane * 16) = kh ? a[1] : a[3];
    lds_signal(L, A ? S_EXA : S_EXB, w2);
    lds_wait1(L, A ? S_EXB : S_EXA, w2);
    STAMP(15);
    const f32x4 c0 = (kh ? a[2] : a[0]) + *(LAS f32x4*)(L + L_EX + (wave ^ 4) * 2048 + lane * 16);
    const f32x4 c1 = (kh ? a[3] : a[1]) + *(LAS f32x4*)(L + L_EX + (wave ^ 4) * 2048 + 1024 + lane * 16);
    const f32x4 h0 = c0 + f32x4{blo(xr.x), bhi(xr.x), blo(xr.y), bhi(xr.y)}, h1 = c1 + f32x4{blo(xr.z), bhi(xr.z), blo(xr.w), bhi(xr.w)};
    hres = u32x4{pk(h0[0], h0[1]), pk(h0[2], h0[3]), pk(h1[0], h1[1]), filled(pk(h1[2], h1[3]))};
    ST16(rs, hres, hvo, kh * 2048);
    const f32x4 r0 = {blo(hres.x), bhi(hres.x), blo(hres.y), bhi(hres.y)}, r1 = {blo(hres.z), bhi(hres.z), blo(hres.w), bhi(hres.w)};
    const f32x4 a1 = r0 + r1, a2q = r0 * r0 + r1 * r1;
    float s1 = (a1[0] + a1[1]) + (a1[2] + a1[3]), s2 = (a2q[0] + a2q[1]) + (a2q[2] + a2q[3]);
    { const float t = swadd16(s1, s2); const f32x2 a2 = sw32(t); s1 = a2.x + a2.y; }
    { const u32x2 sv = __builtin_bit_cast(u32x2, sw16(s1)); __builtin_amdgcn_raw_buffer_store_b64(u32x2{sv.x, filled(sv.y)}, rs, fvo, sto + kh * 16384, 16); }
    WAITVM(2);
  } else {
    lds_wait2(L, S_W2B, S_WOFA);
    if (CF) { HH_CLEAR(); }
    WIN2();
    if (W2F >= 3) { lds_wait(L, S_WOFB); if (W2F < 5 || A) GUBLK(A ? 3 : 4); }
    if (!CF) { HH_CLEAR(); }
    DRAIN();
    if (!CE2) HH_FLAG();
    if (CE2 && A) __builtin_amdgcn_s_sleep(DLY5);
  }
  if constexpr (A && CSL) CS_OUT(2);
  lds_signal(L, A ? S_HFA : S_HFB, w2);
  STAMP(16);
  u32x4 wdf[NWD][8];
  const int wdo = WS_WD + (cg * 4 + w2) * 57344;
  const u32 po = WS_FHH + ((tile2 * 16 + 4 * w2 + ((lane >> 1) & 3)) * 4 + 2 * tg + (lane & 1)) * 128;
  const u32 ho = WS_HH + ((tile2 * 2 + tg) * 4 + w2) * 57344;
#define WDLOAD(k, bf) dma8(rs, lane * 16, wdo + (k) * 8192, ((bf) + 2) * SLOT + w2 * 7168, L_EX + (bf) * 4096 + w2 * 1024)
#define THR() asm volatile("s_waitcnt vmcnt(%0)" :: "n"(THRN) : "memory")
#define WLOAD1(j, i) wdf[(j) % NWD][i] = __builtin_bit_cast(u32x4, __builtin_amdgcn_raw_buffer_load_b128(rs, lane * 16 + ((i) & 3) * 1024, wdo + (j) * 8192 + ((i) & 4) * 1024, 16))
#define THR0() asm volatile("s_waitcnt vmcnt(%0)" :: "n"(THRN0) : "memory")
#define WLOAD(j) _Pragma("unroll") for (int i = 0; i < 8; ++i) { THR0(); WLOAD1(j, i); }
  u32 have = 0;
#define PLOAD() peek(ws, po, lane < 8, tag + 7)
#define PMIN(pv) do { const u64 d = (pv) - tag; have = max(have, min8(d <= 8 ? (u32)d : 0u)); } while (0)
#define NEEDP(n) do { _Pragma("nounroll") for (u32 sp = 0; have < (n) && sp < SPIN_CAP; ++sp) { PMIN(PLOAD()); if (PSLP && have < (n)) __builtin_amdgcn_s_sleep(PSLP); } \
    __builtin_amdgcn_fence(__ATOMIC_ACQUIRE, "wavefront"); } while (0)
#define HBASE() u32 hb3 = w2 * 7168 + lane * 16, hb0 = hb3 + 2 * SLOT, hb2 = hb0 + 2 * SLOT, hb7 = L_EX + w2 * 1024 + lane * 16; asm volatile("" : "+v"(hb0), "+v"(hb2), "+v"(hb7))
#define HFRAG(bf, i) (*(LAS u32x4*)(L + ((i) < 7 ? ((bf) == 3 ? hb3 : (bf) == 2 ? hb2 : hb0 + (bf) * SLOT) + (i) * 1024 : hb7 + (bf) * 4096)))
#define HQLOAD(q, j) _Pragma("unroll") for (int i = 0; i < 8; ++i) hq[q][i] = __builtin_bit_cast(u32x4, __builtin_amdgcn_raw_buffer_load_b128(rs, lane * 16 + (i & 3) * 1024, ho + (j) * 8192 + (i & 4) * 1024, 16))
#define HQLOADT(q, j) _Pragma("unroll") for (int i = 0; i < 8; ++i) { THR(); hq[q][i] = __builtin_bit_cast(u32x4, __builtin_amdgcn_raw_buffer_load_b128(rs, lane * 16 + (i & 3) * 1024, ho + (j) * 8192 + (i & 4) * 1024, 16)); }
#define DOWN16(ac, wf, hf_) _Pragma("unroll") for (int m = 0; m < 16; ++m) ac[(m >> 1) & 1][m & 1] = mfma32(wf[2 * (m >> 2) + (m & 1)], hf_[2 * (m >> 2) + ((m >> 1) & 1)], ac[(m >> 1) & 1][m & 1])
#define PARTIAL_OUT(ac, b0, b1) _Pragma("unroll") for (int n = 0; n < 16; ++n) { const int T = n >> 3, k = (n >> 2) & 1, q = n & 3; \
    *(LAS f32x4*)&HFRAG(T ? b1 : b0, n & 7) = f32x4{ac[T][k][4 * q], ac[T][k][4 * q + 1], ac[T][k][4 * q + 2], ac[T][k][4 * q + 3]}; }
#define FREE(f) do { _Pragma("nounroll") for (u32 sp = 0; sp < (SPIN_CAP << 4); ++sp) { const u32x4 fr = *(volatile LAS u32x4*)(L + L_FLG + S_DONE4 * 16); \
          if (__builtin_amdgcn_readfirstlane(min(min(fr.x, fr.y), min(fr.z, fr.w))) >= (f)) break; __builtin_amdgcn_s_sleep(BSL); } asm volatile("" ::: "memory"); } while (0)
#define HQBAD(q) (!__all(max(max(max(hq[q][0].w, hq[q][1].w), max(hq[q][2].w, hq[q][3].w)), max(max(hq[q][4].w, hq[q][5].w), max(hq[q][6].w, hq[q][7].w))) != EMPTY))
#define HQCHECK(q, j) do { if (__builtin_expect(HQBAD(q), 0)) { _Pragma("nounroll") for (u32 sp = 0; sp < SPIN_CAP; ++sp) { \
      __builtin_amdgcn_s_sleep(RSL); asm volatile("" ::: "memory"); HQLOAD(q, j); if (!HQBAD(q)) break; } } SCHED(); } while (0)
  if constexpr (A) {
    const int T32 = tile * 4 + wave;
    u32x2 st[7];
    u32x4 hb[28];
    u64 fcl;
    const u32 stv = (7 * hh * 2048 + (lane & 31)) * 8;
#define STA(c_) _Pragma("unroll") for (int i = 0; i < 7; ++i) asm volatile("global_load_dwordx2 %0, %1, %2 sc1" : c_(st[i]) : "v"(stv), "s"(ws + WS_ST + (i * 2048 + T32 * 32) * 8) : "memory")
#define STW(k_) asm volatile("s_waitcnt vmcnt(%7)" : "+v"(st[0]), "+v"(st[1]), "+v"(st[2]), "+v"(st[3]), "+v"(st[4]), "+v"(st[5]), "+v"(st[6]) : "n"(k_) : "memory")
#define STBAD() (!__all(max(max(max(st[0].y, st[1].y), max(st[2].y, st[3].y)), max(max(st[4].y, st[5].y), st[6].y)) != EMPTY))
#define HBA(c_, q) _Pragma("unroll") for (int i = 0; i < 4; ++i) asm volatile("global_load_dwordx4 %0, %1, %2 offset:%3 sc1" : c_(hb[4 * (q) + i]) : "v"(lane * 16), "s"(ws + WS_H + (T32 * 28 + 4 * (q)) * 1024), "n"(i * 1024) : "memory")
    lds_wait2(L, S_HFA, S_HFB);
    __builtin_amdgcn_s_sleep(DLY3);
    if (!SPEC || r2 >= 14) {
#pragma nounroll
      for (u32 sp = 0; sp < SPIN_CAP; ++sp) { STA("=v"); STW(0); if (!STBAD()) break; __builtin_amdgcn_s_sleep(RSL); }
    }
    STA("=v");
    lds_signal(L, S_HSEEN, w2);
    STAMP(17);
#pragma unroll
    for (int q = 0; q < 7; ++q) { HBA("=v", q); }
    asm volatile("global_load_dwordx2 %0, %1, %2 sc1" : "=v"(fcl) : "v"(p4 * 128), "s"(ws + WS_FH + (T32 * 32 + 28) * 128) : "memory");
    u32 wb_[3] = {(u32)lane * 16, (u32)lane * 16 + 2 * SLOT, (u32)lane * 16 + 4 * SLOT};
    asm volatile("" : "+v"(wb_[1]), "+v"(wb_[2]));
#define WFRAG(sl, s) (*(LAS u32x4*)(L + wb_[(sl) >> 1] + ((sl) & 1) * SLOT + (s) * 1024))
    LAS char* csl = L + L_CS + hh * 16;
    u32x4 ring[7];
    f32x4 cq[4];
    float nmu, ve, k0, k1;
    u32 mx;
    const int pvo = WS_FHH + ((tile * 16 + r) * 4 + wave) * 128;
#define PROG(n) FLAG(pvo + 131072, n)
    f32x16 acc[2];
    float y[8], gu[8];
    u32x4 yb;
    u32x4 cw;
#define LT(i)
    auto gu_block = [&](auto JC) __attribute__((always_inline)) {
      constexpr int j = JC.value;
      sfor<0, 28>([&](auto SC) LAM {
        constexpr int s = SC.value;
        if constexpr (j == 0) { u32x4 t_ = hb[s]; asm volatile("s_waitcnt vmcnt(%1)" : "+v"(t_) : "n"(28 - s) : "memory"); hb[s] = t_; }
        acc[j & 1] = mfma32(ring[s % 7], hb[s], acc[j & 1]);
        if constexpr (j == 0) mx = max(mx, hb[s].w);
        if constexpr ((j == 2 || j == 4) && s == 12) cw = *(volatile LAS u32x4*)(L + L_FLG + (j == 2 ? S_WIN3 : S_REFILL) * 16);
        if constexpr ((j == 2 || j == 4) && s == 20) { if (__builtin_expect(!__builtin_amdgcn_readfirstlane(cw.x & cw.y & cw.z & cw.w), 0)) lds_wait(L, j == 2 ? S_WIN3 : S_REFILL); asm volatile("" ::: "memory"); }
        if constexpr (j == 1 && s < 7 - NCL) ST16(rs, (u32x4{EMPTY, EMPTY, EMPTY, EMPTY}), lane * 16, hho + (NCL + s) * 8192);
        if constexpr (s + 7 < 28) ring[s % 7] = WFRAG(gu_slot(j), s + 7);
        else if constexpr (j < 6) ring[s % 7] = WFRAG(gu_slot(j + 1), s - 21);
        if constexpr (s == 0 && (j == 1 || j == 2)) lds_signal(L, j == 1 ? S_WSEEN : S_FREE01, wave);
        if constexpr (s == 0 && j >= 3) *(volatile LAS u32*)(L + L_FLG + S_DONE4 * 16 + wave * 4) = j - 2u;
        if constexpr (j * 28 + s == BGO) lds_signal(L, S_KVP, wave);
        if constexpr (j > 0) {
          sfor<0, 8>([&](auto EC) LAM {
            constexpr int i = EC.value, k = s - 1 - 2 * i;
            const float ga = acc[(j - 1) & 1][i];
            if constexpr (k == 0) y[i] = ga * ga;
            if constexpr (k == 1) y[i] = fmaf(y[i], k1, k0);
            if constexpr (k == 2) y[i] = ga * y[i];
            if constexpr (k == 3) y[i] = fexp2(y[i]);
            if constexpr (k == 4) y[i] = fmaf(y[i], ve, ve);
            if constexpr (k == 5) { gu[i] = ga * acc[(j - 1) & 1][i + 8]; PIN(gu[i]); }
            if constexpr (k == 6) y[i] = frcp(y[i]);
            if constexpr (k == 7) y[i] = y[i] * gu[i];
            if constexpr (k >= 0 && k < 8 && k != 5) PIN(y[i]);
          });
          if constexpr (s == 21) { yb[0] = pk(y[0], y[1]); yb[1] = pk(y[2], y[3]); }
          if constexpr (s == 22) yb[2] = pk(y[4], y[5]);
          if constexpr (NCL == 1 && j == 2 && s == 22) WAITVM(2);
          if constexpr (j == 3 && s == 22 && NCL < 7 && NCL > 1) WAITVM(4);
          if constexpr (s == 23) { yb[3] = filled(pk(y[6], y[7])); ST16(rs, yb, lane * 16, hho + (j - 1) * 8192); }
          if constexpr (s == 24 && j <= (HELP > 4 ? HELP : 4)) FLAG(pvo, j);
        }
        if constexpr (j < 6 && s >= 17 && s < 21) cq[s - 17] = *(LAS f32x4*)(csl + (j + 1) * 128 + (s - 17) * 32);
        if constexpr (j < 6 && s >= 22 && s < 27) {
          sfor<0, 16>([&](auto EC) LAM {
            constexpr int e = EC.value;
            if constexpr ((e < 2 ? 22 : e < 4 ? 23 : 23 + (e >> 2)) == s) { float v = nmu * cq[e >> 2][e & 3]; PIN(v); acc[(j + 1) & 1][e] = v; }
          });
        }
        SCHED();
      });
      LT(j);
      if (j == 0) STAMP(18);
      if (j == 3) STAMP(26);
      if (j == 1) STAMP(28);
      if (j == 2) STAMP(29);
      if (j == 4) STAMP(30);
      if (j == 5) STAMP(31);
    };
#define HBW(q) asm volatile("s_waitcnt vmcnt(0)" : "+v"(hb[4 * (q)]), "+v"(hb[4 * (q) + 1]), "+v"(hb[4 * (q) + 2]), "+v"(hb[4 * (q) + 3]) :: "memory")
#define HBBAD(q) (!__all(max(max(hb[4 * (q)].w, hb[4 * (q) + 1].w), max(hb[4 * (q) + 2].w, hb[4 * (q) + 3].w)) != EMPTY))
#define HBC(q, c_) asm volatile("s_bitcmp0_b32 %6, %7\n\ts_cbranch_scc1 1f\n\tglobal_load_dwordx4 %0, %4, %5 sc1\n\tglobal_load_dwordx4 %1, %4, %5 offset:1024 sc1\n\t" \
      "global_load_dwordx4 %2, %4, %5 offset:2048 sc1\n\tglobal_load_dwordx4 %3, %4, %5 offset:3072 sc1\n1:" : "+v"(hb[4 * (q)]), "+v"(hb[4 * (q) + 1]), "+v"(hb[4 * (q) + 2]), "+v"(hb[4 * (q) + 3]) \
      : "v"(lane * 16), "s"(ws + WS_H + (T32 * 28 + 4 * (q)) * 1024), "s"(c_), "n"(q) : "memory", "scc")
#define REDO(m_) do { STA("+v"); _Pragma("unroll") for (int q = 0; q < 7; ++q) HBC(q, m_); STW(0); _Pragma("unroll") for (int q = 0; q < 7; ++q) HBW(q); } while (0)
#define FIX() do { _Pragma("nounroll") for (u32 sp = 0; sp < SPIN_CAP; ++sp) { u32 m = 0; \
        _Pragma("unroll") for (int q = 0; q < 7; ++q) m |= HBBAD(q) ? 1u << q : 0u; \
        if (!m && !STBAD()) break; if (sp) __builtin_amdgcn_s_sleep(RSL); m = __builtin_amdgcn_readfirstlane(m); REDO(m); } } while (0)
    auto first = [&]() LAM {
#pragma unroll
      for (int k = 0; k < 4; ++k) cq[k] = *(LAS f32x4*)(csl + k * 32);
#pragma unroll
      for (int s = 0; s < 7; ++s) ring[s] = WFRAG(0, s);
      f32x2 sf[7];
#pragma unroll
      for (int i = 0; i < 7; ++i) sf[i] = __builtin_bit_cast(f32x2, st[i]);
      float s1 = ((sf[0].x + sf[1].x) + (sf[2].x + sf[3].x)) + ((sf[4].x + sf[5].x) + sf[6].x);
      float s2 = ((sf[0].y + sf[1].y) + (sf[2].y + sf[3].y)) + ((sf[4].y + sf[5].y) + sf[6].y);
      { f32x2 a = sw32(s1); s1 = a.x + a.y; a = sw32(s2); s2 = a.x + a.y; }
      nmu = s1 * (-1.f / 448.f); ve = fmaxf(s2 * (1.f / 448.f) - nmu * nmu, 0.f) + 1e-5f;
      const float rstd = __builtin_amdgcn_rsqf(ve);
      k0 = -2.30220819f * rstd; k1 = -0.10294324f * rstd * rstd * rstd;
#pragma unroll
      for (int i = 0; i < 16; ++i) acc[0][i] = nmu * cq[i >> 2][i & 3];
      mx = 0;
      __builtin_amdgcn_s_setprio(GUPRIO);
      SCHED();
      gu_block(IC<0>{});
    };
    STW(29);
    if (__builtin_expect(STBAD(), 0)) {
      u32 m = 0;
#pragma unroll
      for (int i = 0; i < 7; ++i) { const u64 bm = __builtin_amdgcn_ballot_w64(st[i].y == EMPTY); m |= ((u32)bm ? 1u << (i >> 1) : 0u) | ((u32)(bm >> 32) ? 1u << ((7 + i) >> 1) : 0u); }
      m = __builtin_amdgcn_readfirstlane(m);
      asm volatile("s_waitcnt vmcnt(%0)" :: "n"(FXW) : "memory");
      REDO(m);
      FIX();
    }
    first();
    if (__builtin_expect(!__all(mx != EMPTY), 0)) { FIX(); first(); }
    asm volatile("s_waitcnt vmcnt(0)" : "+v"(fcl) :: "memory");
    if (__builtin_expect(!__all(fcl == tag + 1), 0)) poll_flags(ws, WS_FH + (T32 * 32 + 28 + p4) * 128, lane < 4, tag);
    gu_block(IC<1>{}); gu_block(IC<2>{}); gu_block(IC<3>{}); gu_block(IC<4>{}); gu_block(IC<5>{}); gu_block(IC<6>{});
#pragma unroll
    for (int i = 0; i < 8; ++i) { const float ga = acc[0][i]; y[i] = ga * acc[0][i + 8] * frcp(fmaf(fexp2(ga * fmaf(ga * ga, k1, k0)), ve, ve)); }
    yb = u32x4{pk(y[0], y[1]), pk(y[2], y[3]), pk(y[4], y[5]), filled(pk(y[6], y[7]))};
    ST16(rs, yb, lane * 16, hho + 6 * 8192);
    if (HELP == 7) FLAG(pvo, 7);
    if (BGO == 196) lds_signal(L, S_KVP, wave);
    __builtin_amdgcn_s_setprio(0);
    STAMP(19);
    if (r2 < 14) {
      f32x16 da[2][2] = {};
      u32x4 hq[2][8], wl[2][8];
      HBASE();
      __builtin_amdgcn_s_sleep(ADLY);
      lds_wait1(L, CE == 2 ? S_LAND : CE == 1 ? S_REPSEEN : S_CONS, w2);
      STAMP2(15);
      HQLOAD(0, 5);
      if (A6L == 0) HQLOAD(1, 6);
      if (CE == 2) lds_wait1(L, S_REPSEEN, w2);
#pragma unroll
      for (int i = 0; i < 8; ++i) wl[0][i] = HFRAG(2, i);
      SCHED();
      STAMP2(16);
      HQCHECK(0, 5);
      if (A6L == 1) HQLOAD(1, 6);
      if (CE) lds_wait1(L, S_CONS, w2);
#pragma unroll
      for (int i = 0; i < 8; ++i) wl[1][i] = HFRAG(0, i);
      SCHED();
      DOWN16(da, wl[0], hq[0]);
      SCHED();
      STAMP2(18);
      HQCHECK(1, 6);
      STAMP2(31);
      DOWN16(da, wl[1], hq[1]);
      PARTIAL_OUT(da, 0, 2);
      STAMP(20);
    }
  } else {
    lds_wait2(L, S_HFA, S_HFB);
    if (W2F < 3) { GUBLK(3); if (W3THR) WAITVM(3); }
    if (W2F < 3 || W2F == 5) GUBLK(4);
#define REFILL() do { lds_wait<BSL>(L, S_WSEEN); GUBLK(5); lds_wait<BSL>(L, S_FREE01); GUBLK(6); } while (0)
    if (r2 >= 14) {
      WAITVM(0); lds_signal(L, S_WIN3, w2);
      REFILL();
      WAITVM(0); lds_signal(L, S_REFILL, w2);
    } else {
#pragma unroll
      for (int n = 0; n < 24; ++n) {
        if (n == 8 * NW3) { STAMP2(25); lds_wait(L, S_HSEEN); __builtin_amdgcn_s_sleep(DLY4); }
        asm volatile("s_waitcnt vmcnt(%4)\n\tglobal_load_dwordx4 %0, %1, %2 offset:%3 sc1" : "=v"(wdf[n >> 3][n & 7]) : "v"(lane * 16), "s"(ws + wdo + 4096 + (n >> 3) * 8192), "n"((n & 7) * 1024 - 4096), "n"(THRN0) : "memory");
        if (n == 7) { WAITVM(8); lds_signal(L, S_WIN3, w2); }
      }
      STAMP2(23);
      REFILL();
      f32x16 acc[2][2] = {};
      u32x4 hq[2][8];
      STAMP2(16);
      HBASE();
      u64 pv;
      const char* hs = ws + ho + 4096;
#define PEEK() do { pv = tag + 7; asm volatile("s_mov_b64 exec, 0xff\n\tglobal_atomic_add_x2 %0, %1, %2, %3 sc0\n\ts_mov_b64 exec, -1" : "+v"(pv) : "v"(po), "v"(0ull), "s"(ws) : "memory"); } while (0)
#define PEEKED(k_, n_) do { asm volatile("s_waitcnt vmcnt(%1)" : "+v"(pv) : "n"(k_) : "memory"); PMIN(pv); NEEDP(n_); } while (0)
#define HQA(c_, q, j) _Pragma("unroll") for (int i = 0; i < 8; ++i) asm volatile("global_load_dwordx4 %0, %1, %2 offset:%3 sc1" : c_(hq[q][i]) : "v"(lane * 16), "s"(hs + (j) * 8192), "n"(i * 1024 - 4096) : "memory")
#define HQW(q, k_) asm volatile("s_waitcnt vmcnt(%8)" : "+v"(hq[q][0]), "+v"(hq[q][1]), "+v"(hq[q][2]), "+v"(hq[q][3]), "+v"(hq[q][4]), "+v"(hq[q][5]), "+v"(hq[q][6]), "+v"(hq[q][7]) : "n"(k_) : "memory")
#define HQOK(q, j, k_) do { HQW(q, k_); if (__builtin_expect(HQBAD(q), 0)) { _Pragma("nounroll") for (u32 sp = 0; sp < SPIN_CAP; ++sp) { \
        __builtin_amdgcn_s_sleep(RSL); HQA("+v", q, j); HQW(q, 0); if (!HQBAD(q)) break; } } SCHED(); } while (0)
      PEEK();
      FREE(1); WDLOAD(3, 0);
      WAITVM(8);
#pragma unroll
      for (int k = 0; k < 3; ++k) asm volatile("" : "+v"(wdf[k][0]), "+v"(wdf[k][1]), "+v"(wdf[k][2]), "+v"(wdf[k][3]), "+v"(wdf[k][4]), "+v"(wdf[k][5]), "+v"(wdf[k][6]), "+v"(wdf[k][7]) :: "memory");
      lds_signal(L, S_REFILL, w2);
      PEEKED(8, 1);
      STAMP2(1);
      PEEK();
      HQA("=v", 0, 0);
      FREE(2); WDLOAD(4, 1);
      PEEKED(16, 2);
      STAMP2(2);
      HQA("=v", 1, 1);
      PEEK();
      u32 fr3;
      { const u32x4 fr = *(volatile LAS u32x4*)(L + L_FLG + S_DONE4 * 16); fr3 = !W5D || __builtin_amdgcn_readfirstlane(min(min(fr.x, fr.y), min(fr.z, fr.w))) >= 3; }
      if (fr3) { if (!W5D) FREE(3); WDLOAD(5, 2); }
      if (fr3) WAITVM(25); else WAITVM(17);
      HQOK(0, 0, 25);
      __builtin_amdgcn_s_setprio(DP0);
      DOWN16(acc, wdf[0], hq[0]);
      __builtin_amdgcn_s_setprio(0);
#pragma unroll
      for (int i = 0; i < 8; ++i) wdf[0][i] = HFRAG(0, i);
      SCHED();
      STAMP2(8);
      if (!fr3) { FREE(3); WDLOAD(5, 2); }
      PEEKED(8, 3);
      lds_signal(L, S_LAND, w2);
      STAMP2(3);
      HQA("=v", 0, 2);
      PEEK();
      asm volatile("s_waitcnt lgkmcnt(0)" : "+v"(wdf[0][7]) :: "memory");
      WDLOAD(6, 0);
      HQOK(1, 1, 25);
      __builtin_amdgcn_s_setprio(DP0);
      DOWN16(acc, wdf[1], hq[1]);
#pragma unroll
      for (int i = 0; i < 8; ++i) wdf[1][i] = HFRAG(1, i);
      SCHED();
      STAMP2(9);
      PEEKED(8, 4);
      lds_signal(L, S_REPSEEN, w2);
      STAMP2(4);
      HQA("=v", 1, 3);
      __builtin_amdgcn_s_setprio(DNPRIO);
      HQOK(0, 2, 8);
      lds_signal(L, S_CONS, w2);
      SCHED();
      DOWN16(acc, wdf[2], hq[0]);
      SCHED();
      STAMP2(10);
      FREE(4);
      HQA("=v", 0, 4);
      HQOK(1, 3, 8);
      DOWN16(acc, wdf[0], hq[1]);
      SCHED();
      STAMP2(11);
      HQOK(0, 4, 0);
      DOWN16(acc, wdf[1], hq[0]);
      PARTIAL_OUT(acc, 1, 3);
      STAMP(20);
    }
  }
  if (r2 < 14) {
    BAR();
    STAMP2(7);
    float c[8] = {blo(hres.x), bhi(hres.x), blo(hres.y), bhi(hres.y), blo(hres.z), bhi(hres.z), blo(hres.w), bhi(hres.w)};
    const int rl = (kh * 32 + (tb & 1) * 16 + l15) * 16;
    f32x4 v0 = z4, v1 = z4;
#pragma unroll
    for (int h = 0; h < 2; ++h) {
      const int bf = (tb >> 1) * 2 + h;
      u32 rq = (bf == 3 ? 0 : (bf + 2) * SLOT) + g * 1024 + rl;
      const u32 rq7 = g == 3 ? L_EX + bf * 4096 + rl : rq + 4096, st7 = g == 3 ? 1024 : 7168;
      asm volatile("" : "+v"(rq));
#pragma unroll
      for (int w = 0; w < 4; ++w) { v0 += *(LAS f32x4*)(L + rq + w * 7168); v1 += *(LAS f32x4*)(L + rq7 + w * st7); }
    }
#pragma unroll
    for (int h = 0; h < 2; ++h) {
      const f32x4 v = h ? v1 : v0;
      const f32x2 p = swp32(v[0], v[2]), q = swp32(v[1], v[3]), t0 = swp16(p.x, q.x), t1 = swp16(p.y, q.y);
      c[4 * h] += t0.x; c[4 * h + 1] += t0.y; c[4 * h + 2] += t1.x; c[4 * h + 3] += t1.y;
    }
    { const u32x4 ov = {pk(c[0], c[1]), pk(c[2], c[3]), pk(c[4], c[5]), pk(c[6], c[7])}; asm volatile("global_store_dwordx4 %0, %1, %2 sc1\n\ts_nop 1" :: "v"(xo), "v"(ov), "s"(out) : "memory"); }
    if (A && b == 0 && tid == 0) __hip_atomic_store((GAS u64*)(ws + WS_E), tag, RLX_AGENT);
  }
  STAMP(21);
  };
  if (wave < 4) role(IC<1>{}); else role(IC<0>{});
}

extern "C" void kernel_launch(void* const* d_in, const int* in_sizes, int n_in, void* d_out, int out_size, void* d_ws, size_t ws_size, hipStream_t stream) {
  (void)hipFuncSetAttribute((const void*)fused_block, hipFuncAttributeMaxDynamicSharedMemorySize, LDS_BYTES);
  fused_block<<<256, 512, LDS_BYTES, stream>>>((const char*)d_in[0], (const char*)d_in[1], (const char*)d_in[2], (const char*)d_in[3], (const char*)d_in[5], (const char*)d_in[6],
                                               (const char*)d_in[7], (char*)d_out, (char*)d_ws);
}
```

```cpp
#include <hip/hip_runtime.h>
#include <hip/hip_bf16.h>
#include <stdint.h>

typedef unsigned u32;
typedef unsigned long long u64;
typedef u32 u32x4 __attribute__((ext_vector_type(4)));
typedef u32 u32x2 __attribute__((ext_vector_type(2)));
typedef float f32x2 __attribute__((ext_vector_type(2)));
typedef float f32x4 __attribute__((ext_vector_type(4)));
typedef float f32x16 __attribute__((ext_vector_type(16)));
typedef short s16x4 __attribute__((ext_vector_type(4)));
typedef __bf16 bf16x2 __attribute__((ext_vector_type(2)));
typedef __bf16 bf16x8 __attribute__((ext_vector_type(8)));
#define LAS __attribute__((address_space(3)))
#define GAS __attribute__((address_space(1)))
#define DI __device__ __forceinline__
#define RLX_AGENT __ATOMIC_RELAXED, __HIP_MEMORY_SCOPE_AGENT

constexpr int WS_E = 0;
constexpr int WS_FKV = 8192;
constexpr int WS_FO = WS_FKV + 131072;
constexpr int WS_FH = WS_FO + 131072;
constexpr int WS_FG = WS_FH + 262144;
constexpr int WS_FD = WS_FG + 114688;
constexpr int WS_FHH = WS_FD + 57344;
constexpr int WS_CS = WS_FHH + 262144;
constexpr int WS_ST = WS_CS + 200704;
constexpr int WS_WGU = WS_ST + 229376;
constexpr int WS_WD = WS_WGU + 3211264;
constexpr int WS_KVP = WS_WD + 1605632;
constexpr int WS_O = WS_KVP + 1048576;
constexpr int WS_H = WS_O + 2097152;
constexpr int WS_HH = WS_H + 1835008;
#define UNITX4
#define WDOFF(cg, fb, bl) (((((cg) * 4 + (fb) / 28) * 7 + (fb) % 7) * 4 + (fb) / 7 % 4) * 2 + (bl))
#define HHOFF(T32, fb) (((((T32) >> 1) * 4 + (fb) / 28) * 7 + (fb) % 7) * 8 + ((fb) / 7 & 3) * 2 + ((T32) & 1))
constexpr int WS_STAMP = WS_HH + 7340032;
constexpr int WS_LOG = WS_STAMP + 256 * 8 * 32 * 8;
constexpr int WS_USED = WS_LOG + 4096;
constexpr int SLOT = 28672;
constexpr int L_WK = 4 * SLOT, L_WV = 0, L_WQ = 14336;
constexpr int L_PKV = 2 * SLOT;
constexpr int L_WO = 3 * SLOT;
constexpr int L_EX = 5 * SLOT;
constexpr int L_KVA = 5 * SLOT;
constexpr int L_ST1 = 5 * SLOT + 4096;
constexpr int L_WPHI = 5 * SLOT + 5120;
constexpr int L_CSP = 5 * SLOT + 7168;
constexpr int L_CS = 159744;
constexpr int L_FLG = 160768;
constexpr int L_JUNK = 161280;
constexpr int L_STAMP = 161536;
constexpr int L_RED = SLOT;
constexpr int LDS_BYTES = 163840;
constexpr int gu_slot(int j) { return j % 5; }
constexpr int CODE_PF = 45056;
constexpr u32 SPIN_CAP = 1u << 16;
#define LL 1
#define AP 1

#define STAMP(i) do {} while (0)
#define STAMP2(i) do {} while (0)
#define STAMP2X(i) do {} while (0)

DI f32x4 mfma16(u32x4 a, u32x4 b, f32x4 c) { return __builtin_amdgcn_mfma_f32_16x16x32_bf16(__builtin_bit_cast(bf16x8, a), __builtin_bit_cast(bf16x8, b), c, 0, 0, 0); }
DI f32x16 mfma32(u32x4 a, u32x4 b, f32x16 c) { return __builtin_amdgcn_mfma_f32_32x32x16_bf16(__builtin_bit_cast(bf16x8, a), __builtin_bit_cast(bf16x8, b), c, 0, 0, 0); }
DI u32 pk(float lo, float hi) { f32x2 p = {lo, hi}; return __builtin_bit_cast(u32, __builtin_convertvector(p, bf16x2)); }
DI float blo(u32 v) { return __builtin_bit_cast(float, v << 16); }
DI float bhi(u32 v) { return __builtin_bit_cast(float, v & 0xffff0000u); }
DI float dot2(u32 a, u32 b, float c) { return __builtin_amdgcn_fdot2_f32_bf16(__builtin_bit_cast(bf16x2, a), __builtin_bit_cast(bf16x2, b), c, false); }
DI u32x2 trread(LAS char* p) { return __builtin_bit_cast(u32x2, __builtin_amdgcn_ds_read_tr16_b64_v4i16((LAS s16x4*)p)); }
DI u32x4 trfrag(LAS char* p, int step) { u32x2 a = trread(p), b = trread(p + step); return u32x4{a.x, a.y, b.x, b.y}; }
DI float fexp2(float x) { return __builtin_amdgcn_exp2f(x); }
DI float frcp(float x) { return __builtin_amdgcn_rcpf(x); }
template <int C> DI float dppror(float x) { return __builtin_bit_cast(float, __builtin_amdgcn_update_dpp(0, __builtin_bit_cast(int, x), 0x120 + C, 0xf, 0xf, false)); }
DI float row_max(float x) { x = fmaxf(x, dppror<8>(x)); x = fmaxf(x, dppror<4>(x)); x = fmaxf(x, dppror<2>(x)); return fmaxf(x, dppror<1>(x)); }
DI float row_min(float x) { x = fminf(x, dppror<8>(x)); x = fminf(x, dppror<4>(x)); x = fminf(x, dppror<2>(x)); return fminf(x, dppror<1>(x)); }
DI float row_sum(float x) { x += dppror<8>(x); x += dppror<4>(x); x += dppror<2>(x); return x + dppror<1>(x); }
DI f32x2 sw16(float x) { u32 u = __builtin_bit_cast(u32, x); auto r = __builtin_amdgcn_permlane16_swap(u, u, false, false); return f32x2{__builtin_bit_cast(float, (u32)r[0]), __builtin_bit_cast(float, (u32)r[1])}; }
DI f32x2 sw32(float x) { u32 u = __builtin_bit_cast(u32, x); auto r = __builtin_amdgcn_permlane32_swap(u, u, false, false); return f32x2{__builtin_bit_cast(float, (u32)r[0]), __builtin_bit_cast(float, (u32)r[1])}; }
DI float grp_sum(float x) { f32x2 a = sw16(x); x = a.x + a.y; a = sw32(x); return a.x + a.y; }
DI float grp_max(float x) { f32x2 a = sw16(x); x = fmaxf(a.x, a.y); a = sw32(x); return fmaxf(a.x, a.y); }
DI float grp_min(float x) { f32x2 a = sw16(x); x = fminf(a.x, a.y); a = sw32(x); return fminf(a.x, a.y); }

#define BAR() asm volatile("s_waitcnt lgkmcnt(0)\n\ts_barrier" ::: "memory")
#define WAITVM(n) asm volatile("s_waitcnt vmcnt(" #n ")" ::: "memory")
#define DRAIN() do { asm volatile("s_waitcnt vmcnt(0)" ::: "memory"); __builtin_amdgcn_s_waitcnt(0x0F70); } while (0)
#define SCHED() __builtin_amdgcn_sched_barrier(0)
DI void dma16(const void* base, u32 voff, u32 lds) {
  u32 keep;
  asm volatile("s_mov_b32 %0, m0\n\ts_mov_b32 m0, %3\n\ts_nop 0\n\tglobal_load_lds_dwordx4 %1, %2\n\ts_mov_b32 m0, %0" : "=&s"(keep) : "v"(voff), "s"(base), "s"(lds) : "memory");
}
DI void dma16c(const void* base, u32 voff, u32 lds) {
  u32 keep;
  asm volatile("s_mov_b32 %0, m0\n\ts_mov_b32 m0, %3\n\ts_nop 0\n\tglobal_load_lds_dwordx4 %1, %2 sc1\n\ts_mov_b32 m0, %0" : "=&s"(keep) : "v"(voff), "s"(base), "s"(lds) : "memory");
}
DI void dma4(const void* base, u32 voff, u32 lds) {
  u32 keep;
  asm volatile("s_mov_b32 %0, m0\n\ts_mov_b32 m0, %3\n\ts_nop 0\n\tglobal_load_lds_dword %1, %2\n\ts_mov_b32 m0, %0" : "=&s"(keep) : "v"(voff), "s"(base), "s"(lds) : "memory");
}
DI void dma16c_if(u32 go, const void* base, u32 voff, u32 lds) {
  u32 keep;
  asm volatile("s_cmp_eq_u32 %4, 0\n\ts_cbranch_scc1 1f\n\ts_mov_b32 %0, m0\n\ts_mov_b32 m0, %3\n\ts_nop 0\n\tglobal_load_lds_dwordx4 %1, %2 sc1\n\ts_mov_b32 m0, %0\n1:"
               : "=&s"(keep) : "v"(voff), "s"(base), "s"(lds), "s"(go) : "memory", "scc");
}
template <int SI, int LI> DI void dma7(__amdgpu_buffer_rsrc_t rs, u32 voff, u32 so, u32 lds) {
  u32 t;
  asm volatile("s_add_u32 m0, %4, %6\n\ts_add_u32 %0, %3, %5\n\tbuffer_load_dwordx4 %1, %2, %0 offen sc1 lds\n\tbuffer_load_dwordx4 %1, %2, %0 offen offset:1024 sc1 lds\n\t"
               "buffer_load_dwordx4 %1, %2, %0 offen offset:2048 sc1 lds\n\tbuffer_load_dwordx4 %1, %2, %0 offen offset:3072 sc1 lds\n\t"
               "s_add_u32 m0, m0, 4096\n\ts_add_u32 %0, %0, 4096\n\tbuffer_load_dwordx4 %1, %2, %0 offen sc1 lds\n\tbuffer_load_dwordx4 %1, %2, %0 offen offset:1024 sc1 lds\n\t"
               "buffer_load_dwordx4 %1, %2, %0 offen offset:2048 sc1 lds"
               : "=&s"(t) : "v"(voff), "s"(rs), "s"(so), "s"(lds), "n"(SI), "n"(LI) : "memory", "scc");
}
DI void dma8(__amdgpu_buffer_rsrc_t rs, u32 voff, u32 so, u32 lds, u32 lds7) {
  dma7<0, 0>(rs, voff, so, lds);
  u32 t;
  asm volatile("s_mov_b32 m0, %4\n\ts_add_u32 %0, %3, 7168\n\tbuffer_load_dwordx4 %1, %2, %0 offen sc1 lds" : "=&s"(t) : "v"(voff), "s"(rs), "s"(so), "s"(lds7) : "memory", "scc");
}
DI u32 vm_count() {
  u32 t;
  asm volatile("s_getreg_b32 %0, hwreg(HW_REG_IB_STS)" : "=s"(t) :: "memory");
  return (t & 15u) | ((t >> 18) & 48u);
}
DI u32 min8(u32 c) {
  c = min(c, (u32)__builtin_amdgcn_update_dpp(0, (int)c, 0x121, 0xf, 0xf, false)); c = min(c, (u32)__builtin_amdgcn_update_dpp(0, (int)c, 0x122, 0xf, 0xf, false));
  c = min(c, (u32)__builtin_amdgcn_update_dpp(0, (int)c, 0x124, 0xf, 0xf, false));
  return __builtin_amdgcn_readfirstlane(min(c, (u32)__builtin_amdgcn_update_dpp(0, (int)c, 0x128, 0xf, 0xf, false)));
}
DI void dma4c(const void* base, u32 voff, u32 lds) {
  u32 keep;
  asm volatile("s_mov_b32 %0, m0\n\ts_mov_b32 m0, %3\n\ts_nop 0\n\tglobal_load_lds_dword %1, %2 sc1\n\ts_mov_b32 m0, %0" : "=&s"(keep) : "v"(voff), "s"(base), "s"(lds) : "memory");
}
#define LD16(rs, off) __builtin_bit_cast(u32x4, __builtin_amdgcn_raw_buffer_load_b128(rs, off, 0, 16))
#define ST16(rs, v, off, soff) do { const u32x4 d_ = __builtin_bit_cast(u32x4, v); __builtin_amdgcn_raw_buffer_store_b128(d_, rs, off, soff, 16); asm volatile("s_nop 1" :: "v"(d_) : "memory"); } while (0)

#define FLAG(off, n) __builtin_amdgcn_raw_buffer_store_b64(__builtin_bit_cast(u32x2, tag + (n)), rs, fvo, off, 16)
#define FLAGC(c, off, n) __builtin_amdgcn_raw_buffer_store_b64(__builtin_bit_cast(u32x2, tag + (n)), rs, (c) ? fvo : NOWHERE, off, 16)
constexpr int NOWHERE = 0x7ffffff0;
constexpr u32 EMPTY = 0xffffffffu;
DI u32 filled(u32 w) { return min(w, EMPTY - 1u); }
DI u64 peek(const char* ws, u32 off, bool on, u64 dflt) {
  u64 z = 0, v = dflt;
  asm volatile("" : "+v"(z));
  if (on) v = __hip_atomic_fetch_add((GAS u64*)(ws + off), z, __ATOMIC_RELAXED, AP == 2 ? __HIP_MEMORY_SCOPE_SYSTEM : __HIP_MEMORY_SCOPE_AGENT);
  return v;
}
DI void poll_flags(const char* ws, u32 off, bool on, u64 tag) {
#pragma nounroll
  for (u32 sp = 0; sp < SPIN_CAP; ++sp) {
    u64 v = tag + 1;
    if (on || !LL) v = __hip_atomic_load((const GAS u64*)(ws + off), RLX_AGENT);
    if (__all(v == tag + 1)) break;
  }
  __builtin_amdgcn_fence(__ATOMIC_ACQUIRE, "wavefront");
}
template <int SL = 1> DI void lds_wait(LAS char* L, int sig) {
#pragma nounroll
  for (u32 sp = 0; sp < (SPIN_CAP << 4); ++sp) {
    const u32x4 v = *(volatile LAS u32x4*)(L + L_FLG + sig * 16);
    if (__builtin_amdgcn_readfirstlane(v.x & v.y & v.z & v.w)) break;
    __builtin_amdgcn_s_sleep(SL);
  }
  asm volatile("" ::: "memory");
}
DI void lds_wait2(LAS char* L, int sig, int sig2) {
#pragma nounroll
  for (u32 sp = 0; sp < (SPIN_CAP << 4); ++sp) {
    const u32x4 v = *(volatile LAS u32x4*)(L + L_FLG + sig * 16) & *(volatile LAS u32x4*)(L + L_FLG + sig2 * 16);
    if (__builtin_amdgcn_readfirstlane(v.x & v.y & v.z & v.w)) break;
    __builtin_amdgcn_s_sleep(1);
  }
  asm volatile("" ::: "memory");
}
DI void lds_wait1(LAS char* L, int sig, int w) {
#pragma nounroll
  for (u32 sp = 0; sp < (SPIN_CAP << 4); ++sp) {
    if (__builtin_amdgcn_readfirstlane(*(volatile LAS u32*)(L + L_FLG + sig * 16 + w * 4))) break;
    __builtin_amdgcn_s_sleep(1);
  }
  asm volatile("" ::: "memory");
}
DI void lds_signal(LAS char* L, int sig, int w) {
  asm volatile("" ::: "memory");
  *(volatile LAS u32*)(L + L_FLG + sig * 16 + w * 4) = 1u;
}

template <int N> struct IC { static constexpr int value = N; };
template <int I, int N, class F> DI void sfor(F&& f) { if constexpr (I < N) { f(IC<I>{}); sfor<I + 1, N>(f); } }
#define LAM __attribute__((always_inline))
#define PIN(v) asm volatile("" : "+v"(v))
constexpr float LOG2E = 1.4426950408889634f;
template <int K> DI void macc(f32x4& c, u32x4 a, u32x4 b) {
  if (K == 0) asm volatile("v_mfma_f32_16x16x32_bf16 %0, %1, %2, 0" : "=&v"(c) : "v"(a), "v"(b));
  else if (K == 1) asm volatile("v_mfma_f32_16x16x32_bf16 %0, %1, %2, %0" : "+v"(c) : "v"(a), "v"(b));
  else c = mfma16(a, b, c);
}
template <int GAP, int SIG, int NW, class E, class F> DI void project(LAS char* L, u32 w0, u32 w1, const u32x4 (&xf)[2][14], int tp, int lane, int csl, f32x4 (&t)[2][2], f32x2 (&st)[2], E&& pre, F&& mid) {
  u32 wq = (tp & 1 ? w1 : w0) + (tp >> 1) * (7 * 2048 + GAP), ca = csl + (lane >> 4) * 16, sa = L_ST1 + (tp * 32 + (lane & 15)) * 8, fa = L_FLG + SIG * 16 + (lane & (NW - 1)) * 4;
  u32 cw = ca + tp * 64, fw = L_FLG + SIG * 16 + tp * 4, one = 1;
  u32x4 ones = {0x3f803f80u, 0x3f803f80u, 0x3f803f80u, 0x3f803f80u};
  LAS char *p0 = L + w0, *p1 = L + w1, *pq = L + wq, *pca = L + ca, *psa = L + sa, *pfa = L + fa, *pcw = L + cw, *pfw = L + fw;
  asm volatile("" : "+v"(p0), "+v"(p1), "+v"(pq), "+v"(pca), "+v"(psa), "+v"(pfa), "+v"(pcw), "+v"(pfw), "+v"(ones), "+v"(one));
  pre();
  u32x4 cf[7];
  u32x2 ra[7], rb[7];
  f32x4 cs, cp[4];
  u32 sw;
#define WAT(n) (((n) & 1 ? p1 : p0) + ((n) >> 1) * 2048 + ((n) >= 14 ? GAP : 0))
#pragma unroll
  for (int i = 0; i < 7; ++i) cf[i] = trfrag(pq + i * 2048, 256);
  SCHED();
  sfor<0, 7>([&](auto IC_) LAM {
    constexpr int i = IC_.value;
    macc<i == 0 ? 0 : i == 6 ? 2 : 1>(cs, cf[i], ones);
    SCHED();
    if (i < 6) { ra[i] = trread(WAT(i)); rb[i] = trread(WAT(i) + 256); }
    SCHED();
  });
  sfor<0, 28>([&](auto NC) LAM {
    constexpr int n = NC.value, K = n < 2 ? 0 : n < 26 ? 1 : 2;
    const u32x4 f = {ra[n % 7].x, ra[n % 7].y, rb[n % 7].x, rb[n % 7].y};
    macc<K>(t[0][n & 1], f, xf[0][n >> 1]);
    SCHED();
    if constexpr (n + 6 < 28) ra[(n + 6) % 7] = trread(WAT(n + 6));
    if constexpr (n == 24) st[0] = *(LAS f32x2*)(psa);
    if constexpr (n == 22 || n == 23) cp[n - 22] = *(LAS f32x4*)(pca + (n - 22) * 64);
    if constexpr (n >= 26) { float a = cp[n - 26][0] + cp[n - 24][0], b = cp[n - 26][1] + cp[n - 24][1]; PIN(a); PIN(b); cp[n - 26][0] = a; cp[n - 26][1] = b; }
    SCHED();
    macc<K>(t[1][n & 1], f, xf[1][n >> 1]);
    SCHED();
    if constexpr (n + 6 < 28) rb[(n + 6) % 7] = trread(WAT(n + 6) + 256);
    if constexpr (n == 24) st[1] = *(LAS f32x2*)(psa + 128);
    if constexpr (n == 22 || n == 23) cp[n - 20] = *(LAS f32x4*)(pca + (n - 20) * 64);
    if constexpr (n >= 26) { float a = cp[n - 26][2] + cp[n - 24][2], b = cp[n - 26][3] + cp[n - 24][3]; PIN(a); PIN(b); cp[n - 26][2] = a; cp[n - 26][3] = b; }
    if constexpr (n == 2) { if ((lane & 15) == 0) *(LAS f32x4*)(pcw) = cs; }
    if constexpr (n == 3) { asm volatile("" ::: "memory"); *(volatile LAS u32*)(pfw) = one; }
    if constexpr (n == 16) sw = *(volatile LAS u32*)(pfa);
    if constexpr (n == 21) { if (__builtin_expect(__builtin_amdgcn_ballot_w64(sw == 0) != 0, 0)) { lds_wait(L, SIG); if (NW > 4) lds_wait(L, SIG + 1); } asm volatile("" ::: "memory"); }
    mid(n, pfw - SIG * 16, one);
    SCHED();
  });
#pragma unroll
  for (int tb = 0; tb < 2; ++tb)
#pragma unroll
    for (int k = 0; k < 2; ++k) t[tb][k] += cp[k] * st[tb].x;
}
DI f32x2 swp16(float a, float b) { auto r = __builtin_amdgcn_permlane16_swap(__builtin_bit_cast(u32, a), __builtin_bit_cast(u32, b), false, false); return f32x2{__builtin_bit_cast(float, (u32)r[0]), __builtin_bit_cast(float, (u32)r[1])}; }
DI f32x2 swp32(float a, float b) { auto r = __builtin_amdgcn_permlane32_swap(__builtin_bit_cast(u32, a), __builtin_bit_cast(u32, b), false, false); return f32x2{__builtin_bit_cast(float, (u32)r[0]), __builtin_bit_cast(float, (u32)r[1])}; }
DI float swadd32(float a, float b) { const f32x2 s = swp32(a, b); return s.x + s.y; }
DI float swadd16(float a, float b) { const f32x2 s = swp16(a, b); return s.x + s.y; }
DI float mx2(float a, float b) { float r; asm("v_max_f32 %0, %1, %2" : "=v"(r) : "v"(a), "v"(b)); return r; }
DI float mx3(float a, float b, float c) { return fmaxf(fmaxf(a, b), c); }
DI float mn3(float a, float b, float c) { return fminf(fminf(a, b), c); }
template <class F> DI void feature_map(const f32x4 (&t)[2][2], const f32x2 (&st)[2], const u32x4 (&wpf)[2], int g, u32x4 (&P)[2][2], F&& mid) {
  const float pad = g < 3 ? 0.f : -3.0e38f;
  const f32x4 z4 = {0.f, 0.f, 0.f, 0.f};
  const float sc[2] = {st[0].y * LOG2E, st[1].y * LOG2E}, scg = g < 2 ? sc[0] : sc[1];
  float p[2][8], m[4], mp[4];
#pragma unroll
  for (int tb = 0; tb < 2; ++tb) {
    const u32x4 tq = {pk(t[tb][0][0], t[tb][0][1]), pk(t[tb][0][2], t[tb][0][3]), pk(t[tb][1][0], t[tb][1][1]), pk(t[tb][1][2], t[tb][1][3])};
    const f32x4 p0 = mfma16(wpf[0], tq, z4), p1 = mfma16(wpf[1], tq, z4);
#pragma unroll
    for (int i = 0; i < 8; ++i) p[tb][i] = i < 4 ? p0[i] : p1[i - 4];
  }
  mid(0);
#pragma unroll
  for (int tb = 0; tb < 2; ++tb) {
    const float* q = p[tb];
    m[2 * tb] = mx3(mx3(q[0], q[1], q[2]), mx3(q[3], q[4], q[5]), mx3(q[5], q[6], q[7]));
    m[2 * tb + 1] = mx3(mx3(-q[0], -q[1], -q[2]), mx3(-q[3], -q[4], -q[5]), mx3(-q[5], -q[6], -q[7]));
  }
  {
    const f32x2 a = swp16(m[0], m[1]), b = swp16(m[2], m[3]);
    const f32x2 c = swp32(mx2(a.x, a.y), mx2(b.x, b.y));
    const float d = mx2(c.x, c.y) * -scg;
    const f32x2 e = swp32(d, d), f0 = swp16(e.x, e.x), f1 = swp16(e.y, e.y);
    m[0] = f0.x; m[1] = f0.y; m[2] = f1.x; m[3] = f1.y;
#pragma unroll
    for (int c2 = 0; c2 < 4; ++c2) mp[c2] = m[c2] + pad;
  }
  float ex[4][8], sm[4];
#pragma unroll
  for (int c = 0; c < 4; ++c) {
    mid(1 + c);
#pragma unroll
    for (int i = 0; i < 8; ++i) {
      ex[c][i] = fexp2(fmaf(c & 1 ? -p[c >> 1][i] : p[c >> 1][i], sc[c >> 1], i & 2 ? mp[c] : m[c]));
    }
    sm[c] = ((ex[c][0] + ex[c][1]) + (ex[c][2] + ex[c][3])) + ((ex[c][4] + ex[c][5]) + (ex[c][6] + ex[c][7]));
  }
  mid(5);
  {
    const float d = frcp(swadd32(swadd16(sm[0], sm[1]), swadd16(sm[2], sm[3])));
    const f32x2 e = swp32(d, d), f0 = swp16(e.x, e.x), f1 = swp16(e.y, e.y);
    sm[0] = f0.x; sm[1] = f0.y; sm[2] = f1.x; sm[3] = f1.y;
  }
#pragma unroll
  for (int c = 0; c < 4; ++c)
#pragma unroll
    for (int e = 0; e < 4; ++e) P[c >> 1][c & 1][e] = pk(ex[c][2 * e] * sm[c], ex[c][2 * e + 1] * sm[c]);
}
DI u32x4 piece(const u32x4 (&rp)[8], int e) {
  u32x4 f;
#pragma unroll
  for (int m = 0; m < 4; ++m) f[m] = __builtin_amdgcn_perm(rp[2 * m + 1][e >> 1], rp[2 * m][e >> 1], (e & 1) ? 0x07060302u : 0x05040100u);
  return f;
}
#define RP_OPS "+v"(rp[0]), "+v"(rp[1]), "+v"(rp[2]), "+v"(rp[3]), "+v"(rp[4]), "+v"(rp[5]), "+v"(rp[6]), "+v"(rp[7])
#define PF1 0
#define PF2 84
#define PF3 120
#define TP0 -1
#define TP1 13
#define TP2 -1
#define TOUCH 5
#define NW3 4
#define THRN0 4
#define DNPRIO 3
#define NWD 3
#define MIDCHK 0
#define HELP 0
#define NHW 7
#define W3LATE 0
#define W3WAIT 1
#define ROT 1
#define EXPT 0
#define PFM 12
#define PSLP 4
#define PLAG 0
#define WTOUCH 0
#define REPLATE 0
#define WB0 8
#define WB1 4
#define ULATE 0
#define VPRIO 3
#define VPRIO0 3
#define VPN 27
#define FPRIO 2
#define CLB 0
#define KAT 99
#define STF 0
#define QN0 0
#define QST 4
#define PB0 3
#define WOT 0
#define WTN 2
#define CLF 0
#define KGO -1
#define QEARLY 1
#define DLY1 4
#define W1 1
#define W2C 0
#define WOE 2
#define W3THR 1
#define HW3 1
#define HW1 1
#define SN 4
#define BATCH 1
#define B23 1
#define WD5L 0
#define A56 1
#define BGO 196
#define BALL 0
#define BSL 4
#define RSL 4
#define H5 1
#define H6 1
#define BORD 1
#define DP0 3
#define AIL 1
#define SEEN 3
#define ADLY 16
#define W6E 0
#define SPLIT 1
#define NCL 4
#define B5L 1
#define A6L 1
#define BO2 2
#define TW 0
#define CL 0
#define CE 0
#define W5D 1
#define W2F 5
#define D2A 12
#define D2B 16
#define SPEC 1
#define CLP 28
#define CLS 3
#define CE2 0
#define DLY5 60
#define CSL 1
#define CF 0
#define OPRIO 3
#define PG 1
#define ISK 1
#define T2 70
#define FXW 8
#define DLY4 16
#define PUBS 5
#define THRN 40
#define DLY2 8
#define DLY3 20
#define GUPRIO 2
enum { S_FREE01 = 0, S_REFILL, S_DONE4, S_WIN3, S_WK, S_WV, S_WQ, S_WSEEN, S_KDONE, S_V, S_PHIK, S_CSV, S_KVA, S_WO, S_CSQ, S_PHI, S_X0, S_X1, S_XA, S_CSK, S_CS, S_WOFA, S_WOFB, S_EXA, S_EXB, S_W2A, S_W2B, S_HFA, S_HFB, S_CONS, S_SPARE, S_XF0 };

__global__ void __launch_bounds__(512, 2) fused_block(const char* __restrict__ x, const char* __restrict__ wqkv, const char* __restrict__ wphi, const char* __restrict__ wo,
                                                      const char* __restrict__ wg, const char* __restrict__ wu, const char* __restrict__ wd, char* __restrict__ out, char* ws) {
  extern __shared__ __attribute__((aligned(16))) char smem[];
  LAS char* L = (LAS char*)smem;
  const int tid = threadIdx.x, lane = tid & 63, wave = __builtin_amdgcn_readfirstlane(tid >> 6), w2 = wave & 3;
  const int b = blockIdx.x, li = b >> 3, xp = (b & 7) >> 1, xh = b & 1;
  const int tile = 4 * xp + (li >> 3), r = 8 * xh + (li & 7);
  const int tile2 = 4 * xp + 2 * xh + (li >> 4), r2 = li & 15;
  const int tg = r2 >= 7 ? 1 : 0, cg = r2 - 7 * tg;
  const int l15 = lane & 15, g = lane >> 4, q4 = l15 >> 2, p4 = lane & 3, hh = lane >> 5;
  const int head = r;
  asm volatile("" :: "s"(x), "s"(wqkv), "s"(wphi), "s"(wo), "s"(wg), "s"(wu), "s"(wd), "s"(out), "s"(ws));
  STAMP(0);
  u64 pc_; asm volatile("s_getpc_b64 %0" : "=s"(pc_));
  const char* pc0 = (const char*)(pc_ & ~127ull);
  const __amdgpu_buffer_rsrc_t rs = __builtin_amdgcn_make_buffer_rsrc((void*)ws, 0, WS_USED, 0x00020000);
  const u32x4 ones = {0x3f803f80u, 0x3f803f80u, 0x3f803f80u, 0x3f803f80u};
  const f32x4 z4 = {0.f, 0.f, 0.f, 0.f};
  auto role = [&](auto AC) __attribute__((always_inline)) {
  constexpr bool A = AC.value;
  if (A) asm volatile("; A" ::: "memory"); else asm volatile("; B" ::: "memory");
  __builtin_amdgcn_s_waitcnt(0x0F70);
  u64 epoch;
  u32x4 rp[8];
  const int fvo = lane < 16 ? lane * 8 : NOWHERE;
  const int uidx = (tile * 10 + __builtin_popcount(~0x2A54u & ((1u << head) - 1u))) * 4 + w2, unit = uidx * 147 / 160;
  const bool has_unit = !((0x2A54 >> head) & 1) && (uidx + 1) * 147 / 160 != unit;
  const int u3 = unit / 3, umat = unit - 3 * u3, ukq = u3 / 28, ufl = u3 - 28 * ukq;
  const bool ud = umat == 2;
  const char* gusrc = ws + WS_WGU + 7 * r * SLOT;
  const u32 guo = WS_WGU + 7 * r * SLOT + w2 * 7168;
#define GUBLK(j) dma7<(j) * SLOT, gu_slot(j) * SLOT>(rs, lane * 16, guo, w2 * 7168)
  const u32 wvo = (lane >> 2) * 2688 + (((lane & 3) ^ (2 * (lane >> 5))) * 16);
  const char* wbase = wqkv + head * 56 - (head & 1) * 8 + 16 * w2 * 2688;
  const u32 wr0 = (8 * g + q4) * 64 + (((p4 + (head & 1)) ^ (4 * (g & 1))) * 8), wr1 = (8 * g + q4) * 64 + ((((4 + p4 + (head & 1)) & 7) ^ (4 * (g & 1))) * 8);
  const int tb = w2; constexpr int kh = A ? 0 : 1;
  const int hho = WS_HH + (((tile * 2 + (w2 >> 1)) * 4 + (r >> 2)) * 56 + (r & 3) * 2 + (w2 & 1)) * 1024;
  const int tok = tile2 * 128 + tg * 64 + tb * 16 + l15;
  const int hvo = WS_H + ((tok >> 5) * 28 + 4 * cg + (g >> 1)) * 1024 + ((g & 1) * 32 + (tok & 31)) * 16;
  const int sto = WS_ST + (2 * cg * 2048 + tile2 * 128 + tg * 64 + tb * 16) * 8;
  const u32x4 empty4 = {EMPTY, EMPTY, EMPTY, EMPTY};
#define HH_CLEAR() _Pragma("unroll") for (int i = 0; i < 4 * NCL; ++i) ST16(rs, empty4, lane * 16 + (i & 1) * 2048, WS_HH + (((tile2 * 2 + r2 - 14) * 4 + w2) * 56 + kh + (i >> 1) * 4) * 1024)
#define HH_FLAG() FLAG(WS_FH + ((tile2 * 4 + 2 * (r2 - 14) + kh) * 32 + 28 + w2) * 128, 1)
  u32x4 wof[8][4];
  f32x4 csp[7];
  u64 tag;
#define WOF_READ(i0, i1) do { u32 wa_ = L_WO + (kh * 224 + 4 * g + q4) * 128 + 16 * p4 + 64 * (q4 >> 1), wb_ = wa_ ^ 64, wc_ = wa_ + (g == 3 ? 0 : 2048), wd_ = wc_ ^ 64; \
    asm volatile("" : "+v"(wa_), "+v"(wb_), "+v"(wc_), "+v"(wd_)); \
    _Pragma("unroll") for (int i = i0; i < i1; ++i) _Pragma("unroll") for (int k = 0; k < 4; ++k) { \
      const u32x2 a_ = trread(L + (k >> 1 ? wb_ : wa_) + 28 * i * 128 + (k & 1) * 8), c_ = trread(L + (k >> 1 ? wd_ : wc_) + 28 * i * 128 + (k & 1) * 8); \
      wof[i][k] = u32x4{a_.x, a_.y, c_.x, c_.y}; } } while (0)
#define XF_READ(i) _Pragma("unroll") for (int s = 0; s < 14; ++s) xf[i][s] = *(LAS u32x4*)((s & 1 ? xr1 : xr0) + (i) * 14336 + (s >> 1) * 2048)
#define LN_STATS() do { f32x4 g1 = z4, g2 = z4; \
    _Pragma("unroll") for (int s = 0; s < 14; ++s) { g1 = mfma16(ones, xf[kh][s], g1); g2 = mfma16(xf[kh][s], xf[kh][s], g2); } \
    const float d01 = (p4 & 1) ? g2[1] : g2[0], d23 = (p4 & 1) ? g2[3] : g2[2], s2 = (p4 & 2) ? d23 : d01; \
    const float nmu = g1[0] * (-1.f / 448.f), var = fmaxf(s2 * (1.f / 448.f) - nmu * nmu, 0.f); \
    if (g == q4) *(LAS f32x2*)(L + L_ST1 + (w2 * 32 + kh * 16 + l15) * 8) = f32x2{nmu, __builtin_amdgcn_rsqf(var + 1e-5f)}; } while (0)
#define WPF() do { u32 wp[8]; \
    _Pragma("unroll") for (int j = 0; j < 8; ++j) wp[j] = *(LAS u32*)(L + L_WPHI + 4 * g * 56 + min(l15, 13) * 4 + ((j & 3) + 16 * (j >> 2)) * 56); \
    _Pragma("unroll") for (int e = 0; e < 4; ++e) { const bool z = e >= 2 && g == 3; \
      wpf[0][e] = z ? 0u : __builtin_amdgcn_perm(wp[2 * e + 1], wp[2 * e], 0x05040100u); wpf[1][e] = z ? 0u : __builtin_amdgcn_perm(wp[2 * e + 1], wp[2 * e], 0x07060302u); } \
    asm volatile("" : "+v"(wpf[0]), "+v"(wpf[1])); } while (0)
  {
    u32x4 xf[2][14], wpf[2];
    f32x2 st[2];
    LAS char* xr0 = L + w2 * 28672 + l15 * 128 + ((g ^ (l15 & 7)) * 16);
    LAS char* xr1 = L + w2 * 28672 + l15 * 128 + (((4 + g) ^ (l15 & 7)) * 16);
    LAS char* pw = L + L_PKV + (w2 * 32 + l15) * 224 + g * 8;
    const char* wos = wo + (r2 < 14 ? cg : 0) * 128 + w2 * 56 * 896;
    const u32 wovo = (lane >> 3) * 896 + (((lane & 7) ^ ((lane >> 2) & 4)) * 16);
#define WO_HALF(hf_) _Pragma("unroll") for (int n = 0; n < 7; ++n) dma16(wos + ((hf_) * 224 + n * 8) * 896, wovo, L_WO + ((hf_) * 28 + w2 * 7 + n) * 1024)
    int clo = WS_O + ((tile * 8 + 2 * w2) * 16 + head) * 1024, clh = r2 < 14 ? hvo : NOWHERE, cls = r2 < 14 && lane < 16 ? (lane >> 3) * 16384 + (lane & 7) * 16 : NOWHERE, clt = sto;
    int kvo = WS_KVP + ((head * 16 + tile) * 4 + w2) * 1024, fko = WS_FKV + ((head * 4 + w2) * 16 + tile) * 128;
    u32x4 em4 = empty4;
    u32 wtv = min(lane, 24) * 128;
    const char* wts = wo + min(li * 98 + w2 * 25, 3136 - 25) * 128;
    auto clear = [&](int c) LAM {
      if (c == 0) ST16(rs, em4, lane * 16, clo);
      if (c == 1) ST16(rs, em4, lane * 16, clo + 16384);
      if (c == 2) ST16(rs, em4, clh, 0);
      if (c == 3) ST16(rs, em4, clh, 2048);
      if (c == 4) ST16(rs, em4, cls, clt);
    };
    if constexpr (A) {
      if (CLF == 6 || CLF == 7) { clear(0); clear(1); }
      if (CLF == 6 || CLF == 8) { clear(2); clear(3); clear(4); }
      if (wave < 2) dma4(x + (xp * 512 + li * 16) * 896 + wave * 7168, min(lane, 55) * 128, L_JUNK);
      if (wave == 3) { *(LAS u32*)(L + L_FLG + lane * 4) = 0u; *(LAS u32*)(L + L_FLG + 256 + lane * 4) = 0u; dma4(pc0, min((li % 6) * 8192 + lane * 128, CODE_PF), L_JUNK); }
      if (wave == 2) { dma16(wphi + head * 1568, lane * 16, L_WPHI); dma16(wphi + head * 1568, min(lane + 64, 97) * 16, L_WPHI + 1024); }
      if (WTOUCH && wave < WTOUCH) dma4(wqkv + li * 14 * 2688 + (wave == 2 ? 0 : (wave + 1) * 896) + xh * 384, min(lane >> 2, 13) * 2688 + (lane & 3) * 128, L_JUNK);
      if (CLF == 9) { clear(0); clear(1); clear(2); clear(3); clear(4); }
      BAR();
      asm volatile("s_load_dwordx2 %0, %1, 0x0 ; role A\n\ts_waitcnt lgkmcnt(0)" : "=s"(epoch) : "s"(ws) : "memory");
      tag = epoch * 6364136223846793005ull + 1442695040888963407ull;
      STAMP(1);
#define WKLOAD() _Pragma("unroll") for (int i = 0; i < 7; ++i) dma16(wbase + 896 + 64 * i * 2688, wvo, L_WK + (4 * i + w2) * 1024)
      if (KAT == 3) { WKLOAD(); if (wave == 2) { WAITVM(7); lds_signal(L, S_PHI, 0); } }
      else if (wave == 2) { if (CLF == 9) WAITVM(5); else WAITVM(0); lds_signal(L, S_PHI, 0); }
      if (KAT > 3 && KAT < 28) { lds_wait1(L, S_SPARE, w2); WKLOAD(); }
      lds_wait1(L, S_PHI, 0);
      WPF();
      lds_wait1(L, S_X0, w2);
      XF_READ(0);
      if (w2 < 2) lds_signal(L, S_XF0, w2);
      if (KAT > 28) { WKLOAD(); }
      STAMP(27);
      LN_STATS();
      SCHED();
      STAMP(2);
      lds_wait1(L, S_X1, w2);
      XF_READ(1);
      STAMP(3);
      WAITVM(0);
      STAMP(4);
      lds_signal(L, S_WK, w2);
      f32x4 t[2][2];
      u32 pr = L_PKV + (4 * g + q4) * 224 + 8 * p4;
      asm volatile("" : "+v"(pr), "+v"(pw));
      asm volatile("" : "+s"(clo), "+v"(clh), "+v"(cls), "+s"(clt), "+s"(kvo), "+s"(fko), "+s"(tag), "+v"(em4));
      if (WOT == 5) asm volatile("" : "+v"(wtv), "+s"(wts));
      project<0, S_CSK, 8>(L, L_WK + wr0, L_WK + wr1, xf, w2, lane, L_CSP, t, st, [&]() LAM {
        lds_wait(L, S_WK);
        STAMP(5);
        __builtin_amdgcn_s_setprio(2);
      }, [&](int n, LAS char* sg, u32 one) LAM {
        if (n == KGO) *(volatile LAS u32*)(sg + S_KDONE * 16) = one;
      });
      STAMP(6);
      u32x4 P[2][2];
      __builtin_amdgcn_s_setprio(FPRIO);
      feature_map(t, st, wpf, g, P, [&](int i) LAM {
        if (WOT == 5 && i == 0) { SCHED(); dma4(wts, wtv, L_JUNK); SCHED(); }
        if (CLF == 1 && i > 0) { SCHED(); clear(i - 1); SCHED(); }
        if (CLF == 2 && i == 5) { SCHED(); clear(0); clear(1); clear(2); clear(3); clear(4); SCHED(); }
        if (CLF == 3 && i >= 3) { SCHED(); clear(2 * i - 6); clear(2 * i - 5); SCHED(); }
        if (CLF == 4 && i == 1) { SCHED(); clear(0); clear(1); clear(2); clear(3); clear(4); SCHED(); }
      });
#pragma unroll
      for (int c = 0; c < 4; ++c) {
        LAS char* p = pw + (c >> 1) * 16 * 224 + (c & 1) * 64;
        *(LAS u32x2*)(p) = u32x2{P[c >> 1][c & 1].x, P[c >> 1][c & 1].y}; *(LAS u32x2*)(p + 32) = u32x2{P[c >> 1][c & 1].z, P[c >> 1][c & 1].w};
      }
      lds_signal(L, S_PHIK, w2);
      STAMP(7);
      if (CLF == 0 || CLF == 8) { clear(0); clear(1); }
      if (CLF == 0 || CLF == 7) { clear(2); clear(3); clear(4); }
      lds_wait2(L, S_V, S_PHIK);
      u32x4 pa[4], pb[8];
#pragma unroll
      for (int m = 0; m < 4; ++m) {
        pa[m] = trfrag(L + pr + w2 * 32 + m * 32 * 224, 16 * 224);
        pb[2 * m] = trfrag(L + pr + m * 32 * 224 + 128, 16 * 224); pb[2 * m + 1] = trfrag(L + pr + m * 32 * 224 + 160, 16 * 224);
      }
      SCHED();
      f32x4 kv[2] = {};
#pragma unroll
      for (int m = 0; m < 8; ++m) kv[m & 1] = mfma16(pa[m >> 1], pb[m], kv[m & 1]);
      const u32x4 kvb = {pk(kv[0][0], kv[0][1]), pk(kv[0][2], kv[0][3]), pk(kv[1][0], kv[1][1]), pk(kv[1][2], kv[1][3])};
      ST16(rs, kvb, lane * 16, kvo);
      STAMP(8);
      DRAIN();
      FLAG(fko, 1);
      __builtin_amdgcn_s_setprio(0);
      STAMP(9);
      if (HW1) { lds_signal(L, S_XA, w2); lds_wait(L, S_XA); }
      if (r2 < 14 || !ISK) { WO_HALF(0); WO_HALF(1); } else __builtin_amdgcn_s_sleep(20);
      poll_flags(ws, WS_FKV + ((head * 4 + w2) * 16 + l15) * 128, lane < 16, tag);
      lds_signal(L, S_WO, w2);
      if (CE2 && r2 >= 14) { DRAIN(); HH_FLAG(); }
      STAMP(10);
      u32x4 tt[16];
#pragma unroll
      for (int i = 0; i < 16; ++i) tt[i] = __builtin_bit_cast(u32x4, __builtin_amdgcn_raw_buffer_load_b128(rs, lane * 16, WS_KVP + head * 65536 + w2 * 1024 + i * 4096, 16));
      u32 fgo;
      {
        const int pl = (tile & 1) * 16 + min(lane >> 2, 13);
        fgo = wave == 0 ? WS_FKV + ((l15 * 4 + 2 * tg + (g & 1)) * 16 + tile2) * 128
            : wave == 1 ? WS_FKV + (((((tile >> 1) & 1) * 8 + (pl & 7)) * 4 + p4) * 16 + (tile & 12) + (pl >> 3)) * 128
            : wave == 2 ? WS_FD + (min(cg, 6) * 32 + min(lane & 31, 27)) * 128
                        : WS_FG + (min(((7 * r) >> 2) + g, (7 * r + 6) >> 2) * 16 + (l15 & 8) + min(lane & 7, 6)) * 128;
      }
      u64 fg = __hip_atomic_load((const GAS u64*)(ws + fgo), RLX_AGENT);
      SCHED();
      const u32 one = g == q4 ? ((p4 & 1) ? 0x3f800000u : 0x3f80u) : 0u, s01 = p4 < 2 ? one : 0u, s23 = p4 < 2 ? 0u : one;
      const u32x4 sel0 = {s01, s23, 0u, 0u}, sel1 = {0u, 0u, s01, s23};
      f32x4 s0 = {}, s1 = {};
#pragma unroll
      for (int i = 0; i < 16; ++i) { s0 = mfma16(sel0, tt[i], s0); s1 = mfma16(sel1, tt[i], s1); }
      LAS char* kw = L + L_KVA + (w2 >> 1) * 1024 + lane * 16 + (w2 & 1) * 8;
      *(LAS u32x2*)(kw) = u32x2{pk(s0[0], s0[1]), pk(s0[2], s0[3])};
      *(LAS u32x2*)(kw + 2048) = u32x2{pk(s1[0], s1[1]), pk(s1[2], s1[3])};
      lds_signal(L, S_KVA, w2);
      STAMP(11);
      if (WOE == 3) { WO_HALF(1); WAITVM(0); lds_signal(L, S_WO, w2); }
      asm volatile("" : "+v"(fg) :: "memory");
      if (!__all(fg == tag + 1)) poll_flags(ws, fgo, true, tag);
      __builtin_amdgcn_fence(__ATOMIC_ACQUIRE, "wavefront");
      lds_signal(L, S_WOFA, w2);
      lds_wait1(L, S_WOFA, 3);
      const int fl = min(((7 * r) >> 2) + w2, (7 * r + 6) >> 2);
#pragma unroll
      for (int i = 0; i < 7; ++i) asm volatile("global_load_dwordx4 %0, %1, %2 offset:%3 sc1" : "=v"(csp[i]) : "v"(((7 * r) >> 2) + w2 == fl ? lane * 16 : 0), "s"(ws + WS_CS + fl * 7168 + 4096), "n"(i * 1024 - 4096) : "memory");
      GUBLK(2);
      if (r2 < 14 || !ISK) { lds_wait(L, S_WO); WOF_READ(0, 8); }
      SCHED();
#define CS_OUT(k_) do { asm volatile("s_waitcnt vmcnt(%7)" : "+v"(csp[0]), "+v"(csp[1]), "+v"(csp[2]), "+v"(csp[3]), "+v"(csp[4]), "+v"(csp[5]), "+v"(csp[6]) : "n"(k_) : "memory"); \
        const f32x4 sm = ((csp[0] + csp[1]) + (csp[2] + csp[3])) + ((csp[4] + csp[5]) + csp[6]); \
        const float e0 = swadd32(sm[0], sm[1]), e1 = swadd32(sm[2], sm[3]);     \
        const int j0 = 4 * (((7 * r) >> 2) + w2) + 2 * (lane & 1) - 7 * r, cr = ((g & 1) * 16 + hh * 8 + (l15 >> 1)) * 4; \
        if (j0 >= 0 && j0 < 7) *(LAS float*)(L + L_CS + j0 * 128 + cr) = e0; \
        if (j0 + 1 >= 0 && j0 + 1 < 7) *(LAS float*)(L + L_CS + (j0 + 1) * 128 + cr) = e1; } while (0)
      lds_signal(L, S_WOFB, w2);
      if (!CSL) CS_OUT(7);
      STAMP(24);
    } else {
      const u32 vo = (lane >> 3) * 896 + (((lane & 7) ^ (lane >> 3)) * 16);
      const char* xb = x + (tile * 128 + w2 * 32) * 896;
#pragma unroll
      for (int n = 0; n < 28; ++n) {
        dma16(xb + ((n / 14) * 16 + (n & 1) * 8) * 896 + ((n % 14) >> 1) * 128, vo, w2 * 28672 + n * 1024);
        if (n == 3) { BAR(); asm volatile("s_load_dwordx2 %0, %1, 0x0 ; role B" : "=s"(epoch) : "s"(ws) : "memory"); }
        if (n == 21) { WAITVM(8); lds_signal(L, S_X0, w2); }
        if (n == KAT && KAT > 3) lds_signal(L, S_SPARE, w2);
      }
      asm volatile("s_waitcnt lgkmcnt(0)" : "+s"(epoch)::"memory");
      tag = epoch * 6364136223846793005ull + 1442695040888963407ull;
      STAMP(1);
      XF_READ(0);
      if (w2 < 2) lds_signal(L, S_XF0, 2 + w2);
      WAITVM(0);
      lds_signal(L, S_X1, w2);
      STAMP(2);
      if (STF < 7) lds_wait(L, S_XF0);
      XF_READ(1);
      SCHED();
#define WDST(c) ((c) * 1024 + ((c) >= 14 ? SLOT - 14336 : 0))
#pragma unroll
      for (int i = 0; i < 7 - STF; ++i) dma16(wbase + 1792 + 64 * i * 2688, wvo, L_WV + WDST(4 * i + w2));
      STAMP(3);
      __builtin_amdgcn_s_setprio(PB0);
      LN_STATS();
      lds_signal(L, S_CS, w2);
      if (STF == 7) lds_wait(L, S_XF0);
#pragma unroll
      for (int i = 7 - STF; i < 7; ++i) dma16(wbase + 1792 + 64 * i * 2688, wvo, L_WV + WDST(4 * i + w2));
      STAMP(4);
      const int rg = lane >> 3;
      const u32 lvo = (ud ? (32 * (rg & 1) + (rg & 6)) * 896 : rg * 8 * 3584) + (lane & 7) * 16;
      const char* us = ud ? wd + ufl * 64 * 896 + ukq * 128 : (umat ? wu : wg) + ukq * 64 * 3584 + ufl * 128;
      int ust0 = ud ? 8 * 896 : 3584, ust1 = ud ? 896 : 4 * 3584;
      asm volatile("" : "+s"(us), "+s"(ust0), "+s"(ust1));
      f32x4 t[2][2];
      asm volatile("" : "+v"(pw));
      if (WOT == 4) asm volatile("" : "+v"(wtv), "+s"(wts));
      project<SLOT - 14336, S_CSV, 4>(L, L_WV + wr0, L_WV + wr1, xf, w2, lane, L_CSP + 256, t, st, [&]() LAM {
      if (has_unit) {
#pragma unroll
        for (int j = 0; j < 8; ++j) asm volatile("global_load_dwordx4 %0, %1, %2" : "=v"(rp[j]) : "v"(lvo), "s"(us + (j & 3) * ust0 + (j >> 2) * ust1) : "memory");
      } else WAITVM(0);
      STAMP(28);
      WAITVM(8);
      lds_signal(L, S_WV, w2);
      STAMP(29);
      lds_wait2(L, S_WV, S_WK);
      STAMP(30);
      if (QEARLY == 1) {
#pragma unroll
        for (int i = 0; i < 7; ++i) dma16(wbase + 64 * i * 2688, wvo, L_WQ + WDST(4 * i + w2));
      }
        if (KGO >= 0) lds_wait1(L, S_KDONE, w2);
        STAMP(5);
        __builtin_amdgcn_s_setprio(VPRIO0);
      }, [&](int n, LAS char*, u32) LAM {
        if (n == VPN) __builtin_amdgcn_s_setprio(VPRIO);
        if (WOT == 4 && n == WTN) dma4(wts, wtv, L_JUNK);
      });
#pragma unroll
      for (int c = 0; c < 4; ++c) t[c >> 1][c & 1] *= st[c >> 1].y;
      STAMP(6);
#pragma unroll
      for (int c = 0; c < 2; ++c) {
        LAS char* p = pw + c * 16 * 224 + 128;
        *(LAS u32x2*)(p) = u32x2{pk(t[c][0][0], t[c][0][1]), pk(t[c][0][2], t[c][0][3])};
        *(LAS u32x2*)(p + 32) = g == 3 ? u32x2{0x3f80u, 0u} : u32x2{pk(t[c][1][0], t[c][1][1]), pk(t[c][1][2], t[c][1][3])};
      }
      lds_signal(L, S_V, w2);
      __builtin_amdgcn_s_setprio(0);
      STAMP(7);
      if (!QEARLY) {
#pragma unroll
        for (int i = 0; i < 7; ++i) dma16(wbase + 64 * i * 2688, wvo, L_WQ + WDST(4 * i + w2));
      }
      if (WOE == 0) WO_HALF(0);
      if (WOE < 2) { lds_wait(L, S_KDONE); WO_HALF(1); }
      asm volatile("s_waitcnt vmcnt(%8)" : RP_OPS : "n"(7 + (WOT == 4)) : "memory");
      auto repack = [&]() LAM {
        if (has_unit) {
          const int fb = 4 * ufl + (rg >> 1), pr = fb * 147 >> 10;
          const int so = (ud ? (((pr >> 2) * 7 + fb - 7 * pr) * 4 + (pr & 3)) * 2048 : (rg >> 1) * 1024) + (rg & 1) * 512 + (lane & 7) * 16;
          const int sb = ud ? WS_WD + ukq * 229376 : WS_WGU + 4 * ufl * SLOT + ukq * 4096 + umat * 256;
          const int s1 = ud ? 256 : SLOT, s2 = ud ? 1024 : 2 * SLOT;
          float c[8];
#pragma unroll
          for (int e = 0; e < 8; ++e) {
            const u32x4 f = piece(rp, e);
            ST16(rs, f, so + (e & 1) * 128, sb + ((e >> 1) & 1) * s1 + (e >> 2) * s2);
            c[e] = mfma16(ones, f, z4)[0];
          }
          const f32x4 cv = g & 1 ? f32x4{c[4], c[5], c[6], c[7]} : f32x4{c[0], c[1], c[2], c[3]};
          ST16(rs, cv, g < 2 && !ud ? (l15 >> 3) * 512 + (lane & 7) * 32 + g * 16 : NOWHERE, WS_CS + ufl * 7168 + ukq * 1024 + (umat & 1) * 256);
        }
      };
#define UFLAG() FLAGC(has_unit, ud ? WS_FD + (ukq * 32 + ufl) * 128 : WS_FG + ((ufl * 2 + umat) * 8 + ukq) * 128, 1)
      if (!REPLATE) repack();
      STAMP(8);
      WPF();
      if (!has_unit) asm volatile("s_waitcnt vmcnt(%0)" :: "n"(WOT == 4) : "memory"); else asm volatile("s_waitcnt vmcnt(%0)" :: "n"(9 + (WOT == 4)) : "memory");
      lds_signal(L, S_WQ, w2);
      project<SLOT - 14336, S_CSQ, 4>(L, L_WQ + wr0, L_WQ + wr1, xf, w2, lane, L_CSP + 512, t, st, [&]() LAM {
        lds_wait(L, S_WQ);
        STAMP(9);
      }, [&](int n, LAS char*, u32) LAM {
        if (n == 14 && !REPLATE) { WAITVM(0); UFLAG(); }
      });
      STAMP(10);
      if (REPLATE == 2) repack();
      u32x4 pq[2][2];
      feature_map(t, st, wpf, g, pq, [](int) {});
      asm volatile("" : "+v"(pq[0][0]), "+v"(pq[0][1]), "+v"(pq[1][0]), "+v"(pq[1][1]));
      STAMP(11);
      if (REPLATE == 1) repack();
      if (CE2 && r2 >= 14) { HH_CLEAR(); DRAIN(); HH_FLAG(); }
      if (r2 < 14 || !ISK) { lds_wait(L, S_WO); WOF_READ(0, 8); }
      STAMP(24);
      if (REPLATE == 2) { WAITVM(0); UFLAG(); }
      __builtin_amdgcn_s_setprio(OPRIO);
      lds_wait(L, S_KVA);
      if (REPLATE == 1) { WAITVM(0); UFLAG(); }
      STAMP(27);
      u32x4 ka[4];
#pragma unroll
      for (int i = 0; i < 4; ++i) ka[i] = *(LAS u32x4*)(L + L_KVA + i * 1024 + lane * 16);
      f32x4 o[2][2];
#pragma unroll
      for (int c = 0; c < 4; ++c) o[c >> 1][c & 1] = mfma16(ka[2 * (c & 1) + 1], pq[c >> 1][1], mfma16(ka[2 * (c & 1)], pq[c >> 1][0], z4));
      const f32x2 da = swp16(o[0][1][0], o[0][1][0]), db = swp16(o[1][1][0], o[1][1][0]);
      const float den[2] = {swp32(da.y, da.y).y, swp32(db.y, db.y).y};
#pragma unroll
      for (int c = 0; c < 2; ++c) {
        const float rd = frcp(den[c] + 1e-6f), r3 = g == 3 ? 0.f : rd;
        const u32x4 ob = {pk(o[c][0][0] * rd, o[c][0][1] * rd), pk(o[c][0][2] * rd, o[c][0][3] * rd), pk(o[c][1][0] * r3, o[c][1][1] * r3), filled(pk(o[c][1][2] * r3, o[c][1][3] * r3))};
        ST16(rs, ob, lane * 16, WS_O + ((tile * 8 + 2 * w2 + c) * 16 + head) * 1024);
      }
      STAMP(12);
      if (WOE == 3) lds_wait(L, S_WO);
      *(volatile LAS u32*)(L + L_FLG + S_W2A * 16 + w2 * 4) = (u32)__builtin_amdgcn_s_memrealtime();
      lds_signal(L, S_W2B, w2);
      __builtin_amdgcn_s_setprio(0);
    }
  }
  STAMP(13);
  u32x4 hres = {0u, 0u, 0u, 0u};
  const int xo = tok * 896 + cg * 128 + kh * 64 + g * 16;
  STAMP(23);
  if (r2 < 14) {
    u32x4 xr, of[8];
    asm volatile("global_load_dwordx4 %0, %1, %2" : "=v"(xr) : "v"(xo), "s"(x) : "memory");
    lds_wait2(L, S_W2B, S_WOFA);
#define WIN2() do { if (A ? W1 < 2 : W1 < 3) GUBLK(A ? 0 : 1); } while (0)
    if (W2F == 1 || W2F == 3) WIN2();
    if (W2F == 4 && !A) { GUBLK(0); GUBLK(1); }
    if (W2F == 5 && !A) GUBLK(1);
    if (T2) {
      const u32x4 ts = *(volatile LAS u32x4*)(L + L_FLG + S_W2A * 16);
      const u32 t0 = __builtin_amdgcn_readfirstlane(ts.x), due = t0 + T2 + __builtin_amdgcn_readfirstlane(max(max(0, (int)(ts.y - ts.x)), max((int)(ts.z - ts.x), (int)(ts.w - ts.x))));
#pragma nounroll
      for (u32 sp = 0; sp < 64 && (int)((u32)__builtin_amdgcn_s_memrealtime() - due) < 0; ++sp) __builtin_amdgcn_s_sleep(1);
    } else __builtin_amdgcn_s_sleep(A ? D2A : D2B);
    STAMP(14);
#define OFA(c_) _Pragma("unroll") for (int i = 0; i < 8; ++i) \
      asm volatile("global_load_dwordx4 %0, %1, %2 offset:%3 sc1" : c_(of[i]) : "v"(lane * 16), "s"(ws + WS_O + ((tile2 * 8 + tg * 4 + tb) * 16 + kh * 8 + 4) * 1024), "n"(i * 1024 - 4096) : "memory")
#define OFW(k_) asm volatile("s_waitcnt vmcnt(%9)" : "+v"(of[0]), "+v"(of[1]), "+v"(of[2]), "+v"(of[3]), "+v"(of[4]), "+v"(of[5]), "+v"(of[6]), "+v"(of[7]), "+v"(xr) : "n"(k_) : "memory")
#define OFBAD() (!__all(max(max(max(of[0].w, of[1].w), max(of[2].w, of[3].w)), max(max(of[4].w, of[5].w), max(of[6].w, of[7].w))) != EMPTY))
    OFA("=v");
    f32x4 a[4] = {};
    u32 mx = 0;
#pragma unroll
    for (int i = 0; i < 8; ++i) {
      asm volatile("s_waitcnt vmcnt(%1)" : "+v"(of[i]) : "n"(7 - i) : "memory");
      if (i == PG - 1) { lds_wait(L, S_WOFB); }
      if (i == 7) GUBLK(A ? 0 : 3);
#pragma unroll
      for (int k = 0; k < 4; ++k) a[k] = mfma16(wof[i][k], of[i], a[k]);
      mx = max(mx, of[i].w);
      SCHED();
    }
    asm volatile("" : "+v"(xr));
    if (__builtin_expect(!__all(mx != EMPTY), 0)) {
#pragma nounroll
      for (u32 sp = 0; sp < SPIN_CAP; ++sp) { __builtin_amdgcn_s_sleep(RSL); OFA("+v"); OFW(0); if (!OFBAD()) break; }
#pragma unroll
      for (int i = 0; i < 8; ++i)
#pragma unroll
        for (int k = 0; k < 4; ++k) a[k] = mfma16(wof[i][k], of[i], i ? a[k] : z4);
    }
    STAMP(25);
    *(LAS f32x4*)(L + L_EX + wave * 2048 + lane * 16) = kh ? a[0] : a[2];
    *(LAS f32x4*)(L + L_EX + wave * 2048 + 1024 + lane * 16) = kh ? a[1] : a[3];
    lds_signal(L, A ? S_EXA : S_EXB, w2);
    lds_wait1(L, A ? S_EXB : S_EXA, w2);
    STAMP(15);
    const f32x4 c0 = (kh ? a[2] : a[0]) + *(LAS f32x4*)(L + L_EX + (wave ^ 4) * 2048 + lane * 16);
    const f32x4 c1 = (kh ? a[3] : a[1]) + *(LAS f32x4*)(L + L_EX + (wave ^ 4) * 2048 + 1024 + lane * 16);
    const f32x4 h0 = c0 + f32x4{blo(xr.x), bhi(xr.x), blo(xr.y), bhi(xr.y)}, h1 = c1 + f32x4{blo(xr.z), bhi(xr.z), blo(xr.w), bhi(xr.w)};
    hres = u32x4{pk(h0[0], h0[1]), pk(h0[2], h0[3]), pk(h1[0], h1[1]), filled(pk(h1[2], h1[3]))};
    ST16(rs, hres, hvo, kh * 2048);
    const f32x4 r0 = {blo(hres.x), bhi(hres.x), blo(hres.y), bhi(hres.y)}, r1 = {blo(hres.z), bhi(hres.z), blo(hres.w), bhi(hres.w)};
    const f32x4 a1 = r0 + r1, a2q = r0 * r0 + r1 * r1;
    float s1 = (a1[0] + a1[1]) + (a1[2] + a1[3]), s2 = (a2q[0] + a2q[1]) + (a2q[2] + a2q[3]);
    { const float t = swadd16(s1, s2); const f32x2 a2 = sw32(t); s1 = a2.x + a2.y; }
    { const u32x2 sv = __builtin_bit_cast(u32x2, sw16(s1)); __builtin_amdgcn_raw_buffer_store_b64(u32x2{sv.x, filled(sv.y)}, rs, fvo, sto + kh * 16384, 16); }
    WAITVM(2);
  } else {
    lds_wait2(L, S_W2B, S_WOFA);
    if (CF) { HH_CLEAR(); }
    WIN2();
    if (W2F >= 3) { lds_wait(L, S_WOFB); if (W2F < 5 || A) GUBLK(A ? 3 : 4); }
    if (!CF) { HH_CLEAR(); }
    DRAIN();
    if (!CE2) HH_FLAG();
    if (CE2 && A) __builtin_amdgcn_s_sleep(DLY5);
  }
  if constexpr (A && CSL) CS_OUT(2);
  lds_signal(L, A ? S_HFA : S_HFB, w2);
  STAMP(16);
  u32x4 wdf[NWD][8];
  const int wdo = WS_WD + (cg * 4 + w2) * 57344;
  const u32 po = WS_FHH + ((tile2 * 16 + 4 * w2 + ((lane >> 1) & 3)) * 4 + 2 * tg + (lane & 1)) * 128;
  const u32 ho = WS_HH + ((tile2 * 2 + tg) * 4 + w2) * 57344;
#define WDLOAD(k, bf) dma8(rs, lane * 16, wdo + (k) * 8192, ((bf) + 2) * SLOT + w2 * 7168, L_EX + (bf) * 4096 + w2 * 1024)
#define THR() asm volatile("s_waitcnt vmcnt(%0)" :: "n"(THRN) : "memory")
#define WLOAD1(j, i) wdf[(j) % NWD][i] = __builtin_bit_cast(u32x4, __builtin_amdgcn_raw_buffer_load_b128(rs, lane * 16 + ((i) & 3) * 1024, wdo + (j) * 8192 + ((i) & 4) * 1024, 16))
#define THR0() asm volatile("s_waitcnt vmcnt(%0)" :: "n"(THRN0) : "memory")
#define WLOAD(j) _Pragma("unroll") for (int i = 0; i < 8; ++i) { THR0(); WLOAD1(j, i); }
  u32 have = 0;
#define PLOAD() peek(ws, po, lane < 8, tag + 7)
#define PMIN(pv) do { const u64 d = (pv) - tag; have = max(have, min8(d <= 8 ? (u32)d : 0u)); } while (0)
#define NEEDP(n) do { _Pragma("nounroll") for (u32 sp = 0; have < (n) && sp < SPIN_CAP; ++sp) { PMIN(PLOAD()); if (PSLP && have < (n)) __builtin_amdgcn_s_sleep(PSLP); } \
    __builtin_amdgcn_fence(__ATOMIC_ACQUIRE, "wavefront"); } while (0)
#define HBASE() u32 hb3 = w2 * 7168 + lane * 16, hb0 = hb3 + 2 * SLOT, hb2 = hb0 + 2 * SLOT, hb7 = L_EX + w2 * 1024 + lane * 16; asm volatile("" : "+v"(hb0), "+v"(hb2), "+v"(hb7))
#define HFRAG(bf, i) (*(LAS u32x4*)(L + ((i) < 7 ? ((bf) == 3 ? hb3 : (bf) == 2 ? hb2 : hb0 + (bf) * SLOT) + (i) * 1024 : hb7 + (bf) * 4096)))
#define HQLOAD(q, j) _Pragma("unroll") for (int i = 0; i < 8; ++i) hq[q][i] = __builtin_bit_cast(u32x4, __builtin_amdgcn_raw_buffer_load_b128(rs, lane * 16 + (i & 3) * 1024, ho + (j) * 8192 + (i & 4) * 1024, 16))
#define HQLOADT(q, j) _Pragma("unroll") for (int i = 0; i < 8; ++i) { THR(); hq[q][i] = __builtin_bit_cast(u32x4, __builtin_amdgcn_raw_buffer_load_b128(rs, lane * 16 + (i & 3) * 1024, ho + (j) * 8192 + (i & 4) * 1024, 16)); }
#define DOWN16(ac, wf, hf_) _Pragma("unroll") for (int m = 0; m < 16; ++m) ac[(m >> 1) & 1][m & 1] = mfma32(wf[2 * (m >> 2) + (m & 1)], hf_[2 * (m >> 2) + ((m >> 1) & 1)], ac[(m >> 1) & 1][m & 1])
#define PARTIAL_OUT(ac, b0, b1) _Pragma("unroll") for (int n = 0; n < 16; ++n) { const int T = n >> 3, k = (n >> 2) & 1, q = n & 3; \
    *(LAS f32x4*)&HFRAG(T ? b1 : b0, n & 7) = f32x4{ac[T][k][4 * q], ac[T][k][4 * q + 1], ac[T][k][4 * q + 2], ac[T][k][4 * q + 3]}; }
#define FREE(f) do { _Pragma("nounroll") for (u32 sp = 0; sp < (SPIN_CAP << 4); ++sp) { const u32x4 fr = *(volatile LAS u32x4*)(L + L_FLG + S_DONE4 * 16); \
          if (__builtin_amdgcn_readfirstlane(min(min(fr.x, fr.y), min(fr.z, fr.w))) >= (f)) break; __builtin_amdgcn_s_sleep(BSL); } asm volatile("" ::: "memory"); } while (0)
#define HQBAD(q) (!__all(max(max(max(hq[q][0].w, hq[q][1].w), max(hq[q][2].w, hq[q][3].w)), max(max(hq[q][4].w, hq[q][5].w), max(hq[q][6].w, hq[q][7].w))) != EMPTY))
#define HQCHECK(q, j) do { if (__builtin_expect(HQBAD(q), 0)) { _Pragma("nounroll") for (u32 sp = 0; sp < SPIN_CAP; ++sp) { \
      __builtin_amdgcn_s_sleep(RSL); asm volatile("" ::: "memory"); HQLOAD(q, j); if (!HQBAD(q)) break; } } SCHED(); } while (0)
  if constexpr (A) {
    const int T32 = tile * 4 + wave;
    u32x2 st[7];
    u32x4 hb[28];
    u64 fcl;
    const u32 stv = (7 * hh * 2048 + (lane & 31)) * 8;
#define STA(c_) _Pragma("unroll") for (int i = 0; i < 7; ++i) asm volatile("global_load_dwordx2 %0, %1, %2 sc1" : c_(st[i]) : "v"(stv), "s"(ws + WS_ST + (i * 2048 + T32 * 32) * 8) : "memory")
#define STW(k_) asm volatile("s_waitcnt vmcnt(%7)" : "+v"(st[0]), "+v"(st[1]), "+v"(st[2]), "+v"(st[3]), "+v"(st[4]), "+v"(st[5]), "+v"(st[6]) : "n"(k_) : "memory")
#define STBAD() (!__all(max(max(max(st[0].y, st[1].y), max(st[2].y, st[3].y)), max(max(st[4].y, st[5].y), st[6].y)) != EMPTY))
#define HBA(c_, q) _Pragma("unroll") for (int i = 0; i < 4; ++i) asm volatile("global_load_dwordx4 %0, %1, %2 offset:%3 sc1" : c_(hb[4 * (q) + i]) : "v"(lane * 16), "s"(ws + WS_H + (T32 * 28 + 4 * (q)) * 1024), "n"(i * 1024) : "memory")
    lds_wait2(L, S_HFA, S_HFB);
    __builtin_amdgcn_s_sleep(DLY3);
    if (!SPEC || r2 >= 14) {
#pragma nounroll
      for (u32 sp = 0; sp < SPIN_CAP; ++sp) { STA("=v"); STW(0); if (!STBAD()) break; __builtin_amdgcn_s_sleep(RSL); }
    }
    STA("=v");
    STAMP(17);
#pragma unroll
    for (int q = 0; q < 7; ++q) { HBA("=v", q); }
    asm volatile("global_load_dwordx2 %0, %1, %2 sc1" : "=v"(fcl) : "v"(p4 * 128), "s"(ws + WS_FH + (T32 * 32 + 28) * 128) : "memory");
    u32 wb_[3] = {(u32)lane * 16, (u32)lane * 16 + 2 * SLOT, (u32)lane * 16 + 4 * SLOT};
    asm volatile("" : "+v"(wb_[1]), "+v"(wb_[2]));
#define WFRAG(sl, s) (*(LAS u32x4*)(L + wb_[(sl) >> 1] + ((sl) & 1) * SLOT + (s) * 1024))
    LAS char* csl = L + L_CS + hh * 16;
    u32x4 ring[7];
    f32x4 cq[4];
    float nmu, ve, k0, k1;
    u32 mx;
    const int pvo = WS_FHH + ((tile * 16 + r) * 4 + wave) * 128;
#define PROG(n) FLAG(pvo + 131072, n)
    f32x16 acc[2];
    float y[8], gu[8];
    u32x4 yb;
    u32x4 cw;
#define LT(i)
    auto gu_block = [&](auto JC) __attribute__((always_inline)) {
      constexpr int j = JC.value;
      sfor<0, 28>([&](auto SC) LAM {
        constexpr int s = SC.value;
        if constexpr (j == 0) { u32x4 t_ = hb[s]; asm volatile("s_waitcnt vmcnt(%1)" : "+v"(t_) : "n"(28 - s) : "memory"); hb[s] = t_; }
        acc[j & 1] = mfma32(ring[s % 7], hb[s], acc[j & 1]);
        if constexpr (j == 0) mx = max(mx, hb[s].w);
        if constexpr ((j == 2 || j == 4) && s == 12) cw = *(volatile LAS u32x4*)(L + L_FLG + (j == 2 ? S_WIN3 : S_REFILL) * 16);
        if constexpr ((j == 2 || j == 4) && s == 20) { if (__builtin_expect(!__builtin_amdgcn_readfirstlane(cw.x & cw.y & cw.z & cw.w), 0)) lds_wait(L, j == 2 ? S_WIN3 : S_REFILL); asm volatile("" ::: "memory"); }
        if constexpr (j == 1 && s < 7 - NCL) ST16(rs, (u32x4{EMPTY, EMPTY, EMPTY, EMPTY}), lane * 16, hho + (NCL + s) * 8192);
        if constexpr (s + 7 < 28) ring[s % 7] = WFRAG(gu_slot(j), s + 7);
        else if constexpr (j < 6) ring[s % 7] = WFRAG(gu_slot(j + 1), s - 21);
        if constexpr (s == 0 && (j == 1 || j == 2)) lds_signal(L, j == 1 ? S_WSEEN : S_FREE01, wave);
        if constexpr (s == 0 && j >= 3) *(volatile LAS u32*)(L + L_FLG + S_DONE4 * 16 + wave * 4) = j - 2u;
        if constexpr (j > 0) {
          sfor<0, 8>([&](auto EC) LAM {
            constexpr int i = EC.value, k = s - 1 - 2 * i;
            const float ga = acc[(j - 1) & 1][i];
            if constexpr (k == 0) y[i] = ga * ga;
            if constexpr (k == 1) y[i] = fmaf(y[i], k1, k0);
            if constexpr (k == 2) y[i] = ga * y[i];
            if constexpr (k == 3) y[i] = fexp2(y[i]);
            if constexpr (k == 4) y[i] = fmaf(y[i], ve, ve);
            if constexpr (k == 5) { gu[i] = ga * acc[(j - 1) & 1][i + 8]; PIN(gu[i]); }
            if constexpr (k == 6) y[i] = frcp(y[i]);
            if constexpr (k == 7) y[i] = y[i] * gu[i];
            if constexpr (k >= 0 && k < 8 && k != 5) PIN(y[i]);
          });
          if constexpr (s == 21) { yb[0] = pk(y[0], y[1]); yb[1] = pk(y[2], y[3]); }
          if constexpr (s == 22) yb[2] = pk(y[4], y[5]);
          if constexpr (NCL == 1 && j == 2 && s == 22) WAITVM(2);
          if constexpr (j == 3 && s == 22 && NCL < 7 && NCL > 1) WAITVM(4);
          if constexpr (s == 23) { yb[3] = filled(pk(y[6], y[7])); ST16(rs, yb, lane * 16, hho + (j - 1) * 8192); }
          if constexpr (s == 24 && j <= (HELP > 4 ? HELP : 4)) FLAG(pvo, j);
        }
        if constexpr (j < 6 && s >= 17 && s < 21) cq[s - 17] = *(LAS f32x4*)(csl + (j + 1) * 128 + (s - 17) * 32);
        if constexpr (j < 6 && s >= 22 && s < 27) {
          sfor<0, 16>([&](auto EC) LAM {
            constexpr int e = EC.value;
            if constexpr ((e < 2 ? 22 : e < 4 ? 23 : 23 + (e >> 2)) == s) { float v = nmu * cq[e >> 2][e & 3]; PIN(v); acc[(j + 1) & 1][e] = v; }
          });
        }
        SCHED();
      });
      LT(j);
      if (j == 0) STAMP(18);
      if (j == 3) STAMP(26);
      if (j == 1) STAMP(28);
      if (j == 2) STAMP(29);
      if (j == 4) STAMP(30);
      if (j == 5) STAMP(31);
    };
#define HBW(q) asm volatile("s_waitcnt vmcnt(0)" : "+v"(hb[4 * (q)]), "+v"(hb[4 * (q) + 1]), "+v"(hb[4 * (q) + 2]), "+v"(hb[4 * (q) + 3]) :: "memory")
#define HBBAD(q) (!__all(max(max(hb[4 * (q)].w, hb[4 * (q) + 1].w), max(hb[4 * (q) + 2].w, hb[4 * (q) + 3].w)) != EMPTY))
#define HBC(q, c_) asm volatile("s_bitcmp0_b32 %6, %7\n\ts_cbranch_scc1 1f\n\tglobal_load_dwordx4 %0, %4, %5 sc1\n\tglobal_load_dwordx4 %1, %4, %5 offset:1024 sc1\n\t" \
      "global_load_dwordx4 %2, %4, %5 offset:2048 sc1\n\tglobal_load_dwordx4 %3, %4, %5 offset:3072 sc1\n1:" : "+v"(hb[4 * (q)]), "+v"(hb[4 * (q) + 1]), "+v"(hb[4 * (q) + 2]), "+v"(hb[4 * (q) + 3]) \
      : "v"(lane * 16), "s"(ws + WS_H + (T32 * 28 + 4 * (q)) * 1024), "s"(c_), "n"(q) : "memory", "scc")
#define REDO(m_) do { STA("+v"); _Pragma("unroll") for (int q = 0; q < 7; ++q) HBC(q, m_); STW(0); _Pragma("unroll") for (int q = 0; q < 7; ++q) HBW(q); } while (0)
#define FIX() do { _Pragma("nounroll") for (u32 sp = 0; sp < SPIN_CAP; ++sp) { u32 m = 0; \
        _Pragma("unroll") for (int q = 0; q < 7; ++q) m |= HBBAD(q) ? 1u << q : 0u; \
        if (!m && !STBAD()) break; if (sp) __builtin_amdgcn_s_sleep(RSL); m = __builtin_amdgcn_readfirstlane(m); REDO(m); } } while (0)
    auto first = [&]() LAM {
#pragma unroll
      for (int k = 0; k < 4; ++k) cq[k] = *(LAS f32x4*)(csl + k * 32);
#pragma unroll
      for (int s = 0; s < 7; ++s) ring[s] = WFRAG(0, s);
      f32x2 sf[7];
#pragma unroll
      for (int i = 0; i < 7; ++i) sf[i] = __builtin_bit_cast(f32x2, st[i]);
      float s1 = ((sf[0].x + sf[1].x) + (sf[2].x + sf[3].x)) + ((sf[4].x + sf[5].x) + sf[6].x);
      float s2 = ((sf[0].y + sf[1].y) + (sf[2].y + sf[3].y)) + ((sf[4].y + sf[5].y) + sf[6].y);
      { f32x2 a = sw32(s1); s1 = a.x + a.y; a = sw32(s2); s2 = a.x + a.y; }
      nmu = s1 * (-1.f / 448.f); ve = fmaxf(s2 * (1.f / 448.f) - nmu * nmu, 0.f) + 1e-5f;
      const float rstd = __builtin_amdgcn_rsqf(ve);
      k0 = -2.30220819f * rstd; k1 = -0.10294324f * rstd * rstd * rstd;
#pragma unroll
      for (int i = 0; i < 16; ++i) acc[0][i] = nmu * cq[i >> 2][i & 3];
      mx = 0;
      __builtin_amdgcn_s_setprio(GUPRIO);
      SCHED();
      gu_block(IC<0>{});
    };
    STW(29);
    if (__builtin_expect(STBAD(), 0)) {
      u32 m = 0;
#pragma unroll
      for (int i = 0; i < 7; ++i) { const u64 bm = __builtin_amdgcn_ballot_w64(st[i].y == EMPTY); m |= ((u32)bm ? 1u << (i >> 1) : 0u) | ((u32)(bm >> 32) ? 1u << ((7 + i) >> 1) : 0u); }
      m = __builtin_amdgcn_readfirstlane(m);
      asm volatile("s_waitcnt vmcnt(%0)" :: "n"(FXW) : "memory");
      REDO(m);
      FIX();
    }
    first();
    if (__builtin_expect(!__all(mx != EMPTY), 0)) { FIX(); first(); }
    asm volatile("s_waitcnt vmcnt(0)" : "+v"(fcl) :: "memory");
    if (__builtin_expect(!__all(fcl == tag + 1), 0)) poll_flags(ws, WS_FH + (T32 * 32 + 28 + p4) * 128, lane < 4, tag);
    gu_block(IC<1>{}); gu_block(IC<2>{}); gu_block(IC<3>{}); gu_block(IC<4>{}); gu_block(IC<5>{}); gu_block(IC<6>{});
#pragma unroll
    for (int i = 0; i < 8; ++i) { const float ga = acc[0][i]; y[i] = ga * acc[0][i + 8] * frcp(fmaf(fexp2(ga * fmaf(ga * ga, k1, k0)), ve, ve)); }
    yb = u32x4{pk(y[0], y[1]), pk(y[2], y[3]), pk(y[4], y[5]), filled(pk(y[6], y[7]))};
    ST16(rs, yb, lane * 16, hho + 6 * 8192);
    if (HELP == 7) FLAG(pvo, 7);
    __builtin_amdgcn_s_setprio(0);
    STAMP(19);
    if (r2 < 14) {
      f32x16 da[2][2] = {};
      u32x4 hq[2][8], wl[2][8];
      HBASE();
      __builtin_amdgcn_s_sleep(ADLY);
      lds_wait1(L, S_CONS, w2);
      STAMP2(15);
      HQLOAD(0, 5);
      if (A6L == 0) HQLOAD(1, 6);
#pragma unroll
      for (int i = 0; i < 8; ++i) wl[0][i] = HFRAG(2, i);
      SCHED();
      STAMP2(16);
      HQCHECK(0, 5);
      if (A6L == 1) HQLOAD(1, 6);
#pragma unroll
      for (int i = 0; i < 8; ++i) wl[1][i] = HFRAG(0, i);
      SCHED();
      DOWN16(da, wl[0], hq[0]);
      SCHED();
      STAMP2(18);
      HQCHECK(1, 6);
      STAMP2(31);
      DOWN16(da, wl[1], hq[1]);
      PARTIAL_OUT(da, 0, 2);
      STAMP(20);
    }
  } else {
    lds_wait2(L, S_HFA, S_HFB);
    if (W2F < 3) { GUBLK(3); if (W3THR) WAITVM(3); }
    if (W2F < 3 || W2F == 5) GUBLK(4);
#define REFILL() do { lds_wait<BSL>(L, S_WSEEN); GUBLK(5); lds_wait<BSL>(L, S_FREE01); GUBLK(6); } while (0)
    if (r2 >= 14) {
      WAITVM(0); lds_signal(L, S_WIN3, w2);
      REFILL();
      WAITVM(0); lds_signal(L, S_REFILL, w2);
    } else {
#pragma unroll
      for (int n = 0; n < 24; ++n) {
        asm volatile("s_waitcnt vmcnt(%4)\n\tglobal_load_dwordx4 %0, %1, %2 offset:%3 sc1" : "=v"(wdf[n >> 3][n & 7]) : "v"(lane * 16), "s"(ws + wdo + 4096 + (n >> 3) * 8192), "n"((n & 7) * 1024 - 4096), "n"(THRN0) : "memory");
        if (n == 7) { WAITVM(8); lds_signal(L, S_WIN3, w2); }
      }
      STAMP2(23);
      REFILL();
      f32x16 acc[2][2] = {};
      u32x4 hq[2][8];
      STAMP2(16);
      HBASE();
      u64 pv;
      const char* hs = ws + ho + 4096;
#define PEEK() do { pv = tag + 7; asm volatile("s_mov_b64 exec, 0xff\n\tglobal_atomic_add_x2 %0, %1, %2, %3 sc0\n\ts_mov_b64 exec, -1" : "+v"(pv) : "v"(po), "v"(0ull), "s"(ws) : "memory"); } while (0)
#define PEEKED(k_, n_) do { asm volatile("s_waitcnt vmcnt(%1)" : "+v"(pv) : "n"(k_) : "memory"); PMIN(pv); NEEDP(n_); } while (0)
#define HQA(c_, q, j) _Pragma("unroll") for (int i = 0; i < 8; ++i) asm volatile("global_load_dwordx4 %0, %1, %2 offset:%3 sc1" : c_(hq[q][i]) : "v"(lane * 16), "s"(hs + (j) * 8192), "n"(i * 1024 - 4096) : "memory")
#define HQW(q, k_) asm volatile("s_waitcnt vmcnt(%8)" : "+v"(hq[q][0]), "+v"(hq[q][1]), "+v"(hq[q][2]), "+v"(hq[q][3]), "+v"(hq[q][4]), "+v"(hq[q][5]), "+v"(hq[q][6]), "+v"(hq[q][7]) : "n"(k_) : "memory")
#define HQOK(q, j, k_) do { HQW(q, k_); if (__builtin_expect(HQBAD(q), 0)) { _Pragma("nounroll") for (u32 sp = 0; sp < SPIN_CAP; ++sp) { \
        __builtin_amdgcn_s_sleep(RSL); HQA("+v", q, j); HQW(q, 0); if (!HQBAD(q)) break; } } SCHED(); } while (0)
      PEEK();
      FREE(1); WDLOAD(3, 0);
      WAITVM(8);
#pragma unroll
      for (int k = 0; k < 3; ++k) asm volatile("" : "+v"(wdf[k][0]), "+v"(wdf[k][1]), "+v"(wdf[k][2]), "+v"(wdf[k][3]), "+v"(wdf[k][4]), "+v"(wdf[k][5]), "+v"(wdf[k][6]), "+v"(wdf[k][7]) :: "memory");
      lds_signal(L, S_REFILL, w2);
      PEEKED(8, 1);
      STAMP2(1);
      PEEK();
      HQA("=v", 0, 0);
      FREE(2); WDLOAD(4, 1);
      PEEKED(16, 2);
      STAMP2(2);
      HQA("=v", 1, 1);
      PEEK();
      u32 fr3;
      { const u32x4 fr = *(volatile LAS u32x4*)(L + L_FLG + S_DONE4 * 16); fr3 = !W5D || __builtin_amdgcn_readfirstlane(min(min(fr.x, fr.y), min(fr.z, fr.w))) >= 3; }
      if (fr3) { if (!W5D) FREE(3); WDLOAD(5, 2); }
      if (fr3) WAITVM(25); else WAITVM(17);
      HQOK(0, 0, 25);
      __builtin_amdgcn_s_setprio(DP0);
      DOWN16(acc, wdf[0], hq[0]);
      __builtin_amdgcn_s_setprio(0);
#pragma unroll
      for (int i = 0; i < 8; ++i) wdf[0][i] = HFRAG(0, i);
      SCHED();
      STAMP2(8);
      if (!fr3) { FREE(3); WDLOAD(5, 2); }
      PEEKED(8, 3);
      STAMP2(3);
      HQA("=v", 0, 2);
      PEEK();
      asm volatile("s_waitcnt lgkmcnt(0)" : "+v"(wdf[0][7]) :: "memory");
      WDLOAD(6, 0);
      HQOK(1, 1, 25);
      __builtin_amdgcn_s_setprio(DP0);
      DOWN16(acc, wdf[1], hq[1]);
#pragma unroll
      for (int i = 0; i < 8; ++i) wdf[1][i] = HFRAG(1, i);
      SCHED();
      STAMP2(9);
      PEEKED(8, 4);
      STAMP2(4);
      HQA("=v", 1, 3);
      __builtin_amdgcn_s_setprio(DNPRIO);
      HQOK(0, 2, 8);
      lds_signal(L, S_CONS, w2);
      SCHED();
      DOWN16(acc, wdf[2], hq[0]);
      SCHED();
      STAMP2(10);
      FREE(4);
      HQA("=v", 0, 4);
      HQOK(1, 3, 8);
      DOWN16(acc, wdf[0], hq[1]);
      SCHED();
      STAMP2(11);
      HQOK(0, 4, 0);
      DOWN16(acc, wdf[1], hq[0]);
      PARTIAL_OUT(acc, 1, 3);
      STAMP(20);
    }
  }
  if (r2 < 14) {
    BAR();
    STAMP2(7);
    float c[8] = {blo(hres.x), bhi(hres.x), blo(hres.y), bhi(hres.y), blo(hres.z), bhi(hres.z), blo(hres.w), bhi(hres.w)};
    const int rl = (kh * 32 + (tb & 1) * 16 + l15) * 16;
    f32x4 v0 = z4, v1 = z4;
#pragma unroll
    for (int h = 0; h < 2; ++h) {
      const int bf = (tb >> 1) * 2 + h;
      u32 rq = (bf == 3 ? 0 : (bf + 2) * SLOT) + g * 1024 + rl;
      const u32 rq7 = g == 3 ? L_EX + bf * 4096 + rl : rq + 4096, st7 = g == 3 ? 1024 : 7168;
      asm volatile("" : "+v"(rq));
#pragma unroll
      for (int w = 0; w < 4; ++w) { v0 += *(LAS f32x4*)(L + rq + w * 7168); v1 += *(LAS f32x4*)(L + rq7 + w * st7); }
    }
#pragma unroll
    for (int h = 0; h < 2; ++h) {
      const f32x4 v = h ? v1 : v0;
      const f32x2 p = swp32(v[0], v[2]), q = swp32(v[1], v[3]), t0 = swp16(p.x, q.x), t1 = swp16(p.y, q.y);
      c[4 * h] += t0.x; c[4 * h + 1] += t0.y; c[4 * h + 2] += t1.x; c[4 * h + 3] += t1.y;
    }
    { const u32x4 ov = {pk(c[0], c[1]), pk(c[2], c[3]), pk(c[4], c[5]), pk(c[6], c[7])}; asm volatile("global_store_dwordx4 %0, %1, %2 sc1\n\ts_nop 1" :: "v"(xo), "v"(ov), "s"(out) : "memory"); }
    if (A && b == 0 && tid == 0) __hip_atomic_store((GAS u64*)(ws + WS_E), tag, RLX_AGENT);
  }
  STAMP(21);
  };
  if (wave < 4) role(IC<1>{}); else role(IC<0>{});
}

extern "C" void kernel_launch(void* const* d_in, const int* in_sizes, int n_in, void* d_out, int out_size, void* d_ws, size_t ws_size, hipStream_t stream) {
  (void)hipFuncSetAttribute((const void*)fused_block, hipFuncAttributeMaxDynamicSharedMemorySize, LDS_BYTES);
  fused_block<<<256, 512, LDS_BYTES, stream>>>((const char*)d_in[0], (const char*)d_in[1], (const char*)d_in[2], (const char*)d_in[3], (const char*)d_in[5], (const char*)d_in[6],
                                               (const char*)d_in[7], (char*)d_out, (char*)d_ws);
}
```

```cpp
#include <hip/hip_runtime.h>
#include <hip/hip_bf16.h>
#include <stdint.h>

typedef unsigned u32;
typedef unsigned long long u64;
typedef u32 u32x4 __attribute__((ext_vector_type(4)));
typedef u32 u32x2 __attribute__((ext_vector_type(2)));
typedef float f32x2 __attribute__((ext_vector_type(2)));
typedef float f32x4 __attribute__((ext_vector_type(4)));
typedef float f32x16 __attribute__((ext_vector_type(16)));
typedef short s16x4 __attribute__((ext_vector_type(4)));
typedef __bf16 bf16x2 __attribute__((ext_vector_type(2)));
typedef __bf16 bf16x8 __attribute__((ext_vector_type(8)));
#define LAS __attribute__((address_space(3)))
#define GAS __attribute__((address_space(1)))
#define DI __device__ __forceinline__
#define RLX_AGENT __ATOMIC_RELAXED, __HIP_MEMORY_SCOPE_AGENT

constexpr int WS_E = 0;
constexpr int WS_FKV = 8192;
constexpr int WS_FO = WS_FKV + 131072;
constexpr int WS_FH = WS_FO + 131072;
constexpr int WS_FG = WS_FH + 262144;
constexpr int WS_FD = WS_FG + 114688;
constexpr int WS_FHH = WS_FD + 57344;
constexpr int WS_CS = WS_FHH + 262144;
constexpr int WS_ST = WS_CS + 200704;
constexpr int WS_WGU = WS_ST + 229376;
constexpr int WS_WD = WS_WGU + 3211264;
constexpr int WS_KVP = WS_WD + 1605632;
constexpr int WS_O = WS_KVP + 1048576;
constexpr int WS_H = WS_O + 2097152;
constexpr int WS_HH = WS_H + 1835008;
#define UNITX4
#define NOWD
#define WDOFF(cg, fb, bl) (((((cg) * 4 + (fb) / 28) * 7 + (fb) % 7) * 4 + (fb) / 7 % 4) * 2 + (bl))
#define HHOFF(T32, fb) (((((T32) >> 1) * 4 + (fb) / 28) * 7 + (fb) % 7) * 8 + ((fb) / 7 & 3) * 2 + ((T32) & 1))
constexpr int WS_STAMP = WS_HH + 7340032;
constexpr int WS_LOG = WS_STAMP + 256 * 8 * 32 * 8;
constexpr int WS_USED = WS_LOG + 4096;
constexpr int SLOT = 28672;
constexpr int L_WK = 4 * SLOT, L_WV = 0, L_WQ = 14336;
constexpr int L_PKV = 2 * SLOT;
constexpr int L_WO = 3 * SLOT;
constexpr int L_EX = 5 * SLOT;
constexpr int L_KVA = 5 * SLOT;
constexpr int L_ST1 = 5 * SLOT + 4096;
constexpr int L_WPHI = 5 * SLOT + 5120;
constexpr int L_CSP = 5 * SLOT + 7168;
constexpr int L_CS = 159744;
constexpr int L_FLG = 160768;
constexpr int L_JUNK = 161280;
constexpr int L_STAMP = 161536;
constexpr int L_RED = SLOT;
constexpr int LDS_BYTES = 163840;
constexpr int gu_slot(int j) { return j % 5; }
constexpr int CODE_PF = 45056;
constexpr u32 SPIN_CAP = 1u << 16;
#define LL 1
#define AP 1

#define STAMP(i) do {} while (0)
#define STAMP2(i) do {} while (0)
#define STAMP2X(i) do {} while (0)

DI f32x4 mfma16(u32x4 a, u32x4 b, f32x4 c) { return __builtin_amdgcn_mfma_f32_16x16x32_bf16(__builtin_bit_cast(bf16x8, a), __builtin_bit_cast(bf16x8, b), c, 0, 0, 0); }
DI f32x16 mfma32(u32x4 a, u32x4 b, f32x16 c) { return __builtin_amdgcn_mfma_f32_32x32x16_bf16(__builtin_bit_cast(bf16x8, a), __builtin_bit_cast(bf16x8, b), c, 0, 0, 0); }
DI u32 pk(float lo, float hi) { f32x2 p = {lo, hi}; return __builtin_bit_cast(u32, __builtin_convertvector(p, bf16x2)); }
DI float blo(u32 v) { return __builtin_bit_cast(float, v << 16); }
DI float bhi(u32 v) { return __builtin_bit_cast(float, v & 0xffff0000u); }
DI float dot2(u32 a, u32 b, float c) { return __builtin_amdgcn_fdot2_f32_bf16(__builtin_bit_cast(bf16x2, a), __builtin_bit_cast(bf16x2, b), c, false); }
DI u32x2 trread(LAS char* p) { return __builtin_bit_cast(u32x2, __builtin_amdgcn_ds_read_tr16_b64_v4i16((LAS s16x4*)p)); }
DI u32x4 trfrag(LAS char* p, int step) { u32x2 a = trread(p), b = trread(p + step); return u32x4{a.x, a.y, b.x, b.y}; }
DI float fexp2(float x) { return __builtin_amdgcn_exp2f(x); }
DI float frcp(float x) { return __builtin_amdgcn_rcpf(x); }
template <int C> DI float dppror(float x) { return __builtin_bit_cast(float, __builtin_amdgcn_update_dpp(0, __builtin_bit_cast(int, x), 0x120 + C, 0xf, 0xf, false)); }
DI float row_max(float x) { x = fmaxf(x, dppror<8>(x)); x = fmaxf(x, dppror<4>(x)); x = fmaxf(x, dppror<2>(x)); return fmaxf(x, dppror<1>(x)); }
DI float row_min(float x) { x = fminf(x, dppror<8>(x)); x = fminf(x, dppror<4>(x)); x = fminf(x, dppror<2>(x)); return fminf(x, dppror<1>(x)); }
DI float row_sum(float x) { x += dppror<8>(x); x += dppror<4>(x); x += dppror<2>(x); return x + dppror<1>(x); }
DI f32x2 sw16(float x) { u32 u = __builtin_bit_cast(u32, x); auto r = __builtin_amdgcn_permlane16_swap(u, u, false, false); return f32x2{__builtin_bit_cast(float, (u32)r[0]), __builtin_bit_cast(float, (u32)r[1])}; }
DI f32x2 sw32(float x) { u32 u = __builtin_bit_cast(u32, x); auto r = __builtin_amdgcn_permlane32_swap(u, u, false, false); return f32x2{__builtin_bit_cast(float, (u32)r[0]), __builtin_bit_cast(float, (u32)r[1])}; }
DI float grp_sum(float x) { f32x2 a = sw16(x); x = a.x + a.y; a = sw32(x); return a.x + a.y; }
DI float grp_max(float x) { f32x2 a = sw16(x); x = fmaxf(a.x, a.y); a = sw32(x); return fmaxf(a.x, a.y); }
DI float grp_min(float x) { f32x2 a = sw16(x); x = fminf(a.x, a.y); a = sw32(x); return fminf(a.x, a.y); }

#define BAR() asm volatile("s_waitcnt lgkmcnt(0)\n\ts_barrier" ::: "memory")
#define WAITVM(n) asm volatile("s_waitcnt vmcnt(" #n ")" ::: "memory")
#define DRAIN() do { asm volatile("s_waitcnt vmcnt(0)" ::: "memory"); __builtin_amdgcn_s_waitcnt(0x0F70); } while (0)
#define SCHED() __builtin_amdgcn_sched_barrier(0)
DI void dma16(const void* base, u32 voff, u32 lds) {
  u32 keep;
  asm volatile("s_mov_b32 %0, m0\n\ts_mov_b32 m0, %3\n\ts_nop 0\n\tglobal_load_lds_dwordx4 %1, %2\n\ts_mov_b32 m0, %0" : "=&s"(keep) : "v"(voff), "s"(base), "s"(lds) : "memory");
}
DI void dma16c(const void* base, u32 voff, u32 lds) {
  u32 keep;
  asm volatile("s_mov_b32 %0, m0\n\ts_mov_b32 m0, %3\n\ts_nop 0\n\tglobal_load_lds_dwordx4 %1, %2 sc1\n\ts_mov_b32 m0, %0" : "=&s"(keep) : "v"(voff), "s"(base), "s"(lds) : "memory");
}
DI void dma4(const void* base, u32 voff, u32 lds) {
  u32 keep;
  asm volatile("s_mov_b32 %0, m0\n\ts_mov_b32 m0, %3\n\ts_nop 0\n\tglobal_load_lds_dword %1, %2\n\ts_mov_b32 m0, %0" : "=&s"(keep) : "v"(voff), "s"(base), "s"(lds) : "memory");
}
DI void dma16c_if(u32 go, const void* base, u32 voff, u32 lds) {
  u32 keep;
  asm volatile("s_cmp_eq_u32 %4, 0\n\ts_cbranch_scc1 1f\n\ts_mov_b32 %0, m0\n\ts_mov_b32 m0, %3\n\ts_nop 0\n\tglobal_load_lds_dwordx4 %1, %2 sc1\n\ts_mov_b32 m0, %0\n1:"
               : "=&s"(keep) : "v"(voff), "s"(base), "s"(lds), "s"(go) : "memory", "scc");
}
template <int SI, int LI> DI void dma7(__amdgpu_buffer_rsrc_t rs, u32 voff, u32 so, u32 lds) {
  u32 t;
  asm volatile("s_add_u32 m0, %4, %6\n\ts_add_u32 %0, %3, %5\n\tbuffer_load_dwordx4 %1, %2, %0 offen sc1 lds\n\tbuffer_load_dwordx4 %1, %2, %0 offen offset:1024 sc1 lds\n\t"
               "buffer_load_dwordx4 %1, %2, %0 offen offset:2048 sc1 lds\n\tbuffer_load_dwordx4 %1, %2, %0 offen offset:3072 sc1 lds\n\t"
               "s_add_u32 m0, m0, 4096\n\ts_add_u32 %0, %0, 4096\n\tbuffer_load_dwordx4 %1, %2, %0 offen sc1 lds\n\tbuffer_load_dwordx4 %1, %2, %0 offen offset:1024 sc1 lds\n\t"
               "buffer_load_dwordx4 %1, %2, %0 offen offset:2048 sc1 lds"
               : "=&s"(t) : "v"(voff), "s"(rs), "s"(so), "s"(lds), "n"(SI), "n"(LI) : "memory", "scc");
}
constexpr int wrow(int q, int j, int hf) { return (((7 * q + j) >> 2) * 64 + 2 * ((7 * q + j) & 3) + hf) * 896 + 1024; }
#define DL_(n, o) "s_mov_b32 %0, %" #n "\n\tbuffer_load_dwordx4 %1, %2, %0 offen offset:" #o " lds\n\t"
template <int J> DI void dma_wd(__amdgpu_buffer_rsrc_t rs, u32 voff, u32 lds, u32 lds7) {
  u32 t;
  asm volatile("s_mov_b32 m0, %3\n\t" DL_(5, 0) DL_(6, 1024) DL_(7, 2048) DL_(8, 3072) "s_add_u32 m0, m0, 4096\n\t" DL_(9, 0) DL_(10, 1024) DL_(11, 2048) "s_mov_b32 m0, %4\n\t" DL_(12, 0)
               : "=&s"(t) : "v"(voff), "s"(rs), "s"(lds), "s"(lds7), "n"(wrow(0, J, 0)), "n"(wrow(0, J, 1) - 1024), "n"(wrow(1, J, 0) - 2048), "n"(wrow(1, J, 1) - 3072),
                 "n"(wrow(2, J, 0)), "n"(wrow(2, J, 1) - 1024), "n"(wrow(3, J, 0) - 2048), "n"(wrow(3, J, 1)) : "memory", "scc");
}
template <int N, int LO> DI void dma_wd1(__amdgpu_buffer_rsrc_t rs, u32 voff, u32 lds) {
  u32 t;
  asm volatile("s_add_u32 m0, %3, %4\n\t" DL_(5, 0) : "=&s"(t) : "v"(voff), "s"(rs), "s"(lds), "n"(LO), "n"(wrow(N / 2 % 4, N / 8, N % 2)) : "memory", "scc");
}
DI u32 vm_count() {
  u32 t;
  asm volatile("s_getreg_b32 %0, hwreg(HW_REG_IB_STS)" : "=s"(t) :: "memory");
  return (t & 15u) | ((t >> 18) & 48u);
}
DI u32 min8(u32 c) {
  c = min(c, (u32)__builtin_amdgcn_update_dpp(0, (int)c, 0x121, 0xf, 0xf, false)); c = min(c, (u32)__builtin_amdgcn_update_dpp(0, (int)c, 0x122, 0xf, 0xf, false));
  c = min(c, (u32)__builtin_amdgcn_update_dpp(0, (int)c, 0x124, 0xf, 0xf, false));
  return __builtin_amdgcn_readfirstlane(min(c, (u32)__builtin_amdgcn_update_dpp(0, (int)c, 0x128, 0xf, 0xf, false)));
}
DI void dma4c(const void* base, u32 voff, u32 lds) {
  u32 keep;
  asm volatile("s_mov_b32 %0, m0\n\ts_mov_b32 m0, %3\n\ts_nop 0\n\tglobal_load_lds_dword %1, %2 sc1\n\ts_mov_b32 m0, %0" : "=&s"(keep) : "v"(voff), "s"(base), "s"(lds) : "memory");
}
#define LD16(rs, off) __builtin_bit_cast(u32x4, __builtin_amdgcn_raw_buffer_load_b128(rs, off, 0, 16))
#define ST16(rs, v, off, soff) do { const u32x4 d_ = __builtin_bit_cast(u32x4, v); __builtin_amdgcn_raw_buffer_store_b128(d_, rs, off, soff, 16); asm volatile("s_nop 1" :: "v"(d_) : "memory"); } while (0)

#define FLAG(off, n) __builtin_amdgcn_raw_buffer_store_b64(__builtin_bit_cast(u32x2, tag + (n)), rs, fvo, off, 16)
#define FLAGC(c, off, n) __builtin_amdgcn_raw_buffer_store_b64(__builtin_bit_cast(u32x2, tag + (n)), rs, (c) ? fvo : NOWHERE, off, 16)
constexpr int NOWHERE = 0x7ffffff0;
constexpr u32 EMPTY = 0xffffffffu;
DI u32 filled(u32 w) { return min(w, EMPTY - 1u); }
DI u64 peek(const char* ws, u32 off, bool on, u64 dflt) {
  u64 z = 0, v = dflt;
  asm volatile("" : "+v"(z));
  if (on) v = __hip_atomic_fetch_add((GAS u64*)(ws + off), z, __ATOMIC_RELAXED, AP == 2 ? __HIP_MEMORY_SCOPE_SYSTEM : __HIP_MEMORY_SCOPE_AGENT);
  return v;
}
DI void poll_flags(const char* ws, u32 off, bool on, u64 tag) {
#pragma nounroll
  for (u32 sp = 0; sp < SPIN_CAP; ++sp) {
    u64 v = tag + 1;
    if (on || !LL) v = __hip_atomic_load((const GAS u64*)(ws + off), RLX_AGENT);
    if (__all(v == tag + 1)) break;
  }
  __builtin_amdgcn_fence(__ATOMIC_ACQUIRE, "wavefront");
}
template <int SL = 1> DI void lds_wait(LAS char* L, int sig) {
#pragma nounroll
  for (u32 sp = 0; sp < (SPIN_CAP << 4); ++sp) {
    const u32x4 v = *(volatile LAS u32x4*)(L + L_FLG + sig * 16);
    if (__builtin_amdgcn_readfirstlane(v.x & v.y & v.z & v.w)) break;
    __builtin_amdgcn_s_sleep(SL);
  }
  asm volatile("" ::: "memory");
}
DI void lds_wait2(LAS char* L, int sig, int sig2) {
#pragma nounroll
  for (u32 sp = 0; sp < (SPIN_CAP << 4); ++sp) {
    const u32x4 v = *(volatile LAS u32x4*)(L + L_FLG + sig * 16) & *(volatile LAS u32x4*)(L + L_FLG + sig2 * 16);
    if (__builtin_amdgcn_readfirstlane(v.x & v.y & v.z & v.w)) break;
    __builtin_amdgcn_s_sleep(1);
  }
  asm volatile("" ::: "memory");
}
DI void lds_wait1(LAS char* L, int sig, int w) {
#pragma nounroll
  for (u32 sp = 0; sp < (SPIN_CAP << 4); ++sp) {
    if (__builtin_amdgcn_readfirstlane(*(volatile LAS u32*)(L + L_FLG + sig * 16 + w * 4))) break;
    __builtin_amdgcn_s_sleep(1);
  }
  asm volatile("" ::: "memory");
}
DI void lds_signal(LAS char* L, int sig, int w) {
  asm volatile("" ::: "memory");
  *(volatile LAS u32*)(L + L_FLG + sig * 16 + w * 4) = 1u;
}

template <int N> struct IC { static constexpr int value = N; };
template <int I, int N, class F> DI void sfor(F&& f) { if constexpr (I < N) { f(IC<I>{}); sfor<I + 1, N>(f); } }
#define LAM __attribute__((always_inline))
#define PIN(v) asm volatile("" : "+v"(v))
constexpr float LOG2E = 1.4426950408889634f;
template <int K> DI void macc(f32x4& c, u32x4 a, u32x4 b) {
  if (K == 0) asm volatile("v_mfma_f32_16x16x32_bf16 %0, %1, %2, 0" : "=&v"(c) : "v"(a), "v"(b));
  else if (K == 1) asm volatile("v_mfma_f32_16x16x32_bf16 %0, %1, %2, %0" : "+v"(c) : "v"(a), "v"(b));
  else c = mfma16(a, b, c);
}
template <int GAP, int SIG, int NW, class E, class F> DI void project(LAS char* L, u32 w0, u32 w1, const u32x4 (&xf)[2][14], int tp, int lane, int csl, f32x4 (&t)[2][2], f32x2 (&st)[2], E&& pre, F&& mid) {
  u32 wq = (tp & 1 ? w1 : w0) + (tp >> 1) * (7 * 2048 + GAP), ca = csl + (lane >> 4) * 16, sa = L_ST1 + (tp * 32 + (lane & 15)) * 8, fa = L_FLG + SIG * 16 + (lane & (NW - 1)) * 4;
  u32 cw = ca + tp * 64, fw = L_FLG + SIG * 16 + tp * 4, one = 1;
  u32x4 ones = {0x3f803f80u, 0x3f803f80u, 0x3f803f80u, 0x3f803f80u};
  LAS char *p0 = L + w0, *p1 = L + w1, *pq = L + wq, *pca = L + ca, *psa = L + sa, *pfa = L + fa, *pcw = L + cw, *pfw = L + fw;
  asm volatile("" : "+v"(p0), "+v"(p1), "+v"(pq), "+v"(pca), "+v"(psa), "+v"(pfa), "+v"(pcw), "+v"(pfw), "+v"(ones), "+v"(one));
  pre();
  u32x4 cf[7];
  u32x2 ra[7], rb[7];
  f32x4 cs, cp[4];
  u32 sw;
#define WAT(n) (((n) & 1 ? p1 : p0) + ((n) >> 1) * 2048 + ((n) >= 14 ? GAP : 0))
#pragma unroll
  for (int i = 0; i < 7; ++i) cf[i] = trfrag(pq + i * 2048, 256);
  SCHED();
  sfor<0, 7>([&](auto IC_) LAM {
    constexpr int i = IC_.value;
    macc<i == 0 ? 0 : i == 6 ? 2 : 1>(cs, cf[i], ones);
    SCHED();
    if (i < 6) { ra[i] = trread(WAT(i)); rb[i] = trread(WAT(i) + 256); }
    SCHED();
  });
  sfor<0, 28>([&](auto NC) LAM {
    constexpr int n = NC.value, K = n < 2 ? 0 : n < 26 ? 1 : 2;
    const u32x4 f = {ra[n % 7].x, ra[n % 7].y, rb[n % 7].x, rb[n % 7].y};
    macc<K>(t[0][n & 1], f, xf[0][n >> 1]);
    SCHED();
    if constexpr (n + 6 < 28) ra[(n + 6) % 7] = trread(WAT(n + 6));
    if constexpr (n == 24) st[0] = *(LAS f32x2*)(psa);
    if constexpr (n == 22 || n == 23) cp[n - 22] = *(LAS f32x4*)(pca + (n - 22) * 64);
    if constexpr (n >= 26) { float a = cp[n - 26][0] + cp[n - 24][0], b = cp[n - 26][1] + cp[n - 24][1]; PIN(a); PIN(b); cp[n - 26][0] = a; cp[n - 26][1] = b; }
    SCHED();
    macc<K>(t[1][n & 1], f, xf[1][n >> 1]);
    SCHED();
    if constexpr (n + 6 < 28) rb[(n + 6) % 7] = trread(WAT(n + 6) + 256);
    if constexpr (n == 24) st[1] = *(LAS f32x2*)(psa + 128);
    if constexpr (n == 22 || n == 23) cp[n - 20] = *(LAS f32x4*)(pca + (n - 20) * 64);
    if constexpr (n >= 26) { float a = cp[n - 26][2] + cp[n - 24][2], b = cp[n - 26][3] + cp[n - 24][3]; PIN(a); PIN(b); cp[n - 26][2] = a; cp[n - 26][3] = b; }
    if constexpr (n == 2) { if ((lane & 15) == 0) *(LAS f32x4*)(pcw) = cs; }
    if constexpr (n == 3) { asm volatile("" ::: "memory"); *(volatile LAS u32*)(pfw) = one; }
    if constexpr (n == 16) sw = *(volatile LAS u32*)(pfa);
    if constexpr (n == 21) { if (__builtin_expect(__builtin_amdgcn_ballot_w64(sw == 0) != 0, 0)) { lds_wait(L, SIG); if (NW > 4) lds_wait(L, SIG + 1); } asm volatile("" ::: "memory"); }
    mid(n, pfw - SIG * 16, one);
    SCHED();
  });
#pragma unroll
  for (int tb = 0; tb < 2; ++tb)
#pragma unroll
    for (int k = 0; k < 2; ++k) t[tb][k] += cp[k] * st[tb].x;
}
DI f32x2 swp16(float a, float b) { auto r = __builtin_amdgcn_permlane16_swap(__builtin_bit_cast(u32, a), __builtin_bit_cast(u32, b), false, false); return f32x2{__builtin_bit_cast(float, (u32)r[0]), __builtin_bit_cast(float, (u32)r[1])}; }
DI f32x2 swp32(float a, float b) { auto r = __builtin_amdgcn_permlane32_swap(__builtin_bit_cast(u32, a), __builtin_bit_cast(u32, b), false, false); return f32x2{__builtin_bit_cast(float, (u32)r[0]), __builtin_bit_cast(float, (u32)r[1])}; }
DI float swadd32(float a, float b) { const f32x2 s = swp32(a, b); return s.x + s.y; }
DI float swadd16(float a, float b) { const f32x2 s = swp16(a, b); return s.x + s.y; }
DI float mx2(float a, float b) { float r; asm("v_max_f32 %0, %1, %2" : "=v"(r) : "v"(a), "v"(b)); return r; }
DI float mx3(float a, float b, float c) { return fmaxf(fmaxf(a, b), c); }
DI float mn3(float a, float b, float c) { return fminf(fminf(a, b), c); }
template <class F> DI void feature_map(const f32x4 (&t)[2][2], const f32x2 (&st)[2], const u32x4 (&wpf)[2], int g, u32x4 (&P)[2][2], F&& mid) {
  const float pad = g < 3 ? 0.f : -3.0e38f;
  const f32x4 z4 = {0.f, 0.f, 0.f, 0.f};
  const float sc[2] = {st[0].y * LOG2E, st[1].y * LOG2E}, scg = g < 2 ? sc[0] : sc[1];
  float p[2][8], m[4], mp[4];
#pragma unroll
  for (int tb = 0; tb < 2; ++tb) {
    const u32x4 tq = {pk(t[tb][0][0], t[tb][0][1]), pk(t[tb][0][2], t[tb][0][3]), pk(t[tb][1][0], t[tb][1][1]), pk(t[tb][1][2], t[tb][1][3])};
    const f32x4 p0 = mfma16(wpf[0], tq, z4), p1 = mfma16(wpf[1], tq, z4);
#pragma unroll
    for (int i = 0; i < 8; ++i) p[tb][i] = i < 4 ? p0[i] : p1[i - 4];
  }
  mid(0);
#pragma unroll
  for (int tb = 0; tb < 2; ++tb) {
    const float* q = p[tb];
    m[2 * tb] = mx3(mx3(q[0], q[1], q[2]), mx3(q[3], q[4], q[5]), mx3(q[5], q[6], q[7]));
    m[2 * tb + 1] = mx3(mx3(-q[0], -q[1], -q[2]), mx3(-q[3], -q[4], -q[5]), mx3(-q[5], -q[6], -q[7]));
  }
  {
    const f32x2 a = swp16(m[0], m[1]), b = swp16(m[2], m[3]);
    const f32x2 c = swp32(mx2(a.x, a.y), mx2(b.x, b.y));
    const float d = mx2(c.x, c.y) * -scg;
    const f32x2 e = swp32(d, d), f0 = swp16(e.x, e.x), f1 = swp16(e.y, e.y);
    m[0] = f0.x; m[1] = f0.y; m[2] = f1.x; m[3] = f1.y;
#pragma unroll
    for (int c2 = 0; c2 < 4; ++c2) mp[c2] = m[c2] + pad;
  }
  float ex[4][8], sm[4];
#pragma unroll
  for (int c = 0; c < 4; ++c) {
    mid(1 + c);
#pragma unroll
    for (int i = 0; i < 8; ++i) {
      ex[c][i] = fexp2(fmaf(c & 1 ? -p[c >> 1][i] : p[c >> 1][i], sc[c >> 1], i & 2 ? mp[c] : m[c]));
    }
    sm[c] = ((ex[c][0] + ex[c][1]) + (ex[c][2] + ex[c][3])) + ((ex[c][4] + ex[c][5]) + (ex[c][6] + ex[c][7]));
  }
  mid(5);
  {
    const float d = frcp(swadd32(swadd16(sm[0], sm[1]), swadd16(sm[2], sm[3])));
    const f32x2 e = swp32(d, d), f0 = swp16(e.x, e.x), f1 = swp16(e.y, e.y);
    sm[0] = f0.x; sm[1] = f0.y; sm[2] = f1.x; sm[3] = f1.y;
  }
#pragma unroll
  for (int c = 0; c < 4; ++c)
#pragma unroll
    for (int e = 0; e < 4; ++e) P[c >> 1][c & 1][e] = pk(ex[c][2 * e] * sm[c], ex[c][2 * e + 1] * sm[c]);
}
DI u32x4 piece(const u32x4 (&rp)[8], int e) {
  u32x4 f;
#pragma unroll
  for (int m = 0; m < 4; ++m) f[m] = __builtin_amdgcn_perm(rp[2 * m + 1][e >> 1], rp[2 * m][e >> 1], (e & 1) ? 0x07060302u : 0x05040100u);
  return f;
}
#define RP_OPS "+v"(rp[0]), "+v"(rp[1]), "+v"(rp[2]), "+v"(rp[3]), "+v"(rp[4]), "+v"(rp[5]), "+v"(rp[6]), "+v"(rp[7])
#define PF1 0
#define PF2 84
#define PF3 120
#define TP0 -1
#define TP1 13
#define TP2 -1
#define TOUCH 5
#define NW3 4
#define THRN0 4
#define DNPRIO 3
#define NWD 3
#define MIDCHK 0
#define HELP 0
#define NHW 7
#define W3LATE 0
#define W3WAIT 1
#define ROT 1
#define EXPT 0
#define PFM 12
#define PSLP 4
#define PLAG 0
#define WTOUCH 0
#define REPLATE 0
#define WB0 8
#define WB1 4
#define ULATE 0
#define VPRIO 3
#define VPRIO0 3
#define VPN 27
#define FPRIO 2
#define CLB 0
#define KAT 99
#define STF 0
#define QN0 0
#define QST 4
#define PB0 3
#define WOT 0
#define WTN 2
#define CLF 0
#define TQ 1
#define TU 0
#define TO 1
#define SN_ 49
#define SS_ 128
#define SD_ 0
#define RPO 0
#define REPE 0
#define UFN 14
#define SBA 0
#define TD 2
#define NR5 16
#define NR6 23
#define G4E 0
#define TS0 2
#define TS1 8
#define TWF 4
#define BP3 0
#define W3J 2
#define KGO -1
#define QEARLY 1
#define DLY1 4
#define W1 1
#define W2C 0
#define WOE 2
#define W3THR 1
#define HW3 1
#define HW1 1
#define SN 4
#define BATCH 1
#define B23 1
#define WD5L 0
#define A56 1
#define BGO 196
#define BALL 0
#define BSL 4
#define RSL 4
#define H5 1
#define H6 1
#define BORD 1
#define DP0 3
#define AIL 1
#define SEEN 3
#define ADLY 16
#define W6E 0
#define SPLIT 1
#define NCL 4
#define B5L 1
#define A6L 1
#define BO2 2
#define TW 0
#define CL 0
#define CE 0
#define W5D 1
#define W2F 5
#define D2A 12
#define D2B 16
#define SPEC 1
#define CLP 28
#define CLS 3
#define CE2 0
#define DLY5 60
#define CSL 1
#define CF 0
#define OPRIO 3
#define PG 1
#define ISK 1
#define T2 70
#define FXW 8
#define DLY4 16
#define PUBS 5
#define THRN 40
#define DLY2 8
#define DLY3 20
#define GUPRIO 2
enum { S_FREE01 = 0, S_REFILL, S_DONE4, S_WIN3, S_WK, S_WV, S_WQ, S_WSEEN, S_KDONE, S_V, S_PHIK, S_CSV, S_KVA, S_WO, S_CSQ, S_PHI, S_X0, S_X1, S_XA, S_CSK, S_CS, S_WOFA, S_WOFB, S_EXA, S_EXB, S_W2A, S_W2B, S_HFA, S_HFB, S_CONS, S_SPARE, S_XF0 };

__global__ void __launch_bounds__(512, 2) fused_block(const char* __restrict__ x, const char* __restrict__ wqkv, const char* __restrict__ wphi, const char* __restrict__ wo,
                                                      const char* __restrict__ wg, const char* __restrict__ wu, const char* __restrict__ wd, char* __restrict__ out, char* ws) {
  extern __shared__ __attribute__((aligned(16))) char smem[];
  LAS char* L = (LAS char*)smem;
  const int tid = threadIdx.x, lane = tid & 63, wave = __builtin_amdgcn_readfirstlane(tid >> 6), w2 = wave & 3;
  const int b = blockIdx.x, li = b >> 3, xp = (b & 7) >> 1, xh = b & 1;
  const int tile = 4 * xp + (li >> 3), r = 8 * xh + (li & 7);
  const int tile2 = 4 * xp + 2 * xh + (li >> 4), r2 = li & 15;
  const int tg = r2 >= 7 ? 1 : 0, cg = r2 - 7 * tg;
  const int l15 = lane & 15, g = lane >> 4, q4 = l15 >> 2, p4 = lane & 3, hh = lane >> 5;
  const int head = r;
  asm volatile("" :: "s"(x), "s"(wqkv), "s"(wphi), "s"(wo), "s"(wg), "s"(wu), "s"(wd), "s"(out), "s"(ws));
  STAMP(0);
  u64 pc_; asm volatile("s_getpc_b64 %0" : "=s"(pc_));
  const char* pc0 = (const char*)(pc_ & ~127ull);
  const __amdgpu_buffer_rsrc_t rs = __builtin_amdgcn_make_buffer_rsrc((void*)ws, 0, WS_USED, 0x00020000);
  const u32x4 ones = {0x3f803f80u, 0x3f803f80u, 0x3f803f80u, 0x3f803f80u};
  const f32x4 z4 = {0.f, 0.f, 0.f, 0.f};
  auto role = [&](auto AC) __attribute__((always_inline)) {
  constexpr bool A = AC.value;
  if (A) asm volatile("; A" ::: "memory"); else asm volatile("; B" ::: "memory");
  __builtin_amdgcn_s_waitcnt(0x0F70);
  u64 epoch;
  u32x4 rp[8];
  const int fvo = lane < 16 ? lane * 8 : NOWHERE;
  const int uidx = (tile * 10 + __builtin_popcount(~0x2A54u & ((1u << head) - 1u))) * 4 + w2, unit = uidx * 49 / 80;
  const bool has_unit = !((0x2A54 >> head) & 1) && (uidx + 1) * 49 / 80 != unit;
  const int u3 = unit >> 1, umat = unit & 1, ukq = u3 / 28, ufl = u3 - 28 * ukq;
  const char* gusrc = ws + WS_WGU + 7 * r * SLOT;
  const u32 guo = WS_WGU + 7 * r * SLOT + w2 * 7168;
#define GUBLK(j) dma7<(j) * SLOT, gu_slot(j) * SLOT>(rs, lane * 16, guo, w2 * 7168)
  const u32 wvo = (lane >> 2) * 2688 + (((lane & 3) ^ (2 * (lane >> 5))) * 16);
  const char* wbase = wqkv + head * 56 - (head & 1) * 8 + 16 * w2 * 2688;
  const u32 wr0 = (8 * g + q4) * 64 + (((p4 + (head & 1)) ^ (4 * (g & 1))) * 8), wr1 = (8 * g + q4) * 64 + ((((4 + p4 + (head & 1)) & 7) ^ (4 * (g & 1))) * 8);
  const int tb = w2; constexpr int kh = A ? 0 : 1;
  const int hho = WS_HH + (((tile * 2 + (w2 >> 1)) * 4 + (r >> 2)) * 56 + (r & 3) * 2 + (w2 & 1)) * 1024;
  const int tok = tile2 * 128 + tg * 64 + tb * 16 + l15;
  const int hvo = WS_H + ((tok >> 5) * 28 + 4 * cg + (g >> 1)) * 1024 + ((g & 1) * 32 + (tok & 31)) * 16;
  const int sto = WS_ST + (2 * cg * 2048 + tile2 * 128 + tg * 64 + tb * 16) * 8;
  const u32x4 empty4 = {EMPTY, EMPTY, EMPTY, EMPTY};
#define HH_CLEAR() _Pragma("unroll") for (int i = 0; i < 4 * NCL; ++i) ST16(rs, empty4, lane * 16 + (i & 1) * 2048, WS_HH + (((tile2 * 2 + r2 - 14) * 4 + w2) * 56 + kh + (i >> 1) * 4) * 1024)
#define HH_FLAG() FLAG(WS_FH + ((tile2 * 4 + 2 * (r2 - 14) + kh) * 32 + 28 + w2) * 128, 1)
  u32x4 wof[8][4];
  f32x4 csp[7];
  u64 tag;
#define WOF_READ(i0, i1) do { u32 wa_ = L_WO + (kh * 224 + 4 * g + q4) * 128 + 16 * p4 + 64 * (q4 >> 1), wb_ = wa_ ^ 64, wc_ = wa_ + (g == 3 ? 0 : 2048), wd_ = wc_ ^ 64; \
    asm volatile("" : "+v"(wa_), "+v"(wb_), "+v"(wc_), "+v"(wd_)); \
    _Pragma("unroll") for (int i = i0; i < i1; ++i) _Pragma("unroll") for (int k = 0; k < 4; ++k) { \
      const u32x2 a_ = trread(L + (k >> 1 ? wb_ : wa_) + 28 * i * 128 + (k & 1) * 8), c_ = trread(L + (k >> 1 ? wd_ : wc_) + 28 * i * 128 + (k & 1) * 8); \
      wof[i][k] = u32x4{a_.x, a_.y, c_.x, c_.y}; } } while (0)
#define XF_READ(i) _Pragma("unroll") for (int s = 0; s < 14; ++s) xf[i][s] = *(LAS u32x4*)((s & 1 ? xr1 : xr0) + (i) * 14336 + (s >> 1) * 2048)
#define LN_STATS(kh) do { f32x4 g1 = z4, g2 = z4; \
    _Pragma("unroll") for (int s = 0; s < 14; ++s) { g1 = mfma16(ones, xf[kh][s], g1); g2 = mfma16(xf[kh][s], xf[kh][s], g2); } \
    const float d01 = (p4 & 1) ? g2[1] : g2[0], d23 = (p4 & 1) ? g2[3] : g2[2], s2 = (p4 & 2) ? d23 : d01; \
    const float nmu = g1[0] * (-1.f / 448.f), var = fmaxf(s2 * (1.f / 448.f) - nmu * nmu, 0.f); \
    if (g == q4) *(LAS f32x2*)(L + L_ST1 + (w2 * 32 + kh * 16 + l15) * 8) = f32x2{nmu, __builtin_amdgcn_rsqf(var + 1e-5f)}; } while (0)
#define WPF() do { u32 wp[8]; \
    _Pragma("unroll") for (int j = 0; j < 8; ++j) wp[j] = *(LAS u32*)(L + L_WPHI + 4 * g * 56 + min(l15, 13) * 4 + ((j & 3) + 16 * (j >> 2)) * 56); \
    _Pragma("unroll") for (int e = 0; e < 4; ++e) { const bool z = e >= 2 && g == 3; \
      wpf[0][e] = z ? 0u : __builtin_amdgcn_perm(wp[2 * e + 1], wp[2 * e], 0x05040100u); wpf[1][e] = z ? 0u : __builtin_amdgcn_perm(wp[2 * e + 1], wp[2 * e], 0x07060302u); } \
    asm volatile("" : "+v"(wpf[0]), "+v"(wpf[1])); } while (0)
  {
    u32x4 xf[2][14], wpf[2];
    f32x2 st[2];
    LAS char* xr0 = L + w2 * 28672 + l15 * 128 + ((g ^ (l15 & 7)) * 16);
    LAS char* xr1 = L + w2 * 28672 + l15 * 128 + (((4 + g) ^ (l15 & 7)) * 16);
    LAS char* pw = L + L_PKV + (w2 * 32 + l15) * 224 + g * 8;
    const char* wos = wo + (r2 < 14 ? cg : 0) * 128 + w2 * 56 * 896;
    const u32 wovo = (lane >> 3) * 896 + (((lane & 7) ^ ((lane >> 2) & 4)) * 16);
#define WO_HALF(hf_) _Pragma("unroll") for (int n = 0; n < 7; ++n) dma16(wos + ((hf_) * 224 + n * 8) * 896, wovo, L_WO + ((hf_) * 28 + w2 * 7 + n) * 1024)
    int clo = WS_O + ((tile * 8 + 2 * w2) * 16 + head) * 1024, clh = r2 < 14 ? hvo : NOWHERE, cls = r2 < 14 && lane < 16 ? (lane >> 3) * 16384 + (lane & 7) * 16 : NOWHERE, clt = sto;
    int kvo = WS_KVP + ((head * 16 + tile) * 4 + w2) * 1024, fko = WS_FKV + ((head * 4 + w2) * 16 + tile) * 128;
    u32x4 em4 = empty4;
    u32 wtv = min(lane, 24) * 128;
    const char* wts = wo + min(li * 98 + w2 * 25, 3136 - 25) * 128;
    auto clear = [&](int c) LAM {
      if (c == 0) ST16(rs, em4, lane * 16, clo);
      if (c == 1) ST16(rs, em4, lane * 16, clo + 16384);
      if (c == 2) ST16(rs, em4, clh, 0);
      if (c == 3) ST16(rs, em4, clh, 2048);
      if (c == 4) ST16(rs, em4, cls, clt);
    };
    if constexpr (A) {
      if (CLF == 6 || CLF == 7) { clear(0); clear(1); }
      if (CLF == 6 || CLF == 8) { clear(2); clear(3); clear(4); }
      if (wave < 2) dma4(x + (xp * 512 + li * 16) * 896 + wave * 7168, min(lane, 55) * 128, L_JUNK);
      if (wave == 3) { *(LAS u32*)(L + L_FLG + lane * 4) = 0u; *(LAS u32*)(L + L_FLG + 256 + lane * 4) = 0u; dma4(pc0, min((li % 6) * 8192 + lane * 128, CODE_PF), L_JUNK); }
      if (wave == 2) { dma16(wphi + head * 1568, lane * 16, L_WPHI); dma16(wphi + head * 1568, min(lane + 64, 97) * 16, L_WPHI + 1024); }
      if (TQ == 1 && wave == 2) dma4(wqkv, min(b * 37 + min(lane, 36), 9407) * 128, L_JUNK);
      if (TQ >= 2 && wave < TQ) dma4(wqkv + li * 14 * 2688 + (wave == 2 ? 0 : (wave + 1) * 896) + xh * 384, min(lane >> 2, 13) * 2688 + (lane & 3) * 128, L_JUNK);
      if (TD == 5 && wave == 0) dma4(wd + b * 49 * 128, min(lane, 48) * 128, L_JUNK);
      if (TO == 1 && wave == 3) dma4(wo, min(b * 13 + min(lane, 12), 3135) * 128, L_JUNK);
      if (TO == 2) dma4(wts, wtv, L_JUNK);
      if (CLF == 9) { clear(0); clear(1); clear(2); clear(3); clear(4); }
      BAR();
      asm volatile("s_load_dwordx2 %0, %1, 0x0 ; role A\n\ts_waitcnt lgkmcnt(0)" : "=s"(epoch) : "s"(ws) : "memory");
      tag = epoch * 6364136223846793005ull + 1442695040888963407ull;
      STAMP(1);
#define WKLOAD() _Pragma("unroll") for (int i = 0; i < 7; ++i) dma16(wbase + 896 + 64 * i * 2688, wvo, L_WK + (4 * i + w2) * 1024)
      if (KAT == 3) { WKLOAD(); if (wave == 2) { WAITVM(7); lds_signal(L, S_PHI, 0); } }
      else if (wave == 2) { if (CLF == 9) WAITVM(5); else WAITVM(0); lds_signal(L, S_PHI, 0); }
      if (KAT > 3 && KAT < 28) { lds_wait1(L, S_SPARE, w2); WKLOAD(); }
      lds_wait1(L, S_PHI, 0);
      WPF();
#define DBITE() dma4(wd + min(b * 49 + w2 * 13, 12544 - 13) * 128, min(lane, 12) * 128, L_JUNK)
      if (TD == 9) {
        __builtin_amdgcn_s_sleep(TS0);
        if (w2 & 1) __builtin_amdgcn_s_sleep(TS1);
        if (w2 & 2) { __builtin_amdgcn_s_sleep(TS1); __builtin_amdgcn_s_sleep(TS1); }
        DBITE();
      }
      if (TD == 10 && w2 == 0) DBITE();
      lds_wait1(L, S_X0, w2);
      XF_READ(0);
      if (w2 < 2) lds_signal(L, S_XF0, w2);
      if (TD == 10 && w2 == 1) DBITE();
      if (KAT > 28) { WKLOAD(); }
      if (TD == 10 && w2 == 2) DBITE();
      if (TD == 11) DBITE();
      STAMP(27);
      LN_STATS(0);
      SCHED();
      STAMP(2);
      lds_wait1(L, S_X1, w2);
      XF_READ(1);
      if (QEARLY == 3 && w2 < 2) lds_signal(L, S_SPARE, w2);
      STAMP(3);
      if (SBA) LN_STATS(1);
      WAITVM(0);
      STAMP(4);
      lds_signal(L, S_WK, w2);
      if (TD == 10 && w2 == 3) DBITE();
      if (TD == 12) DBITE();
      f32x4 t[2][2];
      u32 pr = L_PKV + (4 * g + q4) * 224 + 8 * p4;
      asm volatile("" : "+v"(pr), "+v"(pw));
      asm volatile("" : "+s"(clo), "+v"(clh), "+v"(cls), "+s"(clt), "+s"(kvo), "+s"(fko), "+s"(tag), "+v"(em4));
      if (WOT == 5) asm volatile("" : "+v"(wtv), "+s"(wts));
      project<0, S_CSK, 8>(L, L_WK + wr0, L_WK + wr1, xf, w2, lane, L_CSP, t, st, [&]() LAM {
        lds_wait(L, S_WK);
        STAMP(5);
        __builtin_amdgcn_s_setprio(2);
      }, [&](int n, LAS char* sg, u32 one) LAM {
        if (n == KGO) *(volatile LAS u32*)(sg + S_KDONE * 16) = one;
      });
      STAMP(6);
      u32x4 P[2][2];
      __builtin_amdgcn_s_setprio(FPRIO);
      feature_map(t, st, wpf, g, P, [&](int i) LAM {
        if (WOT == 5 && i == 0) { SCHED(); dma4(wts, wtv, L_JUNK); SCHED(); }
        if (CLF == 1 && i > 0) { SCHED(); clear(i - 1); SCHED(); }
        if (CLF == 2 && i == 5) { SCHED(); clear(0); clear(1); clear(2); clear(3); clear(4); SCHED(); }
        if (CLF == 3 && i >= 3) { SCHED(); clear(2 * i - 6); clear(2 * i - 5); SCHED(); }
        if (CLF == 4 && i == 1) { SCHED(); clear(0); clear(1); clear(2); clear(3); clear(4); SCHED(); }
      });
#pragma unroll
      for (int c = 0; c < 4; ++c) {
        LAS char* p = pw + (c >> 1) * 16 * 224 + (c & 1) * 64;
        *(LAS u32x2*)(p) = u32x2{P[c >> 1][c & 1].x, P[c >> 1][c & 1].y}; *(LAS u32x2*)(p + 32) = u32x2{P[c >> 1][c & 1].z, P[c >> 1][c & 1].w};
      }
      lds_signal(L, S_PHIK, w2);
      STAMP(7);
      if (CLF == 0 || CLF == 8) { clear(0); clear(1); }
      if (CLF == 0 || CLF == 7) { clear(2); clear(3); clear(4); }
      lds_wait2(L, S_V, S_PHIK);
      u32x4 pa[4], pb[8];
#pragma unroll
      for (int m = 0; m < 4; ++m) {
        pa[m] = trfrag(L + pr + w2 * 32 + m * 32 * 224, 16 * 224);
        pb[2 * m] = trfrag(L + pr + m * 32 * 224 + 128, 16 * 224); pb[2 * m + 1] = trfrag(L + pr + m * 32 * 224 + 160, 16 * 224);
      }
      SCHED();
      f32x4 kv[2] = {};
#pragma unroll
      for (int m = 0; m < 8; ++m) kv[m & 1] = mfma16(pa[m >> 1], pb[m], kv[m & 1]);
      const u32x4 kvb = {pk(kv[0][0], kv[0][1]), pk(kv[0][2], kv[0][3]), pk(kv[1][0], kv[1][1]), pk(kv[1][2], kv[1][3])};
      ST16(rs, kvb, lane * 16, kvo);
      STAMP(8);
      DRAIN();
      FLAG(fko, 1);
      __builtin_amdgcn_s_setprio(0);
      STAMP(9);
      if (HW1) { lds_signal(L, S_XA, w2); lds_wait(L, S_XA); }
#define DTOUCH() dma4(wd + b * 49 * 128, min(lane, 48) * 128, L_JUNK)
      if (TD == 2 && wave == 0) DTOUCH();
      if (r2 < 14 || !ISK) { WO_HALF(0); WO_HALF(1); } else __builtin_amdgcn_s_sleep(20);
      poll_flags(ws, WS_FKV + ((head * 4 + w2) * 16 + l15) * 128, lane < 16, tag);
      lds_signal(L, S_WO, w2);
      if (CE2 && r2 >= 14) { DRAIN(); HH_FLAG(); }
      STAMP(10);
      u32x4 tt[16];
#pragma unroll
      for (int i = 0; i < 16; ++i) tt[i] = __builtin_bit_cast(u32x4, __builtin_amdgcn_raw_buffer_load_b128(rs, lane * 16, WS_KVP + head * 65536 + w2 * 1024 + i * 4096, 16));
      u32 fgo;
      {
        const int pl = (tile & 1) * 16 + min(lane >> 2, 13);
        fgo = wave == 0 ? WS_FKV + ((l15 * 4 + 2 * tg + (g & 1)) * 16 + tile2) * 128
            : wave == 1 ? WS_FKV + (((((tile >> 1) & 1) * 8 + (pl & 7)) * 4 + p4) * 16 + (tile & 12) + (pl >> 3)) * 128
                        : WS_FG + (min(((7 * r) >> 2) + g, (7 * r + 6) >> 2) * 16 + (l15 & 8) + min(lane & 7, 6)) * 128;
      }
      u64 fg = __hip_atomic_load((const GAS u64*)(ws + fgo), RLX_AGENT);
      SCHED();
      const u32 one = g == q4 ? ((p4 & 1) ? 0x3f800000u : 0x3f80u) : 0u, s01 = p4 < 2 ? one : 0u, s23 = p4 < 2 ? 0u : one;
      const u32x4 sel0 = {s01, s23, 0u, 0u}, sel1 = {0u, 0u, s01, s23};
      f32x4 s0 = {}, s1 = {};
#pragma unroll
      for (int i = 0; i < 16; ++i) { s0 = mfma16(sel0, tt[i], s0); s1 = mfma16(sel1, tt[i], s1); }
      LAS char* kw = L + L_KVA + (w2 >> 1) * 1024 + lane * 16 + (w2 & 1) * 8;
      *(LAS u32x2*)(kw) = u32x2{pk(s0[0], s0[1]), pk(s0[2], s0[3])};
      *(LAS u32x2*)(kw + 2048) = u32x2{pk(s1[0], s1[1]), pk(s1[2], s1[3])};
      lds_signal(L, S_KVA, w2);
      if (TD == 4 && wave == 2) DTOUCH();
      STAMP(11);
      if (WOE == 3) { WO_HALF(1); WAITVM(0); lds_signal(L, S_WO, w2); }
      asm volatile("" : "+v"(fg) :: "memory");
      if (!__all(fg == tag + 1)) poll_flags(ws, fgo, true, tag);
      __builtin_amdgcn_fence(__ATOMIC_ACQUIRE, "wavefront");
      lds_signal(L, S_WOFA, w2);
      lds_wait1(L, S_WOFA, 3);
      const int fl = min(((7 * r) >> 2) + w2, (7 * r + 6) >> 2);
#pragma unroll
      for (int i = 0; i < 7; ++i) asm volatile("global_load_dwordx4 %0, %1, %2 offset:%3 sc1" : "=v"(csp[i]) : "v"(((7 * r) >> 2) + w2 == fl ? lane * 16 : 0), "s"(ws + WS_CS + fl * 7168 + 4096), "n"(i * 1024 - 4096) : "memory");
      GUBLK(2);
      if (r2 < 14 || !ISK) { lds_wait(L, S_WO); WOF_READ(0, 8); }
      SCHED();
#define CS_OUT(k_) do { asm volatile("s_waitcnt vmcnt(%7)" : "+v"(csp[0]), "+v"(csp[1]), "+v"(csp[2]), "+v"(csp[3]), "+v"(csp[4]), "+v"(csp[5]), "+v"(csp[6]) : "n"(k_) : "memory"); \
        const f32x4 sm = ((csp[0] + csp[1]) + (csp[2] + csp[3])) + ((csp[4] + csp[5]) + csp[6]); \
        const float e0 = swadd32(sm[0], sm[1]), e1 = swadd32(sm[2], sm[3]);     \
        const int j0 = 4 * (((7 * r) >> 2) + w2) + 2 * (lane & 1) - 7 * r, cr = ((g & 1) * 16 + hh * 8 + (l15 >> 1)) * 4; \
        if (j0 >= 0 && j0 < 7) *(LAS float*)(L + L_CS + j0 * 128 + cr) = e0; \
        if (j0 + 1 >= 0 && j0 + 1 < 7) *(LAS float*)(L + L_CS + (j0 + 1) * 128 + cr) = e1; } while (0)
      lds_signal(L, S_WOFB, w2);
      if (!CSL) CS_OUT(7);
      STAMP(24);
    } else {
      const u32 vo = (lane >> 3) * 896 + (((lane & 7) ^ (lane >> 3)) * 16);
      const char* xb = x + (tile * 128 + w2 * 32) * 896;
#pragma unroll
      for (int n = 0; n < 28; ++n) {
        dma16(xb + ((n / 14) * 16 + (n & 1) * 8) * 896 + ((n % 14) >> 1) * 128, vo, w2 * 28672 + n * 1024);
        if (n == 3) { BAR(); asm volatile("s_load_dwordx2 %0, %1, 0x0 ; role B" : "=s"(epoch) : "s"(ws) : "memory"); }
        if (n == 21) { WAITVM(8); lds_signal(L, S_X0, w2); }
        if (n == KAT && KAT > 3) lds_signal(L, S_SPARE, w2);
      }
      const int rg = lane >> 3;
      const u32 lvo = rg * 8 * 3584 + (lane & 7) * 16;
      const char* us = (umat ? wu : wg) + ukq * 64 * 3584 + ufl * 128;
      asm volatile("" : "+s"(us));
      auto repack = [&]() LAM {
        if (has_unit) {
          const int so = rg * 512 + (lane & 7) * 16;
          const int sb = WS_WGU + 4 * ufl * SLOT + ukq * 4096 + umat * 256;
          const int s1 = SLOT, s2 = 2 * SLOT;
          float c[8];
#pragma unroll
          for (int e = 0; e < 8; ++e) {
            const u32x4 f = piece(rp, e);
            ST16(rs, f, so + (e & 1) * 128, sb + ((e >> 1) & 1) * s1 + (e >> 2) * s2);
            c[e] = mfma16(ones, f, z4)[0];
          }
          const f32x4 cv = g & 1 ? f32x4{c[4], c[5], c[6], c[7]} : f32x4{c[0], c[1], c[2], c[3]};
          ST16(rs, cv, g < 2 ? (l15 >> 3) * 512 + (lane & 7) * 32 + g * 16 : NOWHERE, WS_CS + ufl * 7168 + ukq * 1024 + (umat & 1) * 256);
        }
      };
#define RPLOAD() _Pragma("unroll") for (int j = 0; j < 8; ++j) asm volatile("global_load_dwordx4 %0, %1, %2" : "=v"(rp[j]) : "v"(lvo), "s"(us + j * 3584) : "memory")
      if (RPO == 2 && has_unit) { RPLOAD(); }
      asm volatile("s_waitcnt lgkmcnt(0)" : "+s"(epoch)::"memory");
      tag = epoch * 6364136223846793005ull + 1442695040888963407ull;
      STAMP(1);
      XF_READ(0);
      if (w2 < 2) lds_signal(L, S_XF0, 2 + w2);
      if (RPO == 2 && has_unit) WAITVM(8); else WAITVM(0);
      lds_signal(L, S_X1, w2);
      if (RPO == 1 && has_unit) { RPLOAD(); }
      STAMP(2);
      if (STF < 7) lds_wait(L, S_XF0);
      XF_READ(1);
      if (QEARLY == 3 && w2 < 2) lds_signal(L, S_SPARE, 2 + w2);
      SCHED();
#define WDST(c) ((c) * 1024 + ((c) >= 14 ? SLOT - 14336 : 0))
#pragma unroll
      for (int i = 0; i < 7 - STF; ++i) dma16(wbase + 1792 + 64 * i * 2688, wvo, L_WV + WDST(4 * i + w2));
      STAMP(3);
      __builtin_amdgcn_s_setprio(PB0);
      if (!SBA) LN_STATS(1);
      lds_signal(L, S_CS, w2);
      if (STF == 7) lds_wait(L, S_XF0);
#pragma unroll
      for (int i = 7 - STF; i < 7; ++i) dma16(wbase + 1792 + 64 * i * 2688, wvo, L_WV + WDST(4 * i + w2));
      STAMP(4);
      f32x4 t[2][2];
      asm volatile("" : "+v"(pw));
      if (WOT == 4) asm volatile("" : "+v"(wtv), "+s"(wts));
      project<SLOT - 14336, S_CSV, 4>(L, L_WV + wr0, L_WV + wr1, xf, w2, lane, L_CSP + 256, t, st, [&]() LAM {
      if (QEARLY == 3) {
        lds_wait(L, S_SPARE);
#pragma unroll
        for (int i = 0; i < 7; ++i) dma16(wbase + 64 * i * 2688, wvo, L_WQ + WDST(4 * i + w2));
      }
      if (REPE == 3) { asm volatile("s_waitcnt vmcnt(%8)" : RP_OPS : "n"(QEARLY == 3 ? 14 : 7) : "memory"); repack(); if (has_unit) { if (QEARLY == 3) WAITVM(16); else WAITVM(9); } }
      if (RPO == 0 && has_unit) { RPLOAD(); } else if (REPE == 3 && has_unit) {} else if (QEARLY == 3) WAITVM(7); else WAITVM(0);
      STAMP(28);
      if (RPO) {} else if (QEARLY == 3) WAITVM(15); else WAITVM(8);
      lds_signal(L, S_WV, w2);
      STAMP(29);
      lds_wait2(L, S_WV, S_WK);
      STAMP(30);
      if (QEARLY == 1) {
#pragma unroll
        for (int i = 0; i < 7; ++i) dma16(wbase + 64 * i * 2688, wvo, L_WQ + WDST(4 * i + w2));
      }
        if (REPE == 2) { asm volatile("s_waitcnt vmcnt(7)" : RP_OPS :: "memory"); repack(); }
        if (TD == 8 && !has_unit && !((0x2A54 >> head) & 1)) dma4(wd + min((uidx - unit) * 51, 12544 - 51) * 128, min(lane, 50) * 128, L_JUNK);
        if (KGO >= 0) lds_wait1(L, S_KDONE, w2);
        STAMP(5);
        __builtin_amdgcn_s_setprio(VPRIO0);
      }, [&](int n, LAS char*, u32) LAM {
        if (n == VPN) __builtin_amdgcn_s_setprio(VPRIO);
        if (WOT == 4 && n == WTN) dma4(wts, wtv, L_JUNK);
        if (QEARLY == 2 && n >= QN0 && (n - QN0) % QST == 0 && (n - QN0) / QST < 7) { const int i = (n - QN0) / QST; dma16(wbase + 64 * i * 2688, wvo, L_WQ + WDST(4 * i + w2)); }
      });
#pragma unroll
      for (int c = 0; c < 4; ++c) t[c >> 1][c & 1] *= st[c >> 1].y;
      STAMP(6);
#pragma unroll
      for (int c = 0; c < 2; ++c) {
        LAS char* p = pw + c * 16 * 224 + 128;
        *(LAS u32x2*)(p) = u32x2{pk(t[c][0][0], t[c][0][1]), pk(t[c][0][2], t[c][0][3])};
        *(LAS u32x2*)(p + 32) = g == 3 ? u32x2{0x3f80u, 0u} : u32x2{pk(t[c][1][0], t[c][1][1]), pk(t[c][1][2], t[c][1][3])};
      }
      lds_signal(L, S_V, w2);
      __builtin_amdgcn_s_setprio(0);
      STAMP(7);
      if (!QEARLY) {
#pragma unroll
        for (int i = 0; i < 7; ++i) dma16(wbase + 64 * i * 2688, wvo, L_WQ + WDST(4 * i + w2));
      }
      if (WOE == 0) WO_HALF(0);
      if (WOE < 2) { lds_wait(L, S_KDONE); WO_HALF(1); }
      if (REPE == 0) asm volatile("s_waitcnt vmcnt(%8)" : RP_OPS : "n"(QEARLY == 3 && !RPO ? 0 : 7) : "memory");
#define UFLAG() FLAGC(has_unit, WS_FG + ((ufl * 2 + umat) * 8 + ukq) * 128, 1)
      if (REPE == 0) repack();
      STAMP(8);
      WPF();
      if (!has_unit || (REPE == 3 && QEARLY != 3)) WAITVM(0); else WAITVM(9);
      if (UFN < 0) { WAITVM(0); UFLAG(); }
      lds_signal(L, S_WQ, w2);
      project<SLOT - 14336, S_CSQ, 4>(L, L_WQ + wr0, L_WQ + wr1, xf, w2, lane, L_CSP + 512, t, st, [&]() LAM {
        lds_wait(L, S_WQ);
        STAMP(9);
      }, [&](int n, LAS char*, u32) LAM {
        if (n == UFN) { WAITVM(0); UFLAG(); }
      });
      STAMP(10);
      if (REPLATE == 2) repack();
      u32x4 pq[2][2];
      feature_map(t, st, wpf, g, pq, [](int) {});
      asm volatile("" : "+v"(pq[0][0]), "+v"(pq[0][1]), "+v"(pq[1][0]), "+v"(pq[1][1]));
      STAMP(11);
      if (REPLATE == 1) repack();
      if (CE2 && r2 >= 14) { HH_CLEAR(); DRAIN(); HH_FLAG(); }
      if (TD == 13 || TD == 14) {
        lds_wait(L, S_WO);
        if (r2 < 14) WOF_READ(0, TWF); else __builtin_amdgcn_s_sleep(2 * TWF);
        SCHED();
        if (TD == 14) DBITE(); else if (w2 == 0) DTOUCH();
        SCHED();
        if (r2 < 14) WOF_READ(TWF, 8);
      } else if (r2 < 14 || !ISK) { lds_wait(L, S_WO); WOF_READ(0, 8); }
      STAMP(24);
      if (REPLATE == 2) { WAITVM(0); UFLAG(); }
      __builtin_amdgcn_s_setprio(OPRIO);
      lds_wait(L, S_KVA);
      if (REPLATE == 1) { WAITVM(0); UFLAG(); }
      STAMP(27);
      u32x4 ka[4];
#pragma unroll
      for (int i = 0; i < 4; ++i) ka[i] = *(LAS u32x4*)(L + L_KVA + i * 1024 + lane * 16);
      f32x4 o[2][2];
#pragma unroll
      for (int c = 0; c < 4; ++c) o[c >> 1][c & 1] = mfma16(ka[2 * (c & 1) + 1], pq[c >> 1][1], mfma16(ka[2 * (c & 1)], pq[c >> 1][0], z4));
      const f32x2 da = swp16(o[0][1][0], o[0][1][0]), db = swp16(o[1][1][0], o[1][1][0]);
      const float den[2] = {swp32(da.y, da.y).y, swp32(db.y, db.y).y};
#pragma unroll
      for (int c = 0; c < 2; ++c) {
        const float rd = frcp(den[c] + 1e-6f), r3 = g == 3 ? 0.f : rd;
        const u32x4 ob = {pk(o[c][0][0] * rd, o[c][0][1] * rd), pk(o[c][0][2] * rd, o[c][0][3] * rd), pk(o[c][1][0] * r3, o[c][1][1] * r3), filled(pk(o[c][1][2] * r3, o[c][1][3] * r3))};
        ST16(rs, ob, lane * 16, WS_O + ((tile * 8 + 2 * w2 + c) * 16 + head) * 1024);
      }
      STAMP(12);
      if (WOE == 3) lds_wait(L, S_WO);
      *(volatile LAS u32*)(L + L_FLG + S_W2A * 16 + w2 * 4) = (u32)__builtin_amdgcn_s_memrealtime();
      lds_signal(L, S_W2B, w2);
      __builtin_amdgcn_s_setprio(0);
      if (TD == 3 && w2 == 0) DTOUCH();
    }
  }
  STAMP(13);
  u32x4 hres = {0u, 0u, 0u, 0u};
  const int xo = tok * 896 + cg * 128 + kh * 64 + g * 16;
  STAMP(23);
  if (r2 < 14) {
    u32x4 xr, of[8];
    asm volatile("global_load_dwordx4 %0, %1, %2" : "=v"(xr) : "v"(xo), "s"(x) : "memory");
    lds_wait2(L, S_W2B, S_WOFA);
#define WIN2() do { if (A ? W1 < 2 : W1 < 3) GUBLK(A ? 0 : 1); } while (0)
    if (W2F == 1 || W2F == 3) WIN2();
    if (W2F == 4 && !A) { GUBLK(0); GUBLK(1); }
    if (W2F == 5 && !A) GUBLK(1);
    if (T2) {
      const u32x4 ts = *(volatile LAS u32x4*)(L + L_FLG + S_W2A * 16);
      const u32 t0 = __builtin_amdgcn_readfirstlane(ts.x), due = t0 + T2 + __builtin_amdgcn_readfirstlane(max(max(0, (int)(ts.y - ts.x)), max((int)(ts.z - ts.x), (int)(ts.w - ts.x))));
#pragma nounroll
      for (u32 sp = 0; sp < 64 && (int)((u32)__builtin_amdgcn_s_memrealtime() - due) < 0; ++sp) __builtin_amdgcn_s_sleep(1);
    } else __builtin_amdgcn_s_sleep(A ? D2A : D2B);
    STAMP(14);
#define OFA(c_) _Pragma("unroll") for (int i = 0; i < 8; ++i) \
      asm volatile("global_load_dwordx4 %0, %1, %2 offset:%3 sc1" : c_(of[i]) : "v"(lane * 16), "s"(ws + WS_O + ((tile2 * 8 + tg * 4 + tb) * 16 + kh * 8 + 4) * 1024), "n"(i * 1024 - 4096) : "memory")
#define OFW(k_) asm volatile("s_waitcnt vmcnt(%9)" : "+v"(of[0]), "+v"(of[1]), "+v"(of[2]), "+v"(of[3]), "+v"(of[4]), "+v"(of[5]), "+v"(of[6]), "+v"(of[7]), "+v"(xr) : "n"(k_) : "memory")
#define OFBAD() (!__all(max(max(max(of[0].w, of[1].w), max(of[2].w, of[3].w)), max(max(of[4].w, of[5].w), max(of[6].w, of[7].w))) != EMPTY))
    OFA("=v");
    f32x4 a[4] = {};
    u32 mx = 0;
#pragma unroll
    for (int i = 0; i < 8; ++i) {
      asm volatile("s_waitcnt vmcnt(%1)" : "+v"(of[i]) : "n"(7 - i) : "memory");
      if (i == PG - 1) { lds_wait(L, S_WOFB); }
      if (i == 7) GUBLK(A ? 0 : 3);
#pragma unroll
      for (int k = 0; k < 4; ++k) a[k] = mfma16(wof[i][k], of[i], a[k]);
      mx = max(mx, of[i].w);
      SCHED();
    }
    asm volatile("" : "+v"(xr));
    if (__builtin_expect(!__all(mx != EMPTY), 0)) {
#pragma nounroll
      for (u32 sp = 0; sp < SPIN_CAP; ++sp) { __builtin_amdgcn_s_sleep(RSL); OFA("+v"); OFW(0); if (!OFBAD()) break; }
#pragma unroll
      for (int i = 0; i < 8; ++i)
#pragma unroll
        for (int k = 0; k < 4; ++k) a[k] = mfma16(wof[i][k], of[i], i ? a[k] : z4);
    }
    STAMP(25);
    *(LAS f32x4*)(L + L_EX + wave * 2048 + lane * 16) = kh ? a[0] : a[2];
    *(LAS f32x4*)(L + L_EX + wave * 2048 + 1024 + lane * 16) = kh ? a[1] : a[3];
    lds_signal(L, A ? S_EXA : S_EXB, w2);
    lds_wait1(L, A ? S_EXB : S_EXA, w2);
    STAMP(15);
    const f32x4 c0 = (kh ? a[2] : a[0]) + *(LAS f32x4*)(L + L_EX + (wave ^ 4) * 2048 + lane * 16);
    const f32x4 c1 = (kh ? a[3] : a[1]) + *(LAS f32x4*)(L + L_EX + (wave ^ 4) * 2048 + 1024 + lane * 16);
    const f32x4 h0 = c0 + f32x4{blo(xr.x), bhi(xr.x), blo(xr.y), bhi(xr.y)}, h1 = c1 + f32x4{blo(xr.z), bhi(xr.z), blo(xr.w), bhi(xr.w)};
    hres = u32x4{pk(h0[0], h0[1]), pk(h0[2], h0[3]), pk(h1[0], h1[1]), filled(pk(h1[2], h1[3]))};
    ST16(rs, hres, hvo, kh * 2048);
    const f32x4 r0 = {blo(hres.x), bhi(hres.x), blo(hres.y), bhi(hres.y)}, r1 = {blo(hres.z), bhi(hres.z), blo(hres.w), bhi(hres.w)};
    const f32x4 a1 = r0 + r1, a2q = r0 * r0 + r1 * r1;
    float s1 = (a1[0] + a1[1]) + (a1[2] + a1[3]), s2 = (a2q[0] + a2q[1]) + (a2q[2] + a2q[3]);
    { const float t = swadd16(s1, s2); const f32x2 a2 = sw32(t); s1 = a2.x + a2.y; }
    { const u32x2 sv = __builtin_bit_cast(u32x2, sw16(s1)); __builtin_amdgcn_raw_buffer_store_b64(u32x2{sv.x, filled(sv.y)}, rs, fvo, sto + kh * 16384, 16); }
    WAITVM(2);
  } else {
    if (TD == 1) dma4(wd + ((tile2 * 2 + r2 - 14) * 8 + wave) * 49 * 128, min(lane, 48) * 128, L_JUNK);
    lds_wait2(L, S_W2B, S_WOFA);
    if (CF) { HH_CLEAR(); }
    WIN2();
    if (W2F >= 3) { lds_wait(L, S_WOFB); if (W2F < 5 || A) GUBLK(A ? 3 : 4); }
    if (!CF) { HH_CLEAR(); }
    DRAIN();
    if (!CE2) HH_FLAG();
    if (CE2 && A) __builtin_amdgcn_s_sleep(DLY5);
  }
  if constexpr (A && CSL) CS_OUT(2);
  lds_signal(L, A ? S_HFA : S_HFB, w2);
  STAMP(16);
  u32x4 wdf[NWD][8];
  const __amdgpu_buffer_rsrc_t rd = __builtin_amdgcn_make_buffer_rsrc((void*)(wd + w2 * 448 * 896 + min(cg, 6) * 128 - 1024), 0, 448 * 896 + 1024, 0x00020000);
  const u32 dvo = (lane >> 3) * 8 * 896 + (((lane & 7) ^ ((lane >> 2) & 4)) * 16);
  u32 da_ = 2 * SLOT + w2 * 7168 + hh * 512 + q4 * 128 + (g & 1) * 32 + p4 * 8 + 64 * (q4 >> 1), db_ = da_ ^ 64, dc_ = da_ + (L_EX - 2 * SLOT - w2 * 6144), dd_ = dc_ ^ 64;
  const u32 po = WS_FHH + ((tile2 * 16 + 4 * w2 + ((lane >> 1) & 3)) * 4 + 2 * tg + (lane & 1)) * 128;
  const u32 ho = WS_HH + ((tile2 * 2 + tg) * 4 + w2) * 57344;
#define WDLOAD(k, bf) dma_wd<k>(rd, dvo, ((bf) + 2) * SLOT + w2 * 7168, L_EX + (bf) * 4096 + w2 * 1024)
#define WDREAD(dst, bf) do { asm volatile("" : "+v"(da_), "+v"(db_), "+v"(dc_), "+v"(dd_)); \
    _Pragma("unroll") for (int i = 0; i < 8; ++i) { LAS char* p_ = L + (i & 1 ? db_ : da_) + (bf) * SLOT + (i >> 1) * 2048; \
      const u32x2 x_ = trread(p_), y_ = trread(i < 6 ? p_ + 1024 : L + (i & 1 ? dd_ : dc_) + (bf) * 4096); dst[i] = u32x4{x_.x, x_.y, y_.x, y_.y}; } } while (0)
#define THR() asm volatile("s_waitcnt vmcnt(%0)" :: "n"(THRN) : "memory")
#define THR0() asm volatile("s_waitcnt vmcnt(%0)" :: "n"(THRN0) : "memory")
  u32 have = 0;
#define PLOAD() peek(ws, po, lane < 8, tag + 7)
#define PMIN(pv) do { const u64 d = (pv) - tag; have = max(have, min8(d <= 8 ? (u32)d : 0u)); } while (0)
#define NEEDP(n) do { _Pragma("nounroll") for (u32 sp = 0; have < (n) && sp < SPIN_CAP; ++sp) { PMIN(PLOAD()); if (PSLP && have < (n)) __builtin_amdgcn_s_sleep(PSLP); } \
    __builtin_amdgcn_fence(__ATOMIC_ACQUIRE, "wavefront"); } while (0)
#define HBASE() u32 hb3 = w2 * 7168 + lane * 16, hb0 = hb3 + 2 * SLOT, hb2 = hb0 + 2 * SLOT, hb7 = L_EX + w2 * 1024 + lane * 16; asm volatile("" : "+v"(hb0), "+v"(hb2), "+v"(hb7))
#define HFRAG(bf, i) (*(LAS u32x4*)(L + ((i) < 7 ? ((bf) == 3 ? hb3 : (bf) == 2 ? hb2 : hb0 + (bf) * SLOT) + (i) * 1024 : hb7 + (bf) * 4096)))
#define HQLOAD(q, j) _Pragma("unroll") for (int i = 0; i < 8; ++i) hq[q][i] = __builtin_bit_cast(u32x4, __builtin_amdgcn_raw_buffer_load_b128(rs, lane * 16 + (i & 3) * 1024, ho + (j) * 8192 + (i & 4) * 1024, 16))
#define HQLOADT(q, j) _Pragma("unroll") for (int i = 0; i < 8; ++i) { THR(); hq[q][i] = __builtin_bit_cast(u32x4, __builtin_amdgcn_raw_buffer_load_b128(rs, lane * 16 + (i & 3) * 1024, ho + (j) * 8192 + (i & 4) * 1024, 16)); }
#define DOWN16(ac, wf, hf_) _Pragma("unroll") for (int m = 0; m < 16; ++m) ac[(m >> 1) & 1][m & 1] = mfma32(wf[2 * (m >> 2) + (m & 1)], hf_[2 * (m >> 2) + ((m >> 1) & 1)], ac[(m >> 1) & 1][m & 1])
#define PARTIAL_OUT(ac, b0, b1) _Pragma("unroll") for (int n = 0; n < 16; ++n) { const int T = n >> 3, k = (n >> 2) & 1, q = n & 3; \
    *(LAS f32x4*)&HFRAG(T ? b1 : b0, n & 7) = f32x4{ac[T][k][4 * q], ac[T][k][4 * q + 1], ac[T][k][4 * q + 2], ac[T][k][4 * q + 3]}; }
#define FREE(f) do { _Pragma("nounroll") for (u32 sp = 0; sp < (SPIN_CAP << 4); ++sp) { const u32x4 fr = *(volatile LAS u32x4*)(L + L_FLG + S_DONE4 * 16); \
          if (__builtin_amdgcn_readfirstlane(min(min(fr.x, fr.y), min(fr.z, fr.w))) >= (f)) break; __builtin_amdgcn_s_sleep(BSL); } asm volatile("" ::: "memory"); } while (0)
#define HQBAD(q) (!__all(max(max(max(hq[q][0].w, hq[q][1].w), max(hq[q][2].w, hq[q][3].w)), max(max(hq[q][4].w, hq[q][5].w), max(hq[q][6].w, hq[q][7].w))) != EMPTY))
#define HQCHECK(q, j) do { if (__builtin_expect(HQBAD(q), 0)) { _Pragma("nounroll") for (u32 sp = 0; sp < SPIN_CAP; ++sp) { \
      __builtin_amdgcn_s_sleep(RSL); asm volatile("" ::: "memory"); HQLOAD(q, j); if (!HQBAD(q)) break; } } SCHED(); } while (0)
  if constexpr (A) {
    const int T32 = tile * 4 + wave;
    u32x2 st[7];
    u32x4 hb[28];
    u64 fcl;
    const u32 stv = (7 * hh * 2048 + (lane & 31)) * 8;
#define STA(c_) _Pragma("unroll") for (int i = 0; i < 7; ++i) asm volatile("global_load_dwordx2 %0, %1, %2 sc1" : c_(st[i]) : "v"(stv), "s"(ws + WS_ST + (i * 2048 + T32 * 32) * 8) : "memory")
#define STW(k_) asm volatile("s_waitcnt vmcnt(%7)" : "+v"(st[0]), "+v"(st[1]), "+v"(st[2]), "+v"(st[3]), "+v"(st[4]), "+v"(st[5]), "+v"(st[6]) : "n"(k_) : "memory")
#define STBAD() (!__all(max(max(max(st[0].y, st[1].y), max(st[2].y, st[3].y)), max(max(st[4].y, st[5].y), st[6].y)) != EMPTY))
#define HBA(c_, q) _Pragma("unroll") for (int i = 0; i < 4; ++i) asm volatile("global_load_dwordx4 %0, %1, %2 offset:%3 sc1" : c_(hb[4 * (q) + i]) : "v"(lane * 16), "s"(ws + WS_H + (T32 * 28 + 4 * (q)) * 1024), "n"(i * 1024) : "memory")
    lds_wait2(L, S_HFA, S_HFB);
    __builtin_amdgcn_s_sleep(DLY3);
    if (!SPEC || r2 >= 14) {
#pragma nounroll
      for (u32 sp = 0; sp < SPIN_CAP; ++sp) { STA("=v"); STW(0); if (!STBAD()) break; __builtin_amdgcn_s_sleep(RSL); }
    }
    STA("=v");
    STAMP(17);
#pragma unroll
    for (int q = 0; q < 7; ++q) { HBA("=v", q); }
    asm volatile("global_load_dwordx2 %0, %1, %2 sc1" : "=v"(fcl) : "v"(p4 * 128), "s"(ws + WS_FH + (T32 * 32 + 28) * 128) : "memory");
    u32 wb_[3] = {(u32)lane * 16, (u32)lane * 16 + 2 * SLOT, (u32)lane * 16 + 4 * SLOT};
    asm volatile("" : "+v"(wb_[1]), "+v"(wb_[2]));
#define WFRAG(sl, s) (*(LAS u32x4*)(L + wb_[(sl) >> 1] + ((sl) & 1) * SLOT + (s) * 1024))
    LAS char* csl = L + L_CS + hh * 16;
    u32x4 ring[7];
    f32x4 cq[4];
    float nmu, ve, k0, k1;
    u32 mx;
    const int pvo = WS_FHH + ((tile * 16 + r) * 4 + wave) * 128;
#define PROG(n) FLAG(pvo + 131072, n)
    f32x16 acc[2];
    float y[8], gu[8];
    u32x4 yb;
    u32x4 cw;
#define LT(i)
    auto gu_block = [&](auto JC) __attribute__((always_inline)) {
      constexpr int j = JC.value;
      sfor<0, 28>([&](auto SC) LAM {
        constexpr int s = SC.value;
        if constexpr (j == 0) { u32x4 t_ = hb[s]; asm volatile("s_waitcnt vmcnt(%1)" : "+v"(t_) : "n"(28 - s) : "memory"); hb[s] = t_; }
        acc[j & 1] = mfma32(ring[s % 7], hb[s], acc[j & 1]);
        if constexpr (j == 0) mx = max(mx, hb[s].w);
        if constexpr ((j == W3J || j == 4) && s == 12) cw = *(volatile LAS u32x4*)(L + L_FLG + (j == W3J ? S_WIN3 : S_REFILL) * 16);
        if constexpr ((j == W3J || j == 4) && s == 20) { if (__builtin_expect(!__builtin_amdgcn_readfirstlane(cw.x & cw.y & cw.z & cw.w), 0)) lds_wait(L, j == W3J ? S_WIN3 : S_REFILL); asm volatile("" ::: "memory"); }
        if constexpr (j == 1 && s < 7 - NCL) ST16(rs, (u32x4{EMPTY, EMPTY, EMPTY, EMPTY}), lane * 16, hho + (NCL + s) * 8192);
        if constexpr (s + 7 < 28) ring[s % 7] = WFRAG(gu_slot(j), s + 7);
        else if constexpr (j < 6) ring[s % 7] = WFRAG(gu_slot(j + 1), s - 21);
        if constexpr (s == 0 && (j == 1 || j == 2)) lds_signal(L, j == 1 ? S_WSEEN : S_FREE01, wave);
        if constexpr (s == 0 && j >= 3) *(volatile LAS u32*)(L + L_FLG + S_DONE4 * 16 + wave * 4) = j - 2u;
        if constexpr (j > 0) {
          sfor<0, 8>([&](auto EC) LAM {
            constexpr int i = EC.value, k = s - 1 - 2 * i;
            const float ga = acc[(j - 1) & 1][i];
            if constexpr (k == 0) y[i] = ga * ga;
            if constexpr (k == 1) y[i] = fmaf(y[i], k1, k0);
            if constexpr (k == 2) y[i] = ga * y[i];
            if constexpr (k == 3) y[i] = fexp2(y[i]);
            if constexpr (k == 4) y[i] = fmaf(y[i], ve, ve);
            if constexpr (k == 5) { gu[i] = ga * acc[(j - 1) & 1][i + 8]; PIN(gu[i]); }
            if constexpr (k == 6) y[i] = frcp(y[i]);
            if constexpr (k == 7) y[i] = y[i] * gu[i];
            if constexpr (k >= 0 && k < 8 && k != 5) PIN(y[i]);
          });
          if constexpr (s == 21) { yb[0] = pk(y[0], y[1]); yb[1] = pk(y[2], y[3]); }
          if constexpr (s == 22) yb[2] = pk(y[4], y[5]);
          if constexpr (NCL == 1 && j == 2 && s == 22) WAITVM(2);
          if constexpr (j == 3 && s == 22 && NCL < 7 && NCL > 1) WAITVM(4);
          if constexpr (s == 23) { yb[3] = filled(pk(y[6], y[7])); ST16(rs, yb, lane * 16, hho + (j - 1) * 8192); }
          if constexpr (s == 24 && j <= (HELP > 4 ? HELP : 4)) FLAG(pvo, j);
        }
        if constexpr (j < 6 && s >= 17 && s < 21) cq[s - 17] = *(LAS f32x4*)(csl + (j + 1) * 128 + (s - 17) * 32);
        if constexpr (j < 6 && s >= 22 && s < 27) {
          sfor<0, 16>([&](auto EC) LAM {
            constexpr int e = EC.value;
            if constexpr ((e < 2 ? 22 : e < 4 ? 23 : 23 + (e >> 2)) == s) { float v = nmu * cq[e >> 2][e & 3]; PIN(v); acc[(j + 1) & 1][e] = v; }
          });
        }
        SCHED();
      });
      LT(j);
      if (j == 0) STAMP(18);
      if (j == 3) STAMP(26);
      if (j == 1) STAMP(28);
      if (j == 2) STAMP(29);
      if (j == 4) STAMP(30);
      if (j == 5) STAMP(31);
    };
#define HBW(q) asm volatile("s_waitcnt vmcnt(0)" : "+v"(hb[4 * (q)]), "+v"(hb[4 * (q) + 1]), "+v"(hb[4 * (q) + 2]), "+v"(hb[4 * (q) + 3]) :: "memory")
#define HBBAD(q) (!__all(max(max(hb[4 * (q)].w, hb[4 * (q) + 1].w), max(hb[4 * (q) + 2].w, hb[4 * (q) + 3].w)) != EMPTY))
#define HBC(q, c_) asm volatile("s_bitcmp0_b32 %6, %7\n\ts_cbranch_scc1 1f\n\tglobal_load_dwordx4 %0, %4, %5 sc1\n\tglobal_load_dwordx4 %1, %4, %5 offset:1024 sc1\n\t" \
      "global_load_dwordx4 %2, %4, %5 offset:2048 sc1\n\tglobal_load_dwordx4 %3, %4, %5 offset:3072 sc1\n1:" : "+v"(hb[4 * (q)]), "+v"(hb[4 * (q) + 1]), "+v"(hb[4 * (q) + 2]), "+v"(hb[4 * (q) + 3]) \
      : "v"(lane * 16), "s"(ws + WS_H + (T32 * 28 + 4 * (q)) * 1024), "s"(c_), "n"(q) : "memory", "scc")
#define REDO(m_) do { STA("+v"); _Pragma("unroll") for (int q = 0; q < 7; ++q) HBC(q, m_); STW(0); _Pragma("unroll") for (int q = 0; q < 7; ++q) HBW(q); } while (0)
#define FIX() do { _Pragma("nounroll") for (u32 sp = 0; sp < SPIN_CAP; ++sp) { u32 m = 0; \
        _Pragma("unroll") for (int q = 0; q < 7; ++q) m |= HBBAD(q) ? 1u << q : 0u; \
        if (!m && !STBAD()) break; if (sp) __builtin_amdgcn_s_sleep(RSL); m = __builtin_amdgcn_readfirstlane(m); REDO(m); } } while (0)
    auto first = [&]() LAM {
#pragma unroll
      for (int k = 0; k < 4; ++k) cq[k] = *(LAS f32x4*)(csl + k * 32);
#pragma unroll
      for (int s = 0; s < 7; ++s) ring[s] = WFRAG(0, s);
      f32x2 sf[7];
#pragma unroll
      for (int i = 0; i < 7; ++i) sf[i] = __builtin_bit_cast(f32x2, st[i]);
      float s1 = ((sf[0].x + sf[1].x) + (sf[2].x + sf[3].x)) + ((sf[4].x + sf[5].x) + sf[6].x);
      float s2 = ((sf[0].y + sf[1].y) + (sf[2].y + sf[3].y)) + ((sf[4].y + sf[5].y) + sf[6].y);
      { f32x2 a = sw32(s1); s1 = a.x + a.y; a = sw32(s2); s2 = a.x + a.y; }
      nmu = s1 * (-1.f / 448.f); ve = fmaxf(s2 * (1.f / 448.f) - nmu * nmu, 0.f) + 1e-5f;
      const float rstd = __builtin_amdgcn_rsqf(ve);
      k0 = -2.30220819f * rstd; k1 = -0.10294324f * rstd * rstd * rstd;
#pragma unroll
      for (int i = 0; i < 16; ++i) acc[0][i] = nmu * cq[i >> 2][i & 3];
      mx = 0;
      __builtin_amdgcn_s_setprio(GUPRIO);
      SCHED();
      gu_block(IC<0>{});
    };
    STW(29);
    if (__builtin_expect(STBAD(), 0)) {
      u32 m = 0;
#pragma unroll
      for (int i = 0; i < 7; ++i) { const u64 bm = __builtin_amdgcn_ballot_w64(st[i].y == EMPTY); m |= ((u32)bm ? 1u << (i >> 1) : 0u) | ((u32)(bm >> 32) ? 1u << ((7 + i) >> 1) : 0u); }
      m = __builtin_amdgcn_readfirstlane(m);
      asm volatile("s_waitcnt vmcnt(%0)" :: "n"(FXW) : "memory");
      REDO(m);
      FIX();
    }
    first();
    if (__builtin_expect(!__all(mx != EMPTY), 0)) { FIX(); first(); }
    asm volatile("s_waitcnt vmcnt(0)" : "+v"(fcl) :: "memory");
    if (__builtin_expect(!__all(fcl == tag + 1), 0)) poll_flags(ws, WS_FH + (T32 * 32 + 28 + p4) * 128, lane < 4, tag);
    gu_block(IC<1>{}); gu_block(IC<2>{}); gu_block(IC<3>{}); gu_block(IC<4>{}); gu_block(IC<5>{}); gu_block(IC<6>{});
#pragma unroll
    for (int i = 0; i < 8; ++i) { const float ga = acc[0][i]; y[i] = ga * acc[0][i + 8] * frcp(fmaf(fexp2(ga * fmaf(ga * ga, k1, k0)), ve, ve)); }
    yb = u32x4{pk(y[0], y[1]), pk(y[2], y[3]), pk(y[4], y[5]), filled(pk(y[6], y[7]))};
    ST16(rs, yb, lane * 16, hho + 6 * 8192);
    if (HELP == 7) FLAG(pvo, 7);
    __builtin_amdgcn_s_setprio(0);
    STAMP(19);
    if (r2 < 14) {
      f32x16 da[2][2] = {};
      u32x4 hq[2][8], wl[2][8];
      HBASE();
      __builtin_amdgcn_s_sleep(ADLY);
      lds_wait1(L, S_CONS, w2);
      STAMP2(15);
      HQLOAD(0, 5);
      if (A6L == 0) HQLOAD(1, 6);
      WDREAD(wl[0], 2);
      SCHED();
      STAMP2(16);
      HQCHECK(0, 5);
      if (A6L == 1) HQLOAD(1, 6);
      WDREAD(wl[1], 0);
      SCHED();
      DOWN16(da, wl[0], hq[0]);
      SCHED();
      STAMP2(18);
      HQCHECK(1, 6);
      STAMP2(31);
      DOWN16(da, wl[1], hq[1]);
      PARTIAL_OUT(da, 0, 2);
      STAMP(20);
    }
  } else {
    lds_wait2(L, S_HFA, S_HFB);
    if (TD == 6 && w2 == 0) DTOUCH();
    if (TD == 7) dma4(wd + min(b * 49 + w2 * 13, 12544 - 13) * 128, min(lane, 12) * 128, L_JUNK);
    __builtin_amdgcn_s_setprio(BP3);
    if (r2 >= 14) GUBLK(4);
#define REFILL() do { lds_wait<BSL>(L, S_WSEEN); GUBLK(5); lds_wait<BSL>(L, S_FREE01); GUBLK(6); } while (0)
    if (r2 >= 14) {
      WAITVM(0); lds_signal(L, S_WIN3, w2);
      REFILL();
      WAITVM(0); lds_signal(L, S_REFILL, w2);
    } else {
      {
        u32x2 e[24][2];
        u32 ea_ = da_ - 2 * SLOT, eb_ = ea_ ^ 64;
        asm volatile("" : "+v"(da_), "+v"(db_), "+v"(dc_), "+v"(dd_), "+v"(ea_), "+v"(eb_));
#define WBL(N0, N1, NS, SL) sfor<N0, N1>([&](auto NC) LAM { constexpr int n = NC.value, k = n - (N0); \
          if constexpr (k < (NS)) dma_wd1<n, (SL) * SLOT + k * 1024>(rd, dvo, w2 * 7168); else dma_wd1<n, L_EX + (k - (NS)) * 4096>(rd, dvo, w2 * 1024); })
#define WBR(N0, N1, NS, SL) do { sfor<N0, N1>([&](auto NC) LAM { constexpr int n = NC.value, k = n - (N0); \
          if constexpr (k >= (NS)) { e[n][0] = trread(L + dc_ + (k - (NS)) * 4096); e[n][1] = trread(L + dd_ + (k - (NS)) * 4096); } \
          else if constexpr ((SL) == 4) { e[n][0] = trread(L + da_ + 2 * SLOT + k * 1024); e[n][1] = trread(L + db_ + 2 * SLOT + k * 1024); } \
          else { e[n][0] = trread(L + ea_ + k * 1024); e[n][1] = trread(L + eb_ + k * 1024); } }); \
          asm volatile("s_waitcnt lgkmcnt(0)" : "+v"(e[(N1) - 1][1]) :: "memory"); } while (0)
        WBL(0, 11, 7, 4);
        WAITVM(0);
        STAMP2(24);
        WBR(0, 11, 7, 4);
        WBL(11, 15, 0, 0);
        if (G4E == 1) { GUBLK(4); WAITVM(7); } else WAITVM(0);
        STAMP2(25);
        WBR(11, 15, 0, 0);
        if (G4E == 0) GUBLK(4);
        lds_wait<BSL>(L, S_WSEEN);
        WBL(15, 24, 7, 0);
        if (G4E == 2) { GUBLK(4); WAITVM(7); } else { WAITVM(0); lds_signal(L, S_WIN3, w2); }
        STAMP2(26);
        WBR(15, 24, 7, 0);
        GUBLK(5);
        if (G4E == 2) { WAITVM(7); lds_signal(L, S_WIN3, w2); }
        lds_wait<BSL>(L, S_FREE01);
        GUBLK(6);
#pragma unroll
        for (int n = 0; n < 24; ++n) wdf[n >> 3][n & 7] = u32x4{e[n & ~1][n & 1].x, e[n & ~1][n & 1].y, e[n | 1][n & 1].x, e[n | 1][n & 1].y};
      }
      STAMP2(23);
      f32x16 acc[2][2] = {};
      u32x4 hq[2][8];
      STAMP2(16);
      HBASE();
      u64 pv;
      const char* hs = ws + ho + 4096;
#define PEEK() do { pv = tag + 7; asm volatile("s_mov_b64 exec, 0xff\n\tglobal_atomic_add_x2 %0, %1, %2, %3 sc0\n\ts_mov_b64 exec, -1" : "+v"(pv) : "v"(po), "v"(0ull), "s"(ws) : "memory"); } while (0)
#define PEEKED(k_, n_) do { asm volatile("s_waitcnt vmcnt(%1)" : "+v"(pv) : "n"(k_) : "memory"); PMIN(pv); NEEDP(n_); } while (0)
#define HQA(c_, q, j) _Pragma("unroll") for (int i = 0; i < 8; ++i) asm volatile("global_load_dwordx4 %0, %1, %2 offset:%3 sc1" : c_(hq[q][i]) : "v"(lane * 16), "s"(hs + (j) * 8192), "n"(i * 1024 - 4096) : "memory")
#define HQW(q, k_) asm volatile("s_waitcnt vmcnt(%8)" : "+v"(hq[q][0]), "+v"(hq[q][1]), "+v"(hq[q][2]), "+v"(hq[q][3]), "+v"(hq[q][4]), "+v"(hq[q][5]), "+v"(hq[q][6]), "+v"(hq[q][7]) : "n"(k_) : "memory")
#define HQOK(q, j, k_) do { HQW(q, k_); if (__builtin_expect(HQBAD(q), 0)) { _Pragma("nounroll") for (u32 sp = 0; sp < SPIN_CAP; ++sp) { \
        __builtin_amdgcn_s_sleep(RSL); HQA("+v", q, j); HQW(q, 0); if (!HQBAD(q)) break; } } SCHED(); } while (0)
      PEEK();
      FREE(1); WDLOAD(3, 0);
      WAITVM(8);
#pragma unroll
      for (int k = 0; k < 3; ++k) asm volatile("" : "+v"(wdf[k][0]), "+v"(wdf[k][1]), "+v"(wdf[k][2]), "+v"(wdf[k][3]), "+v"(wdf[k][4]), "+v"(wdf[k][5]), "+v"(wdf[k][6]), "+v"(wdf[k][7]) :: "memory");
      lds_signal(L, S_REFILL, w2);
      PEEKED(8, 1);
      STAMP2(1);
      PEEK();
      HQA("=v", 0, 0);
      FREE(2); WDLOAD(4, 1);
      PEEKED(16, 2);
      STAMP2(2);
      HQA("=v", 1, 1);
      PEEK();
      u32 fr3;
      { const u32x4 fr = *(volatile LAS u32x4*)(L + L_FLG + S_DONE4 * 16); fr3 = !W5D || __builtin_amdgcn_readfirstlane(min(min(fr.x, fr.y), min(fr.z, fr.w))) >= 3; }
      if (fr3) { if (!W5D) FREE(3); WDLOAD(5, 2); }
      if (fr3) WAITVM(25); else WAITVM(17);
      HQOK(0, 0, 25);
      __builtin_amdgcn_s_setprio(DP0);
      DOWN16(acc, wdf[0], hq[0]);
      __builtin_amdgcn_s_setprio(BP3);
      WDREAD(wdf[0], 0);
      SCHED();
      STAMP2(8);
      if (!fr3) { FREE(3); WDLOAD(5, 2); }
      PEEKED(8, 3);
      STAMP2(3);
      HQA("=v", 0, 2);
      PEEK();
      asm volatile("s_waitcnt lgkmcnt(0)" : "+v"(wdf[0][7]) :: "memory");
      WDLOAD(6, 0);
      HQOK(1, 1, 25);
      __builtin_amdgcn_s_setprio(DP0);
      DOWN16(acc, wdf[1], hq[1]);
      WDREAD(wdf[1], 1);
      SCHED();
      STAMP2(9);
      PEEKED(8, 4);
      STAMP2(4);
      HQA("=v", 1, 3);
      __builtin_amdgcn_s_setprio(DNPRIO);
      HQOK(0, 2, 8);
      lds_signal(L, S_CONS, w2);
      SCHED();
      DOWN16(acc, wdf[2], hq[0]);
      SCHED();
      STAMP2(10);
      FREE(4);
      HQA("=v", 0, 4);
      HQOK(1, 3, 8);
      DOWN16(acc, wdf[0], hq[1]);
      SCHED();
      STAMP2(11);
      HQOK(0, 4, 0);
      DOWN16(acc, wdf[1], hq[0]);
      PARTIAL_OUT(acc, 1, 3);
      STAMP(20);
    }
  }
  if (r2 < 14) {
    BAR();
    STAMP2(7);
    float c[8] = {blo(hres.x), bhi(hres.x), blo(hres.y), bhi(hres.y), blo(hres.z), bhi(hres.z), blo(hres.w), bhi(hres.w)};
    const int rl = ((tb & 1) * 16 + l15) * 16;
    f32x4 v0 = {c[0], c[1], c[2], c[3]}, v1 = {c[4], c[5], c[6], c[7]};
#pragma unroll
    for (int h = 0; h < 2; ++h) {
      const int bf = (tb >> 1) * 2 + h;
      const bool p7 = kh && g == 3;
      u32 rq = (p7 ? L_EX + bf * 4096 : (bf == 3 ? 0 : (bf + 2) * SLOT) + (4 * kh + g) * 1024) + rl;
      const u32 st7 = p7 ? 1024 : 7168;
      asm volatile("" : "+v"(rq));
#pragma unroll
      for (int w = 0; w < 4; ++w) { v0 += *(LAS f32x4*)(L + rq + w * st7); v1 += *(LAS f32x4*)(L + rq + w * st7 + 512); }
    }
#pragma unroll
    for (int i = 0; i < 4; ++i) { c[i] = v0[i]; c[4 + i] = v1[i]; }
    { const u32x4 ov = {pk(c[0], c[1]), pk(c[2], c[3]), pk(c[4], c[5]), pk(c[6], c[7])}; asm volatile("global_store_dwordx4 %0, %1, %2 sc1\n\ts_nop 1" :: "v"(xo), "v"(ov), "s"(out) : "memory"); }
    if (A && b == 0 && tid == 0) __hip_atomic_store((GAS u64*)(ws + WS_E), tag, RLX_AGENT);
  }
  STAMP(21);
  };
  if (wave < 4) role(IC<1>{}); else role(IC<0>{});
}

extern "C" void kernel_launch(void* const* d_in, const int* in_sizes, int n_in, void* d_out, int out_size, void* d_ws, size_t ws_size, hipStream_t stream) {
  (void)hipFuncSetAttribute((const void*)fused_block, hipFuncAttributeMaxDynamicSharedMemorySize, LDS_BYTES);
  fused_block<<<256, 512, LDS_BYTES, stream>>>((const char*)d_in[0], (const char*)d_in[1], (const char*)d_in[2], (const char*)d_in[3], (const char*)d_in[5], (const char*)d_in[6],
                                               (const char*)d_in[7], (char*)d_out, (char*)d_ws);
}
```

```cpp
#include <hip/hip_runtime.h>
#include <hip/hip_bf16.h>
#include <stdint.h>

typedef unsigned u32;
typedef unsigned long long u64;
typedef u32 u32x4 __attribute__((ext_vector_type(4)));
typedef u32 u32x2 __attribute__((ext_vector_type(2)));
typedef float f32x2 __attribute__((ext_vector_type(2)));
typedef float f32x4 __attribute__((ext_vector_type(4)));
typedef float f32x16 __attribute__((ext_vector_type(16)));
typedef short s16x4 __attribute__((ext_vector_type(4)));
typedef __bf16 bf16x2 __attribute__((ext_vector_type(2)));
typedef __bf16 bf16x8 __attribute__((ext_vector_type(8)));
#define LAS __attribute__((address_space(3)))
#define GAS __attribute__((address_space(1)))
#define DI __device__ __forceinline__
#define RLX_AGENT __ATOMIC_RELAXED, __HIP_MEMORY_SCOPE_AGENT

constexpr int WS_E = 0;
constexpr int WS_FKV = 8192;
constexpr int WS_FO = WS_FKV + 131072;
constexpr int WS_FH = WS_FO + 131072;
constexpr int WS_FG = WS_FH + 262144;
constexpr int WS_FD = WS_FG + 114688;
constexpr int WS_FHH = WS_FD + 57344;
constexpr int WS_CS = WS_FHH + 262144;
constexpr int WS_ST = WS_CS + 200704;
constexpr int WS_WGU = WS_ST + 229376;
constexpr int WS_WD = WS_WGU + 3211264;
constexpr int WS_KVP = WS_WD + 1605632;
constexpr int WS_O = WS_KVP + 1048576;
constexpr int WS_H = WS_O + 2097152;
constexpr int WS_HH = WS_H + 1835008;
#define UNITX4
#define NOWD
#define WDOFF(cg, fb, bl) (((((cg) * 4 + (fb) / 28) * 7 + (fb) % 7) * 4 + (fb) / 7 % 4) * 2 + (bl))
#define HHOFF(T32, fb) (((((T32) >> 1) * 4 + (fb) / 28) * 7 + (fb) % 7) * 8 + ((fb) / 7 & 3) * 2 + ((T32) & 1))
constexpr int WS_STAMP = WS_HH + 7340032;
constexpr int WS_LOG = WS_STAMP + 256 * 8 * 32 * 8;
constexpr int WS_USED = WS_LOG + 4096;
constexpr int SLOT = 28672;
constexpr int L_WK = 4 * SLOT, L_WV = 0, L_WQ = 14336;
constexpr int L_PKV = 2 * SLOT;
constexpr int L_WO = 3 * SLOT;
constexpr int L_EX = 5 * SLOT;
constexpr int L_KVA = 5 * SLOT;
constexpr int L_ST1 = 5 * SLOT + 4096;
constexpr int L_WPHI = 5 * SLOT + 5120;
constexpr int L_CSP = 5 * SLOT + 7168;
constexpr int L_CS = 159744;
constexpr int L_FLG = 160768;
constexpr int L_JUNK = 161280;
constexpr int L_STAMP = 161536;
constexpr int L_RED = SLOT;
constexpr int LDS_BYTES = 163840;
constexpr int gu_slot(int j) { return j % 5; }
constexpr int CODE_PF = 49152;
constexpr u32 SPIN_CAP = 1u << 16;
#define LL 1
#define AP 1

#define STAMP(i) do {} while (0)
#define STAMP2(i) do {} while (0)
#define STAMP2X(i) do {} while (0)

DI f32x4 mfma16(u32x4 a, u32x4 b, f32x4 c) { return __builtin_amdgcn_mfma_f32_16x16x32_bf16(__builtin_bit_cast(bf16x8, a), __builtin_bit_cast(bf16x8, b), c, 0, 0, 0); }
DI f32x16 mfma32(u32x4 a, u32x4 b, f32x16 c) { return __builtin_amdgcn_mfma_f32_32x32x16_bf16(__builtin_bit_cast(bf16x8, a), __builtin_bit_cast(bf16x8, b), c, 0, 0, 0); }
DI u32 pk(float lo, float hi) { f32x2 p = {lo, hi}; return __builtin_bit_cast(u32, __builtin_convertvector(p, bf16x2)); }
DI float blo(u32 v) { return __builtin_bit_cast(float, v << 16); }
DI float bhi(u32 v) { return __builtin_bit_cast(float, v & 0xffff0000u); }
DI float dot2(u32 a, u32 b, float c) { return __builtin_amdgcn_fdot2_f32_bf16(__builtin_bit_cast(bf16x2, a), __builtin_bit_cast(bf16x2, b), c, false); }
DI u32x2 trread(LAS char* p) { return __builtin_bit_cast(u32x2, __builtin_amdgcn_ds_read_tr16_b64_v4i16((LAS s16x4*)p)); }
DI u32x4 trfrag(LAS char* p, int step) { u32x2 a = trread(p), b = trread(p + step); return u32x4{a.x, a.y, b.x, b.y}; }
DI float fexp2(float x) { return __builtin_amdgcn_exp2f(x); }
DI float frcp(float x) { return __builtin_amdgcn_rcpf(x); }
template <int C> DI float dppror(float x) { return __builtin_bit_cast(float, __builtin_amdgcn_update_dpp(0, __builtin_bit_cast(int, x), 0x120 + C, 0xf, 0xf, false)); }
DI float row_max(float x) { x = fmaxf(x, dppror<8>(x)); x = fmaxf(x, dppror<4>(x)); x = fmaxf(x, dppror<2>(x)); return fmaxf(x, dppror<1>(x)); }
DI float row_min(float x) { x = fminf(x, dppror<8>(x)); x = fminf(x, dppror<4>(x)); x = fminf(x, dppror<2>(x)); return fminf(x, dppror<1>(x)); }
DI float row_sum(float x) { x += dppror<8>(x); x += dppror<4>(x); x += dppror<2>(x); return x + dppror<1>(x); }
DI f32x2 sw16(float x) { u32 u = __builtin_bit_cast(u32, x); auto r = __builtin_amdgcn_permlane16_swap(u, u, false, false); return f32x2{__builtin_bit_cast(float, (u32)r[0]), __builtin_bit_cast(float, (u32)r[1])}; }
DI f32x2 sw32(float x) { u32 u = __builtin_bit_cast(u32, x); auto r = __builtin_amdgcn_permlane32_swap(u, u, false, false); return f32x2{__builtin_bit_cast(float, (u32)r[0]), __builtin_bit_cast(float, (u32)r[1])}; }
DI float grp_sum(float x) { f32x2 a = sw16(x); x = a.x + a.y; a = sw32(x); return a.x + a.y; }
DI float grp_max(float x) { f32x2 a = sw16(x); x = fmaxf(a.x, a.y); a = sw32(x); return fmaxf(a.x, a.y); }
DI float grp_min(float x) { f32x2 a = sw16(x); x = fminf(a.x, a.y); a = sw32(x); return fminf(a.x, a.y); }

#define BAR() asm volatile("s_waitcnt lgkmcnt(0)\n\ts_barrier" ::: "memory")
#define WAITVM(n) asm volatile("s_waitcnt vmcnt(" #n ")" ::: "memory")
#define DRAIN() do { asm volatile("s_waitcnt vmcnt(0)" ::: "memory"); __builtin_amdgcn_s_waitcnt(0x0F70); } while (0)
#define SCHED() __builtin_amdgcn_sched_barrier(0)
DI void dma16(const void* base, u32 voff, u32 lds) {
  u32 keep;
  asm volatile("s_mov_b32 %0, m0\n\ts_mov_b32 m0, %3\n\ts_nop 0\n\tglobal_load_lds_dwordx4 %1, %2\n\ts_mov_b32 m0, %0" : "=&s"(keep) : "v"(voff), "s"(base), "s"(lds) : "memory");
}
DI void dma16c(const void* base, u32 voff, u32 lds) {
  u32 keep;
  asm volatile("s_mov_b32 %0, m0\n\ts_mov_b32 m0, %3\n\ts_nop 0\n\tglobal_load_lds_dwordx4 %1, %2 sc1\n\ts_mov_b32 m0, %0" : "=&s"(keep) : "v"(voff), "s"(base), "s"(lds) : "memory");
}
DI void dma4(const void* base, u32 voff, u32 lds) {
  u32 keep;
  asm volatile("s_mov_b32 %0, m0\n\ts_mov_b32 m0, %3\n\ts_nop 0\n\tglobal_load_lds_dword %1, %2\n\ts_mov_b32 m0, %0" : "=&s"(keep) : "v"(voff), "s"(base), "s"(lds) : "memory");
}
DI void dma16c_if(u32 go, const void* base, u32 voff, u32 lds) {
  u32 keep;
  asm volatile("s_cmp_eq_u32 %4, 0\n\ts_cbranch_scc1 1f\n\ts_mov_b32 %0, m0\n\ts_mov_b32 m0, %3\n\ts_nop 0\n\tglobal_load_lds_dwordx4 %1, %2 sc1\n\ts_mov_b32 m0, %0\n1:"
               : "=&s"(keep) : "v"(voff), "s"(base), "s"(lds), "s"(go) : "memory", "scc");
}
template <int SI, int LI> DI void dma7(__amdgpu_buffer_rsrc_t rs, u32 voff, u32 so, u32 lds) {
  u32 t;
  asm volatile("s_add_u32 m0, %4, %6\n\ts_add_u32 %0, %3, %5\n\tbuffer_load_dwordx4 %1, %2, %0 offen sc1 lds\n\tbuffer_load_dwordx4 %1, %2, %0 offen offset:1024 sc1 lds\n\t"
               "buffer_load_dwordx4 %1, %2, %0 offen offset:2048 sc1 lds\n\tbuffer_load_dwordx4 %1, %2, %0 offen offset:3072 sc1 lds\n\t"
               "s_add_u32 m0, m0, 4096\n\ts_add_u32 %0, %0, 4096\n\tbuffer_load_dwordx4 %1, %2, %0 offen sc1 lds\n\tbuffer_load_dwordx4 %1, %2, %0 offen offset:1024 sc1 lds\n\t"
               "buffer_load_dwordx4 %1, %2, %0 offen offset:2048 sc1 lds"
               : "=&s"(t) : "v"(voff), "s"(rs), "s"(so), "s"(lds), "n"(SI), "n"(LI) : "memory", "scc");
}
constexpr int wrow(int q, int j, int hf) { return (((7 * q + j) >> 2) * 64 + 2 * ((7 * q + j) & 3) + hf) * 896 + 1024; }
#define DL_(n, o) "s_mov_b32 %0, %" #n "\n\tbuffer_load_dwordx4 %1, %2, %0 offen offset:" #o " lds\n\t"
template <int J> DI void dma_wd(__amdgpu_buffer_rsrc_t rs, u32 voff, u32 lds, u32 lds7) {
  u32 t;
  asm volatile("s_mov_b32 m0, %3\n\t" DL_(5, 0) DL_(6, 1024) DL_(7, 2048) DL_(8, 3072) "s_add_u32 m0, m0, 4096\n\t" DL_(9, 0) DL_(10, 1024) DL_(11, 2048) "s_mov_b32 m0, %4\n\t" DL_(12, 0)
               : "=&s"(t) : "v"(voff), "s"(rs), "s"(lds), "s"(lds7), "n"(wrow(0, J, 0)), "n"(wrow(0, J, 1) - 1024), "n"(wrow(1, J, 0) - 2048), "n"(wrow(1, J, 1) - 3072),
                 "n"(wrow(2, J, 0)), "n"(wrow(2, J, 1) - 1024), "n"(wrow(3, J, 0) - 2048), "n"(wrow(3, J, 1)) : "memory", "scc");
}
template <int N, int LO> DI void dma_wd1(__amdgpu_buffer_rsrc_t rs, u32 voff, u32 lds) {
  u32 t;
  asm volatile("s_add_u32 m0, %3, %4\n\t" DL_(5, 0) : "=&s"(t) : "v"(voff), "s"(rs), "s"(lds), "n"(LO), "n"(wrow(N / 2 % 4, N / 8, N % 2)) : "memory", "scc");
}
DI void touch(const void* base, u32 voff) {
  asm volatile("s_mov_b32 m0, %2\n\ts_nop 0\n\tglobal_load_lds_dword %0, %1" :: "v"(voff), "s"(base), "n"(L_JUNK) : "memory");
}
constexpr int xrow(int n) { return ((n / 14) * 16 + (n & 1) * 8) * 896 + ((n % 14) >> 1) * 128 + 2048 - n % 4 * 1024; }
template <int N> DI void dma_x(__amdgpu_buffer_rsrc_t rs, u32 voff, u32 lds) {
  u32 t;
  asm volatile("s_add_u32 m0, %3, %4\n\t" DL_(5, 0) DL_(6, 1024) DL_(7, 2048) DL_(8, 3072)
               : "=&s"(t) : "v"(voff), "s"(rs), "s"(lds), "n"(N * 1024), "n"(xrow(N)), "n"(xrow(N + 1)), "n"(xrow(N + 2)), "n"(xrow(N + 3)) : "memory", "scc");
}
DI u32 vm_count() {
  u32 t;
  asm volatile("s_getreg_b32 %0, hwreg(HW_REG_IB_STS)" : "=s"(t) :: "memory");
  return (t & 15u) | ((t >> 18) & 48u);
}
DI u32 min8(u32 c) {
  c = min(c, (u32)__builtin_amdgcn_update_dpp(0, (int)c, 0x121, 0xf, 0xf, false)); c = min(c, (u32)__builtin_amdgcn_update_dpp(0, (int)c, 0x122, 0xf, 0xf, false));
  c = min(c, (u32)__builtin_amdgcn_update_dpp(0, (int)c, 0x124, 0xf, 0xf, false));
  return __builtin_amdgcn_readfirstlane(min(c, (u32)__builtin_amdgcn_update_dpp(0, (int)c, 0x128, 0xf, 0xf, false)));
}
DI void dma4c(const void* base, u32 voff, u32 lds) {
  u32 keep;
  asm volatile("s_mov_b32 %0, m0\n\ts_mov_b32 m0, %3\n\ts_nop 0\n\tglobal_load_lds_dword %1, %2 sc1\n\ts_mov_b32 m0, %0" : "=&s"(keep) : "v"(voff), "s"(base), "s"(lds) : "memory");
}
#define LD16(rs, off) __builtin_bit_cast(u32x4, __builtin_amdgcn_raw_buffer_load_b128(rs, off, 0, 16))
#define ST16(rs, v, off, soff) do { const u32x4 d_ = __builtin_bit_cast(u32x4, v); __builtin_amdgcn_raw_buffer_store_b128(d_, rs, off, soff, 16); asm volatile("s_nop 1" :: "v"(d_) : "memory"); } while (0)

#define ST16D(on, rs, v, off, soff) do { const u32x4 d_ = __builtin_bit_cast(u32x4, v); __builtin_amdgcn_raw_buffer_store_b128(d_, rs, off, soff, 16); \
    if (on) __builtin_amdgcn_raw_buffer_store_b128(d_, rs, off, soff, 0); asm volatile("s_nop 1" :: "v"(d_) : "memory"); } while (0)
#define FLAG(off, n) __builtin_amdgcn_raw_buffer_store_b64(__builtin_bit_cast(u32x2, tag + (n)), rs, fvo, off, 16)
#define FLAGC(c, off, n) __builtin_amdgcn_raw_buffer_store_b64(__builtin_bit_cast(u32x2, tag + (n)), rs, (c) ? fvo : NOWHERE, off, 16)
constexpr int NOWHERE = 0x7ffffff0;
constexpr u32 EMPTY = 0xffffffffu;
DI u32 filled(u32 w) { return min(w, EMPTY - 1u); }
DI u64 peek(const char* ws, u32 off, bool on, u64 dflt) {
  u64 z = 0, v = dflt;
  asm volatile("" : "+v"(z));
  if (on) v = __hip_atomic_fetch_add((GAS u64*)(ws + off), z, __ATOMIC_RELAXED, AP == 2 ? __HIP_MEMORY_SCOPE_SYSTEM : __HIP_MEMORY_SCOPE_AGENT);
  return v;
}
DI void poll_flags(const char* ws, u32 off, bool on, u64 tag) {
#pragma nounroll
  for (u32 sp = 0; sp < SPIN_CAP; ++sp) {
    u64 v = tag + 1;
    if (on || !LL) v = __hip_atomic_load((const GAS u64*)(ws + off), RLX_AGENT);
    if (__all(v == tag + 1)) break;
  }
  __builtin_amdgcn_fence(__ATOMIC_ACQUIRE, "wavefront");
}
template <int SL = 1> DI void lds_wait(LAS char* L, int sig) {
#pragma nounroll
  for (u32 sp = 0; sp < (SPIN_CAP << 4); ++sp) {
    const u32x4 v = *(volatile LAS u32x4*)(L + L_FLG + sig * 16);
    if (__builtin_amdgcn_readfirstlane(v.x & v.y & v.z & v.w)) break;
    __builtin_amdgcn_s_sleep(SL);
  }
  asm volatile("" ::: "memory");
}
DI void lds_wait2(LAS char* L, int sig, int sig2) {
#pragma nounroll
  for (u32 sp = 0; sp < (SPIN_CAP << 4); ++sp) {
    const u32x4 v = *(volatile LAS u32x4*)(L + L_FLG + sig * 16) & *(volatile LAS u32x4*)(L + L_FLG + sig2 * 16);
    if (__builtin_amdgcn_readfirstlane(v.x & v.y & v.z & v.w)) break;
    __builtin_amdgcn_s_sleep(1);
  }
  asm volatile("" ::: "memory");
}
DI void lds_wait1(LAS char* L, int sig, int w) {
#pragma nounroll
  for (u32 sp = 0; sp < (SPIN_CAP << 4); ++sp) {
    if (__builtin_amdgcn_readfirstlane(*(volatile LAS u32*)(L + L_FLG + sig * 16 + w * 4))) break;
    __builtin_amdgcn_s_sleep(1);
  }
  asm volatile("" ::: "memory");
}
DI void lds_signal(LAS char* L, int sig, int w) {
  asm volatile("" ::: "memory");
  *(volatile LAS u32*)(L + L_FLG + sig * 16 + w * 4) = 1u;
}

template <int N> struct IC { static constexpr int value = N; };
template <int I, int N, class F> DI void sfor(F&& f) { if constexpr (I < N) { f(IC<I>{}); sfor<I + 1, N>(f); } }
#define LAM __attribute__((always_inline))
#define PIN(v) asm volatile("" : "+v"(v))
constexpr float LOG2E = 1.4426950408889634f;
template <int K> DI void macc(f32x4& c, u32x4 a, u32x4 b) {
  if (K == 0) asm volatile("v_mfma_f32_16x16x32_bf16 %0, %1, %2, 0" : "=&v"(c) : "v"(a), "v"(b));
  else if (K == 1) asm volatile("v_mfma_f32_16x16x32_bf16 %0, %1, %2, %0" : "+v"(c) : "v"(a), "v"(b));
  else c = mfma16(a, b, c);
}
template <int GAP, int SIG, int NW, class E, class F> DI void project(LAS char* L, u32 w0, u32 w1, const u32x4 (&xf)[2][14], int tp, int lane, int csl, f32x4 (&t)[2][2], f32x2 (&st)[2], E&& pre, F&& mid) {
  u32 wq = (tp & 1 ? w1 : w0) + (tp >> 1) * (7 * 2048 + GAP), ca = csl + (lane >> 4) * 16, sa = L_ST1 + (tp * 32 + (lane & 15)) * 8, fa = L_FLG + SIG * 16 + (lane & (NW - 1)) * 4;
  u32 cw = ca + tp * 64, fw = L_FLG + SIG * 16 + tp * 4, one = 1;
  u32x4 ones = {0x3f803f80u, 0x3f803f80u, 0x3f803f80u, 0x3f803f80u};
  LAS char *p0 = L + w0, *p1 = L + w1, *pq = L + wq, *pca = L + ca, *psa = L + sa, *pfa = L + fa, *pcw = L + cw, *pfw = L + fw;
  asm volatile("" : "+v"(p0), "+v"(p1), "+v"(pq), "+v"(pca), "+v"(psa), "+v"(pfa), "+v"(pcw), "+v"(pfw), "+v"(ones), "+v"(one));
  pre();
  u32x4 cf[7];
  u32x2 ra[7], rb[7];
  f32x4 cs, cp[4];
  u32 sw;
#define WAT(n) (((n) & 1 ? p1 : p0) + ((n) >> 1) * 2048 + ((n) >= 14 ? GAP : 0))
#pragma unroll
  for (int i = 0; i < 7; ++i) cf[i] = trfrag(pq + i * 2048, 256);
  SCHED();
  sfor<0, 7>([&](auto IC_) LAM {
    constexpr int i = IC_.value;
    macc<i == 0 ? 0 : i == 6 ? 2 : 1>(cs, cf[i], ones);
    SCHED();
    if (i < 6) { ra[i] = trread(WAT(i)); rb[i] = trread(WAT(i) + 256); }
    SCHED();
  });
  sfor<0, 28>([&](auto NC) LAM {
    constexpr int n = NC.value, K = n < 2 ? 0 : n < 26 ? 1 : 2;
    const u32x4 f = {ra[n % 7].x, ra[n % 7].y, rb[n % 7].x, rb[n % 7].y};
    macc<K>(t[0][n & 1], f, xf[0][n >> 1]);
    SCHED();
    if constexpr (n + 6 < 28) ra[(n + 6) % 7] = trread(WAT(n + 6));
    if constexpr (n == 24) st[0] = *(LAS f32x2*)(psa);
    if constexpr (n == 22 || n == 23) cp[n - 22] = *(LAS f32x4*)(pca + (n - 22) * 64);
    if constexpr (n >= 26) { float a = cp[n - 26][0] + cp[n - 24][0], b = cp[n - 26][1] + cp[n - 24][1]; PIN(a); PIN(b); cp[n - 26][0] = a; cp[n - 26][1] = b; }
    SCHED();
    macc<K>(t[1][n & 1], f, xf[1][n >> 1]);
    SCHED();
    if constexpr (n + 6 < 28) rb[(n + 6) % 7] = trread(WAT(n + 6) + 256);
    if constexpr (n == 24) st[1] = *(LAS f32x2*)(psa + 128);
    if constexpr (n == 22 || n == 23) cp[n - 20] = *(LAS f32x4*)(pca + (n - 20) * 64);
    if constexpr (n >= 26) { float a = cp[n - 26][2] + cp[n - 24][2], b = cp[n - 26][3] + cp[n - 24][3]; PIN(a); PIN(b); cp[n - 26][2] = a; cp[n - 26][3] = b; }
    if constexpr (n == 2) { if ((lane & 15) == 0) *(LAS f32x4*)(pcw) = cs; }
    if constexpr (n == 3) { asm volatile("" ::: "memory"); *(volatile LAS u32*)(pfw) = one; }
    if constexpr (n == 16) sw = *(volatile LAS u32*)(pfa);
    if constexpr (n == 21) { if (__builtin_expect(__builtin_amdgcn_ballot_w64(sw == 0) != 0, 0)) { lds_wait(L, SIG); if (NW > 4) lds_wait(L, SIG + 1); } asm volatile("" ::: "memory"); }
    mid(n, pfw - SIG * 16, one);
    SCHED();
  });
#pragma unroll
  for (int tb = 0; tb < 2; ++tb)
#pragma unroll
    for (int k = 0; k < 2; ++k) t[tb][k] += cp[k] * st[tb].x;
}
DI f32x2 swp16(float a, float b) { auto r = __builtin_amdgcn_permlane16_swap(__builtin_bit_cast(u32, a), __builtin_bit_cast(u32, b), false, false); return f32x2{__builtin_bit_cast(float, (u32)r[0]), __builtin_bit_cast(float, (u32)r[1])}; }
DI f32x2 swp32(float a, float b) { auto r = __builtin_amdgcn_permlane32_swap(__builtin_bit_cast(u32, a), __builtin_bit_cast(u32, b), false, false); return f32x2{__builtin_bit_cast(float, (u32)r[0]), __builtin_bit_cast(float, (u32)r[1])}; }
DI float swadd32(float a, float b) { const f32x2 s = swp32(a, b); return s.x + s.y; }
DI float swadd16(float a, float b) { const f32x2 s = swp16(a, b); return s.x + s.y; }
DI float mx2(float a, float b) { float r; asm("v_max_f32 %0, %1, %2" : "=v"(r) : "v"(a), "v"(b)); return r; }
DI float mx3(float a, float b, float c) { return fmaxf(fmaxf(a, b), c); }
DI float mn3(float a, float b, float c) { return fminf(fminf(a, b), c); }
template <class F> DI void feature_map(const f32x4 (&t)[2][2], const f32x2 (&st)[2], const u32x4 (&wpf)[2], int g, u32x4 (&P)[2][2], F&& mid) {
  const float pad = g < 3 ? 0.f : -3.0e38f;
  const f32x4 z4 = {0.f, 0.f, 0.f, 0.f};
  const float sc[2] = {st[0].y * LOG2E, st[1].y * LOG2E}, scg = g < 2 ? sc[0] : sc[1];
  float p[2][8], m[4], mp[4];
#pragma unroll
  for (int tb = 0; tb < 2; ++tb) {
    const u32x4 tq = {pk(t[tb][0][0], t[tb][0][1]), pk(t[tb][0][2], t[tb][0][3]), pk(t[tb][1][0], t[tb][1][1]), pk(t[tb][1][2], t[tb][1][3])};
    const f32x4 p0 = mfma16(wpf[0], tq, z4), p1 = mfma16(wpf[1], tq, z4);
#pragma unroll
    for (int i = 0; i < 8; ++i) p[tb][i] = i < 4 ? p0[i] : p1[i - 4];
  }
  mid(0);
#pragma unroll
  for (int tb = 0; tb < 2; ++tb) {
    const float* q = p[tb];
    m[2 * tb] = mx3(mx3(q[0], q[1], q[2]), mx3(q[3], q[4], q[5]), mx3(q[5], q[6], q[7]));
    m[2 * tb + 1] = mx3(mx3(-q[0], -q[1], -q[2]), mx3(-q[3], -q[4], -q[5]), mx3(-q[5], -q[6], -q[7]));
  }
  {
    const f32x2 a = swp16(m[0], m[1]), b = swp16(m[2], m[3]);
    const f32x2 c = swp32(mx2(a.x, a.y), mx2(b.x, b.y));
    const float d = mx2(c.x, c.y) * -scg;
    const f32x2 e = swp32(d, d), f0 = swp16(e.x, e.x), f1 = swp16(e.y, e.y);
    m[0] = f0.x; m[1] = f0.y; m[2] = f1.x; m[3] = f1.y;
#pragma unroll
    for (int c2 = 0; c2 < 4; ++c2) mp[c2] = m[c2] + pad;
  }
  float ex[4][8], sm[4];
#pragma unroll
  for (int c = 0; c < 4; ++c) {
    mid(1 + c);
#pragma unroll
    for (int i = 0; i < 8; ++i) {
      ex[c][i] = fexp2(fmaf(c & 1 ? -p[c >> 1][i] : p[c >> 1][i], sc[c >> 1], i & 2 ? mp[c] : m[c]));
    }
    sm[c] = ((ex[c][0] + ex[c][1]) + (ex[c][2] + ex[c][3])) + ((ex[c][4] + ex[c][5]) + (ex[c][6] + ex[c][7]));
  }
  mid(5);
  {
    const float d = frcp(swadd32(swadd16(sm[0], sm[1]), swadd16(sm[2], sm[3])));
    const f32x2 e = swp32(d, d), f0 = swp16(e.x, e.x), f1 = swp16(e.y, e.y);
    sm[0] = f0.x; sm[1] = f0.y; sm[2] = f1.x; sm[3] = f1.y;
  }
#pragma unroll
  for (int c = 0; c < 4; ++c)
#pragma unroll
    for (int e = 0; e < 4; ++e) P[c >> 1][c & 1][e] = pk(ex[c][2 * e] * sm[c], ex[c][2 * e + 1] * sm[c]);
}
DI u32x4 piece(const u32x4 (&rp)[8], int e) {
  u32x4 f;
#pragma unroll
  for (int m = 0; m < 4; ++m) f[m] = __builtin_amdgcn_perm(rp[2 * m + 1][e >> 1], rp[2 * m][e >> 1], (e & 1) ? 0x07060302u : 0x05040100u);
  return f;
}
#define RP_OPS "+v"(rp[0]), "+v"(rp[1]), "+v"(rp[2]), "+v"(rp[3]), "+v"(rp[4]), "+v"(rp[5]), "+v"(rp[6]), "+v"(rp[7])
#define PF1 0
#define PF2 84
#define PF3 120
#define TP0 -1
#define TP1 13
#define TP2 -1
#define TOUCH 5
#define NW3 4
#define THRN0 4
#define DNPRIO 3
#define NWD 3
#define MIDCHK 0
#define HELP 0
#define NHW 7
#define W3LATE 0
#define W3WAIT 1
#define ROT 1
#define EXPT 0
#define PFM 12
#define PSLP 4
#define PLAG 0
#define WTOUCH 0
#define REPLATE 0
#define WB0 8
#define WB1 4
#define ULATE 0
#define VPRIO 3
#define VPRIO0 3
#define VPN 27
#define FPRIO 2
#define CLB 0
#define KAT 99
#define STF 0
#define QN0 0
#define QST 4
#define PB0 3
#define WOT 0
#define WTN 2
#define CLF 0
#define TQ 1
#define TU 0
#define TO 1
#define SN_ 49
#define SS_ 128
#define SD_ 0
#define RPO 0
#define REPE 0
#define UFN 14
#define SBA 0
#define TD 2
#define NR5 16
#define NR6 23
#define G4E 0
#define TS0 2
#define TS1 8
#define TWF 4
#define BP3 0
#define W3J 2
#define TQD -1
#define TOD 0
#define TDD 10
#define CPN 7
#define X0A 5
#define NE 0
#define G6E 0
#define HW 1
#define PACE 1
#define PS 28
#define PJ0 1
#define PJ1 6
#define MAP 0
#define DUP 0
#define XT 1
#define KGO -1
#define QEARLY 1
#define DLY1 4
#define W1 1
#define W2C 0
#define WOE 2
#define W3THR 1
#define HW3 1
#define HW1 1
#define SN 4
#define BATCH 1
#define B23 1
#define WD5L 0
#define A56 1
#define BGO 196
#define BALL 0
#define BSL 4
#define RSL 4
#define H5 1
#define H6 1
#define BORD 1
#define DP0 3
#define AIL 1
#define SEEN 3
#define ADLY 16
#define W6E 0
#define SPLIT 1
#define NCL 4
#define B5L 1
#define A6L 1
#define BO2 2
#define TW 0
#define CL 0
#define CE 0
#define W5D 1
#define W2F 5
#define D2A 12
#define D2B 16
#define SPEC 1
#define CLP 28
#define CLS 3
#define CE2 0
#define DLY5 60
#define CSL 1
#define CF 0
#define OPRIO 3
#define PG 1
#define ISK 1
#define T2 70
#define FXW 8
#define DLY4 16
#define PUBS 5
#define THRN 40
#define DLY2 8
#define DLY3 15
#define GUPRIO 2
enum { S_FREE01 = 0, S_REFILL, S_DONE4, S_WIN3, S_WK, S_WV, S_WQ, S_WSEEN, S_KDONE, S_V, S_PHIK, S_CSV, S_KVA, S_WO, S_CSQ, S_PHI, S_X0, S_X1, S_XA, S_CSK, S_CS, S_WOFA, S_WOFB, S_EXA, S_EXB, S_W2A, S_W2B, S_HFA, S_HFB, S_CONS, S_SPARE, S_XF0 };

__global__ void __launch_bounds__(512, 2) fused_block(const char* __restrict__ x, const char* __restrict__ wqkv, const char* __restrict__ wphi, const char* __restrict__ wo,
                                                      const char* __restrict__ wg, const char* __restrict__ wu, const char* __restrict__ wd, char* __restrict__ out, char* ws) {
  extern __shared__ __attribute__((aligned(16))) char smem[];
  LAS char* L = (LAS char*)smem;
  const int tid = threadIdx.x, lane = tid & 63, wave = __builtin_amdgcn_readfirstlane(tid >> 6), w2 = wave & 3;
  const int b = blockIdx.x, li = b >> 3, xp = (b & 7) >> 1, xh = b & 1;
  const int tile = MAP == 2 ? (b & 4) * 2 + (li >> 2) : MAP ? 2 * (b & 7) + (li >> 4) : 4 * xp + (li >> 3), r = MAP == 2 ? 4 * (b & 3) + (li & 3) : MAP ? li & 15 : 8 * xh + (li & 7);
  const int tile2 = 4 * xp + 2 * xh + (li >> 4), r2 = li & 15;
  const int tg = r2 >= 7 ? 1 : 0, cg = r2 - 7 * tg;
  const int l15 = lane & 15, g = lane >> 4, q4 = l15 >> 2, p4 = lane & 3, hh = lane >> 5;
  const int head = r;
  {
    u64 pc_; asm volatile("s_getpc_b64 %0" : "=s"(pc_));
    touch((const char*)pc_, min((b & ~7) * 256 + (tid >> 5) * 128, CODE_PF));
    *(LAS u32*)(L + L_FLG + (tid & 127) * 4) = 0u;
  }
  int tn = wave < 2 ? (XT ? 28 : 56) : 85 - 24 * wave, ti = (XT ? b & 7 : xp) * 64 + li * 2 + wave;
  asm volatile("" : "+s"(tn), "+s"(ti));
  ti = wave < 2 ? ti : b;
  u32 tvo = min(ti * tn + min(lane, tn - 1), wave == 3 ? 3135 : wave == 2 ? 9407 : 14335) * 128, xvo = (tile * 128 + w2 * 32 + (lane >> 3)) * 896 + (((lane & 7) ^ (lane >> 3)) * 16);
  asm volatile("s_waitcnt lgkmcnt(0)\n\ts_barrier" : "+v"(tvo), "+v"(xvo) :: "memory");
  asm volatile("" :: "s"(x), "s"(wqkv), "s"(wphi), "s"(wo), "s"(wg), "s"(wu), "s"(wd), "s"(out), "s"(ws));
  STAMP(0);
  u64 epoch;
  if (wave < 4) {
    const char* tb_ = wave < 2 ? x : wave == 2 ? wqkv : wo;
    asm volatile("" : "+s"(tb_));
    touch(tb_, tvo);
  }
  asm volatile("s_load_dwordx2 %0, %1, 0x0" : "=s"(epoch) : "s"(ws) : "memory");
  if (wave >= 4) {
    const __amdgpu_buffer_rsrc_t rx = __builtin_amdgcn_make_buffer_rsrc((void*)(x - 2048), 0, 2048 * 896 + 2048, 0x00020000);
    sfor<0, 7>([&](auto NC) LAM {
      dma_x<4 * NC.value>(rx, xvo, w2 * 28672);
      if (NC.value == X0A) { asm volatile("s_waitcnt vmcnt(%0)" :: "n"(4 * X0A - 10) : "memory"); lds_signal(L, S_X0, w2); }
    });
  }
  asm volatile("s_waitcnt lgkmcnt(0)" : "+s"(epoch) :: "memory");
  const __amdgpu_buffer_rsrc_t rs = __builtin_amdgcn_make_buffer_rsrc((void*)ws, 0, WS_USED, 0x00020000);
  const u32x4 ones = {0x3f803f80u, 0x3f803f80u, 0x3f803f80u, 0x3f803f80u};
  const f32x4 z4 = {0.f, 0.f, 0.f, 0.f};
  auto role = [&](auto AC) __attribute__((always_inline)) {
  constexpr bool A = AC.value;
  if (A) asm volatile("; A" ::: "memory"); else asm volatile("; B" ::: "memory");
  u32x4 rp[8];
  const int fvo = lane < 16 ? lane * 8 : NOWHERE;
  const int uidx = (tile * 10 + __builtin_popcount(~0x2A54u & ((1u << head) - 1u))) * 4 + w2, unit = uidx * 49 / 80;
  const bool has_unit = !((0x2A54 >> head) & 1) && (uidx + 1) * 49 / 80 != unit;
  const int u3 = unit >> 1, umat = unit & 1, ukq = u3 / 28, ufl = u3 - 28 * ukq;
  const char* gusrc = ws + WS_WGU + 7 * r * SLOT;
  const u32 guo = WS_WGU + 7 * r * SLOT + w2 * 7168;
#define GUBLK(j) dma7<(j) * SLOT, gu_slot(j) * SLOT>(rs, lane * 16, guo, w2 * 7168)
  const u32 wvo = (lane >> 2) * 2688 + (((lane & 3) ^ (2 * (lane >> 5))) * 16);
  const char* wbase = wqkv + head * 56 - (head & 1) * 8 + 16 * w2 * 2688;
  const u32 wr0 = (8 * g + q4) * 64 + (((p4 + (head & 1)) ^ (4 * (g & 1))) * 8), wr1 = (8 * g + q4) * 64 + ((((4 + p4 + (head & 1)) & 7) ^ (4 * (g & 1))) * 8);
  const int tb = w2; constexpr int kh = A ? 0 : 1;
  const int hho = WS_HH + (((tile * 2 + (w2 >> 1)) * 4 + (r >> 2)) * 56 + (r & 3) * 2 + (w2 & 1)) * 1024;
  const int tok = tile2 * 128 + tg * 64 + tb * 16 + l15;
  const int hvo = WS_H + ((tok >> 5) * 28 + 4 * cg + (g >> 1)) * 1024 + ((g & 1) * 32 + (tok & 31)) * 16;
  const int sto = WS_ST + (2 * cg * 2048 + tile2 * 128 + tg * 64 + tb * 16) * 8;
  const u32x4 empty4 = {EMPTY, EMPTY, EMPTY, EMPTY};
#define HH_CLEAR(i0, i1) _Pragma("unroll") for (int i = i0; i < i1; ++i) ST16(rs, empty4, lane * 16 + (i & 1) * 2048, WS_HH + (((tile2 * 2 + r2 - 14) * 4 + w2) * 56 + kh + (i >> 1) * 4) * 1024)
#define HH_FLAG() FLAG(WS_FH + ((tile2 * 4 + 2 * (r2 - 14) + kh) * 32 + 28 + w2) * 128, 1)
  u32x4 wof[8][4];
  f32x4 csp[7];
  u64 tag;
#define WOF_READ(i0, i1) do { u32 wa_ = L_WO + (kh * 224 + 4 * g + q4) * 128 + 16 * p4 + 64 * (q4 >> 1), wb_ = wa_ ^ 64, wc_ = wa_ + (g == 3 ? 0 : 2048), wd_ = wc_ ^ 64; \
    asm volatile("" : "+v"(wa_), "+v"(wb_), "+v"(wc_), "+v"(wd_)); \
    _Pragma("unroll") for (int i = i0; i < i1; ++i) _Pragma("unroll") for (int k = 0; k < 4; ++k) { \
      const u32x2 a_ = trread(L + (k >> 1 ? wb_ : wa_) + 28 * i * 128 + (k & 1) * 8), c_ = trread(L + (k >> 1 ? wd_ : wc_) + 28 * i * 128 + (k & 1) * 8); \
      wof[i][k] = u32x4{a_.x, a_.y, c_.x, c_.y}; } } while (0)
#define XF_READ(i) _Pragma("unroll") for (int s = 0; s < 14; ++s) xf[i][s] = *(LAS u32x4*)((s & 1 ? xr1 : xr0) + (i) * 14336 + (s >> 1) * 2048)
#define LN_STATS(kh) do { f32x4 g1 = z4, g2 = z4; \
    _Pragma("unroll") for (int s = 0; s < 14; ++s) { g1 = mfma16(ones, xf[kh][s], g1); g2 = mfma16(xf[kh][s], xf[kh][s], g2); } \
    const float d01 = (p4 & 1) ? g2[1] : g2[0], d23 = (p4 & 1) ? g2[3] : g2[2], s2 = (p4 & 2) ? d23 : d01; \
    const float nmu = g1[0] * (-1.f / 448.f), var = fmaxf(s2 * (1.f / 448.f) - nmu * nmu, 0.f); \
    if (g == q4) *(LAS f32x2*)(L + L_ST1 + (w2 * 32 + kh * 16 + l15) * 8) = f32x2{nmu, __builtin_amdgcn_rsqf(var + 1e-5f)}; } while (0)
#define WPF() do { u32 wp[8]; \
    _Pragma("unroll") for (int j = 0; j < 8; ++j) wp[j] = *(LAS u32*)(L + L_WPHI + 4 * g * 56 + min(l15, 13) * 4 + ((j & 3) + 16 * (j >> 2)) * 56); \
    _Pragma("unroll") for (int e = 0; e < 4; ++e) { const bool z = e >= 2 && g == 3; \
      wpf[0][e] = z ? 0u : __builtin_amdgcn_perm(wp[2 * e + 1], wp[2 * e], 0x05040100u); wpf[1][e] = z ? 0u : __builtin_amdgcn_perm(wp[2 * e + 1], wp[2 * e], 0x07060302u); } \
    asm volatile("" : "+v"(wpf[0]), "+v"(wpf[1])); } while (0)
  {
    u32x4 xf[2][14], wpf[2];
    f32x2 st[2];
    LAS char* xr0 = L + w2 * 28672 + l15 * 128 + ((g ^ (l15 & 7)) * 16);
    LAS char* xr1 = L + w2 * 28672 + l15 * 128 + (((4 + g) ^ (l15 & 7)) * 16);
    LAS char* pw = L + L_PKV + (w2 * 32 + l15) * 224 + g * 8;
    const char* wos = wo + (r2 < 14 ? cg : 0) * 128 + w2 * 56 * 896;
    const u32 wovo = (lane >> 3) * 896 + (((lane & 7) ^ ((lane >> 2) & 4)) * 16);
#define WO_HALF(hf_) _Pragma("unroll") for (int n = 0; n < 7; ++n) dma16(wos + ((hf_) * 224 + n * 8) * 896, wovo, L_WO + ((hf_) * 28 + w2 * 7 + n) * 1024)
    int clo = WS_O + ((tile * 8 + 2 * w2) * 16 + head) * 1024, clh = r2 < 14 ? hvo : NOWHERE, cls = r2 < 14 && lane < 16 ? (lane >> 3) * 16384 + (lane & 7) * 16 : NOWHERE, clt = sto;
    int kvo = WS_KVP + ((head * 16 + tile) * 4 + w2) * 1024, fko = WS_FKV + ((head * 4 + w2) * 16 + tile) * 128;
    u32x4 em4 = empty4;
    u32 wtv = min(lane, 24) * 128;
    const char* wts = wo + min(li * 98 + w2 * 25, 3136 - 25) * 128;
    auto clear = [&](int c) LAM {
      if (c == 0) ST16(rs, em4, lane * 16, clo);
      if (c == 1) ST16(rs, em4, lane * 16, clo + 16384);
      if (c == 2) ST16(rs, em4, clh, 0);
      if (c == 3) ST16(rs, em4, clh, 2048);
      if (c == 4) ST16(rs, em4, cls, clt);
    };
    if constexpr (A) {
      if (wave == 2) { dma16(wphi + head * 1568, lane * 16, L_WPHI); dma16(wphi + head * 1568, min(lane + 64, 97) * 16, L_WPHI + 1024); }
      __builtin_amdgcn_s_waitcnt(0x0F70);
      tag = epoch * 6364136223846793005ull + 1442695040888963407ull;
      STAMP(1);
#define WKLOAD() _Pragma("unroll") for (int i = 0; i < 7; ++i) dma16(wbase + 896 + 64 * i * 2688, wvo, L_WK + (4 * i + w2) * 1024)
      if (KAT == 3) { WKLOAD(); if (wave == 2) { WAITVM(7); lds_signal(L, S_PHI, 0); } }
      else if (wave == 2) { if (CLF == 9) WAITVM(5); else WAITVM(0); lds_signal(L, S_PHI, 0); }
      if (KAT > 3 && KAT < 28) { lds_wait1(L, S_SPARE, w2); WKLOAD(); }
      lds_wait1(L, S_PHI, 0);
      WPF();
#define DBITE() dma4(wd + min(b * 49 + w2 * 13, 12544 - 13) * 128, min(lane, 12) * 128, L_JUNK)
      if (TD == 9) {
        __builtin_amdgcn_s_sleep(TS0);
        if (w2 & 1) __builtin_amdgcn_s_sleep(TS1);
        if (w2 & 2) { __builtin_amdgcn_s_sleep(TS1); __builtin_amdgcn_s_sleep(TS1); }
        DBITE();
      }
      if (TD == 10 && w2 == 0) DBITE();
      lds_wait1(L, S_X0, w2);
      XF_READ(0);
      if (w2 < 2) lds_signal(L, S_XF0, w2);
      if (TD == 10 && w2 == 1) DBITE();
      if (KAT > 28) { WKLOAD(); }
      if (TD == 10 && w2 == 2) DBITE();
      if (TD == 11) DBITE();
      STAMP(27);
      LN_STATS(0);
      SCHED();
      STAMP(2);
      lds_wait1(L, S_X1, w2);
      XF_READ(1);
      if (QEARLY == 3 && w2 < 2) lds_signal(L, S_SPARE, w2);
      STAMP(3);
      if (SBA) LN_STATS(1);
      WAITVM(0);
      STAMP(4);
      lds_signal(L, S_WK, w2);
      if (TD == 10 && w2 == 3) DBITE();
      if (TD == 12) DBITE();
      f32x4 t[2][2];
      u32 pr = L_PKV + (4 * g + q4) * 224 + 8 * p4;
      asm volatile("" : "+v"(pr), "+v"(pw));
      asm volatile("" : "+s"(clo), "+v"(clh), "+v"(cls), "+s"(clt), "+s"(kvo), "+s"(fko), "+s"(tag), "+v"(em4));
      if (WOT == 5) asm volatile("" : "+v"(wtv), "+s"(wts));
      project<0, S_CSK, 8>(L, L_WK + wr0, L_WK + wr1, xf, w2, lane, L_CSP, t, st, [&]() LAM {
        lds_wait(L, S_WK);
        STAMP(5);
        __builtin_amdgcn_s_setprio(2);
      }, [&](int n, LAS char* sg, u32 one) LAM {
        if (n == KGO) *(volatile LAS u32*)(sg + S_KDONE * 16) = one;
      });
      STAMP(6);
      u32x4 P[2][2];
      __builtin_amdgcn_s_setprio(FPRIO);
      feature_map(t, st, wpf, g, P, [&](int i) LAM {
        if (WOT == 5 && i == 0) { SCHED(); dma4(wts, wtv, L_JUNK); SCHED(); }
        if (CLF == 1 && i > 0) { SCHED(); clear(i - 1); SCHED(); }
        if (CLF == 2 && i == 5) { SCHED(); clear(0); clear(1); clear(2); clear(3); clear(4); SCHED(); }
        if (CLF == 3 && i >= 3) { SCHED(); clear(2 * i - 6); clear(2 * i - 5); SCHED(); }
        if (CLF == 4 && i == 1) { SCHED(); clear(0); clear(1); clear(2); clear(3); clear(4); SCHED(); }
      });
#pragma unroll
      for (int c = 0; c < 4; ++c) {
        LAS char* p = pw + (c >> 1) * 16 * 224 + (c & 1) * 64;
        *(LAS u32x2*)(p) = u32x2{P[c >> 1][c & 1].x, P[c >> 1][c & 1].y}; *(LAS u32x2*)(p + 32) = u32x2{P[c >> 1][c & 1].z, P[c >> 1][c & 1].w};
      }
      lds_signal(L, S_PHIK, w2);
      STAMP(7);
      if (CLF == 0 || CLF == 8) { clear(0); clear(1); }
      if (CLF == 0 || CLF == 7) { clear(2); clear(3); clear(4); }
      lds_wait2(L, S_V, S_PHIK);
      u32x4 pa[4], pb[8];
#pragma unroll
      for (int m = 0; m < 4; ++m) {
        pa[m] = trfrag(L + pr + w2 * 32 + m * 32 * 224, 16 * 224);
        pb[2 * m] = trfrag(L + pr + m * 32 * 224 + 128, 16 * 224); pb[2 * m + 1] = trfrag(L + pr + m * 32 * 224 + 160, 16 * 224);
      }
      SCHED();
      f32x4 kv[2] = {};
#pragma unroll
      for (int m = 0; m < 8; ++m) kv[m & 1] = mfma16(pa[m >> 1], pb[m], kv[m & 1]);
      const u32x4 kvb = {pk(kv[0][0], kv[0][1]), pk(kv[0][2], kv[0][3]), pk(kv[1][0], kv[1][1]), pk(kv[1][2], kv[1][3])};
      ST16(rs, kvb, lane * 16, kvo);
      STAMP(8);
      DRAIN();
      FLAG(fko, 1);
      __builtin_amdgcn_s_setprio(0);
      STAMP(9);
      if (HW1) { lds_signal(L, S_XA, w2); lds_wait(L, S_XA); }
#define DTOUCH() dma4(wd + b * 49 * 128, min(lane, 48) * 128, L_JUNK)
      if (TD == 2 && wave == 0) DTOUCH();
      if (r2 < 14 || !ISK) { WO_HALF(0); WO_HALF(1); } else { HH_CLEAR(0, NE); if (NE < 8) __builtin_amdgcn_s_sleep(20); }
      poll_flags(ws, WS_FKV + ((head * 4 + w2) * 16 + l15) * 128, lane < 16, tag);
      lds_signal(L, S_WO, w2);
      STAMP(10);
      u32x4 tt[16];
#pragma unroll
      for (int i = 0; i < 16; ++i) tt[i] = __builtin_bit_cast(u32x4, __builtin_amdgcn_raw_buffer_load_b128(rs, lane * 16, WS_KVP + head * 65536 + w2 * 1024 + i * 4096, 16));
      u32 fgo;
      {
        const int pl = (tile & 1) * 16 + min(lane >> 2, 13);
        fgo = wave == 0 ? WS_FKV + ((l15 * 4 + 2 * tg + (g & 1)) * 16 + tile2) * 128
            : wave == 1 ? (MAP == 2 ? WS_FKV + (((4 * ((tile >> 1) & 3) + (pl & 3)) * 4 + p4) * 16 + (tile & 8) + (pl >> 2)) * 128 : MAP ? WS_FKV + ((min(lane >> 2, 13) * 4 + p4) * 16 + tile) * 128 : WS_FKV + (((((tile >> 1) & 1) * 8 + (pl & 7)) * 4 + p4) * 16 + (tile & 12) + (pl >> 3)) * 128)
                        : WS_FG + (min(((7 * r) >> 2) + g, (7 * r + 6) >> 2) * 16 + (l15 & 8) + min(lane & 7, 6)) * 128;
      }
      u64 fg = __hip_atomic_load((const GAS u64*)(ws + fgo), RLX_AGENT);
      SCHED();
      const u32 one = g == q4 ? ((p4 & 1) ? 0x3f800000u : 0x3f80u) : 0u, s01 = p4 < 2 ? one : 0u, s23 = p4 < 2 ? 0u : one;
      const u32x4 sel0 = {s01, s23, 0u, 0u}, sel1 = {0u, 0u, s01, s23};
      f32x4 s0 = {}, s1 = {};
#pragma unroll
      for (int i = 0; i < 16; ++i) { s0 = mfma16(sel0, tt[i], s0); s1 = mfma16(sel1, tt[i], s1); }
      LAS char* kw = L + L_KVA + (w2 >> 1) * 1024 + lane * 16 + (w2 & 1) * 8;
      *(LAS u32x2*)(kw) = u32x2{pk(s0[0], s0[1]), pk(s0[2], s0[3])};
      *(LAS u32x2*)(kw + 2048) = u32x2{pk(s1[0], s1[1]), pk(s1[2], s1[3])};
      lds_signal(L, S_KVA, w2);
      if (TD == 4 && wave == 2) DTOUCH();
      STAMP(11);
      if (WOE == 3) { WO_HALF(1); WAITVM(0); lds_signal(L, S_WO, w2); }
      asm volatile("" : "+v"(fg) :: "memory");
      if (!__all(fg == tag + 1)) poll_flags(ws, fgo, true, tag);
      __builtin_amdgcn_fence(__ATOMIC_ACQUIRE, "wavefront");
      lds_signal(L, S_WOFA, w2);
      lds_wait1(L, S_WOFA, 3);
      const int fl = min(((7 * r) >> 2) + w2, (7 * r + 6) >> 2);
#pragma unroll
      for (int i = 0; i < 7; ++i) asm volatile("global_load_dwordx4 %0, %1, %2 offset:%3 sc1" : "=v"(csp[i]) : "v"(((7 * r) >> 2) + w2 == fl ? lane * 16 : 0), "s"(ws + WS_CS + fl * 7168 + 4096), "n"(i * 1024 - 4096) : "memory");
      GUBLK(2);
      if (r2 < 14 || !ISK) { lds_wait(L, S_WO); WOF_READ(0, 8); }
      SCHED();
#define CS_OUT(k_) do { asm volatile("s_waitcnt vmcnt(%7)" : "+v"(csp[0]), "+v"(csp[1]), "+v"(csp[2]), "+v"(csp[3]), "+v"(csp[4]), "+v"(csp[5]), "+v"(csp[6]) : "n"(k_) : "memory"); \
        const f32x4 sm = ((csp[0] + csp[1]) + (csp[2] + csp[3])) + ((csp[4] + csp[5]) + csp[6]); \
        const float e0 = swadd32(sm[0], sm[1]), e1 = swadd32(sm[2], sm[3]);     \
        const int j0 = 4 * (((7 * r) >> 2) + w2) + 2 * (lane & 1) - 7 * r, cr = ((g & 1) * 16 + hh * 8 + (l15 >> 1)) * 4; \
        if (j0 >= 0 && j0 < 7) *(LAS float*)(L + L_CS + j0 * 128 + cr) = e0; \
        if (j0 + 1 >= 0 && j0 + 1 < 7) *(LAS float*)(L + L_CS + (j0 + 1) * 128 + cr) = e1; } while (0)
      lds_signal(L, S_WOFB, w2);
      if (!CSL) CS_OUT(7);
      STAMP(24);
    } else {
      const int rg = lane >> 3;
      const u32 lvo = rg * 8 * 3584 + (lane & 7) * 16;
      const char* us = (umat ? wu : wg) + ukq * 64 * 3584 + ufl * 128;
      asm volatile("" : "+s"(us));
      auto repack = [&]() LAM {
        if (has_unit) {
          const int so = rg * 512 + (lane & 7) * 16;
          const int sb = WS_WGU + 4 * ufl * SLOT + ukq * 4096 + umat * 256;
          const int s1 = SLOT, s2 = 2 * SLOT;
          float c[8];
#pragma unroll
          for (int e = 0; e < 8; ++e) {
            const u32x4 f = piece(rp, e);
            ST16(rs, f, so + (e & 1) * 128, sb + ((e >> 1) & 1) * s1 + (e >> 2) * s2);
            c[e] = mfma16(ones, f, z4)[0];
          }
          const f32x4 cv = g & 1 ? f32x4{c[4], c[5], c[6], c[7]} : f32x4{c[0], c[1], c[2], c[3]};
          ST16(rs, cv, g < 2 ? (l15 >> 3) * 512 + (lane & 7) * 32 + g * 16 : NOWHERE, WS_CS + ufl * 7168 + ukq * 1024 + (umat & 1) * 256);
        }
      };
#define RPLOAD() _Pragma("unroll") for (int j = 0; j < 8; ++j) asm volatile("global_load_dwordx4 %0, %1, %2" : "=v"(rp[j]) : "v"(lvo), "s"(us + j * 3584) : "memory")
      if (RPO == 2 && has_unit) { RPLOAD(); }
      tag = epoch * 6364136223846793005ull + 1442695040888963407ull;
      STAMP(1);
      XF_READ(0);
      if (w2 < 2) lds_signal(L, S_XF0, 2 + w2);
      if (RPO == 2 && has_unit) WAITVM(8); else WAITVM(0);
      lds_signal(L, S_X1, w2);
      if (RPO == 1 && has_unit) { RPLOAD(); }
      STAMP(2);
      if (STF < 7) lds_wait(L, S_XF0);
      XF_READ(1);
      if (QEARLY == 3 && w2 < 2) lds_signal(L, S_SPARE, 2 + w2);
      SCHED();
#define WDST(c) ((c) * 1024 + ((c) >= 14 ? SLOT - 14336 : 0))
#pragma unroll
      for (int i = 0; i < 7 - STF; ++i) dma16(wbase + 1792 + 64 * i * 2688, wvo, L_WV + WDST(4 * i + w2));
      STAMP(3);
      __builtin_amdgcn_s_setprio(PB0);
      if (!SBA) LN_STATS(1);
      lds_signal(L, S_CS, w2);
      if (STF == 7) lds_wait(L, S_XF0);
#pragma unroll
      for (int i = 7 - STF; i < 7; ++i) dma16(wbase + 1792 + 64 * i * 2688, wvo, L_WV + WDST(4 * i + w2));
      STAMP(4);
      f32x4 t[2][2];
      asm volatile("" : "+v"(pw));
      if (WOT == 4) asm volatile("" : "+v"(wtv), "+s"(wts));
      project<SLOT - 14336, S_CSV, 4>(L, L_WV + wr0, L_WV + wr1, xf, w2, lane, L_CSP + 256, t, st, [&]() LAM {
      if (QEARLY == 3) {
        lds_wait(L, S_SPARE);
#pragma unroll
        for (int i = 0; i < 7; ++i) dma16(wbase + 64 * i * 2688, wvo, L_WQ + WDST(4 * i + w2));
      }
      if (REPE == 3) { asm volatile("s_waitcnt vmcnt(%8)" : RP_OPS : "n"(QEARLY == 3 ? 14 : 7) : "memory"); repack(); if (has_unit) { if (QEARLY == 3) WAITVM(16); else WAITVM(9); } }
      if (RPO == 0 && has_unit) { RPLOAD(); } else if (REPE == 3 && has_unit) {} else if (QEARLY == 3) WAITVM(7); else WAITVM(0);
      STAMP(28);
      if (RPO) {} else if (QEARLY == 3) WAITVM(15); else WAITVM(8);
      lds_signal(L, S_WV, w2);
      STAMP(29);
      lds_wait2(L, S_WV, S_WK);
      STAMP(30);
      if (QEARLY == 1) {
#pragma unroll
        for (int i = 0; i < 7; ++i) dma16(wbase + 64 * i * 2688, wvo, L_WQ + WDST(4 * i + w2));
      }
        if (REPE == 2) { asm volatile("s_waitcnt vmcnt(7)" : RP_OPS :: "memory"); repack(); }
        if (TD == 8 && !has_unit && !((0x2A54 >> head) & 1)) dma4(wd + min((uidx - unit) * 51, 12544 - 51) * 128, min(lane, 50) * 128, L_JUNK);
        if (KGO >= 0) lds_wait1(L, S_KDONE, w2);
        STAMP(5);
        __builtin_amdgcn_s_setprio(VPRIO0);
      }, [&](int n, LAS char*, u32) LAM {
        if (n == VPN) __builtin_amdgcn_s_setprio(VPRIO);
        if (WOT == 4 && n == WTN) dma4(wts, wtv, L_JUNK);
        if (QEARLY == 2 && n >= QN0 && (n - QN0) % QST == 0 && (n - QN0) / QST < 7) { const int i = (n - QN0) / QST; dma16(wbase + 64 * i * 2688, wvo, L_WQ + WDST(4 * i + w2)); }
      });
#pragma unroll
      for (int c = 0; c < 4; ++c) t[c >> 1][c & 1] *= st[c >> 1].y;
      STAMP(6);
#pragma unroll
      for (int c = 0; c < 2; ++c) {
        LAS char* p = pw + c * 16 * 224 + 128;
        *(LAS u32x2*)(p) = u32x2{pk(t[c][0][0], t[c][0][1]), pk(t[c][0][2], t[c][0][3])};
        *(LAS u32x2*)(p + 32) = g == 3 ? u32x2{0x3f80u, 0u} : u32x2{pk(t[c][1][0], t[c][1][1]), pk(t[c][1][2], t[c][1][3])};
      }
      lds_signal(L, S_V, w2);
      __builtin_amdgcn_s_setprio(0);
      STAMP(7);
      if (!QEARLY) {
#pragma unroll
        for (int i = 0; i < 7; ++i) dma16(wbase + 64 * i * 2688, wvo, L_WQ + WDST(4 * i + w2));
      }
      if (WOE == 0) WO_HALF(0);
      if (WOE < 2) { lds_wait(L, S_KDONE); WO_HALF(1); }
      if (REPE == 0) asm volatile("s_waitcnt vmcnt(%8)" : RP_OPS : "n"(QEARLY == 3 && !RPO ? 0 : 7) : "memory");
#define UFLAG() FLAGC(has_unit, WS_FG + ((ufl * 2 + umat) * 8 + ukq) * 128, 1)
      if (REPE == 0) repack();
      STAMP(8);
      WPF();
      if (!has_unit || (REPE == 3 && QEARLY != 3)) WAITVM(0); else WAITVM(9);
      if (UFN < 0) { WAITVM(0); UFLAG(); }
      lds_signal(L, S_WQ, w2);
      project<SLOT - 14336, S_CSQ, 4>(L, L_WQ + wr0, L_WQ + wr1, xf, w2, lane, L_CSP + 512, t, st, [&]() LAM {
        lds_wait(L, S_WQ);
        STAMP(9);
      }, [&](int n, LAS char*, u32) LAM {
        if (n == UFN) { WAITVM(0); UFLAG(); }
      });
      STAMP(10);
      if (REPLATE == 2) repack();
      u32x4 pq[2][2];
      feature_map(t, st, wpf, g, pq, [](int) {});
      asm volatile("" : "+v"(pq[0][0]), "+v"(pq[0][1]), "+v"(pq[1][0]), "+v"(pq[1][1]));
      STAMP(11);
      if (REPLATE == 1) repack();
      if (TD == 13 || TD == 14) {
        lds_wait(L, S_WO);
        if (r2 < 14) WOF_READ(0, TWF); else __builtin_amdgcn_s_sleep(2 * TWF);
        SCHED();
        if (TD == 14) DBITE(); else if (w2 == 0) DTOUCH();
        SCHED();
        if (r2 < 14) WOF_READ(TWF, 8);
      } else if (r2 < 14 || !ISK) { lds_wait(L, S_WO); WOF_READ(0, 8); }
      STAMP(24);
      if (REPLATE == 2) { WAITVM(0); UFLAG(); }
      __builtin_amdgcn_s_setprio(OPRIO);
      lds_wait(L, S_KVA);
      if (REPLATE == 1) { WAITVM(0); UFLAG(); }
      STAMP(27);
      u32x4 ka[4];
#pragma unroll
      for (int i = 0; i < 4; ++i) ka[i] = *(LAS u32x4*)(L + L_KVA + i * 1024 + lane * 16);
      f32x4 o[2][2];
#pragma unroll
      for (int c = 0; c < 4; ++c) o[c >> 1][c & 1] = mfma16(ka[2 * (c & 1) + 1], pq[c >> 1][1], mfma16(ka[2 * (c & 1)], pq[c >> 1][0], z4));
      const f32x2 da = swp16(o[0][1][0], o[0][1][0]), db = swp16(o[1][1][0], o[1][1][0]);
      const float den[2] = {swp32(da.y, da.y).y, swp32(db.y, db.y).y};
#pragma unroll
      for (int c = 0; c < 2; ++c) {
        const float rd = frcp(den[c] + 1e-6f), r3 = g == 3 ? 0.f : rd;
        const u32x4 ob = {pk(o[c][0][0] * rd, o[c][0][1] * rd), pk(o[c][0][2] * rd, o[c][0][3] * rd), pk(o[c][1][0] * r3, o[c][1][1] * r3), filled(pk(o[c][1][2] * r3, o[c][1][3] * r3))};
        ST16D(DUP & 1, rs, ob, lane * 16, WS_O + ((tile * 8 + 2 * w2 + c) * 16 + head) * 1024);
      }
      STAMP(12);
      if (WOE == 3) lds_wait(L, S_WO);
      *(volatile LAS u32*)(L + L_FLG + S_W2A * 16 + w2 * 4) = (u32)__builtin_amdgcn_s_memrealtime();
      lds_signal(L, S_W2B, w2);
      __builtin_amdgcn_s_setprio(0);
      if (TD == 3 && w2 == 0) DTOUCH();
    }
  }
  STAMP(13);
  u32x4 hres = {0u, 0u, 0u, 0u};
  const int xo = tok * 896 + cg * 128 + kh * 64 + g * 16;
  STAMP(23);
  if (r2 < 14) {
    u32x4 xr, of[8];
    asm volatile("global_load_dwordx4 %0, %1, %2" : "=v"(xr) : "v"(xo), "s"(x) : "memory");
    lds_wait2(L, S_W2B, S_WOFA);
#define WIN2() do { if (A ? W1 < 2 : W1 < 3) GUBLK(A ? 0 : 1); } while (0)
    if (W2F == 1 || W2F == 3) WIN2();
    if (W2F == 4 && !A) { GUBLK(0); GUBLK(1); }
    if (W2F == 5 && !A) GUBLK(1);
    if (T2) {
      const u32x4 ts = *(volatile LAS u32x4*)(L + L_FLG + S_W2A * 16);
      const u32 t0 = __builtin_amdgcn_readfirstlane(ts.x), due = t0 + T2 + __builtin_amdgcn_readfirstlane(max(max(0, (int)(ts.y - ts.x)), max((int)(ts.z - ts.x), (int)(ts.w - ts.x))));
#pragma nounroll
      for (u32 sp = 0; sp < 64 && (int)((u32)__builtin_amdgcn_s_memrealtime() - due) < 0; ++sp) __builtin_amdgcn_s_sleep(1);
    } else __builtin_amdgcn_s_sleep(A ? D2A : D2B);
    STAMP(14);
#define OFA(c_) _Pragma("unroll") for (int i = 0; i < 8; ++i) \
      asm volatile("global_load_dwordx4 %0, %1, %2 offset:%3 sc1" : c_(of[i]) : "v"(lane * 16), "s"(ws + WS_O + ((tile2 * 8 + tg * 4 + tb) * 16 + kh * 8 + 4) * 1024), "n"(i * 1024 - 4096) : "memory")
#define OFW(k_) asm volatile("s_waitcnt vmcnt(%9)" : "+v"(of[0]), "+v"(of[1]), "+v"(of[2]), "+v"(of[3]), "+v"(of[4]), "+v"(of[5]), "+v"(of[6]), "+v"(of[7]), "+v"(xr) : "n"(k_) : "memory")
#define OFBAD() (!__all(max(max(max(of[0].w, of[1].w), max(of[2].w, of[3].w)), max(max(of[4].w, of[5].w), max(of[6].w, of[7].w))) != EMPTY))
    OFA("=v");
    f32x4 a[4] = {};
    u32 mx = 0;
#pragma unroll
    for (int i = 0; i < 8; ++i) {
      asm volatile("s_waitcnt vmcnt(%1)" : "+v"(of[i]) : "n"(7 - i) : "memory");
      if (i == PG - 1) { lds_wait(L, S_WOFB); }
      if (i == 7) GUBLK(A ? 0 : 3);
#pragma unroll
      for (int k = 0; k < 4; ++k) a[k] = mfma16(wof[i][k], of[i], a[k]);
      mx = max(mx, of[i].w);
      SCHED();
    }
    asm volatile("" : "+v"(xr));
    if (__builtin_expect(!__all(mx != EMPTY), 0)) {
#pragma nounroll
      for (u32 sp = 0; sp < SPIN_CAP; ++sp) { __builtin_amdgcn_s_sleep(RSL); OFA("+v"); OFW(0); if (!OFBAD()) break; }
#pragma unroll
      for (int i = 0; i < 8; ++i)
#pragma unroll
        for (int k = 0; k < 4; ++k) a[k] = mfma16(wof[i][k], of[i], i ? a[k] : z4);
    }
    STAMP(25);
    *(LAS f32x4*)(L + L_EX + wave * 2048 + lane * 16) = kh ? a[0] : a[2];
    *(LAS f32x4*)(L + L_EX + wave * 2048 + 1024 + lane * 16) = kh ? a[1] : a[3];
    lds_signal(L, A ? S_EXA : S_EXB, w2);
    lds_wait1(L, A ? S_EXB : S_EXA, w2);
    STAMP(15);
    const f32x4 c0 = (kh ? a[2] : a[0]) + *(LAS f32x4*)(L + L_EX + (wave ^ 4) * 2048 + lane * 16);
    const f32x4 c1 = (kh ? a[3] : a[1]) + *(LAS f32x4*)(L + L_EX + (wave ^ 4) * 2048 + 1024 + lane * 16);
    const f32x4 h0 = c0 + f32x4{blo(xr.x), bhi(xr.x), blo(xr.y), bhi(xr.y)}, h1 = c1 + f32x4{blo(xr.z), bhi(xr.z), blo(xr.w), bhi(xr.w)};
    hres = u32x4{pk(h0[0], h0[1]), pk(h0[2], h0[3]), pk(h1[0], h1[1]), filled(pk(h1[2], h1[3]))};
    ST16D(DUP & 2, rs, hres, hvo, kh * 2048);
    const f32x4 r0 = {blo(hres.x), bhi(hres.x), blo(hres.y), bhi(hres.y)}, r1 = {blo(hres.z), bhi(hres.z), blo(hres.w), bhi(hres.w)};
    const f32x4 a1 = r0 + r1, a2q = r0 * r0 + r1 * r1;
    float s1 = (a1[0] + a1[1]) + (a1[2] + a1[3]), s2 = (a2q[0] + a2q[1]) + (a2q[2] + a2q[3]);
    { const float t = swadd16(s1, s2); const f32x2 a2 = sw32(t); s1 = a2.x + a2.y; }
    { const u32x2 sv = __builtin_bit_cast(u32x2, sw16(s1)); __builtin_amdgcn_raw_buffer_store_b64(u32x2{sv.x, filled(sv.y)}, rs, fvo, sto + kh * 16384, 16);
      if (DUP & 2) __builtin_amdgcn_raw_buffer_store_b64(u32x2{sv.x, filled(sv.y)}, rs, fvo, sto + kh * 16384, 0); }
    if (DUP & 2) WAITVM(4); else WAITVM(2);
  } else {
    if (TD == 1) dma4(wd + ((tile2 * 2 + r2 - 14) * 8 + wave) * 49 * 128, min(lane, 48) * 128, L_JUNK);
    lds_wait2(L, S_W2B, S_WOFA);
    if (CF) { HH_CLEAR(A ? NE : 0, 4 * NCL); }
    WIN2();
    if (W2F >= 3) { lds_wait(L, S_WOFB); if (W2F < 5 || A) GUBLK(A ? 3 : 4); }
    if (!CF) { HH_CLEAR(A ? NE : 0, 4 * NCL); }
    DRAIN();
    if (!CE2) HH_FLAG();
    if (CE2 && A) __builtin_amdgcn_s_sleep(DLY5);
  }
  if constexpr (A && CSL) CS_OUT(DUP & 2 ? 4 : 2);
  lds_signal(L, A ? S_HFA : S_HFB, w2);
  STAMP(16);
  u32x4 wdf[NWD][8];
  const __amdgpu_buffer_rsrc_t rd = __builtin_amdgcn_make_buffer_rsrc((void*)(wd + w2 * 448 * 896 + min(cg, 6) * 128 - 1024), 0, 448 * 896 + 1024, 0x00020000);
  const u32 dvo = (lane >> 3) * 8 * 896 + (((lane & 7) ^ ((lane >> 2) & 4)) * 16);
  u32 da_ = 2 * SLOT + w2 * 7168 + hh * 512 + q4 * 128 + (g & 1) * 32 + p4 * 8 + 64 * (q4 >> 1), db_ = da_ ^ 64, dc_ = da_ + (L_EX - 2 * SLOT - w2 * 6144), dd_ = dc_ ^ 64;
  const u32 po = WS_FHH + ((tile2 * 16 + 4 * w2 + ((lane >> 1) & 3)) * 4 + 2 * tg + (lane & 1)) * 128;
  const u32 ho = WS_HH + ((tile2 * 2 + tg) * 4 + w2) * 57344;
#define WDLOAD(k, bf) dma_wd<k>(rd, dvo, ((bf) + 2) * SLOT + w2 * 7168, L_EX + (bf) * 4096 + w2 * 1024)
#define WDREAD(dst, bf) do { asm volatile("" : "+v"(da_), "+v"(db_), "+v"(dc_), "+v"(dd_)); \
    _Pragma("unroll") for (int i = 0; i < 8; ++i) { LAS char* p_ = L + (i & 1 ? db_ : da_) + (bf) * SLOT + (i >> 1) * 2048; \
      const u32x2 x_ = trread(p_), y_ = trread(i < 6 ? p_ + 1024 : L + (i & 1 ? dd_ : dc_) + (bf) * 4096); dst[i] = u32x4{x_.x, x_.y, y_.x, y_.y}; } } while (0)
#define THR() asm volatile("s_waitcnt vmcnt(%0)" :: "n"(THRN) : "memory")
#define THR0() asm volatile("s_waitcnt vmcnt(%0)" :: "n"(THRN0) : "memory")
  u32 have = 0;
#define PLOAD() peek(ws, po, lane < 8, tag + 7)
#define PMIN(pv) do { const u64 d = (pv) - tag; have = max(have, min8(d <= 8 ? (u32)d : 0u)); } while (0)
#define NEEDP(n) do { _Pragma("nounroll") for (u32 sp = 0; have < (n) && sp < SPIN_CAP; ++sp) { PMIN(PLOAD()); if (PSLP && have < (n)) __builtin_amdgcn_s_sleep(PSLP); } \
    __builtin_amdgcn_fence(__ATOMIC_ACQUIRE, "wavefront"); } while (0)
#define HBASE() u32 hb3 = w2 * 7168 + lane * 16, hb0 = hb3 + 2 * SLOT, hb2 = hb0 + 2 * SLOT, hb7 = L_EX + w2 * 1024 + lane * 16; asm volatile("" : "+v"(hb0), "+v"(hb2), "+v"(hb7))
#define HFRAG(bf, i) (*(LAS u32x4*)(L + ((i) < 7 ? ((bf) == 3 ? hb3 : (bf) == 2 ? hb2 : hb0 + (bf) * SLOT) + (i) * 1024 : hb7 + (bf) * 4096)))
#define HQLOAD(q, j) _Pragma("unroll") for (int i = 0; i < 8; ++i) hq[q][i] = __builtin_bit_cast(u32x4, __builtin_amdgcn_raw_buffer_load_b128(rs, lane * 16 + (i & 3) * 1024, ho + (j) * 8192 + (i & 4) * 1024, 16))
#define HQLOADT(q, j) _Pragma("unroll") for (int i = 0; i < 8; ++i) { THR(); hq[q][i] = __builtin_bit_cast(u32x4, __builtin_amdgcn_raw_buffer_load_b128(rs, lane * 16 + (i & 3) * 1024, ho + (j) * 8192 + (i & 4) * 1024, 16)); }
#define DOWN16(ac, wf, hf_) _Pragma("unroll") for (int m = 0; m < 16; ++m) ac[(m >> 1) & 1][m & 1] = mfma32(wf[2 * (m >> 2) + (m & 1)], hf_[2 * (m >> 2) + ((m >> 1) & 1)], ac[(m >> 1) & 1][m & 1])
#define PARTIAL_OUT(ac, b0, b1) _Pragma("unroll") for (int n = 0; n < 16; ++n) { const int T = n >> 3, k = (n >> 2) & 1, q = n & 3; \
    *(LAS f32x4*)&HFRAG(T ? b1 : b0, n & 7) = f32x4{ac[T][k][4 * q], ac[T][k][4 * q + 1], ac[T][k][4 * q + 2], ac[T][k][4 * q + 3]}; }
#define FREE(f) do { _Pragma("nounroll") for (u32 sp = 0; sp < (SPIN_CAP << 4); ++sp) { const u32x4 fr = *(volatile LAS u32x4*)(L + L_FLG + S_DONE4 * 16); \
          if (__builtin_amdgcn_readfirstlane(min(min(fr.x, fr.y), min(fr.z, fr.w))) >= (f)) break; __builtin_amdgcn_s_sleep(BSL); } asm volatile("" ::: "memory"); } while (0)
#define HQBAD(q) (!__all(max(max(max(hq[q][0].w, hq[q][1].w), max(hq[q][2].w, hq[q][3].w)), max(max(hq[q][4].w, hq[q][5].w), max(hq[q][6].w, hq[q][7].w))) != EMPTY))
#define HQCHECK(q, j) do { if (__builtin_expect(HQBAD(q), 0)) { _Pragma("nounroll") for (u32 sp = 0; sp < SPIN_CAP; ++sp) { \
      __builtin_amdgcn_s_sleep(RSL); asm volatile("" ::: "memory"); HQLOAD(q, j); if (!HQBAD(q)) break; } } SCHED(); } while (0)
  if constexpr (A) {
    const int T32 = tile * 4 + wave;
    u32x2 st[7];
    u32x4 hb[28];
    u64 fcl;
    const u32 stv = (7 * hh * 2048 + (lane & 31)) * 8;
#define STA(c_) _Pragma("unroll") for (int i = 0; i < 7; ++i) asm volatile("global_load_dwordx2 %0, %1, %2 sc1" : c_(st[i]) : "v"(stv), "s"(ws + WS_ST + (i * 2048 + T32 * 32) * 8) : "memory")
#define STW(k_) asm volatile("s_waitcnt vmcnt(%7)" : "+v"(st[0]), "+v"(st[1]), "+v"(st[2]), "+v"(st[3]), "+v"(st[4]), "+v"(st[5]), "+v"(st[6]) : "n"(k_) : "memory")
#define STBAD() (!__all(max(max(max(st[0].y, st[1].y), max(st[2].y, st[3].y)), max(max(st[4].y, st[5].y), st[6].y)) != EMPTY))
#define HBA(c_, q) _Pragma("unroll") for (int i = 0; i < 4; ++i) asm volatile("global_load_dwordx4 %0, %1, %2 offset:%3 sc1" : c_(hb[4 * (q) + i]) : "v"(lane * 16), "s"(ws + WS_H + (T32 * 28 + 4 * (q)) * 1024), "n"(i * 1024) : "memory")
    lds_wait2(L, S_HFA, S_HFB);
    __builtin_amdgcn_s_sleep(DLY3);
    if (!SPEC || r2 >= 14) {
#pragma nounroll
      for (u32 sp = 0; sp < SPIN_CAP; ++sp) { STA("=v"); STW(0); if (!STBAD()) break; __builtin_amdgcn_s_sleep(RSL); }
    }
    STA("=v");
    STAMP(17);
#pragma unroll
    for (int q = 0; q < 7; ++q) { HBA("=v", q); }
    asm volatile("global_load_dwordx2 %0, %1, %2 sc1" : "=v"(fcl) : "v"(p4 * 128), "s"(ws + WS_FH + (T32 * 32 + 28) * 128) : "memory");
    u32 wb_[3] = {(u32)lane * 16, (u32)lane * 16 + 2 * SLOT, (u32)lane * 16 + 4 * SLOT};
    asm volatile("" : "+v"(wb_[1]), "+v"(wb_[2]));
#define WFRAG(sl, s) (*(LAS u32x4*)(L + wb_[(sl) >> 1] + ((sl) & 1) * SLOT + (s) * 1024))
    LAS char* csl = L + L_CS + hh * 16;
    u32x4 ring[7];
    f32x4 cq[4];
    float nmu, ve, k0, k1;
    u32 mx;
    const int pvo = WS_FHH + ((tile * 16 + r) * 4 + wave) * 128;
#define PROG(n) FLAG(pvo + 131072, n)
    f32x16 acc[2];
    float y[8], gu[8];
    u32x4 yb;
    u32x4 cw;
#define LT(i)
    auto gu_block = [&](auto JC) __attribute__((always_inline)) {
      constexpr int j = JC.value;
      sfor<0, 28>([&](auto SC) LAM {
        constexpr int s = SC.value;
        if constexpr (j == 0) { u32x4 t_ = hb[s]; asm volatile("s_waitcnt vmcnt(%1)" : "+v"(t_) : "n"(28 - s) : "memory"); hb[s] = t_; }
        if constexpr (PACE > 0 && j >= PJ0 && j <= PJ1 && s % PS == 0) __builtin_amdgcn_s_sleep(PACE);
        if constexpr (HW && j > 0 && s % 2 == HW - 1 && (s < 27 || j < 6)) asm volatile("" :: "v"(ring[(s + 1) % 7]));
        acc[j & 1] = mfma32(ring[s % 7], hb[s], acc[j & 1]);
        if constexpr (j == 0) mx = max(mx, hb[s].w);
        if constexpr ((j == W3J || j == 4) && s == 12) cw = *(volatile LAS u32x4*)(L + L_FLG + (j == W3J ? S_WIN3 : S_REFILL) * 16);
        if constexpr ((j == W3J || j == 4) && s == 20) { if (__builtin_expect(!__builtin_amdgcn_readfirstlane(cw.x & cw.y & cw.z & cw.w), 0)) lds_wait(L, j == W3J ? S_WIN3 : S_REFILL); asm volatile("" ::: "memory"); }
        if constexpr (j == 1 && s < 7 - NCL) ST16(rs, (u32x4{EMPTY, EMPTY, EMPTY, EMPTY}), lane * 16, hho + (NCL + s) * 8192);
        if constexpr (s + 7 < 28) ring[s % 7] = WFRAG(gu_slot(j), s + 7);
        else if constexpr (j < 6) ring[s % 7] = WFRAG(gu_slot(j + 1), s - 21);
        if constexpr (s == 0 && (j == 1 || j == 2)) lds_signal(L, j == 1 ? S_WSEEN : S_FREE01, wave);
        if constexpr (s == 0 && j >= 3) *(volatile LAS u32*)(L + L_FLG + S_DONE4 * 16 + wave * 4) = j - 2u;
        if constexpr (j > 0) {
          sfor<0, 8>([&](auto EC) LAM {
            constexpr int i = EC.value, k = s - 1 - 2 * i;
            const float ga = acc[(j - 1) & 1][i];
            if constexpr (k == 0) y[i] = ga * ga;
            if constexpr (k == 1) y[i] = fmaf(y[i], k1, k0);
            if constexpr (k == 2) y[i] = ga * y[i];
            if constexpr (k == 3) y[i] = fexp2(y[i]);
            if constexpr (k == 4) y[i] = fmaf(y[i], ve, ve);
            if constexpr (k == 5) { gu[i] = ga * acc[(j - 1) & 1][i + 8]; PIN(gu[i]); }
            if constexpr (k == 6) y[i] = frcp(y[i]);
            if constexpr (k == 7) y[i] = y[i] * gu[i];
            if constexpr (k >= 0 && k < 8 && k != 5) PIN(y[i]);
          });
          if constexpr (s == 21) { yb[0] = pk(y[0], y[1]); yb[1] = pk(y[2], y[3]); }
          if constexpr (s == 22) yb[2] = pk(y[4], y[5]);
          if constexpr (NCL == 1 && j == 2 && s == 22) WAITVM(2);
          if constexpr (j == 3 && s == 22 && NCL < 7 && NCL > 1) { if (DUP & 4) WAITVM(6); else WAITVM(4); }
          if constexpr (s == 23) { yb[3] = filled(pk(y[6], y[7])); ST16D(DUP & 4, rs, yb, lane * 16, hho + (j - 1) * 8192); }
          if constexpr (s == 24 && j <= (HELP > 4 ? HELP : 4)) FLAG(pvo, j);
        }
        if constexpr (j < 6 && s >= 17 && s < 21) cq[s - 17] = *(LAS f32x4*)(csl + (j + 1) * 128 + (s - 17) * 32);
        if constexpr (j < 6 && s >= 22 && s < 27) {
          sfor<0, 16>([&](auto EC) LAM {
            constexpr int e = EC.value;
            if constexpr ((e < 2 ? 22 : e < 4 ? 23 : 23 + (e >> 2)) == s) { float v = nmu * cq[e >> 2][e & 3]; PIN(v); acc[(j + 1) & 1][e] = v; }
          });
        }
        SCHED();
      });
      LT(j);
      if (j == 0) STAMP(18);
      if (j == 3) STAMP(26);
      if (j == 1) STAMP(28);
      if (j == 2) STAMP(29);
      if (j == 4) STAMP(30);
      if (j == 5) STAMP(31);
    };
#define HBW(q) asm volatile("s_waitcnt vmcnt(0)" : "+v"(hb[4 * (q)]), "+v"(hb[4 * (q) + 1]), "+v"(hb[4 * (q) + 2]), "+v"(hb[4 * (q) + 3]) :: "memory")
#define HBBAD(q) (!__all(max(max(hb[4 * (q)].w, hb[4 * (q) + 1].w), max(hb[4 * (q) + 2].w, hb[4 * (q) + 3].w)) != EMPTY))
#define HBC(q, c_) asm volatile("s_bitcmp0_b32 %6, %7\n\ts_cbranch_scc1 1f\n\tglobal_load_dwordx4 %0, %4, %5 sc1\n\tglobal_load_dwordx4 %1, %4, %5 offset:1024 sc1\n\t" \
      "global_load_dwordx4 %2, %4, %5 offset:2048 sc1\n\tglobal_load_dwordx4 %3, %4, %5 offset:3072 sc1\n1:" : "+v"(hb[4 * (q)]), "+v"(hb[4 * (q) + 1]), "+v"(hb[4 * (q) + 2]), "+v"(hb[4 * (q) + 3]) \
      : "v"(lane * 16), "s"(ws + WS_H + (T32 * 28 + 4 * (q)) * 1024), "s"(c_), "n"(q) : "memory", "scc")
#define REDO(m_) do { STA("+v"); _Pragma("unroll") for (int q = 0; q < 7; ++q) HBC(q, m_); STW(0); _Pragma("unroll") for (int q = 0; q < 7; ++q) HBW(q); } while (0)
#define FIX() do { _Pragma("nounroll") for (u32 sp = 0; sp < SPIN_CAP; ++sp) { u32 m = 0; \
        _Pragma("unroll") for (int q = 0; q < 7; ++q) m |= HBBAD(q) ? 1u << q : 0u; \
        if (!m && !STBAD()) break; if (sp) __builtin_amdgcn_s_sleep(RSL); m = __builtin_amdgcn_readfirstlane(m); REDO(m); } } while (0)
    auto first = [&]() LAM {
#pragma unroll
      for (int k = 0; k < 4; ++k) cq[k] = *(LAS f32x4*)(csl + k * 32);
#pragma unroll
      for (int s = 0; s < 7; ++s) ring[s] = WFRAG(0, s);
      f32x2 sf[7];
#pragma unroll
      for (int i = 0; i < 7; ++i) sf[i] = __builtin_bit_cast(f32x2, st[i]);
      float s1 = ((sf[0].x + sf[1].x) + (sf[2].x + sf[3].x)) + ((sf[4].x + sf[5].x) + sf[6].x);
      float s2 = ((sf[0].y + sf[1].y) + (sf[2].y + sf[3].y)) + ((sf[4].y + sf[5].y) + sf[6].y);
      { f32x2 a = sw32(s1); s1 = a.x + a.y; a = sw32(s2); s2 = a.x + a.y; }
      nmu = s1 * (-1.f / 448.f); ve = fmaxf(s2 * (1.f / 448.f) - nmu * nmu, 0.f) + 1e-5f;
      const float rstd = __builtin_amdgcn_rsqf(ve);
      k0 = -2.30220819f * rstd; k1 = -0.10294324f * rstd * rstd * rstd;
#pragma unroll
      for (int i = 0; i < 16; ++i) acc[0][i] = nmu * cq[i >> 2][i & 3];
      mx = 0;
      __builtin_amdgcn_s_setprio(GUPRIO);
      SCHED();
      gu_block(IC<0>{});
    };
    STW(29);
    if (__builtin_expect(STBAD(), 0)) {
      u32 m = 0;
#pragma unroll
      for (int i = 0; i < 7; ++i) { const u64 bm = __builtin_amdgcn_ballot_w64(st[i].y == EMPTY); m |= ((u32)bm ? 1u << (i >> 1) : 0u) | ((u32)(bm >> 32) ? 1u << ((7 + i) >> 1) : 0u); }
      m = __builtin_amdgcn_readfirstlane(m);
      asm volatile("s_waitcnt vmcnt(%0)" :: "n"(FXW) : "memory");
      REDO(m);
      FIX();
    }
    first();
    if (__builtin_expect(!__all(mx != EMPTY), 0)) { FIX(); first(); }
    asm volatile("s_waitcnt vmcnt(0)" : "+v"(fcl) :: "memory");
    if (__builtin_expect(!__all(fcl == tag + 1), 0)) poll_flags(ws, WS_FH + (T32 * 32 + 28 + p4) * 128, lane < 4, tag);
    gu_block(IC<1>{}); gu_block(IC<2>{}); gu_block(IC<3>{}); gu_block(IC<4>{}); gu_block(IC<5>{}); gu_block(IC<6>{});
#pragma unroll
    for (int i = 0; i < 8; ++i) { const float ga = acc[0][i]; y[i] = ga * acc[0][i + 8] * frcp(fmaf(fexp2(ga * fmaf(ga * ga, k1, k0)), ve, ve)); }
    yb = u32x4{pk(y[0], y[1]), pk(y[2], y[3]), pk(y[4], y[5]), filled(pk(y[6], y[7]))};
    ST16D(DUP & 4, rs, yb, lane * 16, hho + 6 * 8192);
    if (HELP == 7) FLAG(pvo, 7);
    __builtin_amdgcn_s_setprio(0);
    STAMP(19);
    if (r2 < 14) {
      f32x16 da[2][2] = {};
      u32x4 hq[2][8], wl[2][8];
      HBASE();
      __builtin_amdgcn_s_sleep(ADLY);
      lds_wait1(L, S_CONS, w2);
      STAMP2(15);
      HQLOAD(0, 5);
      if (A6L == 0) HQLOAD(1, 6);
      WDREAD(wl[0], 2);
      SCHED();
      STAMP2(16);
      HQCHECK(0, 5);
      if (A6L == 1) HQLOAD(1, 6);
      WDREAD(wl[1], 0);
      SCHED();
      DOWN16(da, wl[0], hq[0]);
      SCHED();
      STAMP2(18);
      HQCHECK(1, 6);
      STAMP2(31);
      DOWN16(da, wl[1], hq[1]);
      PARTIAL_OUT(da, 0, 2);
      STAMP(20);
    }
  } else {
    lds_wait2(L, S_HFA, S_HFB);
    if (TD == 6 && w2 == 0) DTOUCH();
    if (TD == 7) dma4(wd + min(b * 49 + w2 * 13, 12544 - 13) * 128, min(lane, 12) * 128, L_JUNK);
    __builtin_amdgcn_s_setprio(BP3);
    if (r2 >= 14) GUBLK(4);
#define REFILL() do { lds_wait<BSL>(L, S_WSEEN); GUBLK(5); lds_wait<BSL>(L, S_FREE01); GUBLK(6); } while (0)
    if (r2 >= 14) {
      WAITVM(0); lds_signal(L, S_WIN3, w2);
      REFILL();
      WAITVM(0); lds_signal(L, S_REFILL, w2);
    } else {
      {
        u32x2 e[24][2];
        u32 ea_ = da_ - 2 * SLOT, eb_ = ea_ ^ 64;
        asm volatile("" : "+v"(da_), "+v"(db_), "+v"(dc_), "+v"(dd_), "+v"(ea_), "+v"(eb_));
#define WBL(N0, N1, NS, SL) sfor<N0, N1>([&](auto NC) LAM { constexpr int n = NC.value, k = n - (N0); \
          if constexpr (k < (NS)) dma_wd1<n, (SL) * SLOT + k * 1024>(rd, dvo, w2 * 7168); else dma_wd1<n, L_EX + (k - (NS)) * 4096>(rd, dvo, w2 * 1024); })
#define WBR(N0, N1, NS, SL) do { sfor<N0, N1>([&](auto NC) LAM { constexpr int n = NC.value, k = n - (N0); \
          if constexpr (k >= (NS)) { e[n][0] = trread(L + dc_ + (k - (NS)) * 4096); e[n][1] = trread(L + dd_ + (k - (NS)) * 4096); } \
          else if constexpr ((SL) == 4) { e[n][0] = trread(L + da_ + 2 * SLOT + k * 1024); e[n][1] = trread(L + db_ + 2 * SLOT + k * 1024); } \
          else { e[n][0] = trread(L + ea_ + k * 1024); e[n][1] = trread(L + eb_ + k * 1024); } }); \
          asm volatile("s_waitcnt lgkmcnt(0)" : "+v"(e[(N1) - 1][1]) :: "memory"); } while (0)
        WBL(0, 11, 7, 4);
        WAITVM(0);
        STAMP2(24);
        WBR(0, 11, 7, 4);
        WBL(11, 15, 0, 0);
        if (G4E == 1) { GUBLK(4); WAITVM(7); } else WAITVM(0);
        STAMP2(25);
        WBR(11, 15, 0, 0);
        if (G4E == 0) GUBLK(4);
        lds_wait<BSL>(L, S_WSEEN);
        WBL(15, 24, 7, 0);
        if (G6E) { lds_wait<BSL>(L, S_FREE01); GUBLK(6); WAITVM(7); lds_signal(L, S_WIN3, w2); }
        else if (G4E == 2) { GUBLK(4); WAITVM(7); } else { WAITVM(0); lds_signal(L, S_WIN3, w2); }
        STAMP2(26);
        WBR(15, 24, 7, 0);
        GUBLK(5);
        if (G4E == 2) { WAITVM(7); lds_signal(L, S_WIN3, w2); }
        if (!G6E) { lds_wait<BSL>(L, S_FREE01); GUBLK(6); }
#pragma unroll
        for (int n = 0; n < 24; ++n) wdf[n >> 3][n & 7] = u32x4{e[n & ~1][n & 1].x, e[n & ~1][n & 1].y, e[n | 1][n & 1].x, e[n | 1][n & 1].y};
      }
      STAMP2(23);
      f32x16 acc[2][2] = {};
      u32x4 hq[2][8];
      STAMP2(16);
      HBASE();
      u64 pv;
      const char* hs = ws + ho + 4096;
#define PEEK() do { pv = tag + 7; asm volatile("s_mov_b64 exec, 0xff\n\tglobal_atomic_add_x2 %0, %1, %2, %3 sc0\n\ts_mov_b64 exec, -1" : "+v"(pv) : "v"(po), "v"(0ull), "s"(ws) : "memory"); } while (0)
#define PEEKED(k_, n_) do { asm volatile("s_waitcnt vmcnt(%1)" : "+v"(pv) : "n"(k_) : "memory"); PMIN(pv); NEEDP(n_); } while (0)
#define HQA(c_, q, j) _Pragma("unroll") for (int i = 0; i < 8; ++i) asm volatile("global_load_dwordx4 %0, %1, %2 offset:%3 sc1" : c_(hq[q][i]) : "v"(lane * 16), "s"(hs + (j) * 8192), "n"(i * 1024 - 4096) : "memory")
#define HQW(q, k_) asm volatile("s_waitcnt vmcnt(%8)" : "+v"(hq[q][0]), "+v"(hq[q][1]), "+v"(hq[q][2]), "+v"(hq[q][3]), "+v"(hq[q][4]), "+v"(hq[q][5]), "+v"(hq[q][6]), "+v"(hq[q][7]) : "n"(k_) : "memory")
#define HQOK(q, j, k_) do { HQW(q, k_); if (__builtin_expect(HQBAD(q), 0)) { _Pragma("nounroll") for (u32 sp = 0; sp < SPIN_CAP; ++sp) { \
        __builtin_amdgcn_s_sleep(RSL); HQA("+v", q, j); HQW(q, 0); if (!HQBAD(q)) break; } } SCHED(); } while (0)
      PEEK();
      FREE(1); WDLOAD(3, 0);
      WAITVM(8);
#pragma unroll
      for (int k = 0; k < 3; ++k) asm volatile("" : "+v"(wdf[k][0]), "+v"(wdf[k][1]), "+v"(wdf[k][2]), "+v"(wdf[k][3]), "+v"(wdf[k][4]), "+v"(wdf[k][5]), "+v"(wdf[k][6]), "+v"(wdf[k][7]) :: "memory");
      lds_signal(L, S_REFILL, w2);
      PEEKED(8, 1);
      STAMP2(1);
      PEEK();
      HQA("=v", 0, 0);
      FREE(2); WDLOAD(4, 1);
      PEEKED(16, 2);
      STAMP2(2);
      HQA("=v", 1, 1);
      PEEK();
      u32 fr3;
      { const u32x4 fr = *(volatile LAS u32x4*)(L + L_FLG + S_DONE4 * 16); fr3 = !W5D || __builtin_amdgcn_readfirstlane(min(min(fr.x, fr.y), min(fr.z, fr.w))) >= 3; }
      if (fr3) { if (!W5D) FREE(3); WDLOAD(5, 2); }
      if (fr3) WAITVM(25); else WAITVM(17);
      HQOK(0, 0, 25);
      __builtin_amdgcn_s_setprio(DP0);
      DOWN16(acc, wdf[0], hq[0]);
      __builtin_amdgcn_s_setprio(BP3);
      WDREAD(wdf[0], 0);
      SCHED();
      STAMP2(8);
      if (!fr3) { FREE(3); WDLOAD(5, 2); }
      PEEKED(8, 3);
      STAMP2(3);
      HQA("=v", 0, 2);
      PEEK();
      asm volatile("s_waitcnt lgkmcnt(0)" : "+v"(wdf[0][7]) :: "memory");
      WDLOAD(6, 0);
      HQOK(1, 1, 25);
      __builtin_amdgcn_s_setprio(DP0);
      DOWN16(acc, wdf[1], hq[1]);
      WDREAD(wdf[1], 1);
      SCHED();
      STAMP2(9);
      PEEKED(8, 4);
      STAMP2(4);
      HQA("=v", 1, 3);
      __builtin_amdgcn_s_setprio(DNPRIO);
      HQOK(0, 2, 8);
      lds_signal(L, S_CONS, w2);
      SCHED();
      DOWN16(acc, wdf[2], hq[0]);
      SCHED();
      STAMP2(10);
      FREE(4);
      HQA("=v", 0, 4);
      HQOK(1, 3, 8);
      DOWN16(acc, wdf[0], hq[1]);
      SCHED();
      STAMP2(11);
      HQOK(0, 4, 0);
      DOWN16(acc, wdf[1], hq[0]);
      PARTIAL_OUT(acc, 1, 3);
      STAMP(20);
    }
  }
  if (r2 < 14) {
    BAR();
    STAMP2(7);
    float c[8] = {blo(hres.x), bhi(hres.x), blo(hres.y), bhi(hres.y), blo(hres.z), bhi(hres.z), blo(hres.w), bhi(hres.w)};
    const int rl = ((tb & 1) * 16 + l15) * 16;
    f32x4 v0 = {c[0], c[1], c[2], c[3]}, v1 = {c[4], c[5], c[6], c[7]};
#pragma unroll
    for (int h = 0; h < 2; ++h) {
      const int bf = (tb >> 1) * 2 + h;
      const bool p7 = kh && g == 3;
      u32 rq = (p7 ? L_EX + bf * 4096 : (bf == 3 ? 0 : (bf + 2) * SLOT) + (4 * kh + g) * 1024) + rl;
      const u32 st7 = p7 ? 1024 : 7168;
      asm volatile("" : "+v"(rq));
#pragma unroll
      for (int w = 0; w < 4; ++w) { v0 += *(LAS f32x4*)(L + rq + w * st7); v1 += *(LAS f32x4*)(L + rq + w * st7 + 512); }
    }
#pragma unroll
    for (int i = 0; i < 4; ++i) { c[i] = v0[i]; c[4 + i] = v1[i]; }
    { const u32x4 ov = {pk(c[0], c[1]), pk(c[2], c[3]), pk(c[4], c[5]), pk(c[6], c[7])}; asm volatile("global_store_dwordx4 %0, %1, %2 sc1\n\ts_nop 1" :: "v"(xo), "v"(ov), "s"(out) : "memory"); }
    if (A && b == 0 && tid == 0) __hip_atomic_store((GAS u64*)(ws + WS_E), tag, RLX_AGENT);
  }
  STAMP(21);
  };
  if (wave < 4) role(IC<1>{}); else role(IC<0>{});
}

extern "C" void kernel_launch(void* const* d_in, const int* in_sizes, int n_in, void* d_out, int out_size, void* d_ws, size_t ws_size, hipStream_t stream) {
  (void)hipFuncSetAttribute((const void*)fused_block, hipFuncAttributeMaxDynamicSharedMemorySize, LDS_BYTES);
  fused_block<<<256, 512, LDS_BYTES, stream>>>((const char*)d_in[0], (const char*)d_in[1], (const char*)d_in[2], (const char*)d_in[3], (const char*)d_in[5], (const char*)d_in[6],
                                               (const char*)d_in[7], (char*)d_out, (char*)d_ws);
}
```

```cpp
#define PROBE 102
#include <hip/hip_runtime.h>
#include <hip/hip_bf16.h>
#include <stdint.h>

typedef unsigned u32;
typedef unsigned long long u64;
typedef u32 u32x4 __attribute__((ext_vector_type(4)));
typedef u32 u32x2 __attribute__((ext_vector_type(2)));
typedef float f32x2 __attribute__((ext_vector_type(2)));
typedef float f32x4 __attribute__((ext_vector_type(4)));
typedef float f32x16 __attribute__((ext_vector_type(16)));
typedef short s16x4 __attribute__((ext_vector_type(4)));
typedef __bf16 bf16x2 __attribute__((ext_vector_type(2)));
typedef __bf16 bf16x8 __attribute__((ext_vector_type(8)));
#define LAS __attribute__((address_space(3)))
#define GAS __attribute__((address_space(1)))
#define DI __device__ __forceinline__
#define RLX_AGENT __ATOMIC_RELAXED, __HIP_MEMORY_SCOPE_AGENT

constexpr int WS_E = 0;
constexpr int WS_FKV = 8192;
constexpr int WS_FO = WS_FKV + 131072;
constexpr int WS_FH = WS_FO + 131072;
constexpr int WS_FG = WS_FH + 262144;
constexpr int WS_FD = WS_FG + 114688;
constexpr int WS_FHH = WS_FD + 57344;
constexpr int WS_CS = WS_FHH + 262144;
constexpr int WS_ST = WS_CS + 200704;
constexpr int WS_WGU = WS_ST + 229376;
constexpr int WS_WD = WS_WGU + 3211264;
constexpr int WS_KVP = WS_WD + 1605632;
constexpr int WS_O = WS_KVP + 1048576;
constexpr int WS_H = WS_O + 2097152;
constexpr int WS_HH = WS_H + 1835008;
#define UNITX4
#define NOWD
#define WDOFF(cg, fb, bl) (((((cg) * 4 + (fb) / 28) * 7 + (fb) % 7) * 4 + (fb) / 7 % 4) * 2 + (bl))
#define HHOFF(T32, fb) (((((T32) >> 1) * 4 + (fb) / 28) * 7 + (fb) % 7) * 8 + ((fb) / 7 & 3) * 2 + ((T32) & 1))
constexpr int WS_STAMP = WS_HH + 7340032;
constexpr int WS_LOG = WS_STAMP + 256 * 8 * 32 * 8;
constexpr int WS_USED = WS_LOG + 4096;
constexpr int SLOT = 28672;
constexpr int L_WK = 4 * SLOT, L_WV = 0, L_WQ = 14336;
constexpr int L_PKV = 2 * SLOT;
constexpr int L_WO = 3 * SLOT;
constexpr int L_EX = 5 * SLOT;
constexpr int L_KVA = 5 * SLOT;
constexpr int L_ST1 = 5 * SLOT + 4096;
constexpr int L_WPHI = 5 * SLOT + 5120;
constexpr int L_CSP = 5 * SLOT + 7168;
constexpr int L_CS = 159744;
constexpr int L_FLG = 160768;
constexpr int L_JUNK = 161280;
constexpr int L_STAMP = 161536;
constexpr int L_RED = SLOT;
constexpr int LDS_BYTES = 163840;
constexpr int gu_slot(int j) { return j % 5; }
constexpr int CODE_PF = 49152;
constexpr u32 SPIN_CAP = 1u << 16;
#define LL 1
#define AP 1

#define STAMP(i) do { if ((i) == PROBE % 100 && (wave >= 4) == (PROBE >= 100)) *(volatile LAS u32*)(L + L_STAMP + wave * 4) = (u32)__builtin_amdgcn_s_memrealtime(); } while (0)
#define STAMP2(i) do {} while (0)
#define STAMP2X(i) do {} while (0)

DI f32x4 mfma16(u32x4 a, u32x4 b, f32x4 c) { return __builtin_amdgcn_mfma_f32_16x16x32_bf16(__builtin_bit_cast(bf16x8, a), __builtin_bit_cast(bf16x8, b), c, 0, 0, 0); }
DI f32x16 mfma32(u32x4 a, u32x4 b, f32x16 c) { return __builtin_amdgcn_mfma_f32_32x32x16_bf16(__builtin_bit_cast(bf16x8, a), __builtin_bit_cast(bf16x8, b), c, 0, 0, 0); }
DI u32 pk(float lo, float hi) { f32x2 p = {lo, hi}; return __builtin_bit_cast(u32, __builtin_convertvector(p, bf16x2)); }
DI float blo(u32 v) { return __builtin_bit_cast(float, v << 16); }
DI float bhi(u32 v) { return __builtin_bit_cast(float, v & 0xffff0000u); }
DI float dot2(u32 a, u32 b, float c) { return __builtin_amdgcn_fdot2_f32_bf16(__builtin_bit_cast(bf16x2, a), __builtin_bit_cast(bf16x2, b), c, false); }
DI u32x2 trread(LAS char* p) { return __builtin_bit_cast(u32x2, __builtin_amdgcn_ds_read_tr16_b64_v4i16((LAS s16x4*)p)); }
DI u32x4 trfrag(LAS char* p, int step) { u32x2 a = trread(p), b = trread(p + step); return u32x4{a.x, a.y, b.x, b.y}; }
DI float fexp2(float x) { return __builtin_amdgcn_exp2f(x); }
DI float frcp(float x) { return __builtin_amdgcn_rcpf(x); }
template <int C> DI float dppror(float x) { return __builtin_bit_cast(float, __builtin_amdgcn_update_dpp(0, __builtin_bit_cast(int, x), 0x120 + C, 0xf, 0xf, false)); }
DI float row_max(float x) { x = fmaxf(x, dppror<8>(x)); x = fmaxf(x, dppror<4>(x)); x = fmaxf(x, dppror<2>(x)); return fmaxf(x, dppror<1>(x)); }
DI float row_min(float x) { x = fminf(x, dppror<8>(x)); x = fminf(x, dppror<4>(x)); x = fminf(x, dppror<2>(x)); return fminf(x, dppror<1>(x)); }
DI float row_sum(float x) { x += dppror<8>(x); x += dppror<4>(x); x += dppror<2>(x); return x + dppror<1>(x); }
DI f32x2 sw16(float x) { u32 u = __builtin_bit_cast(u32, x); auto r = __builtin_amdgcn_permlane16_swap(u, u, false, false); return f32x2{__builtin_bit_cast(float, (u32)r[0]), __builtin_bit_cast(float, (u32)r[1])}; }
DI f32x2 sw32(float x) { u32 u = __builtin_bit_cast(u32, x); auto r = __builtin_amdgcn_permlane32_swap(u, u, false, false); return f32x2{__builtin_bit_cast(float, (u32)r[0]), __builtin_bit_cast(float, (u32)r[1])}; }
DI float grp_sum(float x) { f32x2 a = sw16(x); x = a.x + a.y; a = sw32(x); return a.x + a.y; }
DI float grp_max(float x) { f32x2 a = sw16(x); x = fmaxf(a.x, a.y); a = sw32(x); return fmaxf(a.x, a.y); }
DI float grp_min(float x) { f32x2 a = sw16(x); x = fminf(a.x, a.y); a = sw32(x); return fminf(a.x, a.y); }

#define BAR() asm volatile("s_waitcnt lgkmcnt(0)\n\ts_barrier" ::: "memory")
#define WAITVM(n) asm volatile("s_waitcnt vmcnt(" #n ")" ::: "memory")
#define DRAIN() do { asm volatile("s_waitcnt vmcnt(0)" ::: "memory"); __builtin_amdgcn_s_waitcnt(0x0F70); } while (0)
#define SCHED() __builtin_amdgcn_sched_barrier(0)
DI void dma16(const void* base, u32 voff, u32 lds) {
  u32 keep;
  asm volatile("s_mov_b32 %0, m0\n\ts_mov_b32 m0, %3\n\ts_nop 0\n\tglobal_load_lds_dwordx4 %1, %2\n\ts_mov_b32 m0, %0" : "=&s"(keep) : "v"(voff), "s"(base), "s"(lds) : "memory");
}
DI void dma16c(const void* base, u32 voff, u32 lds) {
  u32 keep;
  asm volatile("s_mov_b32 %0, m0\n\ts_mov_b32 m0, %3\n\ts_nop 0\n\tglobal_load_lds_dwordx4 %1, %2 sc1\n\ts_mov_b32 m0, %0" : "=&s"(keep) : "v"(voff), "s"(base), "s"(lds) : "memory");
}
DI void dma4(const void* base, u32 voff, u32 lds) {
  u32 keep;
  asm volatile("s_mov_b32 %0, m0\n\ts_mov_b32 m0, %3\n\ts_nop 0\n\tglobal_load_lds_dword %1, %2\n\ts_mov_b32 m0, %0" : "=&s"(keep) : "v"(voff), "s"(base), "s"(lds) : "memory");
}
DI void dma16c_if(u32 go, const void* base, u32 voff, u32 lds) {
  u32 keep;
  asm volatile("s_cmp_eq_u32 %4, 0\n\ts_cbranch_scc1 1f\n\ts_mov_b32 %0, m0\n\ts_mov_b32 m0, %3\n\ts_nop 0\n\tglobal_load_lds_dwordx4 %1, %2 sc1\n\ts_mov_b32 m0, %0\n1:"
               : "=&s"(keep) : "v"(voff), "s"(base), "s"(lds), "s"(go) : "memory", "scc");
}
template <int SI, int LI> DI void dma7(__amdgpu_buffer_rsrc_t rs, u32 voff, u32 so, u32 lds) {
  u32 t;
  asm volatile("s_add_u32 m0, %4, %6\n\ts_add_u32 %0, %3, %5\n\tbuffer_load_dwordx4 %1, %2, %0 offen sc1 lds\n\tbuffer_load_dwordx4 %1, %2, %0 offen offset:1024 sc1 lds\n\t"
               "buffer_load_dwordx4 %1, %2, %0 offen offset:2048 sc1 lds\n\tbuffer_load_dwordx4 %1, %2, %0 offen offset:3072 sc1 lds\n\t"
               "s_add_u32 m0, m0, 4096\n\ts_add_u32 %0, %0, 4096\n\tbuffer_load_dwordx4 %1, %2, %0 offen sc1 lds\n\tbuffer_load_dwordx4 %1, %2, %0 offen offset:1024 sc1 lds\n\t"
               "buffer_load_dwordx4 %1, %2, %0 offen offset:2048 sc1 lds"
               : "=&s"(t) : "v"(voff), "s"(rs), "s"(so), "s"(lds), "n"(SI), "n"(LI) : "memory", "scc");
}
constexpr int wrow(int q, int j, int hf) { return (((7 * q + j) >> 2) * 64 + 2 * ((7 * q + j) & 3) + hf) * 896 + 1024; }
#define DL_(n, o) "s_mov_b32 %0, %" #n "\n\tbuffer_load_dwordx4 %1, %2, %0 offen offset:" #o " lds\n\t"
template <int J> DI void dma_wd(__amdgpu_buffer_rsrc_t rs, u32 voff, u32 lds, u32 lds7) {
  u32 t;
  asm volatile("s_mov_b32 m0, %3\n\t" DL_(5, 0) DL_(6, 1024) DL_(7, 2048) DL_(8, 3072) "s_add_u32 m0, m0, 4096\n\t" DL_(9, 0) DL_(10, 1024) DL_(11, 2048) "s_mov_b32 m0, %4\n\t" DL_(12, 0)
               : "=&s"(t) : "v"(voff), "s"(rs), "s"(lds), "s"(lds7), "n"(wrow(0, J, 0)), "n"(wrow(0, J, 1) - 1024), "n"(wrow(1, J, 0) - 2048), "n"(wrow(1, J, 1) - 3072),
                 "n"(wrow(2, J, 0)), "n"(wrow(2, J, 1) - 1024), "n"(wrow(3, J, 0) - 2048), "n"(wrow(3, J, 1)) : "memory", "scc");
}
template <int N, int LO> DI void dma_wd1(__amdgpu_buffer_rsrc_t rs, u32 voff, u32 lds) {
  u32 t;
  asm volatile("s_add_u32 m0, %3, %4\n\t" DL_(5, 0) : "=&s"(t) : "v"(voff), "s"(rs), "s"(lds), "n"(LO), "n"(wrow(N / 2 % 4, N / 8, N % 2)) : "memory", "scc");
}
DI void touch(const void* base, u32 voff) {
  asm volatile("s_mov_b32 m0, %2\n\ts_nop 0\n\tglobal_load_lds_dword %0, %1" :: "v"(voff), "s"(base), "n"(L_JUNK) : "memory");
}
constexpr int xrow(int n) { return ((n / 14) * 16 + (n & 1) * 8) * 896 + ((n % 14) >> 1) * 128 + 2048 - n % 4 * 1024; }
template <int N> DI void dma_x(__amdgpu_buffer_rsrc_t rs, u32 voff, u32 lds) {
  u32 t;
  asm volatile("s_add_u32 m0, %3, %4\n\t" DL_(5, 0) DL_(6, 1024) DL_(7, 2048) DL_(8, 3072)
               : "=&s"(t) : "v"(voff), "s"(rs), "s"(lds), "n"(N * 1024), "n"(xrow(N)), "n"(xrow(N + 1)), "n"(xrow(N + 2)), "n"(xrow(N + 3)) : "memory", "scc");
}
DI u32 vm_count() {
  u32 t;
  asm volatile("s_getreg_b32 %0, hwreg(HW_REG_IB_STS)" : "=s"(t) :: "memory");
  return (t & 15u) | ((t >> 18) & 48u);
}
DI u32 min8(u32 c) {
  c = min(c, (u32)__builtin_amdgcn_update_dpp(0, (int)c, 0x121, 0xf, 0xf, false)); c = min(c, (u32)__builtin_amdgcn_update_dpp(0, (int)c, 0x122, 0xf, 0xf, false));
  c = min(c, (u32)__builtin_amdgcn_update_dpp(0, (int)c, 0x124, 0xf, 0xf, false));
  return __builtin_amdgcn_readfirstlane(min(c, (u32)__builtin_amdgcn_update_dpp(0, (int)c, 0x128, 0xf, 0xf, false)));
}
DI void dma4c(const void* base, u32 voff, u32 lds) {
  u32 keep;
  asm volatile("s_mov_b32 %0, m0\n\ts_mov_b32 m0, %3\n\ts_nop 0\n\tglobal_load_lds_dword %1, %2 sc1\n\ts_mov_b32 m0, %0" : "=&s"(keep) : "v"(voff), "s"(base), "s"(lds) : "memory");
}
#define LD16(rs, off) __builtin_bit_cast(u32x4, __builtin_amdgcn_raw_buffer_load_b128(rs, off, 0, 16))
#define ST16(rs, v, off, soff) do { const u32x4 d_ = __builtin_bit_cast(u32x4, v); __builtin_amdgcn_raw_buffer_store_b128(d_, rs, off, soff, 16); asm volatile("s_nop 1" :: "v"(d_) : "memory"); } while (0)

#define ST16D(on, rs, v, off, soff) do { const u32x4 d_ = __builtin_bit_cast(u32x4, v); __builtin_amdgcn_raw_buffer_store_b128(d_, rs, off, soff, 16); \
    if (on) __builtin_amdgcn_raw_buffer_store_b128(d_, rs, off, soff, 0); asm volatile("s_nop 1" :: "v"(d_) : "memory"); } while (0)
#define FLAG(off, n) __builtin_amdgcn_raw_buffer_store_b64(__builtin_bit_cast(u32x2, tag + (n)), rs, fvo, off, 16)
#define FLAGC(c, off, n) __builtin_amdgcn_raw_buffer_store_b64(__builtin_bit_cast(u32x2, tag + (n)), rs, (c) ? fvo : NOWHERE, off, 16)
constexpr int NOWHERE = 0x7ffffff0;
constexpr u32 EMPTY = 0xffffffffu;
DI u32 filled(u32 w) { return min(w, EMPTY - 1u); }
DI u64 peek(const char* ws, u32 off, bool on, u64 dflt) {
  u64 z = 0, v = dflt;
  asm volatile("" : "+v"(z));
  if (on) v = __hip_atomic_fetch_add((GAS u64*)(ws + off), z, __ATOMIC_RELAXED, AP == 2 ? __HIP_MEMORY_SCOPE_SYSTEM : __HIP_MEMORY_SCOPE_AGENT);
  return v;
}
DI void poll_flags(const char* ws, u32 off, bool on, u64 tag) {
#pragma nounroll
  for (u32 sp = 0; sp < SPIN_CAP; ++sp) {
    u64 v = tag + 1;
    if (on || !LL) v = __hip_atomic_load((const GAS u64*)(ws + off), RLX_AGENT);
    if (__all(v == tag + 1)) break;
  }
  __builtin_amdgcn_fence(__ATOMIC_ACQUIRE, "wavefront");
}
template <int SL = 1> DI void lds_wait(LAS char* L, int sig) {
#pragma nounroll
  for (u32 sp = 0; sp < (SPIN_CAP << 4); ++sp) {
    const u32x4 v = *(volatile LAS u32x4*)(L + L_FLG + sig * 16);
    if (__builtin_amdgcn_readfirstlane(v.x & v.y & v.z & v.w)) break;
    __builtin_amdgcn_s_sleep(SL);
  }
  asm volatile("" ::: "memory");
}
DI void lds_wait2(LAS char* L, int sig, int sig2) {
#pragma nounroll
  for (u32 sp = 0; sp < (SPIN_CAP << 4); ++sp) {
    const u32x4 v = *(volatile LAS u32x4*)(L + L_FLG + sig * 16) & *(volatile LAS u32x4*)(L + L_FLG + sig2 * 16);
    if (__builtin_amdgcn_readfirstlane(v.x & v.y & v.z & v.w)) break;
    __builtin_amdgcn_s_sleep(1);
  }
  asm volatile("" ::: "memory");
}
DI void lds_wait1(LAS char* L, int sig, int w) {
#pragma nounroll
  for (u32 sp = 0; sp < (SPIN_CAP << 4); ++sp) {
    if (__builtin_amdgcn_readfirstlane(*(volatile LAS u32*)(L + L_FLG + sig * 16 + w * 4))) break;
    __builtin_amdgcn_s_sleep(1);
  }
  asm volatile("" ::: "memory");
}
DI void lds_signal(LAS char* L, int sig, int w) {
  asm volatile("" ::: "memory");
  *(volatile LAS u32*)(L + L_FLG + sig * 16 + w * 4) = 1u;
}

template <int N> struct IC { static constexpr int value = N; };
template <int I, int N, class F> DI void sfor(F&& f) { if constexpr (I < N) { f(IC<I>{}); sfor<I + 1, N>(f); } }
#define LAM __attribute__((always_inline))
#define PIN(v) asm volatile("" : "+v"(v))
constexpr float LOG2E = 1.4426950408889634f;
template <int K> DI void macc(f32x4& c, u32x4 a, u32x4 b) {
  if (K == 0) asm volatile("v_mfma_f32_16x16x32_bf16 %0, %1, %2, 0" : "=&v"(c) : "v"(a), "v"(b));
  else if (K == 1) asm volatile("v_mfma_f32_16x16x32_bf16 %0, %1, %2, %0" : "+v"(c) : "v"(a), "v"(b));
  else c = mfma16(a, b, c);
}
template <int GAP, int SIG, int NW, class E, class F> DI void project(LAS char* L, u32 w0, u32 w1, const u32x4 (&xf)[2][14], int tp, int lane, int csl, f32x4 (&t)[2][2], f32x2 (&st)[2], E&& pre, F&& mid) {
  u32 wq = (tp & 1 ? w1 : w0) + (tp >> 1) * (7 * 2048 + GAP), ca = csl + (lane >> 4) * 16, sa = L_ST1 + (tp * 32 + (lane & 15)) * 8, fa = L_FLG + SIG * 16 + (lane & (NW - 1)) * 4;
  u32 cw = ca + tp * 64, fw = L_FLG + SIG * 16 + tp * 4, one = 1;
  u32x4 ones = {0x3f803f80u, 0x3f803f80u, 0x3f803f80u, 0x3f803f80u};
  LAS char *p0 = L + w0, *p1 = L + w1, *pq = L + wq, *pca = L + ca, *psa = L + sa, *pfa = L + fa, *pcw = L + cw, *pfw = L + fw;
  asm volatile("" : "+v"(p0), "+v"(p1), "+v"(pq), "+v"(pca), "+v"(psa), "+v"(pfa), "+v"(pcw), "+v"(pfw), "+v"(ones), "+v"(one));
  pre();
  u32x4 cf[7];
  u32x2 ra[7], rb[7];
  f32x4 cs, cp[4];
  u32 sw;
#define WAT(n) (((n) & 1 ? p1 : p0) + ((n) >> 1) * 2048 + ((n) >= 14 ? GAP : 0))
#pragma unroll
  for (int i = 0; i < 7; ++i) cf[i] = trfrag(pq + i * 2048, 256);
  SCHED();
  sfor<0, 7>([&](auto IC_) LAM {
    constexpr int i = IC_.value;
    macc<i == 0 ? 0 : i == 6 ? 2 : 1>(cs, cf[i], ones);
    SCHED();
    if (i < 6) { ra[i] = trread(WAT(i)); rb[i] = trread(WAT(i) + 256); }
    SCHED();
  });
  sfor<0, 28>([&](auto NC) LAM {
    constexpr int n = NC.value, K = n < 2 ? 0 : n < 26 ? 1 : 2;
    const u32x4 f = {ra[n % 7].x, ra[n % 7].y, rb[n % 7].x, rb[n % 7].y};
    macc<K>(t[0][n & 1], f, xf[0][n >> 1]);
    SCHED();
    if constexpr (n + 6 < 28) ra[(n + 6) % 7] = trread(WAT(n + 6));
    if constexpr (n == 24) st[0] = *(LAS f32x2*)(psa);
    if constexpr (n == 22 || n == 23) cp[n - 22] = *(LAS f32x4*)(pca + (n - 22) * 64);
    if constexpr (n >= 26) { float a = cp[n - 26][0] + cp[n - 24][0], b = cp[n - 26][1] + cp[n - 24][1]; PIN(a); PIN(b); cp[n - 26][0] = a; cp[n - 26][1] = b; }
    SCHED();
    macc<K>(t[1][n & 1], f, xf[1][n >> 1]);
    SCHED();
    if constexpr (n + 6 < 28) rb[(n + 6) % 7] = trread(WAT(n + 6) + 256);
    if constexpr (n == 24) st[1] = *(LAS f32x2*)(psa + 128);
    if constexpr (n == 22 || n == 23) cp[n - 20] = *(LAS f32x4*)(pca + (n - 20) * 64);
    if constexpr (n >= 26) { float a = cp[n - 26][2] + cp[n - 24][2], b = cp[n - 26][3] + cp[n - 24][3]; PIN(a); PIN(b); cp[n - 26][2] = a; cp[n - 26][3] = b; }
    if constexpr (n == 2) { if ((lane & 15) == 0) *(LAS f32x4*)(pcw) = cs; }
    if constexpr (n == 3) { asm volatile("" ::: "memory"); *(volatile LAS u32*)(pfw) = one; }
    if constexpr (n == 16) sw = *(volatile LAS u32*)(pfa);
    if constexpr (n == 21) { if (__builtin_expect(__builtin_amdgcn_ballot_w64(sw == 0) != 0, 0)) { lds_wait(L, SIG); if (NW > 4) lds_wait(L, SIG + 1); } asm volatile("" ::: "memory"); }
    mid(n, pfw - SIG * 16, one);
    SCHED();
  });
#pragma unroll
  for (int tb = 0; tb < 2; ++tb)
#pragma unroll
    for (int k = 0; k < 2; ++k) t[tb][k] += cp[k] * st[tb].x;
}
DI f32x2 swp16(float a, float b) { auto r = __builtin_amdgcn_permlane16_swap(__builtin_bit_cast(u32, a), __builtin_bit_cast(u32, b), false, false); return f32x2{__builtin_bit_cast(float, (u32)r[0]), __builtin_bit_cast(float, (u32)r[1])}; }
DI f32x2 swp32(float a, float b) { auto r = __builtin_amdgcn_permlane32_swap(__builtin_bit_cast(u32, a), __builtin_bit_cast(u32, b), false, false); return f32x2{__builtin_bit_cast(float, (u32)r[0]), __builtin_bit_cast(float, (u32)r[1])}; }
DI float swadd32(float a, float b) { const f32x2 s = swp32(a, b); return s.x + s.y; }
DI float swadd16(float a, float b) { const f32x2 s = swp16(a, b); return s.x + s.y; }
DI float mx2(float a, float b) { float r; asm("v_max_f32 %0, %1, %2" : "=v"(r) : "v"(a), "v"(b)); return r; }
DI float mx3(float a, float b, float c) { return fmaxf(fmaxf(a, b), c); }
DI float mn3(float a, float b, float c) { return fminf(fminf(a, b), c); }
template <class F> DI void feature_map(const f32x4 (&t)[2][2], const f32x2 (&st)[2], const u32x4 (&wpf)[2], int g, u32x4 (&P)[2][2], F&& mid) {
  const float pad = g < 3 ? 0.f : -3.0e38f;
  const f32x4 z4 = {0.f, 0.f, 0.f, 0.f};
  const float sc[2] = {st[0].y * LOG2E, st[1].y * LOG2E}, scg = g < 2 ? sc[0] : sc[1];
  float p[2][8], m[4], mp[4];
#pragma unroll
  for (int tb = 0; tb < 2; ++tb) {
    const u32x4 tq = {pk(t[tb][0][0], t[tb][0][1]), pk(t[tb][0][2], t[tb][0][3]), pk(t[tb][1][0], t[tb][1][1]), pk(t[tb][1][2], t[tb][1][3])};
    const f32x4 p0 = mfma16(wpf[0], tq, z4), p1 = mfma16(wpf[1], tq, z4);
#pragma unroll
    for (int i = 0; i < 8; ++i) p[tb][i] = i < 4 ? p0[i] : p1[i - 4];
  }
  mid(0);
#pragma unroll
  for (int tb = 0; tb < 2; ++tb) {
    const float* q = p[tb];
    m[2 * tb] = mx3(mx3(q[0], q[1], q[2]), mx3(q[3], q[4], q[5]), mx3(q[5], q[6], q[7]));
    m[2 * tb + 1] = mx3(mx3(-q[0], -q[1], -q[2]), mx3(-q[3], -q[4], -q[5]), mx3(-q[5], -q[6], -q[7]));
  }
  {
    const f32x2 a = swp16(m[0], m[1]), b = swp16(m[2], m[3]);
    const f32x2 c = swp32(mx2(a.x, a.y), mx2(b.x, b.y));
    const float d = mx2(c.x, c.y) * -scg;
    const f32x2 e = swp32(d, d), f0 = swp16(e.x, e.x), f1 = swp16(e.y, e.y);
    m[0] = f0.x; m[1] = f0.y; m[2] = f1.x; m[3] = f1.y;
#pragma unroll
    for (int c2 = 0; c2 < 4; ++c2) mp[c2] = m[c2] + pad;
  }
  float ex[4][8], sm[4];
#pragma unroll
  for (int c = 0; c < 4; ++c) {
    mid(1 + c);
#pragma unroll
    for (int i = 0; i < 8; ++i) {
      ex[c][i] = fexp2(fmaf(c & 1 ? -p[c >> 1][i] : p[c >> 1][i], sc[c >> 1], i & 2 ? mp[c] : m[c]));
    }
    sm[c] = ((ex[c][0] + ex[c][1]) + (ex[c][2] + ex[c][3])) + ((ex[c][4] + ex[c][5]) + (ex[c][6] + ex[c][7]));
  }
  mid(5);
  {
    const float d = frcp(swadd32(swadd16(sm[0], sm[1]), swadd16(sm[2], sm[3])));
    const f32x2 e = swp32(d, d), f0 = swp16(e.x, e.x), f1 = swp16(e.y, e.y);
    sm[0] = f0.x; sm[1] = f0.y; sm[2] = f1.x; sm[3] = f1.y;
  }
#pragma unroll
  for (int c = 0; c < 4; ++c)
#pragma unroll
    for (int e = 0; e < 4; ++e) P[c >> 1][c & 1][e] = pk(ex[c][2 * e] * sm[c], ex[c][2 * e + 1] * sm[c]);
}
DI u32x4 piece(const u32x4 (&rp)[8], int e) {
  u32x4 f;
#pragma unroll
  for (int m = 0; m < 4; ++m) f[m] = __builtin_amdgcn_perm(rp[2 * m + 1][e >> 1], rp[2 * m][e >> 1], (e & 1) ? 0x07060302u : 0x05040100u);
  return f;
}
#define RP_OPS "+v"(rp[0]), "+v"(rp[1]), "+v"(rp[2]), "+v"(rp[3]), "+v"(rp[4]), "+v"(rp[5]), "+v"(rp[6]), "+v"(rp[7])
#define PF1 0
#define PF2 84
#define PF3 120
#define TP0 -1
#define TP1 13
#define TP2 -1
#define TOUCH 5
#define NW3 4
#define THRN0 4
#define DNPRIO 3
#define NWD 3
#define MIDCHK 0
#define HELP 0
#define NHW 7
#define W3LATE 0
#define W3WAIT 1
#define ROT 1
#define EXPT 0
#define PFM 12
#define PSLP 4
#define PLAG 0
#define WTOUCH 0
#define REPLATE 0
#define WB0 8
#define WB1 4
#define ULATE 0
#define VPRIO 3
#define VPRIO0 3
#define VPN 27
#define FPRIO 2
#define CLB 0
#define KAT 99
#define STF 0
#define QN0 0
#define QST 4
#define PB0 3
#define WOT 0
#define WTN 2
#define CLF 0
#define TQ 1
#define TU 0
#define TO 1
#define SN_ 49
#define SS_ 128
#define SD_ 0
#define RPO 0
#define REPE 0
#define UFN 14
#define SBA 0
#define TD 2
#define NR5 16
#define NR6 23
#define G4E 0
#define TS0 2
#define TS1 8
#define TWF 4
#define BP3 0
#define W3J 2
#define TQD -1
#define TOD 0
#define TDD 10
#define CPN 7
#define X0A 5
#define NE 0
#define G6E 0
#define HW 1
#define PACE 1
#define PS 28
#define PJ0 1
#define PJ1 6
#define MAP 0
#define DUP 0
#define XT 1
#define KGO -1
#define QEARLY 1
#define DLY1 4
#define W1 1
#define W2C 0
#define WOE 2
#define W3THR 1
#define HW3 1
#define HW1 1
#define SN 4
#define BATCH 1
#define B23 1
#define WD5L 0
#define A56 1
#define BGO 196
#define BALL 0
#define BSL 4
#define RSL 4
#define H5 1
#define H6 1
#define BORD 1
#define DP0 3
#define AIL 1
#define SEEN 3
#define ADLY 16
#define W6E 0
#define SPLIT 1
#define NCL 4
#define B5L 1
#define A6L 1
#define BO2 2
#define TW 0
#define CL 0
#define CE 0
#define W5D 1
#define W2F 5
#define D2A 12
#define D2B 16
#define SPEC 1
#define CLP 28
#define CLS 3
#define CE2 0
#define DLY5 60
#define CSL 1
#define CF 0
#define OPRIO 3
#define PG 1
#define ISK 1
#define T2 70
#define FXW 8
#define DLY4 16
#define PUBS 5
#define THRN 40
#define DLY2 8
#define DLY3 15
#define GUPRIO 2
enum { S_FREE01 = 0, S_REFILL, S_DONE4, S_WIN3, S_WK, S_WV, S_WQ, S_WSEEN, S_KDONE, S_V, S_PHIK, S_CSV, S_KVA, S_WO, S_CSQ, S_PHI, S_X0, S_X1, S_XA, S_CSK, S_CS, S_WOFA, S_WOFB, S_EXA, S_EXB, S_W2A, S_W2B, S_HFA, S_HFB, S_CONS, S_SPARE, S_XF0 };

__global__ void __launch_bounds__(512, 2) fused_block(const char* __restrict__ x, const char* __restrict__ wqkv, const char* __restrict__ wphi, const char* __restrict__ wo,
                                                      const char* __restrict__ wg, const char* __restrict__ wu, const char* __restrict__ wd, char* __restrict__ out, char* ws) {
  extern __shared__ __attribute__((aligned(16))) char smem[];
  LAS char* L = (LAS char*)smem;
  const int tid = threadIdx.x, lane = tid & 63, wave = __builtin_amdgcn_readfirstlane(tid >> 6), w2 = wave & 3;
  const int b = blockIdx.x, li = b >> 3, xp = (b & 7) >> 1, xh = b & 1;
  const int tile = MAP == 2 ? (b & 4) * 2 + (li >> 2) : MAP ? 2 * (b & 7) + (li >> 4) : 4 * xp + (li >> 3), r = MAP == 2 ? 4 * (b & 3) + (li & 3) : MAP ? li & 15 : 8 * xh + (li & 7);
  const int tile2 = 4 * xp + 2 * xh + (li >> 4), r2 = li & 15;
  const int tg = r2 >= 7 ? 1 : 0, cg = r2 - 7 * tg;
  const int l15 = lane & 15, g = lane >> 4, q4 = l15 >> 2, p4 = lane & 3, hh = lane >> 5;
  const int head = r;
  {
    u64 pc_; asm volatile("s_getpc_b64 %0" : "=s"(pc_));
    touch((const char*)pc_, min((b & ~7) * 256 + (tid >> 5) * 128, CODE_PF));
    *(LAS u32*)(L + L_FLG + (tid & 127) * 4) = 0u;
  }
  int tn = wave < 2 ? (XT ? 28 : 56) : 85 - 24 * wave, ti = (XT ? b & 7 : xp) * 64 + li * 2 + wave;
  asm volatile("" : "+s"(tn), "+s"(ti));
  ti = wave < 2 ? ti : b;
  u32 tvo = min(ti * tn + min(lane, tn - 1), wave == 3 ? 3135 : wave == 2 ? 9407 : 14335) * 128, xvo = (tile * 128 + w2 * 32 + (lane >> 3)) * 896 + (((lane & 7) ^ (lane >> 3)) * 16);
  asm volatile("s_waitcnt lgkmcnt(0)\n\ts_barrier" : "+v"(tvo), "+v"(xvo) :: "memory");
  asm volatile("" :: "s"(x), "s"(wqkv), "s"(wphi), "s"(wo), "s"(wg), "s"(wu), "s"(wd), "s"(out), "s"(ws));
  const u32 fix0_ = (u32)__builtin_amdgcn_s_memrealtime();
  *(volatile LAS u32*)(L + L_STAMP + wave * 4) = fix0_;
  u64 epoch;
  if (wave < 4) {
    const char* tb_ = wave < 2 ? x : wave == 2 ? wqkv : wo;
    asm volatile("" : "+s"(tb_));
    touch(tb_, tvo);
  }
  asm volatile("s_load_dwordx2 %0, %1, 0x0" : "=s"(epoch) : "s"(ws) : "memory");
  if (wave >= 4) {
    const __amdgpu_buffer_rsrc_t rx = __builtin_amdgcn_make_buffer_rsrc((void*)(x - 2048), 0, 2048 * 896 + 2048, 0x00020000);
    sfor<0, 7>([&](auto NC) LAM {
      dma_x<4 * NC.value>(rx, xvo, w2 * 28672);
      if (NC.value == X0A) { asm volatile("s_waitcnt vmcnt(%0)" :: "n"(4 * X0A - 10) : "memory"); lds_signal(L, S_X0, w2); }
    });
  }
  asm volatile("s_waitcnt lgkmcnt(0)" : "+s"(epoch) :: "memory");
  const __amdgpu_buffer_rsrc_t rs = __builtin_amdgcn_make_buffer_rsrc((void*)ws, 0, WS_USED, 0x00020000);
  const u32x4 ones = {0x3f803f80u, 0x3f803f80u, 0x3f803f80u, 0x3f803f80u};
  const f32x4 z4 = {0.f, 0.f, 0.f, 0.f};
  auto role = [&](auto AC) __attribute__((always_inline)) {
  constexpr bool A = AC.value;
  if (A) asm volatile("; A" ::: "memory"); else asm volatile("; B" ::: "memory");
  u32x4 rp[8];
  const int fvo = lane < 16 ? lane * 8 : NOWHERE;
  const int uidx = (tile * 10 + __builtin_popcount(~0x2A54u & ((1u << head) - 1u))) * 4 + w2, unit = uidx * 49 / 80;
  const bool has_unit = !((0x2A54 >> head) & 1) && (uidx + 1) * 49 / 80 != unit;
  const int u3 = unit >> 1, umat = unit & 1, ukq = u3 / 28, ufl = u3 - 28 * ukq;
  const char* gusrc = ws + WS_WGU + 7 * r * SLOT;
  const u32 guo = WS_WGU + 7 * r * SLOT + w2 * 7168;
#define GUBLK(j) dma7<(j) * SLOT, gu_slot(j) * SLOT>(rs, lane * 16, guo, w2 * 7168)
  const u32 wvo = (lane >> 2) * 2688 + (((lane & 3) ^ (2 * (lane >> 5))) * 16);
  const char* wbase = wqkv + head * 56 - (head & 1) * 8 + 16 * w2 * 2688;
  const u32 wr0 = (8 * g + q4) * 64 + (((p4 + (head & 1)) ^ (4 * (g & 1))) * 8), wr1 = (8 * g + q4) * 64 + ((((4 + p4 + (head & 1)) & 7) ^ (4 * (g & 1))) * 8);
  const int tb = w2; constexpr int kh = A ? 0 : 1;
  const int hho = WS_HH + (((tile * 2 + (w2 >> 1)) * 4 + (r >> 2)) * 56 + (r & 3) * 2 + (w2 & 1)) * 1024;
  const int tok = tile2 * 128 + tg * 64 + tb * 16 + l15;
  const int hvo = WS_H + ((tok >> 5) * 28 + 4 * cg + (g >> 1)) * 1024 + ((g & 1) * 32 + (tok & 31)) * 16;
  const int sto = WS_ST + (2 * cg * 2048 + tile2 * 128 + tg * 64 + tb * 16) * 8;
  const u32x4 empty4 = {EMPTY, EMPTY, EMPTY, EMPTY};
#define HH_CLEAR(i0, i1) _Pragma("unroll") for (int i = i0; i < i1; ++i) ST16(rs, empty4, lane * 16 + (i & 1) * 2048, WS_HH + (((tile2 * 2 + r2 - 14) * 4 + w2) * 56 + kh + (i >> 1) * 4) * 1024)
#define HH_FLAG() FLAG(WS_FH + ((tile2 * 4 + 2 * (r2 - 14) + kh) * 32 + 28 + w2) * 128, 1)
  u32x4 wof[8][4];
  f32x4 csp[7];
  u64 tag;
#define WOF_READ(i0, i1) do { u32 wa_ = L_WO + (kh * 224 + 4 * g + q4) * 128 + 16 * p4 + 64 * (q4 >> 1), wb_ = wa_ ^ 64, wc_ = wa_ + (g == 3 ? 0 : 2048), wd_ = wc_ ^ 64; \
    asm volatile("" : "+v"(wa_), "+v"(wb_), "+v"(wc_), "+v"(wd_)); \
    _Pragma("unroll") for (int i = i0; i < i1; ++i) _Pragma("unroll") for (int k = 0; k < 4; ++k) { \
      const u32x2 a_ = trread(L + (k >> 1 ? wb_ : wa_) + 28 * i * 128 + (k & 1) * 8), c_ = trread(L + (k >> 1 ? wd_ : wc_) + 28 * i * 128 + (k & 1) * 8); \
      wof[i][k] = u32x4{a_.x, a_.y, c_.x, c_.y}; } } while (0)
#define XF_READ(i) _Pragma("unroll") for (int s = 0; s < 14; ++s) xf[i][s] = *(LAS u32x4*)((s & 1 ? xr1 : xr0) + (i) * 14336 + (s >> 1) * 2048)
#define LN_STATS(kh) do { f32x4 g1 = z4, g2 = z4; \
    _Pragma("unroll") for (int s = 0; s < 14; ++s) { g1 = mfma16(ones, xf[kh][s], g1); g2 = mfma16(xf[kh][s], xf[kh][s], g2); } \
    const float d01 = (p4 & 1) ? g2[1] : g2[0], d23 = (p4 & 1) ? g2[3] : g2[2], s2 = (p4 & 2) ? d23 : d01; \
    const float nmu = g1[0] * (-1.f / 448.f), var = fmaxf(s2 * (1.f / 448.f) - nmu * nmu, 0.f); \
    if (g == q4) *(LAS f32x2*)(L + L_ST1 + (w2 * 32 + kh * 16 + l15) * 8) = f32x2{nmu, __builtin_amdgcn_rsqf(var + 1e-5f)}; } while (0)
#define WPF() do { u32 wp[8]; \
    _Pragma("unroll") for (int j = 0; j < 8; ++j) wp[j] = *(LAS u32*)(L + L_WPHI + 4 * g * 56 + min(l15, 13) * 4 + ((j & 3) + 16 * (j >> 2)) * 56); \
    _Pragma("unroll") for (int e = 0; e < 4; ++e) { const bool z = e >= 2 && g == 3; \
      wpf[0][e] = z ? 0u : __builtin_amdgcn_perm(wp[2 * e + 1], wp[2 * e], 0x05040100u); wpf[1][e] = z ? 0u : __builtin_amdgcn_perm(wp[2 * e + 1], wp[2 * e], 0x07060302u); } \
    asm volatile("" : "+v"(wpf[0]), "+v"(wpf[1])); } while (0)
  {
    u32x4 xf[2][14], wpf[2];
    f32x2 st[2];
    LAS char* xr0 = L + w2 * 28672 + l15 * 128 + ((g ^ (l15 & 7)) * 16);
    LAS char* xr1 = L + w2 * 28672 + l15 * 128 + (((4 + g) ^ (l15 & 7)) * 16);
    LAS char* pw = L + L_PKV + (w2 * 32 + l15) * 224 + g * 8;
    const char* wos = wo + (r2 < 14 ? cg : 0) * 128 + w2 * 56 * 896;
    const u32 wovo = (lane >> 3) * 896 + (((lane & 7) ^ ((lane >> 2) & 4)) * 16);
#define WO_HALF(hf_) _Pragma("unroll") for (int n = 0; n < 7; ++n) dma16(wos + ((hf_) * 224 + n * 8) * 896, wovo, L_WO + ((hf_) * 28 + w2 * 7 + n) * 1024)
    int clo = WS_O + ((tile * 8 + 2 * w2) * 16 + head) * 1024, clh = r2 < 14 ? hvo : NOWHERE, cls = r2 < 14 && lane < 16 ? (lane >> 3) * 16384 + (lane & 7) * 16 : NOWHERE, clt = sto;
    int kvo = WS_KVP + ((head * 16 + tile) * 4 + w2) * 1024, fko = WS_FKV + ((head * 4 + w2) * 16 + tile) * 128;
    u32x4 em4 = empty4;
    u32 wtv = min(lane, 24) * 128;
    const char* wts = wo + min(li * 98 + w2 * 25, 3136 - 25) * 128;
    auto clear = [&](int c) LAM {
      if (c == 0) ST16(rs, em4, lane * 16, clo);
      if (c == 1) ST16(rs, em4, lane * 16, clo + 16384);
      if (c == 2) ST16(rs, em4, clh, 0);
      if (c == 3) ST16(rs, em4, clh, 2048);
      if (c == 4) ST16(rs, em4, cls, clt);
    };
    if constexpr (A) {
      if (wave == 2) { dma16(wphi + head * 1568, lane * 16, L_WPHI); dma16(wphi + head * 1568, min(lane + 64, 97) * 16, L_WPHI + 1024); }
      __builtin_amdgcn_s_waitcnt(0x0F70);
      tag = epoch * 6364136223846793005ull + 1442695040888963407ull;
      STAMP(1);
#define WKLOAD() _Pragma("unroll") for (int i = 0; i < 7; ++i) dma16(wbase + 896 + 64 * i * 2688, wvo, L_WK + (4 * i + w2) * 1024)
      if (KAT == 3) { WKLOAD(); if (wave == 2) { WAITVM(7); lds_signal(L, S_PHI, 0); } }
      else if (wave == 2) { if (CLF == 9) WAITVM(5); else WAITVM(0); lds_signal(L, S_PHI, 0); }
      if (KAT > 3 && KAT < 28) { lds_wait1(L, S_SPARE, w2); WKLOAD(); }
      lds_wait1(L, S_PHI, 0);
      WPF();
#define DBITE() dma4(wd + min(b * 49 + w2 * 13, 12544 - 13) * 128, min(lane, 12) * 128, L_JUNK)
      if (TD == 9) {
        __builtin_amdgcn_s_sleep(TS0);
        if (w2 & 1) __builtin_amdgcn_s_sleep(TS1);
        if (w2 & 2) { __builtin_amdgcn_s_sleep(TS1); __builtin_amdgcn_s_sleep(TS1); }
        DBITE();
      }
      if (TD == 10 && w2 == 0) DBITE();
      lds_wait1(L, S_X0, w2);
      XF_READ(0);
      if (w2 < 2) lds_signal(L, S_XF0, w2);
      if (TD == 10 && w2 == 1) DBITE();
      if (KAT > 28) { WKLOAD(); }
      if (TD == 10 && w2 == 2) DBITE();
      if (TD == 11) DBITE();
      STAMP(27);
      LN_STATS(0);
      SCHED();
      STAMP(2);
      lds_wait1(L, S_X1, w2);
      XF_READ(1);
      if (QEARLY == 3 && w2 < 2) lds_signal(L, S_SPARE, w2);
      STAMP(3);
      if (SBA) LN_STATS(1);
      WAITVM(0);
      STAMP(4);
      lds_signal(L, S_WK, w2);
      if (TD == 10 && w2 == 3) DBITE();
      if (TD == 12) DBITE();
      f32x4 t[2][2];
      u32 pr = L_PKV + (4 * g + q4) * 224 + 8 * p4;
      asm volatile("" : "+v"(pr), "+v"(pw));
      asm volatile("" : "+s"(clo), "+v"(clh), "+v"(cls), "+s"(clt), "+s"(kvo), "+s"(fko), "+s"(tag), "+v"(em4));
      if (WOT == 5) asm volatile("" : "+v"(wtv), "+s"(wts));
      project<0, S_CSK, 8>(L, L_WK + wr0, L_WK + wr1, xf, w2, lane, L_CSP, t, st, [&]() LAM {
        lds_wait(L, S_WK);
        STAMP(5);
        __builtin_amdgcn_s_setprio(2);
      }, [&](int n, LAS char* sg, u32 one) LAM {
        if (n == KGO) *(volatile LAS u32*)(sg + S_KDONE * 16) = one;
      });
      STAMP(6);
      u32x4 P[2][2];
      __builtin_amdgcn_s_setprio(FPRIO);
      feature_map(t, st, wpf, g, P, [&](int i) LAM {
        if (WOT == 5 && i == 0) { SCHED(); dma4(wts, wtv, L_JUNK); SCHED(); }
        if (CLF == 1 && i > 0) { SCHED(); clear(i - 1); SCHED(); }
        if (CLF == 2 && i == 5) { SCHED(); clear(0); clear(1); clear(2); clear(3); clear(4); SCHED(); }
        if (CLF == 3 && i >= 3) { SCHED(); clear(2 * i - 6); clear(2 * i - 5); SCHED(); }
        if (CLF == 4 && i == 1) { SCHED(); clear(0); clear(1); clear(2); clear(3); clear(4); SCHED(); }
      });
#pragma unroll
      for (int c = 0; c < 4; ++c) {
        LAS char* p = pw + (c >> 1) * 16 * 224 + (c & 1) * 64;
        *(LAS u32x2*)(p) = u32x2{P[c >> 1][c & 1].x, P[c >> 1][c & 1].y}; *(LAS u32x2*)(p + 32) = u32x2{P[c >> 1][c & 1].z, P[c >> 1][c & 1].w};
      }
      lds_signal(L, S_PHIK, w2);
      STAMP(7);
      if (CLF == 0 || CLF == 8) { clear(0); clear(1); }
      if (CLF == 0 || CLF == 7) { clear(2); clear(3); clear(4); }
      lds_wait2(L, S_V, S_PHIK);
      u32x4 pa[4], pb[8];
#pragma unroll
      for (int m = 0; m < 4; ++m) {
        pa[m] = trfrag(L + pr + w2 * 32 + m * 32 * 224, 16 * 224);
        pb[2 * m] = trfrag(L + pr + m * 32 * 224 + 128, 16 * 224); pb[2 * m + 1] = trfrag(L + pr + m * 32 * 224 + 160, 16 * 224);
      }
      SCHED();
      f32x4 kv[2] = {};
#pragma unroll
      for (int m = 0; m < 8; ++m) kv[m & 1] = mfma16(pa[m >> 1], pb[m], kv[m & 1]);
      const u32x4 kvb = {pk(kv[0][0], kv[0][1]), pk(kv[0][2], kv[0][3]), pk(kv[1][0], kv[1][1]), pk(kv[1][2], kv[1][3])};
      ST16(rs, kvb, lane * 16, kvo);
      STAMP(8);
      DRAIN();
      FLAG(fko, 1);
      __builtin_amdgcn_s_setprio(0);
      STAMP(9);
      if (HW1) { lds_signal(L, S_XA, w2); lds_wait(L, S_XA); }
#define DTOUCH() dma4(wd + b * 49 * 128, min(lane, 48) * 128, L_JUNK)
      if (TD == 2 && wave == 0) DTOUCH();
      if (r2 < 14 || !ISK) { WO_HALF(0); WO_HALF(1); } else { HH_CLEAR(0, NE); if (NE < 8) __builtin_amdgcn_s_sleep(20); }
      poll_flags(ws, WS_FKV + ((head * 4 + w2) * 16 + l15) * 128, lane < 16, tag);
      lds_signal(L, S_WO, w2);
      STAMP(10);
      u32x4 tt[16];
#pragma unroll
      for (int i = 0; i < 16; ++i) tt[i] = __builtin_bit_cast(u32x4, __builtin_amdgcn_raw_buffer_load_b128(rs, lane * 16, WS_KVP + head * 65536 + w2 * 1024 + i * 4096, 16));
      u32 fgo;
      {
        const int pl = (tile & 1) * 16 + min(lane >> 2, 13);
        fgo = wave == 0 ? WS_FKV + ((l15 * 4 + 2 * tg + (g & 1)) * 16 + tile2) * 128
            : wave == 1 ? (MAP == 2 ? WS_FKV + (((4 * ((tile >> 1) & 3) + (pl & 3)) * 4 + p4) * 16 + (tile & 8) + (pl >> 2)) * 128 : MAP ? WS_FKV + ((min(lane >> 2, 13) * 4 + p4) * 16 + tile) * 128 : WS_FKV + (((((tile >> 1) & 1) * 8 + (pl & 7)) * 4 + p4) * 16 + (tile & 12) + (pl >> 3)) * 128)
                        : WS_FG + (min(((7 * r) >> 2) + g, (7 * r + 6) >> 2) * 16 + (l15 & 8) + min(lane & 7, 6)) * 128;
      }
      u64 fg = __hip_atomic_load((const GAS u64*)(ws + fgo), RLX_AGENT);
      SCHED();
      const u32 one = g == q4 ? ((p4 & 1) ? 0x3f800000u : 0x3f80u) : 0u, s01 = p4 < 2 ? one : 0u, s23 = p4 < 2 ? 0u : one;
      const u32x4 sel0 = {s01, s23, 0u, 0u}, sel1 = {0u, 0u, s01, s23};
      f32x4 s0 = {}, s1 = {};
#pragma unroll
      for (int i = 0; i < 16; ++i) { s0 = mfma16(sel0, tt[i], s0); s1 = mfma16(sel1, tt[i], s1); }
      LAS char* kw = L + L_KVA + (w2 >> 1) * 1024 + lane * 16 + (w2 & 1) * 8;
      *(LAS u32x2*)(kw) = u32x2{pk(s0[0], s0[1]), pk(s0[2], s0[3])};
      *(LAS u32x2*)(kw + 2048) = u32x2{pk(s1[0], s1[1]), pk(s1[2], s1[3])};
      lds_signal(L, S_KVA, w2);
      if (TD == 4 && wave == 2) DTOUCH();
      STAMP(11);
      if (WOE == 3) { WO_HALF(1); WAITVM(0); lds_signal(L, S_WO, w2); }
      asm volatile("" : "+v"(fg) :: "memory");
      if (!__all(fg == tag + 1)) poll_flags(ws, fgo, true, tag);
      __builtin_amdgcn_fence(__ATOMIC_ACQUIRE, "wavefront");
      lds_signal(L, S_WOFA, w2);
      lds_wait1(L, S_WOFA, 3);
      const int fl = min(((7 * r) >> 2) + w2, (7 * r + 6) >> 2);
#pragma unroll
      for (int i = 0; i < 7; ++i) asm volatile("global_load_dwordx4 %0, %1, %2 offset:%3 sc1" : "=v"(csp[i]) : "v"(((7 * r) >> 2) + w2 == fl ? lane * 16 : 0), "s"(ws + WS_CS + fl * 7168 + 4096), "n"(i * 1024 - 4096) : "memory");
      GUBLK(2);
      if (r2 < 14 || !ISK) { lds_wait(L, S_WO); WOF_READ(0, 8); }
      SCHED();
#define CS_OUT(k_) do { asm volatile("s_waitcnt vmcnt(%7)" : "+v"(csp[0]), "+v"(csp[1]), "+v"(csp[2]), "+v"(csp[3]), "+v"(csp[4]), "+v"(csp[5]), "+v"(csp[6]) : "n"(k_) : "memory"); \
        const f32x4 sm = ((csp[0] + csp[1]) + (csp[2] + csp[3])) + ((csp[4] + csp[5]) + csp[6]); \
        const float e0 = swadd32(sm[0], sm[1]), e1 = swadd32(sm[2], sm[3]);     \
        const int j0 = 4 * (((7 * r) >> 2) + w2) + 2 * (lane & 1) - 7 * r, cr = ((g & 1) * 16 + hh * 8 + (l15 >> 1)) * 4; \
        if (j0 >= 0 && j0 < 7) *(LAS float*)(L + L_CS + j0 * 128 + cr) = e0; \
        if (j0 + 1 >= 0 && j0 + 1 < 7) *(LAS float*)(L + L_CS + (j0 + 1) * 128 + cr) = e1; } while (0)
      lds_signal(L, S_WOFB, w2);
      if (!CSL) CS_OUT(7);
      STAMP(24);
    } else {
      const int rg = lane >> 3;
      const u32 lvo = rg * 8 * 3584 + (lane & 7) * 16;
      const char* us = (umat ? wu : wg) + ukq * 64 * 3584 + ufl * 128;
      asm volatile("" : "+s"(us));
      auto repack = [&]() LAM {
        if (has_unit) {
          const int so = rg * 512 + (lane & 7) * 16;
          const int sb = WS_WGU + 4 * ufl * SLOT + ukq * 4096 + umat * 256;
          const int s1 = SLOT, s2 = 2 * SLOT;
          float c[8];
#pragma unroll
          for (int e = 0; e < 8; ++e) {
            const u32x4 f = piece(rp, e);
            ST16(rs, f, so + (e & 1) * 128, sb + ((e >> 1) & 1) * s1 + (e >> 2) * s2);
            c[e] = mfma16(ones, f, z4)[0];
          }
          const f32x4 cv = g & 1 ? f32x4{c[4], c[5], c[6], c[7]} : f32x4{c[0], c[1], c[2], c[3]};
          ST16(rs, cv, g < 2 ? (l15 >> 3) * 512 + (lane & 7) * 32 + g * 16 : NOWHERE, WS_CS + ufl * 7168 + ukq * 1024 + (umat & 1) * 256);
        }
      };
#define RPLOAD() _Pragma("unroll") for (int j = 0; j < 8; ++j) asm volatile("global_load_dwordx4 %0, %1, %2" : "=v"(rp[j]) : "v"(lvo), "s"(us + j * 3584) : "memory")
      if (RPO == 2 && has_unit) { RPLOAD(); }
      tag = epoch * 6364136223846793005ull + 1442695040888963407ull;
      STAMP(1);
      XF_READ(0);
      if (w2 < 2) lds_signal(L, S_XF0, 2 + w2);
      if (RPO == 2 && has_unit) WAITVM(8); else WAITVM(0);
      lds_signal(L, S_X1, w2);
      if (RPO == 1 && has_unit) { RPLOAD(); }
      STAMP(2);
      if (STF < 7) lds_wait(L, S_XF0);
      XF_READ(1);
      if (QEARLY == 3 && w2 < 2) lds_signal(L, S_SPARE, 2 + w2);
      SCHED();
#define WDST(c) ((c) * 1024 + ((c) >= 14 ? SLOT - 14336 : 0))
#pragma unroll
      for (int i = 0; i < 7 - STF; ++i) dma16(wbase + 1792 + 64 * i * 2688, wvo, L_WV + WDST(4 * i + w2));
      STAMP(3);
      __builtin_amdgcn_s_setprio(PB0);
      if (!SBA) LN_STATS(1);
      lds_signal(L, S_CS, w2);
      if (STF == 7) lds_wait(L, S_XF0);
#pragma unroll
      for (int i = 7 - STF; i < 7; ++i) dma16(wbase + 1792 + 64 * i * 2688, wvo, L_WV + WDST(4 * i + w2));
      STAMP(4);
      f32x4 t[2][2];
      asm volatile("" : "+v"(pw));
      if (WOT == 4) asm volatile("" : "+v"(wtv), "+s"(wts));
      project<SLOT - 14336, S_CSV, 4>(L, L_WV + wr0, L_WV + wr1, xf, w2, lane, L_CSP + 256, t, st, [&]() LAM {
      if (QEARLY == 3) {
        lds_wait(L, S_SPARE);
#pragma unroll
        for (int i = 0; i < 7; ++i) dma16(wbase + 64 * i * 2688, wvo, L_WQ + WDST(4 * i + w2));
      }
      if (REPE == 3) { asm volatile("s_waitcnt vmcnt(%8)" : RP_OPS : "n"(QEARLY == 3 ? 14 : 7) : "memory"); repack(); if (has_unit) { if (QEARLY == 3) WAITVM(16); else WAITVM(9); } }
      if (RPO == 0 && has_unit) { RPLOAD(); } else if (REPE == 3 && has_unit) {} else if (QEARLY == 3) WAITVM(7); else WAITVM(0);
      STAMP(28);
      if (RPO) {} else if (QEARLY == 3) WAITVM(15); else WAITVM(8);
      lds_signal(L, S_WV, w2);
      STAMP(29);
      lds_wait2(L, S_WV, S_WK);
      STAMP(30);
      if (QEARLY == 1) {
#pragma unroll
        for (int i = 0; i < 7; ++i) dma16(wbase + 64 * i * 2688, wvo, L_WQ + WDST(4 * i + w2));
      }
        if (REPE == 2) { asm volatile("s_waitcnt vmcnt(7)" : RP_OPS :: "memory"); repack(); }
        if (TD == 8 && !has_unit && !((0x2A54 >> head) & 1)) dma4(wd + min((uidx - unit) * 51, 12544 - 51) * 128, min(lane, 50) * 128, L_JUNK);
        if (KGO >= 0) lds_wait1(L, S_KDONE, w2);
        STAMP(5);
        __builtin_amdgcn_s_setprio(VPRIO0);
      }, [&](int n, LAS char*, u32) LAM {
        if (n == VPN) __builtin_amdgcn_s_setprio(VPRIO);
        if (WOT == 4 && n == WTN) dma4(wts, wtv, L_JUNK);
        if (QEARLY == 2 && n >= QN0 && (n - QN0) % QST == 0 && (n - QN0) / QST < 7) { const int i = (n - QN0) / QST; dma16(wbase + 64 * i * 2688, wvo, L_WQ + WDST(4 * i + w2)); }
      });
#pragma unroll
      for (int c = 0; c < 4; ++c) t[c >> 1][c & 1] *= st[c >> 1].y;
      STAMP(6);
#pragma unroll
      for (int c = 0; c < 2; ++c) {
        LAS char* p = pw + c * 16 * 224 + 128;
        *(LAS u32x2*)(p) = u32x2{pk(t[c][0][0], t[c][0][1]), pk(t[c][0][2], t[c][0][3])};
        *(LAS u32x2*)(p + 32) = g == 3 ? u32x2{0x3f80u, 0u} : u32x2{pk(t[c][1][0], t[c][1][1]), pk(t[c][1][2], t[c][1][3])};
      }
      lds_signal(L, S_V, w2);
      __builtin_amdgcn_s_setprio(0);
      STAMP(7);
      if (!QEARLY) {
#pragma unroll
        for (int i = 0; i < 7; ++i) dma16(wbase + 64 * i * 2688, wvo, L_WQ + WDST(4 * i + w2));
      }
      if (WOE == 0) WO_HALF(0);
      if (WOE < 2) { lds_wait(L, S_KDONE); WO_HALF(1); }
      if (REPE == 0) asm volatile("s_waitcnt vmcnt(%8)" : RP_OPS : "n"(QEARLY == 3 && !RPO ? 0 : 7) : "memory");
#define UFLAG() FLAGC(has_unit, WS_FG + ((ufl * 2 + umat) * 8 + ukq) * 128, 1)
      if (REPE == 0) repack();
      STAMP(8);
      WPF();
      if (!has_unit || (REPE == 3 && QEARLY != 3)) WAITVM(0); else WAITVM(9);
      if (UFN < 0) { WAITVM(0); UFLAG(); }
      lds_signal(L, S_WQ, w2);
      project<SLOT - 14336, S_CSQ, 4>(L, L_WQ + wr0, L_WQ + wr1, xf, w2, lane, L_CSP + 512, t, st, [&]() LAM {
        lds_wait(L, S_WQ);
        STAMP(9);
      }, [&](int n, LAS char*, u32) LAM {
        if (n == UFN) { WAITVM(0); UFLAG(); }
      });
      STAMP(10);
      if (REPLATE == 2) repack();
      u32x4 pq[2][2];
      feature_map(t, st, wpf, g, pq, [](int) {});
      asm volatile("" : "+v"(pq[0][0]), "+v"(pq[0][1]), "+v"(pq[1][0]), "+v"(pq[1][1]));
      STAMP(11);
      if (REPLATE == 1) repack();
      if (TD == 13 || TD == 14) {
        lds_wait(L, S_WO);
        if (r2 < 14) WOF_READ(0, TWF); else __builtin_amdgcn_s_sleep(2 * TWF);
        SCHED();
        if (TD == 14) DBITE(); else if (w2 == 0) DTOUCH();
        SCHED();
        if (r2 < 14) WOF_READ(TWF, 8);
      } else if (r2 < 14 || !ISK) { lds_wait(L, S_WO); WOF_READ(0, 8); }
      STAMP(24);
      if (REPLATE == 2) { WAITVM(0); UFLAG(); }
      __builtin_amdgcn_s_setprio(OPRIO);
      lds_wait(L, S_KVA);
      if (REPLATE == 1) { WAITVM(0); UFLAG(); }
      STAMP(27);
      u32x4 ka[4];
#pragma unroll
      for (int i = 0; i < 4; ++i) ka[i] = *(LAS u32x4*)(L + L_KVA + i * 1024 + lane * 16);
      f32x4 o[2][2];
#pragma unroll
      for (int c = 0; c < 4; ++c) o[c >> 1][c & 1] = mfma16(ka[2 * (c & 1) + 1], pq[c >> 1][1], mfma16(ka[2 * (c & 1)], pq[c >> 1][0], z4));
      const f32x2 da = swp16(o[0][1][0], o[0][1][0]), db = swp16(o[1][1][0], o[1][1][0]);
      const float den[2] = {swp32(da.y, da.y).y, swp32(db.y, db.y).y};
#pragma unroll
      for (int c = 0; c < 2; ++c) {
        const float rd = frcp(den[c] + 1e-6f), r3 = g == 3 ? 0.f : rd;
        const u32x4 ob = {pk(o[c][0][0] * rd, o[c][0][1] * rd), pk(o[c][0][2] * rd, o[c][0][3] * rd), pk(o[c][1][0] * r3, o[c][1][1] * r3), filled(pk(o[c][1][2] * r3, o[c][1][3] * r3))};
        ST16D(DUP & 1, rs, ob, lane * 16, WS_O + ((tile * 8 + 2 * w2 + c) * 16 + head) * 1024);
      }
      STAMP(12);
      if (WOE == 3) lds_wait(L, S_WO);
      *(volatile LAS u32*)(L + L_FLG + S_W2A * 16 + w2 * 4) = (u32)__builtin_amdgcn_s_memrealtime();
      lds_signal(L, S_W2B, w2);
      __builtin_amdgcn_s_setprio(0);
      if (TD == 3 && w2 == 0) DTOUCH();
    }
  }
  STAMP(13);
  u32x4 hres = {0u, 0u, 0u, 0u};
  const int xo = tok * 896 + cg * 128 + kh * 64 + g * 16;
  STAMP(23);
  if (r2 < 14) {
    u32x4 xr, of[8];
    asm volatile("global_load_dwordx4 %0, %1, %2" : "=v"(xr) : "v"(xo), "s"(x) : "memory");
    lds_wait2(L, S_W2B, S_WOFA);
#define WIN2() do { if (A ? W1 < 2 : W1 < 3) GUBLK(A ? 0 : 1); } while (0)
    if (W2F == 1 || W2F == 3) WIN2();
    if (W2F == 4 && !A) { GUBLK(0); GUBLK(1); }
    if (W2F == 5 && !A) GUBLK(1);
    if (T2) {
      const u32x4 ts = *(volatile LAS u32x4*)(L + L_FLG + S_W2A * 16);
      const u32 t0 = __builtin_amdgcn_readfirstlane(ts.x), due = t0 + T2 + __builtin_amdgcn_readfirstlane(max(max(0, (int)(ts.y - ts.x)), max((int)(ts.z - ts.x), (int)(ts.w - ts.x))));
#pragma nounroll
      for (u32 sp = 0; sp < 64 && (int)((u32)__builtin_amdgcn_s_memrealtime() - due) < 0; ++sp) __builtin_amdgcn_s_sleep(1);
    } else __builtin_amdgcn_s_sleep(A ? D2A : D2B);
    STAMP(14);
#define OFA(c_) _Pragma("unroll") for (int i = 0; i < 8; ++i) \
      asm volatile("global_load_dwordx4 %0, %1, %2 offset:%3 sc1" : c_(of[i]) : "v"(lane * 16), "s"(ws + WS_O + ((tile2 * 8 + tg * 4 + tb) * 16 + kh * 8 + 4) * 1024), "n"(i * 1024 - 4096) : "memory")
#define OFW(k_) asm volatile("s_waitcnt vmcnt(%9)" : "+v"(of[0]), "+v"(of[1]), "+v"(of[2]), "+v"(of[3]), "+v"(of[4]), "+v"(of[5]), "+v"(of[6]), "+v"(of[7]), "+v"(xr) : "n"(k_) : "memory")
#define OFBAD() (!__all(max(max(max(of[0].w, of[1].w), max(of[2].w, of[3].w)), max(max(of[4].w, of[5].w), max(of[6].w, of[7].w))) != EMPTY))
    OFA("=v");
    f32x4 a[4] = {};
    u32 mx = 0;
#pragma unroll
    for (int i = 0; i < 8; ++i) {
      asm volatile("s_waitcnt vmcnt(%1)" : "+v"(of[i]) : "n"(7 - i) : "memory");
      if (i == PG - 1) { lds_wait(L, S_WOFB); }
      if (i == 7) GUBLK(A ? 0 : 3);
#pragma unroll
      for (int k = 0; k < 4; ++k) a[k] = mfma16(wof[i][k], of[i], a[k]);
      mx = max(mx, of[i].w);
      SCHED();
    }
    asm volatile("" : "+v"(xr));
    if (__builtin_expect(!__all(mx != EMPTY), 0)) {
#pragma nounroll
      for (u32 sp = 0; sp < SPIN_CAP; ++sp) { __builtin_amdgcn_s_sleep(RSL); OFA("+v"); OFW(0); if (!OFBAD()) break; }
#pragma unroll
      for (int i = 0; i < 8; ++i)
#pragma unroll
        for (int k = 0; k < 4; ++k) a[k] = mfma16(wof[i][k], of[i], i ? a[k] : z4);
    }
    STAMP(25);
    *(LAS f32x4*)(L + L_EX + wave * 2048 + lane * 16) = kh ? a[0] : a[2];
    *(LAS f32x4*)(L + L_EX + wave * 2048 + 1024 + lane * 16) = kh ? a[1] : a[3];
    lds_signal(L, A ? S_EXA : S_EXB, w2);
    lds_wait1(L, A ? S_EXB : S_EXA, w2);
    STAMP(15);
    const f32x4 c0 = (kh ? a[2] : a[0]) + *(LAS f32x4*)(L + L_EX + (wave ^ 4) * 2048 + lane * 16);
    const f32x4 c1 = (kh ? a[3] : a[1]) + *(LAS f32x4*)(L + L_EX + (wave ^ 4) * 2048 + 1024 + lane * 16);
    const f32x4 h0 = c0 + f32x4{blo(xr.x), bhi(xr.x), blo(xr.y), bhi(xr.y)}, h1 = c1 + f32x4{blo(xr.z), bhi(xr.z), blo(xr.w), bhi(xr.w)};
    hres = u32x4{pk(h0[0], h0[1]), pk(h0[2], h0[3]), pk(h1[0], h1[1]), filled(pk(h1[2], h1[3]))};
    ST16D(DUP & 2, rs, hres, hvo, kh * 2048);
    const f32x4 r0 = {blo(hres.x), bhi(hres.x), blo(hres.y), bhi(hres.y)}, r1 = {blo(hres.z), bhi(hres.z), blo(hres.w), bhi(hres.w)};
    const f32x4 a1 = r0 + r1, a2q = r0 * r0 + r1 * r1;
    float s1 = (a1[0] + a1[1]) + (a1[2] + a1[3]), s2 = (a2q[0] + a2q[1]) + (a2q[2] + a2q[3]);
    { const float t = swadd16(s1, s2); const f32x2 a2 = sw32(t); s1 = a2.x + a2.y; }
    { const u32x2 sv = __builtin_bit_cast(u32x2, sw16(s1)); __builtin_amdgcn_raw_buffer_store_b64(u32x2{sv.x, filled(sv.y)}, rs, fvo, sto + kh * 16384, 16);
      if (DUP & 2) __builtin_amdgcn_raw_buffer_store_b64(u32x2{sv.x, filled(sv.y)}, rs, fvo, sto + kh * 16384, 0); }
    if (DUP & 2) WAITVM(4); else WAITVM(2);
  } else {
    if (TD == 1) dma4(wd + ((tile2 * 2 + r2 - 14) * 8 + wave) * 49 * 128, min(lane, 48) * 128, L_JUNK);
    lds_wait2(L, S_W2B, S_WOFA);
    if (CF) { HH_CLEAR(A ? NE : 0, 4 * NCL); }
    WIN2();
    if (W2F >= 3) { lds_wait(L, S_WOFB); if (W2F < 5 || A) GUBLK(A ? 3 : 4); }
    if (!CF) { HH_CLEAR(A ? NE : 0, 4 * NCL); }
    DRAIN();
    if (!CE2) HH_FLAG();
    if (CE2 && A) __builtin_amdgcn_s_sleep(DLY5);
  }
  if constexpr (A && CSL) CS_OUT(DUP & 2 ? 4 : 2);
  lds_signal(L, A ? S_HFA : S_HFB, w2);
  STAMP(16);
  u32x4 wdf[NWD][8];
  const __amdgpu_buffer_rsrc_t rd = __builtin_amdgcn_make_buffer_rsrc((void*)(wd + w2 * 448 * 896 + min(cg, 6) * 128 - 1024), 0, 448 * 896 + 1024, 0x00020000);
  const u32 dvo = (lane >> 3) * 8 * 896 + (((lane & 7) ^ ((lane >> 2) & 4)) * 16);
  u32 da_ = 2 * SLOT + w2 * 7168 + hh * 512 + q4 * 128 + (g & 1) * 32 + p4 * 8 + 64 * (q4 >> 1), db_ = da_ ^ 64, dc_ = da_ + (L_EX - 2 * SLOT - w2 * 6144), dd_ = dc_ ^ 64;
  const u32 po = WS_FHH + ((tile2 * 16 + 4 * w2 + ((lane >> 1) & 3)) * 4 + 2 * tg + (lane & 1)) * 128;
  const u32 ho = WS_HH + ((tile2 * 2 + tg) * 4 + w2) * 57344;
#define WDLOAD(k, bf) dma_wd<k>(rd, dvo, ((bf) + 2) * SLOT + w2 * 7168, L_EX + (bf) * 4096 + w2 * 1024)
#define WDREAD(dst, bf) do { asm volatile("" : "+v"(da_), "+v"(db_), "+v"(dc_), "+v"(dd_)); \
    _Pragma("unroll") for (int i = 0; i < 8; ++i) { LAS char* p_ = L + (i & 1 ? db_ : da_) + (bf) * SLOT + (i >> 1) * 2048; \
      const u32x2 x_ = trread(p_), y_ = trread(i < 6 ? p_ + 1024 : L + (i & 1 ? dd_ : dc_) + (bf) * 4096); dst[i] = u32x4{x_.x, x_.y, y_.x, y_.y}; } } while (0)
#define THR() asm volatile("s_waitcnt vmcnt(%0)" :: "n"(THRN) : "memory")
#define THR0() asm volatile("s_waitcnt vmcnt(%0)" :: "n"(THRN0) : "memory")
  u32 have = 0;
#define PLOAD() peek(ws, po, lane < 8, tag + 7)
#define PMIN(pv) do { const u64 d = (pv) - tag; have = max(have, min8(d <= 8 ? (u32)d : 0u)); } while (0)
#define NEEDP(n) do { _Pragma("nounroll") for (u32 sp = 0; have < (n) && sp < SPIN_CAP; ++sp) { PMIN(PLOAD()); if (PSLP && have < (n)) __builtin_amdgcn_s_sleep(PSLP); } \
    __builtin_amdgcn_fence(__ATOMIC_ACQUIRE, "wavefront"); } while (0)
#define HBASE() u32 hb3 = w2 * 7168 + lane * 16, hb0 = hb3 + 2 * SLOT, hb2 = hb0 + 2 * SLOT, hb7 = L_EX + w2 * 1024 + lane * 16; asm volatile("" : "+v"(hb0), "+v"(hb2), "+v"(hb7))
#define HFRAG(bf, i) (*(LAS u32x4*)(L + ((i) < 7 ? ((bf) == 3 ? hb3 : (bf) == 2 ? hb2 : hb0 + (bf) * SLOT) + (i) * 1024 : hb7 + (bf) * 4096)))
#define HQLOAD(q, j) _Pragma("unroll") for (int i = 0; i < 8; ++i) hq[q][i] = __builtin_bit_cast(u32x4, __builtin_amdgcn_raw_buffer_load_b128(rs, lane * 16 + (i & 3) * 1024, ho + (j) * 8192 + (i & 4) * 1024, 16))
#define HQLOADT(q, j) _Pragma("unroll") for (int i = 0; i < 8; ++i) { THR(); hq[q][i] = __builtin_bit_cast(u32x4, __builtin_amdgcn_raw_buffer_load_b128(rs, lane * 16 + (i & 3) * 1024, ho + (j) * 8192 + (i & 4) * 1024, 16)); }
#define DOWN16(ac, wf, hf_) _Pragma("unroll") for (int m = 0; m < 16; ++m) ac[(m >> 1) & 1][m & 1] = mfma32(wf[2 * (m >> 2) + (m & 1)], hf_[2 * (m >> 2) + ((m >> 1) & 1)], ac[(m >> 1) & 1][m & 1])
#define PARTIAL_OUT(ac, b0, b1) _Pragma("unroll") for (int n = 0; n < 16; ++n) { const int T = n >> 3, k = (n >> 2) & 1, q = n & 3; \
    *(LAS f32x4*)&HFRAG(T ? b1 : b0, n & 7) = f32x4{ac[T][k][4 * q], ac[T][k][4 * q + 1], ac[T][k][4 * q + 2], ac[T][k][4 * q + 3]}; }
#define FREE(f) do { _Pragma("nounroll") for (u32 sp = 0; sp < (SPIN_CAP << 4); ++sp) { const u32x4 fr = *(volatile LAS u32x4*)(L + L_FLG + S_DONE4 * 16); \
          if (__builtin_amdgcn_readfirstlane(min(min(fr.x, fr.y), min(fr.z, fr.w))) >= (f)) break; __builtin_amdgcn_s_sleep(BSL); } asm volatile("" ::: "memory"); } while (0)
#define HQBAD(q) (!__all(max(max(max(hq[q][0].w, hq[q][1].w), max(hq[q][2].w, hq[q][3].w)), max(max(hq[q][4].w, hq[q][5].w), max(hq[q][6].w, hq[q][7].w))) != EMPTY))
#define HQCHECK(q, j) do { if (__builtin_expect(HQBAD(q), 0)) { _Pragma("nounroll") for (u32 sp = 0; sp < SPIN_CAP; ++sp) { \
      __builtin_amdgcn_s_sleep(RSL); asm volatile("" ::: "memory"); HQLOAD(q, j); if (!HQBAD(q)) break; } } SCHED(); } while (0)
  if constexpr (A) {
    const int T32 = tile * 4 + wave;
    u32x2 st[7];
    u32x4 hb[28];
    u64 fcl;
    const u32 stv = (7 * hh * 2048 + (lane & 31)) * 8;
#define STA(c_) _Pragma("unroll") for (int i = 0; i < 7; ++i) asm volatile("global_load_dwordx2 %0, %1, %2 sc1" : c_(st[i]) : "v"(stv), "s"(ws + WS_ST + (i * 2048 + T32 * 32) * 8) : "memory")
#define STW(k_) asm volatile("s_waitcnt vmcnt(%7)" : "+v"(st[0]), "+v"(st[1]), "+v"(st[2]), "+v"(st[3]), "+v"(st[4]), "+v"(st[5]), "+v"(st[6]) : "n"(k_) : "memory")
#define STBAD() (!__all(max(max(max(st[0].y, st[1].y), max(st[2].y, st[3].y)), max(max(st[4].y, st[5].y), st[6].y)) != EMPTY))
#define HBA(c_, q) _Pragma("unroll") for (int i = 0; i < 4; ++i) asm volatile("global_load_dwordx4 %0, %1, %2 offset:%3 sc1" : c_(hb[4 * (q) + i]) : "v"(lane * 16), "s"(ws + WS_H + (T32 * 28 + 4 * (q)) * 1024), "n"(i * 1024) : "memory")
    lds_wait2(L, S_HFA, S_HFB);
    __builtin_amdgcn_s_sleep(DLY3);
    if (!SPEC || r2 >= 14) {
#pragma nounroll
      for (u32 sp = 0; sp < SPIN_CAP; ++sp) { STA("=v"); STW(0); if (!STBAD()) break; __builtin_amdgcn_s_sleep(RSL); }
    }
    STA("=v");
    STAMP(17);
#pragma unroll
    for (int q = 0; q < 7; ++q) { HBA("=v", q); }
    asm volatile("global_load_dwordx2 %0, %1, %2 sc1" : "=v"(fcl) : "v"(p4 * 128), "s"(ws + WS_FH + (T32 * 32 + 28) * 128) : "memory");
    u32 wb_[3] = {(u32)lane * 16, (u32)lane * 16 + 2 * SLOT, (u32)lane * 16 + 4 * SLOT};
    asm volatile("" : "+v"(wb_[1]), "+v"(wb_[2]));
#define WFRAG(sl, s) (*(LAS u32x4*)(L + wb_[(sl) >> 1] + ((sl) & 1) * SLOT + (s) * 1024))
    LAS char* csl = L + L_CS + hh * 16;
    u32x4 ring[7];
    f32x4 cq[4];
    float nmu, ve, k0, k1;
    u32 mx;
    const int pvo = WS_FHH + ((tile * 16 + r) * 4 + wave) * 128;
#define PROG(n) FLAG(pvo + 131072, n)
    f32x16 acc[2];
    float y[8], gu[8];
    u32x4 yb;
    u32x4 cw;
#define LT(i)
    auto gu_block = [&](auto JC) __attribute__((always_inline)) {
      constexpr int j = JC.value;
      sfor<0, 28>([&](auto SC) LAM {
        constexpr int s = SC.value;
        if constexpr (j == 0) { u32x4 t_ = hb[s]; asm volatile("s_waitcnt vmcnt(%1)" : "+v"(t_) : "n"(28 - s) : "memory"); hb[s] = t_; }
        if constexpr (PACE > 0 && j >= PJ0 && j <= PJ1 && s % PS == 0) __builtin_amdgcn_s_sleep(PACE);
        if constexpr (HW && j > 0 && s % 2 == HW - 1 && (s < 27 || j < 6)) asm volatile("" :: "v"(ring[(s + 1) % 7]));
        acc[j & 1] = mfma32(ring[s % 7], hb[s], acc[j & 1]);
        if constexpr (j == 0) mx = max(mx, hb[s].w);
        if constexpr ((j == W3J || j == 4) && s == 12) cw = *(volatile LAS u32x4*)(L + L_FLG + (j == W3J ? S_WIN3 : S_REFILL) * 16);
        if constexpr ((j == W3J || j == 4) && s == 20) { if (__builtin_expect(!__builtin_amdgcn_readfirstlane(cw.x & cw.y & cw.z & cw.w), 0)) lds_wait(L, j == W3J ? S_WIN3 : S_REFILL); asm volatile("" ::: "memory"); }
        if constexpr (j == 1 && s < 7 - NCL) ST16(rs, (u32x4{EMPTY, EMPTY, EMPTY, EMPTY}), lane * 16, hho + (NCL + s) * 8192);
        if constexpr (s + 7 < 28) ring[s % 7] = WFRAG(gu_slot(j), s + 7);
        else if constexpr (j < 6) ring[s % 7] = WFRAG(gu_slot(j + 1), s - 21);
        if constexpr (s == 0 && (j == 1 || j == 2)) lds_signal(L, j == 1 ? S_WSEEN : S_FREE01, wave);
        if constexpr (s == 0 && j >= 3) *(volatile LAS u32*)(L + L_FLG + S_DONE4 * 16 + wave * 4) = j - 2u;
        if constexpr (j > 0) {
          sfor<0, 8>([&](auto EC) LAM {
            constexpr int i = EC.value, k = s - 1 - 2 * i;
            const float ga = acc[(j - 1) & 1][i];
            if constexpr (k == 0) y[i] = ga * ga;
            if constexpr (k == 1) y[i] = fmaf(y[i], k1, k0);
            if constexpr (k == 2) y[i] = ga * y[i];
            if constexpr (k == 3) y[i] = fexp2(y[i]);
            if constexpr (k == 4) y[i] = fmaf(y[i], ve, ve);
            if constexpr (k == 5) { gu[i] = ga * acc[(j - 1) & 1][i + 8]; PIN(gu[i]); }
            if constexpr (k == 6) y[i] = frcp(y[i]);
            if constexpr (k == 7) y[i] = y[i] * gu[i];
            if constexpr (k >= 0 && k < 8 && k != 5) PIN(y[i]);
          });
          if constexpr (s == 21) { yb[0] = pk(y[0], y[1]); yb[1] = pk(y[2], y[3]); }
          if constexpr (s == 22) yb[2] = pk(y[4], y[5]);
          if constexpr (NCL == 1 && j == 2 && s == 22) WAITVM(2);
          if constexpr (j == 3 && s == 22 && NCL < 7 && NCL > 1) { if (DUP & 4) WAITVM(6); else WAITVM(4); }
          if constexpr (s == 23) { yb[3] = filled(pk(y[6], y[7])); ST16D(DUP & 4, rs, yb, lane * 16, hho + (j - 1) * 8192); }
          if constexpr (s == 24 && j <= (HELP > 4 ? HELP : 4)) FLAG(pvo, j);
        }
        if constexpr (j < 6 && s >= 17 && s < 21) cq[s - 17] = *(LAS f32x4*)(csl + (j + 1) * 128 + (s - 17) * 32);
        if constexpr (j < 6 && s >= 22 && s < 27) {
          sfor<0, 16>([&](auto EC) LAM {
            constexpr int e = EC.value;
            if constexpr ((e < 2 ? 22 : e < 4 ? 23 : 23 + (e >> 2)) == s) { float v = nmu * cq[e >> 2][e & 3]; PIN(v); acc[(j + 1) & 1][e] = v; }
          });
        }
        SCHED();
      });
      LT(j);
      if (j == 0) STAMP(18);
      if (j == 3) STAMP(26);
      if (j == 1) STAMP(28);
      if (j == 2) STAMP(29);
      if (j == 4) STAMP(30);
      if (j == 5) STAMP(31);
    };
#define HBW(q) asm volatile("s_waitcnt vmcnt(0)" : "+v"(hb[4 * (q)]), "+v"(hb[4 * (q) + 1]), "+v"(hb[4 * (q) + 2]), "+v"(hb[4 * (q) + 3]) :: "memory")
#define HBBAD(q) (!__all(max(max(hb[4 * (q)].w, hb[4 * (q) + 1].w), max(hb[4 * (q) + 2].w, hb[4 * (q) + 3].w)) != EMPTY))
#define HBC(q, c_) asm volatile("s_bitcmp0_b32 %6, %7\n\ts_cbranch_scc1 1f\n\tglobal_load_dwordx4 %0, %4, %5 sc1\n\tglobal_load_dwordx4 %1, %4, %5 offset:1024 sc1\n\t" \
      "global_load_dwordx4 %2, %4, %5 offset:2048 sc1\n\tglobal_load_dwordx4 %3, %4, %5 offset:3072 sc1\n1:" : "+v"(hb[4 * (q)]), "+v"(hb[4 * (q) + 1]), "+v"(hb[4 * (q) + 2]), "+v"(hb[4 * (q) + 3]) \
      : "v"(lane * 16), "s"(ws + WS_H + (T32 * 28 + 4 * (q)) * 1024), "s"(c_), "n"(q) : "memory", "scc")
#define REDO(m_) do { STA("+v"); _Pragma("unroll") for (int q = 0; q < 7; ++q) HBC(q, m_); STW(0); _Pragma("unroll") for (int q = 0; q < 7; ++q) HBW(q); } while (0)
#define FIX() do { _Pragma("nounroll") for (u32 sp = 0; sp < SPIN_CAP; ++sp) { u32 m = 0; \
        _Pragma("unroll") for (int q = 0; q < 7; ++q) m |= HBBAD(q) ? 1u << q : 0u; \
        if (!m && !STBAD()) break; if (sp) __builtin_amdgcn_s_sleep(RSL); m = __builtin_amdgcn_readfirstlane(m); REDO(m); } } while (0)
    auto first = [&]() LAM {
#pragma unroll
      for (int k = 0; k < 4; ++k) cq[k] = *(LAS f32x4*)(csl + k * 32);
#pragma unroll
      for (int s = 0; s < 7; ++s) ring[s] = WFRAG(0, s);
      f32x2 sf[7];
#pragma unroll
      for (int i = 0; i < 7; ++i) sf[i] = __builtin_bit_cast(f32x2, st[i]);
      float s1 = ((sf[0].x + sf[1].x) + (sf[2].x + sf[3].x)) + ((sf[4].x + sf[5].x) + sf[6].x);
      float s2 = ((sf[0].y + sf[1].y) + (sf[2].y + sf[3].y)) + ((sf[4].y + sf[5].y) + sf[6].y);
      { f32x2 a = sw32(s1); s1 = a.x + a.y; a = sw32(s2); s2 = a.x + a.y; }
      nmu = s1 * (-1.f / 448.f); ve = fmaxf(s2 * (1.f / 448.f) - nmu * nmu, 0.f) + 1e-5f;
      const float rstd = __builtin_amdgcn_rsqf(ve);
      k0 = -2.30220819f * rstd; k1 = -0.10294324f * rstd * rstd * rstd;
#pragma unroll
      for (int i = 0; i < 16; ++i) acc[0][i] = nmu * cq[i >> 2][i & 3];
      mx = 0;
      __builtin_amdgcn_s_setprio(GUPRIO);
      SCHED();
      gu_block(IC<0>{});
    };
    STW(29);
    if (__builtin_expect(STBAD(), 0)) {
      u32 m = 0;
#pragma unroll
      for (int i = 0; i < 7; ++i) { const u64 bm = __builtin_amdgcn_ballot_w64(st[i].y == EMPTY); m |= ((u32)bm ? 1u << (i >> 1) : 0u) | ((u32)(bm >> 32) ? 1u << ((7 + i) >> 1) : 0u); }
      m = __builtin_amdgcn_readfirstlane(m);
      asm volatile("s_waitcnt vmcnt(%0)" :: "n"(FXW) : "memory");
      REDO(m);
      FIX();
    }
    first();
    if (__builtin_expect(!__all(mx != EMPTY), 0)) { FIX(); first(); }
    asm volatile("s_waitcnt vmcnt(0)" : "+v"(fcl) :: "memory");
    if (__builtin_expect(!__all(fcl == tag + 1), 0)) poll_flags(ws, WS_FH + (T32 * 32 + 28 + p4) * 128, lane < 4, tag);
    gu_block(IC<1>{}); gu_block(IC<2>{}); gu_block(IC<3>{}); gu_block(IC<4>{}); gu_block(IC<5>{}); gu_block(IC<6>{});
#pragma unroll
    for (int i = 0; i < 8; ++i) { const float ga = acc[0][i]; y[i] = ga * acc[0][i + 8] * frcp(fmaf(fexp2(ga * fmaf(ga * ga, k1, k0)), ve, ve)); }
    yb = u32x4{pk(y[0], y[1]), pk(y[2], y[3]), pk(y[4], y[5]), filled(pk(y[6], y[7]))};
    ST16D(DUP & 4, rs, yb, lane * 16, hho + 6 * 8192);
    if (HELP == 7) FLAG(pvo, 7);
    __builtin_amdgcn_s_setprio(0);
    STAMP(19);
    if (r2 < 14) {
      f32x16 da[2][2] = {};
      u32x4 hq[2][8], wl[2][8];
      HBASE();
      __builtin_amdgcn_s_sleep(ADLY);
      lds_wait1(L, S_CONS, w2);
      STAMP2(15);
      HQLOAD(0, 5);
      if (A6L == 0) HQLOAD(1, 6);
      WDREAD(wl[0], 2);
      SCHED();
      STAMP2(16);
      HQCHECK(0, 5);
      if (A6L == 1) HQLOAD(1, 6);
      WDREAD(wl[1], 0);
      SCHED();
      DOWN16(da, wl[0], hq[0]);
      SCHED();
      STAMP2(18);
      HQCHECK(1, 6);
      STAMP2(31);
      DOWN16(da, wl[1], hq[1]);
      PARTIAL_OUT(da, 0, 2);
      STAMP(20);
    }
  } else {
    lds_wait2(L, S_HFA, S_HFB);
    if (TD == 6 && w2 == 0) DTOUCH();
    if (TD == 7) dma4(wd + min(b * 49 + w2 * 13, 12544 - 13) * 128, min(lane, 12) * 128, L_JUNK);
    __builtin_amdgcn_s_setprio(BP3);
    if (r2 >= 14) GUBLK(4);
#define REFILL() do { lds_wait<BSL>(L, S_WSEEN); GUBLK(5); lds_wait<BSL>(L, S_FREE01); GUBLK(6); } while (0)
    if (r2 >= 14) {
      WAITVM(0); lds_signal(L, S_WIN3, w2);
      REFILL();
      WAITVM(0); lds_signal(L, S_REFILL, w2);
    } else {
      {
        u32x2 e[24][2];
        u32 ea_ = da_ - 2 * SLOT, eb_ = ea_ ^ 64;
        asm volatile("" : "+v"(da_), "+v"(db_), "+v"(dc_), "+v"(dd_), "+v"(ea_), "+v"(eb_));
#define WBL(N0, N1, NS, SL) sfor<N0, N1>([&](auto NC) LAM { constexpr int n = NC.value, k = n - (N0); \
          if constexpr (k < (NS)) dma_wd1<n, (SL) * SLOT + k * 1024>(rd, dvo, w2 * 7168); else dma_wd1<n, L_EX + (k - (NS)) * 4096>(rd, dvo, w2 * 1024); })
#define WBR(N0, N1, NS, SL) do { sfor<N0, N1>([&](auto NC) LAM { constexpr int n = NC.value, k = n - (N0); \
          if constexpr (k >= (NS)) { e[n][0] = trread(L + dc_ + (k - (NS)) * 4096); e[n][1] = trread(L + dd_ + (k - (NS)) * 4096); } \
          else if constexpr ((SL) == 4) { e[n][0] = trread(L + da_ + 2 * SLOT + k * 1024); e[n][1] = trread(L + db_ + 2 * SLOT + k * 1024); } \
          else { e[n][0] = trread(L + ea_ + k * 1024); e[n][1] = trread(L + eb_ + k * 1024); } }); \
          asm volatile("s_waitcnt lgkmcnt(0)" : "+v"(e[(N1) - 1][1]) :: "memory"); } while (0)
        WBL(0, 11, 7, 4);
        WAITVM(0);
        STAMP2(24);
        WBR(0, 11, 7, 4);
        WBL(11, 15, 0, 0);
        if (G4E == 1) { GUBLK(4); WAITVM(7); } else WAITVM(0);
        STAMP2(25);
        WBR(11, 15, 0, 0);
        if (G4E == 0) GUBLK(4);
        lds_wait<BSL>(L, S_WSEEN);
        WBL(15, 24, 7, 0);
        if (G6E) { lds_wait<BSL>(L, S_FREE01); GUBLK(6); WAITVM(7); lds_signal(L, S_WIN3, w2); }
        else if (G4E == 2) { GUBLK(4); WAITVM(7); } else { WAITVM(0); lds_signal(L, S_WIN3, w2); }
        STAMP2(26);
        WBR(15, 24, 7, 0);
        GUBLK(5);
        if (G4E == 2) { WAITVM(7); lds_signal(L, S_WIN3, w2); }
        if (!G6E) { lds_wait<BSL>(L, S_FREE01); GUBLK(6); }
#pragma unroll
        for (int n = 0; n < 24; ++n) wdf[n >> 3][n & 7] = u32x4{e[n & ~1][n & 1].x, e[n & ~1][n & 1].y, e[n | 1][n & 1].x, e[n | 1][n & 1].y};
      }
      STAMP2(23);
      f32x16 acc[2][2] = {};
      u32x4 hq[2][8];
      STAMP2(16);
      HBASE();
      u64 pv;
      const char* hs = ws + ho + 4096;
#define PEEK() do { pv = tag + 7; asm volatile("s_mov_b64 exec, 0xff\n\tglobal_atomic_add_x2 %0, %1, %2, %3 sc0\n\ts_mov_b64 exec, -1" : "+v"(pv) : "v"(po), "v"(0ull), "s"(ws) : "memory"); } while (0)
#define PEEKED(k_, n_) do { asm volatile("s_waitcnt vmcnt(%1)" : "+v"(pv) : "n"(k_) : "memory"); PMIN(pv); NEEDP(n_); } while (0)
#define HQA(c_, q, j) _Pragma("unroll") for (int i = 0; i < 8; ++i) asm volatile("global_load_dwordx4 %0, %1, %2 offset:%3 sc1" : c_(hq[q][i]) : "v"(lane * 16), "s"(hs + (j) * 8192), "n"(i * 1024 - 4096) : "memory")
#define HQW(q, k_) asm volatile("s_waitcnt vmcnt(%8)" : "+v"(hq[q][0]), "+v"(hq[q][1]), "+v"(hq[q][2]), "+v"(hq[q][3]), "+v"(hq[q][4]), "+v"(hq[q][5]), "+v"(hq[q][6]), "+v"(hq[q][7]) : "n"(k_) : "memory")
#define HQOK(q, j, k_) do { HQW(q, k_); if (__builtin_expect(HQBAD(q), 0)) { _Pragma("nounroll") for (u32 sp = 0; sp < SPIN_CAP; ++sp) { \
        __builtin_amdgcn_s_sleep(RSL); HQA("+v", q, j); HQW(q, 0); if (!HQBAD(q)) break; } } SCHED(); } while (0)
      PEEK();
      FREE(1); WDLOAD(3, 0);
      WAITVM(8);
#pragma unroll
      for (int k = 0; k < 3; ++k) asm volatile("" : "+v"(wdf[k][0]), "+v"(wdf[k][1]), "+v"(wdf[k][2]), "+v"(wdf[k][3]), "+v"(wdf[k][4]), "+v"(wdf[k][5]), "+v"(wdf[k][6]), "+v"(wdf[k][7]) :: "memory");
      lds_signal(L, S_REFILL, w2);
      PEEKED(8, 1);
      STAMP2(1);
      PEEK();
      HQA("=v", 0, 0);
      FREE(2); WDLOAD(4, 1);
      PEEKED(16, 2);
      STAMP2(2);
      HQA("=v", 1, 1);
      PEEK();
      u32 fr3;
      { const u32x4 fr = *(volatile LAS u32x4*)(L + L_FLG + S_DONE4 * 16); fr3 = !W5D || __builtin_amdgcn_readfirstlane(min(min(fr.x, fr.y), min(fr.z, fr.w))) >= 3; }
      if (fr3) { if (!W5D) FREE(3); WDLOAD(5, 2); }
      if (fr3) WAITVM(25); else WAITVM(17);
      HQOK(0, 0, 25);
      __builtin_amdgcn_s_setprio(DP0);
      DOWN16(acc, wdf[0], hq[0]);
      __builtin_amdgcn_s_setprio(BP3);
      WDREAD(wdf[0], 0);
      SCHED();
      STAMP2(8);
      if (!fr3) { FREE(3); WDLOAD(5, 2); }
      PEEKED(8, 3);
      STAMP2(3);
      HQA("=v", 0, 2);
      PEEK();
      asm volatile("s_waitcnt lgkmcnt(0)" : "+v"(wdf[0][7]) :: "memory");
      WDLOAD(6, 0);
      HQOK(1, 1, 25);
      __builtin_amdgcn_s_setprio(DP0);
      DOWN16(acc, wdf[1], hq[1]);
      WDREAD(wdf[1], 1);
      SCHED();
      STAMP2(9);
      PEEKED(8, 4);
      STAMP2(4);
      HQA("=v", 1, 3);
      __builtin_amdgcn_s_setprio(DNPRIO);
      HQOK(0, 2, 8);
      lds_signal(L, S_CONS, w2);
      SCHED();
      DOWN16(acc, wdf[2], hq[0]);
      SCHED();
      STAMP2(10);
      FREE(4);
      HQA("=v", 0, 4);
      HQOK(1, 3, 8);
      DOWN16(acc, wdf[0], hq[1]);
      SCHED();
      STAMP2(11);
      HQOK(0, 4, 0);
      DOWN16(acc, wdf[1], hq[0]);
      PARTIAL_OUT(acc, 1, 3);
      STAMP(20);
    }
  }
  if (r2 < 14) {
    BAR();
    STAMP2(7);
    float c[8] = {blo(hres.x), bhi(hres.x), blo(hres.y), bhi(hres.y), blo(hres.z), bhi(hres.z), blo(hres.w), bhi(hres.w)};
    const int rl = ((tb & 1) * 16 + l15) * 16;
    f32x4 v0 = {c[0], c[1], c[2], c[3]}, v1 = {c[4], c[5], c[6], c[7]};
#pragma unroll
    for (int h = 0; h < 2; ++h) {
      const int bf = (tb >> 1) * 2 + h;
      const bool p7 = kh && g == 3;
      u32 rq = (p7 ? L_EX + bf * 4096 : (bf == 3 ? 0 : (bf + 2) * SLOT) + (4 * kh + g) * 1024) + rl;
      const u32 st7 = p7 ? 1024 : 7168;
      asm volatile("" : "+v"(rq));
#pragma unroll
      for (int w = 0; w < 4; ++w) { v0 += *(LAS f32x4*)(L + rq + w * st7); v1 += *(LAS f32x4*)(L + rq + w * st7 + 512); }
    }
#pragma unroll
    for (int i = 0; i < 4; ++i) { c[i] = v0[i]; c[4 + i] = v1[i]; }
    { const u32x4 ov = {pk(c[0], c[1]), pk(c[2], c[3]), pk(c[4], c[5]), pk(c[6], c[7])}; asm volatile("global_store_dwordx4 %0, %1, %2 sc1\n\ts_nop 1" :: "v"(xo), "v"(ov), "s"(out) : "memory"); }
    if (A && b == 0 && tid == 0) __hip_atomic_store((GAS u64*)(ws + WS_E), tag, RLX_AGENT);
  }
  STAMP(21);
  };
  if (wave < 4) role(IC<1>{}); else role(IC<0>{});
  { const int due_ = 3000 + 4 * (int)(__builtin_amdgcn_readfirstlane(*(volatile LAS u32*)(L + L_STAMP + wave * 4)) - fix0_);
#pragma nounroll
  for (u32 sp = 0; sp < 100000 && (int)((u32)__builtin_amdgcn_s_memrealtime() - fix0_) < due_; ++sp) __builtin_amdgcn_s_sleep(2); }
}

extern "C" void kernel_launch(void* const* d_in, const int* in_sizes, int n_in, void* d_out, int out_size, void* d_ws, size_t ws_size, hipStream_t stream) {
  (void)hipFuncSetAttribute((const void*)fused_block, hipFuncAttributeMaxDynamicSharedMemorySize, LDS_BYTES);
  fused_block<<<256, 512, LDS_BYTES, stream>>>((const char*)d_in[0], (const char*)d_in[1], (const char*)d_in[2], (const char*)d_in[3], (const char*)d_in[5], (const char*)d_in[6],
                                               (const char*)d_in[7], (char*)d_out, (char*)d_ws);
}
```

```cpp
#include <hip/hip_runtime.h>
#include <hip/hip_bf16.h>
#include <stdint.h>

typedef unsigned u32;
typedef unsigned long long u64;
typedef u32 u32x4 __attribute__((ext_vector_type(4)));
typedef u32 u32x2 __attribute__((ext_vector_type(2)));
typedef float f32x2 __attribute__((ext_vector_type(2)));
typedef float f32x4 __attribute__((ext_vector_type(4)));
typedef float f32x16 __attribute__((ext_vector_type(16)));
typedef short s16x4 __attribute__((ext_vector_type(4)));
typedef __bf16 bf16x2 __attribute__((ext_vector_type(2)));
typedef __bf16 bf16x8 __attribute__((ext_vector_type(8)));
#define LAS __attribute__((address_space(3)))
#define GAS __attribute__((address_space(1)))
#define DI __device__ __forceinline__
#define RLX_AGENT __ATOMIC_RELAXED, __HIP_MEMORY_SCOPE_AGENT

constexpr int WS_E = 0;
constexpr int WS_FKV = 8192;
constexpr int WS_FO = WS_FKV + 131072;
constexpr int WS_FH = WS_FO + 131072;
constexpr int WS_FG = WS_FH + 262144;
constexpr int WS_FD = WS_FG + 114688;
constexpr int WS_FHH = WS_FD + 57344;
constexpr int WS_CS = WS_FHH + 262144;
constexpr int WS_ST = WS_CS + 200704;
constexpr int WS_WGU = WS_ST + 229376;
constexpr int WS_WD = WS_WGU + 3211264;
constexpr int WS_KVP = WS_WD + 1605632;
constexpr int WS_O = WS_KVP + 1048576;
constexpr int WS_H = WS_O + 2097152;
constexpr int WS_HH = WS_H + 1835008;
#define UNITX4
#define NOWD
#define WDOFF(cg, fb, bl) (((((cg) * 4 + (fb) / 28) * 7 + (fb) % 7) * 4 + (fb) / 7 % 4) * 2 + (bl))
#define HHOFF(T32, fb) (((((T32) >> 1) * 4 + (fb) / 28) * 7 + (fb) % 7) * 8 + ((fb) / 7 & 3) * 2 + ((T32) & 1))
constexpr int WS_STAMP = WS_HH + 7340032;
constexpr int WS_LOG = WS_STAMP + 256 * 8 * 32 * 8;
constexpr int WS_USED = WS_LOG + 4096;
constexpr int SLOT = 28672;
constexpr int L_WK = 4 * SLOT, L_WV = 0, L_WQ = 14336;
constexpr int L_PKV = 2 * SLOT;
constexpr int L_WO = 3 * SLOT;
constexpr int L_EX = 5 * SLOT;
constexpr int L_KVA = 5 * SLOT;
constexpr int L_ST1 = 5 * SLOT + 4096;
constexpr int L_WPHI = 5 * SLOT + 5120;
constexpr int L_CSP = 5 * SLOT + 7168;
constexpr int L_CS = 159744;
constexpr int L_FLG = 160768;
constexpr int L_JUNK = 161280;
constexpr int L_STAMP = 161536;
constexpr int L_RED = SLOT;
constexpr int LDS_BYTES = 163840;
constexpr int gu_slot(int j) { return j % 5; }
constexpr int CODE_PF = 48384;
constexpr u32 SPIN_CAP = 1u << 16;
#define LL 1
#define AP 1

#define STAMP(i) do {} while (0)
#define STAMP2(i) do {} while (0)
#define STAMP2X(i) do {} while (0)

DI f32x4 mfma16(u32x4 a, u32x4 b, f32x4 c) { return __builtin_amdgcn_mfma_f32_16x16x32_bf16(__builtin_bit_cast(bf16x8, a), __builtin_bit_cast(bf16x8, b), c, 0, 0, 0); }
DI f32x16 mfma32(u32x4 a, u32x4 b, f32x16 c) { return __builtin_amdgcn_mfma_f32_32x32x16_bf16(__builtin_bit_cast(bf16x8, a), __builtin_bit_cast(bf16x8, b), c, 0, 0, 0); }
DI u32 pk(float lo, float hi) { f32x2 p = {lo, hi}; return __builtin_bit_cast(u32, __builtin_convertvector(p, bf16x2)); }
DI float blo(u32 v) { return __builtin_bit_cast(float, v << 16); }
DI float bhi(u32 v) { return __builtin_bit_cast(float, v & 0xffff0000u); }
DI float dot2(u32 a, u32 b, float c) { return __builtin_amdgcn_fdot2_f32_bf16(__builtin_bit_cast(bf16x2, a), __builtin_bit_cast(bf16x2, b), c, false); }
DI u32x2 trread(LAS char* p) { return __builtin_bit_cast(u32x2, __builtin_amdgcn_ds_read_tr16_b64_v4i16((LAS s16x4*)p)); }
DI u32x4 trfrag(LAS char* p, int step) { u32x2 a = trread(p), b = trread(p + step); return u32x4{a.x, a.y, b.x, b.y}; }
DI float fexp2(float x) { return __builtin_amdgcn_exp2f(x); }
DI float frcp(float x) { return __builtin_amdgcn_rcpf(x); }
template <int C> DI float dppror(float x) { return __builtin_bit_cast(float, __builtin_amdgcn_update_dpp(0, __builtin_bit_cast(int, x), 0x120 + C, 0xf, 0xf, false)); }
DI float row_max(float x) { x = fmaxf(x, dppror<8>(x)); x = fmaxf(x, dppror<4>(x)); x = fmaxf(x, dppror<2>(x)); return fmaxf(x, dppror<1>(x)); }
DI float row_min(float x) { x = fminf(x, dppror<8>(x)); x = fminf(x, dppror<4>(x)); x = fminf(x, dppror<2>(x)); return fminf(x, dppror<1>(x)); }
DI float row_sum(float x) { x += dppror<8>(x); x += dppror<4>(x); x += dppror<2>(x); return x + dppror<1>(x); }
DI f32x2 sw16(float x) { u32 u = __builtin_bit_cast(u32, x); auto r = __builtin_amdgcn_permlane16_swap(u, u, false, false); return f32x2{__builtin_bit_cast(float, (u32)r[0]), __builtin_bit_cast(float, (u32)r[1])}; }
DI f32x2 sw32(float x) { u32 u = __builtin_bit_cast(u32, x); auto r = __builtin_amdgcn_permlane32_swap(u, u, false, false); return f32x2{__builtin_bit_cast(float, (u32)r[0]), __builtin_bit_cast(float, (u32)r[1])}; }
DI float grp_sum(float x) { f32x2 a = sw16(x); x = a.x + a.y; a = sw32(x); return a.x + a.y; }
DI float grp_max(float x) { f32x2 a = sw16(x); x = fmaxf(a.x, a.y); a = sw32(x); return fmaxf(a.x, a.y); }
DI float grp_min(float x) { f32x2 a = sw16(x); x = fminf(a.x, a.y); a = sw32(x); return fminf(a.x, a.y); }

#define BAR() asm volatile("s_waitcnt lgkmcnt(0)\n\ts_barrier" ::: "memory")
#define WAITVM(n) asm volatile("s_waitcnt vmcnt(" #n ")" ::: "memory")
#define DRAIN() do { asm volatile("s_waitcnt vmcnt(0)" ::: "memory"); __builtin_amdgcn_s_waitcnt(0x0F70); } while (0)
#define SCHED() __builtin_amdgcn_sched_barrier(0)
DI void dma16(const void* base, u32 voff, u32 lds) {
  u32 keep;
  asm volatile("s_mov_b32 %0, m0\n\ts_mov_b32 m0, %3\n\ts_nop 0\n\tglobal_load_lds_dwordx4 %1, %2\n\ts_mov_b32 m0, %0" : "=&s"(keep) : "v"(voff), "s"(base), "s"(lds) : "memory");
}
DI void dma16c(const void* base, u32 voff, u32 lds) {
  u32 keep;
  asm volatile("s_mov_b32 %0, m0\n\ts_mov_b32 m0, %3\n\ts_nop 0\n\tglobal_load_lds_dwordx4 %1, %2 sc1\n\ts_mov_b32 m0, %0" : "=&s"(keep) : "v"(voff), "s"(base), "s"(lds) : "memory");
}
DI void dma4(const void* base, u32 voff, u32 lds) {
  u32 keep;
  asm volatile("s_mov_b32 %0, m0\n\ts_mov_b32 m0, %3\n\ts_nop 0\n\tglobal_load_lds_dword %1, %2\n\ts_mov_b32 m0, %0" : "=&s"(keep) : "v"(voff), "s"(base), "s"(lds) : "memory");
}
DI void dma16c_if(u32 go, const void* base, u32 voff, u32 lds) {
  u32 keep;
  asm volatile("s_cmp_eq_u32 %4, 0\n\ts_cbranch_scc1 1f\n\ts_mov_b32 %0, m0\n\ts_mov_b32 m0, %3\n\ts_nop 0\n\tglobal_load_lds_dwordx4 %1, %2 sc1\n\ts_mov_b32 m0, %0\n1:"
               : "=&s"(keep) : "v"(voff), "s"(base), "s"(lds), "s"(go) : "memory", "scc");
}
template <int SI, int LI> DI void dma7(__amdgpu_buffer_rsrc_t rs, u32 voff, u32 so, u32 lds) {
  u32 t;
  asm volatile("s_add_u32 m0, %4, %6\n\ts_add_u32 %0, %3, %5\n\tbuffer_load_dwordx4 %1, %2, %0 offen sc1 lds\n\tbuffer_load_dwordx4 %1, %2, %0 offen offset:1024 sc1 lds\n\t"
               "buffer_load_dwordx4 %1, %2, %0 offen offset:2048 sc1 lds\n\tbuffer_load_dwordx4 %1, %2, %0 offen offset:3072 sc1 lds\n\t"
               "s_add_u32 m0, m0, 4096\n\ts_add_u32 %0, %0, 4096\n\tbuffer_load_dwordx4 %1, %2, %0 offen sc1 lds\n\tbuffer_load_dwordx4 %1, %2, %0 offen offset:1024 sc1 lds\n\t"
               "buffer_load_dwordx4 %1, %2, %0 offen offset:2048 sc1 lds"
               : "=&s"(t) : "v"(voff), "s"(rs), "s"(so), "s"(lds), "n"(SI), "n"(LI) : "memory", "scc");
}
constexpr int wrow(int q, int j, int hf) { return (((7 * q + j) >> 2) * 64 + 2 * ((7 * q + j) & 3) + hf) * 896 + 1024; }
#define DL_(n, o) "s_mov_b32 %0, %" #n "\n\tbuffer_load_dwordx4 %1, %2, %0 offen offset:" #o " lds\n\t"
template <int J> DI void dma_wd(__amdgpu_buffer_rsrc_t rs, u32 voff, u32 lds, u32 lds7) {
  u32 t;
  asm volatile("s_mov_b32 m0, %3\n\t" DL_(5, 0) DL_(6, 1024) DL_(7, 2048) DL_(8, 3072) "s_add_u32 m0, m0, 4096\n\t" DL_(9, 0) DL_(10, 1024) DL_(11, 2048) "s_mov_b32 m0, %4\n\t" DL_(12, 0)
               : "=&s"(t) : "v"(voff), "s"(rs), "s"(lds), "s"(lds7), "n"(wrow(0, J, 0)), "n"(wrow(0, J, 1) - 1024), "n"(wrow(1, J, 0) - 2048), "n"(wrow(1, J, 1) - 3072),
                 "n"(wrow(2, J, 0)), "n"(wrow(2, J, 1) - 1024), "n"(wrow(3, J, 0) - 2048), "n"(wrow(3, J, 1)) : "memory", "scc");
}
template <int N, int LO> DI void dma_wd1(__amdgpu_buffer_rsrc_t rs, u32 voff, u32 lds) {
  u32 t;
  asm volatile("s_add_u32 m0, %3, %4\n\t" DL_(5, 0) : "=&s"(t) : "v"(voff), "s"(rs), "s"(lds), "n"(LO), "n"(wrow(N / 2 % 4, N / 8, N % 2)) : "memory", "scc");
}
DI void touch(const void* base, u32 voff) {
  asm volatile("s_mov_b32 m0, %2\n\ts_nop 0\n\tglobal_load_lds_dword %0, %1" :: "v"(voff), "s"(base), "n"(L_JUNK) : "memory");
}
constexpr int xrow(int n) { return ((n / 14) * 16 + (n & 1) * 8) * 896 + ((n % 14) >> 1) * 128 + 2048 - n % 4 * 1024; }
template <int N> DI void dma_x(__amdgpu_buffer_rsrc_t rs, u32 voff, u32 lds) {
  u32 t;
  asm volatile("s_add_u32 m0, %3, %4\n\t" DL_(5, 0) DL_(6, 1024) DL_(7, 2048) DL_(8, 3072)
               : "=&s"(t) : "v"(voff), "s"(rs), "s"(lds), "n"(N * 1024), "n"(xrow(N)), "n"(xrow(N + 1)), "n"(xrow(N + 2)), "n"(xrow(N + 3)) : "memory", "scc");
}
DI u32 vm_count() {
  u32 t;
  asm volatile("s_getreg_b32 %0, hwreg(HW_REG_IB_STS)" : "=s"(t) :: "memory");
  return (t & 15u) | ((t >> 18) & 48u);
}
DI u32 min8(u32 c) {
  c = min(c, (u32)__builtin_amdgcn_update_dpp(0, (int)c, 0x121, 0xf, 0xf, false)); c = min(c, (u32)__builtin_amdgcn_update_dpp(0, (int)c, 0x122, 0xf, 0xf, false));
  c = min(c, (u32)__builtin_amdgcn_update_dpp(0, (int)c, 0x124, 0xf, 0xf, false));
  return __builtin_amdgcn_readfirstlane(min(c, (u32)__builtin_amdgcn_update_dpp(0, (int)c, 0x128, 0xf, 0xf, false)));
}
DI void dma4c(const void* base, u32 voff, u32 lds) {
  u32 keep;
  asm volatile("s_mov_b32 %0, m0\n\ts_mov_b32 m0, %3\n\ts_nop 0\n\tglobal_load_lds_dword %1, %2 sc1\n\ts_mov_b32 m0, %0" : "=&s"(keep) : "v"(voff), "s"(base), "s"(lds) : "memory");
}
#define LD16(rs, off) __builtin_bit_cast(u32x4, __builtin_amdgcn_raw_buffer_load_b128(rs, off, 0, 16))
#define ST16(rs, v, off, soff) do { const u32x4 d_ = __builtin_bit_cast(u32x4, v); __builtin_amdgcn_raw_buffer_store_b128(d_, rs, off, soff, 16); asm volatile("s_nop 1" :: "v"(d_) : "memory"); } while (0)

#define FLAG(off, n) __builtin_amdgcn_raw_buffer_store_b64(__builtin_bit_cast(u32x2, tag + (n)), rs, fvo, off, 16)
#define FLAGC(c, off, n) __builtin_amdgcn_raw_buffer_store_b64(__builtin_bit_cast(u32x2, tag + (n)), rs, (c) ? fvo : NOWHERE, off, 16)
constexpr int NOWHERE = 0x7ffffff0;
constexpr u32 EMPTY = 0xffffffffu;
DI u32 filled(u32 w) { return min(w, EMPTY - 1u); }
DI u64 peek(const char* ws, u32 off, bool on, u64 dflt) {
  u64 z = 0, v = dflt;
  asm volatile("" : "+v"(z));
  if (on) v = __hip_atomic_fetch_add((GAS u64*)(ws + off), z, __ATOMIC_RELAXED, AP == 2 ? __HIP_MEMORY_SCOPE_SYSTEM : __HIP_MEMORY_SCOPE_AGENT);
  return v;
}
DI void poll_flags(const char* ws, u32 off, bool on, u64 tag) {
#pragma nounroll
  for (u32 sp = 0; sp < SPIN_CAP; ++sp) {
    u64 v = tag + 1;
    if (on || !LL) v = __hip_atomic_load((const GAS u64*)(ws + off), RLX_AGENT);
    if (__all(v == tag + 1)) break;
  }
  __builtin_amdgcn_fence(__ATOMIC_ACQUIRE, "wavefront");
}
template <int SL = 1> DI void lds_wait(LAS char* L, int sig) {
#pragma nounroll
  for (u32 sp = 0; sp < (SPIN_CAP << 4); ++sp) {
    const u32x4 v = *(volatile LAS u32x4*)(L + L_FLG + sig * 16);
    if (__builtin_amdgcn_readfirstlane(v.x & v.y & v.z & v.w)) break;
    __builtin_amdgcn_s_sleep(SL);
  }
  asm volatile("" ::: "memory");
}
DI void lds_wait2(LAS char* L, int sig, int sig2) {
#pragma nounroll
  for (u32 sp = 0; sp < (SPIN_CAP << 4); ++sp) {
    const u32x4 v = *(volatile LAS u32x4*)(L + L_FLG + sig * 16) & *(volatile LAS u32x4*)(L + L_FLG + sig2 * 16);
    if (__builtin_amdgcn_readfirstlane(v.x & v.y & v.z & v.w)) break;
    __builtin_amdgcn_s_sleep(1);
  }
  asm volatile("" ::: "memory");
}
DI void lds_wait1(LAS char* L, int sig, int w) {
#pragma nounroll
  for (u32 sp = 0; sp < (SPIN_CAP << 4); ++sp) {
    if (__builtin_amdgcn_readfirstlane(*(volatile LAS u32*)(L + L_FLG + sig * 16 + w * 4))) break;
    __builtin_amdgcn_s_sleep(1);
  }
  asm volatile("" ::: "memory");
}
DI void lds_signal(LAS char* L, int sig, int w) {
  asm volatile("" ::: "memory");
  *(volatile LAS u32*)(L + L_FLG + sig * 16 + w * 4) = 1u;
}

template <int N> struct IC { static constexpr int value = N; };
template <int I, int N, class F> DI void sfor(F&& f) { if constexpr (I < N) { f(IC<I>{}); sfor<I + 1, N>(f); } }
#define LAM __attribute__((always_inline))
#define PIN(v) asm volatile("" : "+v"(v))
constexpr float LOG2E = 1.4426950408889634f;
template <int K> DI void macc(f32x4& c, u32x4 a, u32x4 b) {
  if (K == 0) asm volatile("v_mfma_f32_16x16x32_bf16 %0, %1, %2, 0" : "=&v"(c) : "v"(a), "v"(b));
  else if (K == 1) asm volatile("v_mfma_f32_16x16x32_bf16 %0, %1, %2, %0" : "+v"(c) : "v"(a), "v"(b));
  else c = mfma16(a, b, c);
}
template <int K> DI void macc(f32x16& c, u32x4 a, u32x4 b) {
  if (K == 0) asm volatile("v_mfma_f32_32x32x16_bf16 %0, %1, %2, 0" : "=&v"(c) : "v"(a), "v"(b));
  else if (K == 1) asm volatile("v_mfma_f32_32x32x16_bf16 %0, %1, %2, %0" : "+v"(c) : "v"(a), "v"(b));
  else c = mfma32(a, b, c);
}
template <int GAP, int SIG, int NW, class E, class F> DI void project(LAS char* L, u32 w0, u32 w1, const u32x4 (&xf)[2][14], int tp, int lane, int csl, f32x4 (&t)[2][2], f32x2 (&st)[2], E&& pre, F&& mid) {
  u32 wq = (tp & 1 ? w1 : w0) + (tp >> 1) * (7 * 2048 + GAP), ca = csl + (lane >> 4) * 16, sa = L_ST1 + (tp * 32 + (lane & 15)) * 8, fa = L_FLG + SIG * 16 + (lane & (NW - 1)) * 4;
  u32 cw = ca + tp * 64, fw = L_FLG + SIG * 16 + tp * 4, one = 1;
  u32x4 ones = {0x3f803f80u, 0x3f803f80u, 0x3f803f80u, 0x3f803f80u};
  LAS char *p0 = L + w0, *p1 = L + w1, *pq = L + wq, *pca = L + ca, *psa = L + sa, *pfa = L + fa, *pcw = L + cw, *pfw = L + fw;
  asm volatile("" : "+v"(p0), "+v"(p1), "+v"(pq), "+v"(pca), "+v"(psa), "+v"(pfa), "+v"(pcw), "+v"(pfw), "+v"(ones), "+v"(one));
  pre();
  u32x4 cf[7];
  u32x2 ra[7], rb[7];
  f32x4 cs, cp[4];
  u32 sw;
#define WAT(n) (((n) & 1 ? p1 : p0) + ((n) >> 1) * 2048 + ((n) >= 14 ? GAP : 0))
#pragma unroll
  for (int i = 0; i < 7; ++i) cf[i] = trfrag(pq + i * 2048, 256);
  SCHED();
  sfor<0, 7>([&](auto IC_) LAM {
    constexpr int i = IC_.value;
    macc<i == 0 ? 0 : i == 6 ? 2 : 1>(cs, cf[i], ones);
    SCHED();
    if (i < 6) { ra[i] = trread(WAT(i)); rb[i] = trread(WAT(i) + 256); }
    SCHED();
  });
  sfor<0, 28>([&](auto NC) LAM {
    constexpr int n = NC.value, K = n < 2 ? 0 : n < 26 ? 1 : 2;
    const u32x4 f = {ra[n % 7].x, ra[n % 7].y, rb[n % 7].x, rb[n % 7].y};
    macc<K>(t[0][n & 1], f, xf[0][n >> 1]);
    SCHED();
    if constexpr (n + 6 < 28) ra[(n + 6) % 7] = trread(WAT(n + 6));
    if constexpr (n == 24) st[0] = *(LAS f32x2*)(psa);
    if constexpr (n == 22 || n == 23) cp[n - 22] = *(LAS f32x4*)(pca + (n - 22) * 64);
    if constexpr (n >= 26) { float a = cp[n - 26][0] + cp[n - 24][0], b = cp[n - 26][1] + cp[n - 24][1]; PIN(a); PIN(b); cp[n - 26][0] = a; cp[n - 26][1] = b; }
    SCHED();
    macc<K>(t[1][n & 1], f, xf[1][n >> 1]);
    SCHED();
    if constexpr (n + 6 < 28) rb[(n + 6) % 7] = trread(WAT(n + 6) + 256);
    if constexpr (n == 24) st[1] = *(LAS f32x2*)(psa + 128);
    if constexpr (n == 22 || n == 23) cp[n - 20] = *(LAS f32x4*)(pca + (n - 20) * 64);
    if constexpr (n >= 26) { float a = cp[n - 26][2] + cp[n - 24][2], b = cp[n - 26][3] + cp[n - 24][3]; PIN(a); PIN(b); cp[n - 26][2] = a; cp[n - 26][3] = b; }
    if constexpr (n == 2) { if ((lane & 15) == 0) *(LAS f32x4*)(pcw) = cs; }
    if constexpr (n == 3) { asm volatile("" ::: "memory"); *(volatile LAS u32*)(pfw) = one; }
    if constexpr (n == 16) sw = *(volatile LAS u32*)(pfa);
    if constexpr (n == 21) { if (__builtin_expect(__builtin_amdgcn_ballot_w64(sw == 0) != 0, 0)) { lds_wait(L, SIG); if (NW > 4) lds_wait(L, SIG + 1); } asm volatile("" ::: "memory"); }
    mid(n, pfw - SIG * 16, one);
    SCHED();
  });
#pragma unroll
  for (int tb = 0; tb < 2; ++tb)
#pragma unroll
    for (int k = 0; k < 2; ++k) t[tb][k] += cp[k] * st[tb].x;
}
DI f32x2 swp16(float a, float b) { auto r = __builtin_amdgcn_permlane16_swap(__builtin_bit_cast(u32, a), __builtin_bit_cast(u32, b), false, false); return f32x2{__builtin_bit_cast(float, (u32)r[0]), __builtin_bit_cast(float, (u32)r[1])}; }
DI f32x2 swp32(float a, float b) { auto r = __builtin_amdgcn_permlane32_swap(__builtin_bit_cast(u32, a), __builtin_bit_cast(u32, b), false, false); return f32x2{__builtin_bit_cast(float, (u32)r[0]), __builtin_bit_cast(float, (u32)r[1])}; }
DI float swadd32(float a, float b) { const f32x2 s = swp32(a, b); return s.x + s.y; }
DI float swadd16(float a, float b) { const f32x2 s = swp16(a, b); return s.x + s.y; }
DI float mx2(float a, float b) { float r; asm("v_max_f32 %0, %1, %2" : "=v"(r) : "v"(a), "v"(b)); return r; }
DI float mx3(float a, float b, float c) { return fmaxf(fmaxf(a, b), c); }
DI float mn3(float a, float b, float c) { return fminf(fminf(a, b), c); }
template <class F> DI void feature_map(const f32x4 (&t)[2][2], const f32x2 (&st)[2], const u32x4 (&wpf)[2], int g, u32x4 (&P)[2][2], F&& mid) {
  const float pad = g < 3 ? 0.f : -3.0e38f;
  const f32x4 z4 = {0.f, 0.f, 0.f, 0.f};
  const float sc[2] = {st[0].y * LOG2E, st[1].y * LOG2E}, scg = g < 2 ? sc[0] : sc[1];
  float p[2][8], m[4], mp[4];
#pragma unroll
  for (int tb = 0; tb < 2; ++tb) {
    const u32x4 tq = {pk(t[tb][0][0], t[tb][0][1]), pk(t[tb][0][2], t[tb][0][3]), pk(t[tb][1][0], t[tb][1][1]), pk(t[tb][1][2], t[tb][1][3])};
    const f32x4 p0 = mfma16(wpf[0], tq, z4), p1 = mfma16(wpf[1], tq, z4);
#pragma unroll
    for (int i = 0; i < 8; ++i) p[tb][i] = i < 4 ? p0[i] : p1[i - 4];
  }
  mid(0);
#pragma unroll
  for (int tb = 0; tb < 2; ++tb) {
    const float* q = p[tb];
    m[2 * tb] = mx3(mx3(q[0], q[1], q[2]), mx3(q[3], q[4], q[5]), mx3(q[5], q[6], q[7]));
    m[2 * tb + 1] = mx3(mx3(-q[0], -q[1], -q[2]), mx3(-q[3], -q[4], -q[5]), mx3(-q[5], -q[6], -q[7]));
  }
  {
    const f32x2 a = swp16(m[0], m[1]), b = swp16(m[2], m[3]);
    const f32x2 c = swp32(mx2(a.x, a.y), mx2(b.x, b.y));
    const float d = mx2(c.x, c.y) * -scg;
    const f32x2 e = swp32(d, d), f0 = swp16(e.x, e.x), f1 = swp16(e.y, e.y);
    m[0] = f0.x; m[1] = f0.y; m[2] = f1.x; m[3] = f1.y;
#pragma unroll
    for (int c2 = 0; c2 < 4; ++c2) mp[c2] = m[c2] + pad;
  }
  float ex[4][8], sm[4];
#pragma unroll
  for (int c = 0; c < 4; ++c) {
    mid(1 + c);
#pragma unroll
    for (int i = 0; i < 8; ++i) {
      ex[c][i] = fexp2(fmaf(c & 1 ? -p[c >> 1][i] : p[c >> 1][i], sc[c >> 1], i & 2 ? mp[c] : m[c]));
    }
    sm[c] = ((ex[c][0] + ex[c][1]) + (ex[c][2] + ex[c][3])) + ((ex[c][4] + ex[c][5]) + (ex[c][6] + ex[c][7]));
  }
  mid(5);
  {
    const float d = frcp(swadd32(swadd16(sm[0], sm[1]), swadd16(sm[2], sm[3])));
    const f32x2 e = swp32(d, d), f0 = swp16(e.x, e.x), f1 = swp16(e.y, e.y);
    sm[0] = f0.x; sm[1] = f0.y; sm[2] = f1.x; sm[3] = f1.y;
  }
#pragma unroll
  for (int c = 0; c < 4; ++c)
#pragma unroll
    for (int e = 0; e < 4; ++e) P[c >> 1][c & 1][e] = pk(ex[c][2 * e] * sm[c], ex[c][2 * e + 1] * sm[c]);
}
DI u32x4 piece(const u32x4 (&rp)[8], int e) {
  u32x4 f;
#pragma unroll
  for (int m = 0; m < 4; ++m) f[m] = __builtin_amdgcn_perm(rp[2 * m + 1][e >> 1], rp[2 * m][e >> 1], (e & 1) ? 0x07060302u : 0x05040100u);
  return f;
}
#define RP_OPS "+v"(rp[0]), "+v"(rp[1]), "+v"(rp[2]), "+v"(rp[3]), "+v"(rp[4]), "+v"(rp[5]), "+v"(rp[6]), "+v"(rp[7])
#define PF1 0
#define PF2 84
#define PF3 120
#define TP0 -1
#define TP1 13
#define TP2 -1
#define TOUCH 5
#define NW3 4
#define THRN0 4
#define DNPRIO 3
#define NWD 3
#define MIDCHK 0
#define HELP 0
#define NHW 7
#define W3LATE 0
#define W3WAIT 1
#define ROT 1
#define EXPT 0
#define PFM 12
#define PSLP 4
#define PLAG 0
#define WTOUCH 0
#define REPLATE 0
#define WB0 8
#define WB1 4
#define ULATE 0
#define VPRIO 3
#define VPRIO0 3
#define VPN 27
#define FPRIO 2
#define CLB 0
#define KAT 99
#define STF 0
#define QN0 0
#define QST 4
#define PB0 3
#define WOT 0
#define WTN 2
#define CLF 0
#define TQ 1
#define TU 0
#define TO 1
#define SN_ 49
#define SS_ 128
#define SD_ 0
#define RPO 0
#define REPE 0
#define UFN 14
#define SBA 0
#define TD 2
#define NR5 16
#define NR6 23
#define G4E 0
#define TS0 2
#define TS1 8
#define TWF 4
#define BP3 0
#define W3J 2
#define TQD -1
#define TOD 0
#define TDD 10
#define CPN 7
#define X0A 5
#define NE 0
#define G6E 0
#define B5E 2
#define B5D 0
#define HW 1
#define PACE 1
#define PS 28
#define PJ0 1
#define PJ1 6
#define ATP 3
#define P1E 0
#define HLA 12
#define FLG13 1
#define H1F 1
#define OLAG 5
#define W4O 0
#define CLW 2
#define CN 50
#define KGO -1
#define QEARLY 1
#define DLY1 4
#define W1 1
#define W2C 0
#define WOE 2
#define W3THR 1
#define HW3 1
#define HW1 1
#define SN 4
#define BATCH 1
#define B23 1
#define WD5L 0
#define A56 1
#define BGO 196
#define BALL 0
#define BSL 4
#define RSL 4
#define H5 1
#define H6 1
#define BORD 1
#define DP0 3
#define AIL 1
#define SEEN 3
#define ADLY 4
#define W6E 0
#define SPLIT 1
#define NCL 4
#define B5L 1
#define A6L 1
#define BO2 2
#define TW 0
#define CL 0
#define CE 1
#define W5D 1
#define W2F 5
#define D2A 12
#define D2B 16
#define SPEC 1
#define CLP 28
#define CLS 3
#define CE2 0
#define DLY5 60
#define CSL 1
#define CF 0
#define OPRIO 3
#define PG 1
#define ISK 1
#define T2 70
#define FXW 8
#define DLY4 16
#define PUBS 5
#define THRN 40
#define DLY2 8
#define DLY3 15
#define GUPRIO 2
enum { S_FREE01 = 0, S_REFILL, S_DONE4, S_WIN3, S_WK, S_WV, S_WQ, S_WSEEN, S_KDONE, S_V, S_PHIK, S_CSV, S_KVA, S_WO, S_CSQ, S_PHI, S_X0, S_X1, S_XA, S_CSK, S_CS, S_WOFA, S_WOFB, S_EXA, S_EXB, S_W2A, S_W2B, S_HFA, S_HFB, S_CONS, S_SPARE, S_XF0 };

__global__ void __launch_bounds__(512, 2) fused_block(const char* __restrict__ x, const char* __restrict__ wqkv, const char* __restrict__ wphi, const char* __restrict__ wo,
                                                      const char* __restrict__ wg, const char* __restrict__ wu, const char* __restrict__ wd, char* __restrict__ out, char* ws) {
  extern __shared__ __attribute__((aligned(16))) char smem[];
  LAS char* L = (LAS char*)smem;
  const int tid = threadIdx.x, lane = tid & 63, wave = __builtin_amdgcn_readfirstlane(tid >> 6), w2 = wave & 3;
  const int b = blockIdx.x, li = b >> 3, xp = (b & 7) >> 1, xh = b & 1;
  const int tile = 4 * xp + (li >> 3), r = 8 * xh + (li & 7);
  const int tile2 = 4 * xp + 2 * xh + (li >> 4), r2 = li & 15;
  const int tg = r2 >= 7 ? 1 : 0, cg = r2 - 7 * tg;
  const int l15 = lane & 15, g = lane >> 4, q4 = l15 >> 2, p4 = lane & 3, hh = lane >> 5;
  const int head = r;
  {
    u64 pc_; asm volatile("s_getpc_b64 %0" : "=s"(pc_));
    touch((const char*)pc_, min((b & ~7) * 256 + (tid >> 5) * 128, CODE_PF));
    *(LAS u32*)(L + L_FLG + (tid & 127) * 4) = 0u;
  }
  int tn = wave < 2 ? 28 : 85 - 24 * wave, ti = (b & 7) * 64 + li * 2 + wave;
  asm volatile("" : "+s"(tn), "+s"(ti));
  ti = wave < 2 ? ti : b;
  u32 tvo = min(ti * tn + min(lane, tn - 1), wave == 3 ? 3135 : wave == 2 ? 9407 : 14335) * 128, xvo = (tile * 128 + w2 * 32 + (lane >> 3)) * 896 + (((lane & 7) ^ (lane >> 3)) * 16);
  asm volatile("s_waitcnt lgkmcnt(0)\n\ts_barrier" : "+v"(tvo), "+v"(xvo) :: "memory");
  asm volatile("" :: "s"(x), "s"(wqkv), "s"(wphi), "s"(wo), "s"(wg), "s"(wu), "s"(wd), "s"(out), "s"(ws));
  STAMP(0);
  u64 epoch;
  if (wave < 4) {
    const char* tb_ = wave < 2 ? x : wave == 2 ? wqkv : wo;
    asm volatile("" : "+s"(tb_));
    touch(tb_, tvo);
  }
  asm volatile("s_load_dwordx2 %0, %1, 0x0" : "=s"(epoch) : "s"(ws) : "memory");
  if (wave >= 4) {
    const __amdgpu_buffer_rsrc_t rx = __builtin_amdgcn_make_buffer_rsrc((void*)(x - 2048), 0, 2048 * 896 + 2048, 0x00020000);
    sfor<0, 7>([&](auto NC) LAM {
      dma_x<4 * NC.value>(rx, xvo, w2 * 28672);
      if (NC.value == X0A) { asm volatile("s_waitcnt vmcnt(%0)" :: "n"(4 * X0A - 10) : "memory"); lds_signal(L, S_X0, w2); }
    });
  }
  asm volatile("s_waitcnt lgkmcnt(0)" : "+s"(epoch) :: "memory");
  const __amdgpu_buffer_rsrc_t rs = __builtin_amdgcn_make_buffer_rsrc((void*)ws, 0, WS_USED, 0x00020000);
  const u32x4 ones = {0x3f803f80u, 0x3f803f80u, 0x3f803f80u, 0x3f803f80u};
  const f32x4 z4 = {0.f, 0.f, 0.f, 0.f};
  auto role = [&](auto AC) __attribute__((always_inline)) {
  constexpr bool A = AC.value;
  if (A) asm volatile("; A" ::: "memory"); else asm volatile("; B" ::: "memory");
  u32x4 rp[8];
  const int fvo = lane < 16 ? lane * 8 : NOWHERE;
  const int uidx = (tile * 10 + __builtin_popcount(~0x2A54u & ((1u << head) - 1u))) * 4 + w2, unit = uidx * 49 / 80;
  const bool has_unit = !((0x2A54 >> head) & 1) && (uidx + 1) * 49 / 80 != unit;
  const int u3 = unit >> 1, umat = unit & 1, ukq = u3 / 28, ufl = u3 - 28 * ukq;
  const char* gusrc = ws + WS_WGU + 7 * r * SLOT;
  const u32 guo = WS_WGU + 7 * r * SLOT + w2 * 7168;
#define GUBLK(j) dma7<(j) * SLOT, gu_slot(j) * SLOT>(rs, lane * 16, guo, w2 * 7168)
  const u32 wvo = (lane >> 2) * 2688 + (((lane & 3) ^ (2 * (lane >> 5))) * 16);
  const char* wbase = wqkv + head * 56 - (head & 1) * 8 + 16 * w2 * 2688;
  const u32 wr0 = (8 * g + q4) * 64 + (((p4 + (head & 1)) ^ (4 * (g & 1))) * 8), wr1 = (8 * g + q4) * 64 + ((((4 + p4 + (head & 1)) & 7) ^ (4 * (g & 1))) * 8);
  const int tb = w2; constexpr int kh = A ? 0 : 1;
  const int hho = WS_HH + (((tile * 2 + (w2 >> 1)) * 4 + (r >> 2)) * 56 + (r & 3) * 2 + (w2 & 1)) * 1024;
  const int tok = tile2 * 128 + tg * 64 + tb * 16 + l15;
  const int hvo = WS_H + ((tok >> 5) * 28 + 4 * cg + (g >> 1)) * 1024 + ((g & 1) * 32 + (tok & 31)) * 16;
  const int sto = WS_ST + (2 * cg * 2048 + tile2 * 128 + tg * 64 + tb * 16) * 8;
  const u32x4 empty4 = {EMPTY, EMPTY, EMPTY, EMPTY};
#define HH_CLEAR(i0, i1) _Pragma("unroll") for (int i = i0; i < i1; ++i) ST16(rs, empty4, lane * 16 + (i & 1) * 2048, WS_HH + (((tile2 * 2 + r2 - 14) * 4 + w2) * 56 + kh + (i >> 1) * 4) * 1024)
#define HH_FLAG() FLAG(WS_FH + ((tile2 * 4 + 2 * (r2 - 14) + kh) * 32 + 28 + w2) * 128, 1)
  u32x4 wof[8][4];
  f32x4 csp[7];
  u64 tag;
#define WOF_READ(i0, i1) do { u32 wa_ = L_WO + (kh * 224 + 4 * g + q4) * 128 + 16 * p4 + 64 * (q4 >> 1), wb_ = wa_ ^ 64, wc_ = wa_ + (g == 3 ? 0 : 2048), wd_ = wc_ ^ 64; \
    asm volatile("" : "+v"(wa_), "+v"(wb_), "+v"(wc_), "+v"(wd_)); \
    _Pragma("unroll") for (int i = i0; i < i1; ++i) _Pragma("unroll") for (int k = 0; k < 4; ++k) { \
      const u32x2 a_ = trread(L + (k >> 1 ? wb_ : wa_) + 28 * i * 128 + (k & 1) * 8), c_ = trread(L + (k >> 1 ? wd_ : wc_) + 28 * i * 128 + (k & 1) * 8); \
      wof[i][k] = u32x4{a_.x, a_.y, c_.x, c_.y}; } } while (0)
#define XF_READ(i) _Pragma("unroll") for (int s = 0; s < 14; ++s) xf[i][s] = *(LAS u32x4*)((s & 1 ? xr1 : xr0) + (i) * 14336 + (s >> 1) * 2048)
#define LN_STATS(kh) do { f32x4 g1 = z4, g2 = z4; \
    _Pragma("unroll") for (int s = 0; s < 14; ++s) { g1 = mfma16(ones, xf[kh][s], g1); g2 = mfma16(xf[kh][s], xf[kh][s], g2); } \
    const float d01 = (p4 & 1) ? g2[1] : g2[0], d23 = (p4 & 1) ? g2[3] : g2[2], s2 = (p4 & 2) ? d23 : d01; \
    const float nmu = g1[0] * (-1.f / 448.f), var = fmaxf(s2 * (1.f / 448.f) - nmu * nmu, 0.f); \
    if (g == q4) *(LAS f32x2*)(L + L_ST1 + (w2 * 32 + kh * 16 + l15) * 8) = f32x2{nmu, __builtin_amdgcn_rsqf(var + 1e-5f)}; } while (0)
#define WPF() do { u32 wp[8]; \
    _Pragma("unroll") for (int j = 0; j < 8; ++j) wp[j] = *(LAS u32*)(L + L_WPHI + 4 * g * 56 + min(l15, 13) * 4 + ((j & 3) + 16 * (j >> 2)) * 56); \
    _Pragma("unroll") for (int e = 0; e < 4; ++e) { const bool z = e >= 2 && g == 3; \
      wpf[0][e] = z ? 0u : __builtin_amdgcn_perm(wp[2 * e + 1], wp[2 * e], 0x05040100u); wpf[1][e] = z ? 0u : __builtin_amdgcn_perm(wp[2 * e + 1], wp[2 * e], 0x07060302u); } \
    asm volatile("" : "+v"(wpf[0]), "+v"(wpf[1])); } while (0)
  {
    u32x4 xf[2][14], wpf[2];
    f32x2 st[2];
    LAS char* xr0 = L + w2 * 28672 + l15 * 128 + ((g ^ (l15 & 7)) * 16);
    LAS char* xr1 = L + w2 * 28672 + l15 * 128 + (((4 + g) ^ (l15 & 7)) * 16);
    LAS char* pw = L + L_PKV + (w2 * 32 + l15) * 224 + g * 8;
    const char* wos = wo + (r2 < 14 ? cg : 0) * 128 + w2 * 56 * 896;
    const u32 wovo = (lane >> 3) * 896 + (((lane & 7) ^ ((lane >> 2) & 4)) * 16);
#define WO_HALF(hf_) _Pragma("unroll") for (int n = 0; n < 7; ++n) dma16(wos + ((hf_) * 224 + n * 8) * 896, wovo, L_WO + ((hf_) * 28 + w2 * 7 + n) * 1024)
    int clo = WS_O + ((tile * 8 + 2 * w2) * 16 + head) * 1024, clh = r2 < 14 ? hvo : NOWHERE, cls = r2 < 14 && lane < 16 ? (lane >> 3) * 16384 + (lane & 7) * 16 : NOWHERE, clt = sto;
    int kvo = WS_KVP + ((head * 16 + tile) * 4 + w2) * 1024, fko = WS_FKV + ((head * 4 + w2) * 16 + tile) * 128;
    u32x4 em4 = empty4;
    u32 wtv = min(lane, 24) * 128;
    const char* wts = wo + min(li * 98 + w2 * 25, 3136 - 25) * 128;
    auto clear = [&](int c) LAM {
      if (c == 0) ST16(rs, em4, lane * 16, clo);
      if (c == 1) ST16(rs, em4, lane * 16, clo + 16384);
      if (c == 2) ST16(rs, em4, clh, 0);
      if (c == 3) ST16(rs, em4, clh, 2048);
      if (c == 4) ST16(rs, em4, cls, clt);
    };
    if constexpr (A) {
      if (wave == 2) { dma16(wphi + head * 1568, lane * 16, L_WPHI); dma16(wphi + head * 1568, min(lane + 64, 97) * 16, L_WPHI + 1024); }
      __builtin_amdgcn_s_waitcnt(0x0F70);
      tag = epoch * 6364136223846793005ull + 1442695040888963407ull;
      STAMP(1);
#define WKLOAD() _Pragma("unroll") for (int i = 0; i < 7; ++i) dma16(wbase + 896 + 64 * i * 2688, wvo, L_WK + (4 * i + w2) * 1024)
      if (KAT == 3) { WKLOAD(); if (wave == 2) { WAITVM(7); lds_signal(L, S_PHI, 0); } }
      else if (wave == 2) { if (CLF == 9) WAITVM(5); else WAITVM(0); lds_signal(L, S_PHI, 0); }
      if (KAT > 3 && KAT < 28) { lds_wait1(L, S_SPARE, w2); WKLOAD(); }
      lds_wait1(L, S_PHI, 0);
      WPF();
#define DBITE() dma4(wd + min(b * 49 + w2 * 13, 12544 - 13) * 128, min(lane, 12) * 128, L_JUNK)
      if (TD == 9) {
        __builtin_amdgcn_s_sleep(TS0);
        if (w2 & 1) __builtin_amdgcn_s_sleep(TS1);
        if (w2 & 2) { __builtin_amdgcn_s_sleep(TS1); __builtin_amdgcn_s_sleep(TS1); }
        DBITE();
      }
      if (TD == 10 && w2 == 0) DBITE();
      lds_wait1(L, S_X0, w2);
      XF_READ(0);
      if (w2 < 2) lds_signal(L, S_XF0, w2);
      if (TD == 10 && w2 == 1) DBITE();
      if (KAT > 28) { WKLOAD(); }
      if (TD == 10 && w2 == 2) DBITE();
      if (TD == 11) DBITE();
      STAMP(27);
      LN_STATS(0);
      SCHED();
      STAMP(2);
      lds_wait1(L, S_X1, w2);
      XF_READ(1);
      if (QEARLY == 3 && w2 < 2) lds_signal(L, S_SPARE, w2);
      STAMP(3);
      if (SBA) LN_STATS(1);
      WAITVM(0);
      STAMP(4);
      lds_signal(L, S_WK, w2);
      if (TD == 10 && w2 == 3) DBITE();
      if (TD == 12) DBITE();
      f32x4 t[2][2];
      u32 pr = L_PKV + (4 * g + q4) * 224 + 8 * p4;
      asm volatile("" : "+v"(pr), "+v"(pw));
      asm volatile("" : "+s"(clo), "+v"(clh), "+v"(cls), "+s"(clt), "+s"(kvo), "+s"(fko), "+s"(tag), "+v"(em4));
      if (WOT == 5) asm volatile("" : "+v"(wtv), "+s"(wts));
      project<0, S_CSK, 8>(L, L_WK + wr0, L_WK + wr1, xf, w2, lane, L_CSP, t, st, [&]() LAM {
        lds_wait(L, S_WK);
        STAMP(5);
        __builtin_amdgcn_s_setprio(2);
      }, [&](int n, LAS char* sg, u32 one) LAM {
        if (n == KGO) *(volatile LAS u32*)(sg + S_KDONE * 16) = one;
      });
      STAMP(6);
      u32x4 P[2][2];
      __builtin_amdgcn_s_setprio(FPRIO);
      feature_map(t, st, wpf, g, P, [&](int i) LAM {
        if (WOT == 5 && i == 0) { SCHED(); dma4(wts, wtv, L_JUNK); SCHED(); }
        if (CLF == 1 && i > 0) { SCHED(); clear(i - 1); SCHED(); }
        if (CLF == 2 && i == 5) { SCHED(); clear(0); clear(1); clear(2); clear(3); clear(4); SCHED(); }
        if (CLF == 3 && i >= 3) { SCHED(); clear(2 * i - 6); clear(2 * i - 5); SCHED(); }
        if (CLF == 4 && i == 1) { SCHED(); clear(0); clear(1); clear(2); clear(3); clear(4); SCHED(); }
      });
#pragma unroll
      for (int c = 0; c < 4; ++c) {
        LAS char* p = pw + (c >> 1) * 16 * 224 + (c & 1) * 64;
        *(LAS u32x2*)(p) = u32x2{P[c >> 1][c & 1].x, P[c >> 1][c & 1].y}; *(LAS u32x2*)(p + 32) = u32x2{P[c >> 1][c & 1].z, P[c >> 1][c & 1].w};
      }
      lds_signal(L, S_PHIK, w2);
      STAMP(7);
      if (CLF == 0 || CLF == 8) { clear(0); clear(1); }
      if (CLF == 0 || CLF == 7) { clear(2); clear(3); clear(4); }
      lds_wait2(L, S_V, S_PHIK);
      u32x4 pa[4], pb[8];
#pragma unroll
      for (int m = 0; m < 4; ++m) {
        pa[m] = trfrag(L + pr + w2 * 32 + m * 32 * 224, 16 * 224);
        pb[2 * m] = trfrag(L + pr + m * 32 * 224 + 128, 16 * 224); pb[2 * m + 1] = trfrag(L + pr + m * 32 * 224 + 160, 16 * 224);
      }
      SCHED();
      f32x4 kv[2] = {};
#pragma unroll
      for (int m = 0; m < 8; ++m) kv[m & 1] = mfma16(pa[m >> 1], pb[m], kv[m & 1]);
      const u32x4 kvb = {pk(kv[0][0], kv[0][1]), pk(kv[0][2], kv[0][3]), pk(kv[1][0], kv[1][1]), pk(kv[1][2], kv[1][3])};
      ST16(rs, kvb, lane * 16, kvo);
      STAMP(8);
      DRAIN();
      FLAG(fko, 1);
      __builtin_amdgcn_s_setprio(0);
      STAMP(9);
      if (HW1) { lds_signal(L, S_XA, w2); lds_wait(L, S_XA); }
#define DTOUCH() dma4(wd + b * 49 * 128, min(lane, 48) * 128, L_JUNK)
      if (TD == 2 && wave == 0) DTOUCH();
      if (r2 < 14 || !ISK) { WO_HALF(0); WO_HALF(1); } else { HH_CLEAR(0, NE); if (NE < 8) __builtin_amdgcn_s_sleep(20); }
      poll_flags(ws, WS_FKV + ((head * 4 + w2) * 16 + l15) * 128, lane < 16, tag);
      lds_signal(L, S_WO, w2);
      STAMP(10);
      u32x4 tt[16];
#pragma unroll
      for (int i = 0; i < 16; ++i) tt[i] = __builtin_bit_cast(u32x4, __builtin_amdgcn_raw_buffer_load_b128(rs, lane * 16, WS_KVP + head * 65536 + w2 * 1024 + i * 4096, 16));
      u32 fgo;
      {
        const int pl = (tile & 1) * 16 + min(lane >> 2, 13);
        fgo = wave == 0 ? WS_FKV + ((l15 * 4 + 2 * tg + (g & 1)) * 16 + tile2) * 128
            : wave == 1 ? WS_FKV + (((((tile >> 1) & 1) * 8 + (pl & 7)) * 4 + p4) * 16 + (tile & 12) + (pl >> 3)) * 128
                        : WS_FG + (min(((7 * r) >> 2) + g, (7 * r + 6) >> 2) * 16 + (l15 & 8) + min(lane & 7, 6)) * 128;
      }
      u64 fg = __hip_atomic_load((const GAS u64*)(ws + fgo), RLX_AGENT);
      SCHED();
      const u32 one = g == q4 ? ((p4 & 1) ? 0x3f800000u : 0x3f80u) : 0u, s01 = p4 < 2 ? one : 0u, s23 = p4 < 2 ? 0u : one;
      const u32x4 sel0 = {s01, s23, 0u, 0u}, sel1 = {0u, 0u, s01, s23};
      f32x4 s0 = {}, s1 = {};
#pragma unroll
      for (int i = 0; i < 16; ++i) { s0 = mfma16(sel0, tt[i], s0); s1 = mfma16(sel1, tt[i], s1); }
      LAS char* kw = L + L_KVA + (w2 >> 1) * 1024 + lane * 16 + (w2 & 1) * 8;
      *(LAS u32x2*)(kw) = u32x2{pk(s0[0], s0[1]), pk(s0[2], s0[3])};
      *(LAS u32x2*)(kw + 2048) = u32x2{pk(s1[0], s1[1]), pk(s1[2], s1[3])};
      lds_signal(L, S_KVA, w2);
      if (TD == 4 && wave == 2) DTOUCH();
      STAMP(11);
      if (WOE == 3) { WO_HALF(1); WAITVM(0); lds_signal(L, S_WO, w2); }
      asm volatile("" : "+v"(fg) :: "memory");
      if (!__all(fg == tag + 1)) poll_flags(ws, fgo, true, tag);
      __builtin_amdgcn_fence(__ATOMIC_ACQUIRE, "wavefront");
      lds_signal(L, S_WOFA, w2);
      lds_wait1(L, S_WOFA, 3);
      const int fl = min(((7 * r) >> 2) + w2, (7 * r + 6) >> 2);
#pragma unroll
      for (int i = 0; i < 7; ++i) asm volatile("global_load_dwordx4 %0, %1, %2 offset:%3 sc1" : "=v"(csp[i]) : "v"(((7 * r) >> 2) + w2 == fl ? lane * 16 : 0), "s"(ws + WS_CS + fl * 7168 + 4096), "n"(i * 1024 - 4096) : "memory");
      GUBLK(2);
      if (r2 < 14 || !ISK) { lds_wait(L, S_WO); WOF_READ(0, 8); }
      SCHED();
#define CS_OUT(k_) do { asm volatile("s_waitcnt vmcnt(%7)" : "+v"(csp[0]), "+v"(csp[1]), "+v"(csp[2]), "+v"(csp[3]), "+v"(csp[4]), "+v"(csp[5]), "+v"(csp[6]) : "n"(k_) : "memory"); \
        const f32x4 sm = ((csp[0] + csp[1]) + (csp[2] + csp[3])) + ((csp[4] + csp[5]) + csp[6]); \
        const float e0 = swadd32(sm[0], sm[1]), e1 = swadd32(sm[2], sm[3]);     \
        const int j0 = 4 * (((7 * r) >> 2) + w2) + 2 * (lane & 1) - 7 * r, cr = ((g & 1) * 16 + hh * 8 + (l15 >> 1)) * 4; \
        if (j0 >= 0 && j0 < 7) *(LAS float*)(L + L_CS + j0 * 128 + cr) = e0; \
        if (j0 + 1 >= 0 && j0 + 1 < 7) *(LAS float*)(L + L_CS + (j0 + 1) * 128 + cr) = e1; } while (0)
      lds_signal(L, S_WOFB, w2);
      if (!CSL) CS_OUT(7);
      STAMP(24);
    } else {
      const int rg = lane >> 3;
      const u32 lvo = rg * 8 * 3584 + (lane & 7) * 16;
      const char* us = (umat ? wu : wg) + ukq * 64 * 3584 + ufl * 128;
      asm volatile("" : "+s"(us));
      auto repack = [&]() LAM {
        if (has_unit) {
          const int so = rg * 512 + (lane & 7) * 16;
          const int sb = WS_WGU + 4 * ufl * SLOT + ukq * 4096 + umat * 256;
          const int s1 = SLOT, s2 = 2 * SLOT;
          float c[8];
#pragma unroll
          for (int e = 0; e < 8; ++e) {
            const u32x4 f = piece(rp, e);
            ST16(rs, f, so + (e & 1) * 128, sb + ((e >> 1) & 1) * s1 + (e >> 2) * s2);
            c[e] = mfma16(ones, f, z4)[0];
          }
          const f32x4 cv = g & 1 ? f32x4{c[4], c[5], c[6], c[7]} : f32x4{c[0], c[1], c[2], c[3]};
          ST16(rs, cv, g < 2 ? (l15 >> 3) * 512 + (lane & 7) * 32 + g * 16 : NOWHERE, WS_CS + ufl * 7168 + ukq * 1024 + (umat & 1) * 256);
        }
      };
#define RPLOAD() _Pragma("unroll") for (int j = 0; j < 8; ++j) asm volatile("global_load_dwordx4 %0, %1, %2" : "=v"(rp[j]) : "v"(lvo), "s"(us + j * 3584) : "memory")
      if (RPO == 2 && has_unit) { RPLOAD(); }
      tag = epoch * 6364136223846793005ull + 1442695040888963407ull;
      STAMP(1);
      XF_READ(0);
      if (w2 < 2) lds_signal(L, S_XF0, 2 + w2);
      if (RPO == 2 && has_unit) WAITVM(8); else WAITVM(0);
      lds_signal(L, S_X1, w2);
      if (RPO == 1 && has_unit) { RPLOAD(); }
      STAMP(2);
      if (STF < 7) lds_wait(L, S_XF0);
      XF_READ(1);
      if (QEARLY == 3 && w2 < 2) lds_signal(L, S_SPARE, 2 + w2);
      SCHED();
#define WDST(c) ((c) * 1024 + ((c) >= 14 ? SLOT - 14336 : 0))
#pragma unroll
      for (int i = 0; i < 7 - STF; ++i) dma16(wbase + 1792 + 64 * i * 2688, wvo, L_WV + WDST(4 * i + w2));
      STAMP(3);
      __builtin_amdgcn_s_setprio(PB0);
      if (!SBA) LN_STATS(1);
      lds_signal(L, S_CS, w2);
      if (STF == 7) lds_wait(L, S_XF0);
#pragma unroll
      for (int i = 7 - STF; i < 7; ++i) dma16(wbase + 1792 + 64 * i * 2688, wvo, L_WV + WDST(4 * i + w2));
      STAMP(4);
      f32x4 t[2][2];
      asm volatile("" : "+v"(pw));
      if (WOT == 4) asm volatile("" : "+v"(wtv), "+s"(wts));
      project<SLOT - 14336, S_CSV, 4>(L, L_WV + wr0, L_WV + wr1, xf, w2, lane, L_CSP + 256, t, st, [&]() LAM {
      if (QEARLY == 3) {
        lds_wait(L, S_SPARE);
#pragma unroll
        for (int i = 0; i < 7; ++i) dma16(wbase + 64 * i * 2688, wvo, L_WQ + WDST(4 * i + w2));
      }
      if (REPE == 3) { asm volatile("s_waitcnt vmcnt(%8)" : RP_OPS : "n"(QEARLY == 3 ? 14 : 7) : "memory"); repack(); if (has_unit) { if (QEARLY == 3) WAITVM(16); else WAITVM(9); } }
      if (RPO == 0 && has_unit) { RPLOAD(); } else if (REPE == 3 && has_unit) {} else if (QEARLY == 3) WAITVM(7); else WAITVM(0);
      STAMP(28);
      if (RPO) {} else if (QEARLY == 3) WAITVM(15); else WAITVM(8);
      lds_signal(L, S_WV, w2);
      STAMP(29);
      lds_wait2(L, S_WV, S_WK);
      STAMP(30);
      if (QEARLY == 1) {
#pragma unroll
        for (int i = 0; i < 7; ++i) dma16(wbase + 64 * i * 2688, wvo, L_WQ + WDST(4 * i + w2));
      }
        if (REPE == 2) { asm volatile("s_waitcnt vmcnt(7)" : RP_OPS :: "memory"); repack(); }
        if (TD == 8 && !has_unit && !((0x2A54 >> head) & 1)) dma4(wd + min((uidx - unit) * 51, 12544 - 51) * 128, min(lane, 50) * 128, L_JUNK);
        if (KGO >= 0) lds_wait1(L, S_KDONE, w2);
        STAMP(5);
        __builtin_amdgcn_s_setprio(VPRIO0);
      }, [&](int n, LAS char*, u32) LAM {
        if (n == VPN) __builtin_amdgcn_s_setprio(VPRIO);
        if (WOT == 4 && n == WTN) dma4(wts, wtv, L_JUNK);
        if (QEARLY == 2 && n >= QN0 && (n - QN0) % QST == 0 && (n - QN0) / QST < 7) { const int i = (n - QN0) / QST; dma16(wbase + 64 * i * 2688, wvo, L_WQ + WDST(4 * i + w2)); }
      });
#pragma unroll
      for (int c = 0; c < 4; ++c) t[c >> 1][c & 1] *= st[c >> 1].y;
      STAMP(6);
#pragma unroll
      for (int c = 0; c < 2; ++c) {
        LAS char* p = pw + c * 16 * 224 + 128;
        *(LAS u32x2*)(p) = u32x2{pk(t[c][0][0], t[c][0][1]), pk(t[c][0][2], t[c][0][3])};
        *(LAS u32x2*)(p + 32) = g == 3 ? u32x2{0x3f80u, 0u} : u32x2{pk(t[c][1][0], t[c][1][1]), pk(t[c][1][2], t[c][1][3])};
      }
      lds_signal(L, S_V, w2);
      __builtin_amdgcn_s_setprio(0);
      STAMP(7);
      if (!QEARLY) {
#pragma unroll
        for (int i = 0; i < 7; ++i) dma16(wbase + 64 * i * 2688, wvo, L_WQ + WDST(4 * i + w2));
      }
      if (WOE == 0) WO_HALF(0);
      if (WOE < 2) { lds_wait(L, S_KDONE); WO_HALF(1); }
      if (REPE == 0) asm volatile("s_waitcnt vmcnt(%8)" : RP_OPS : "n"(QEARLY == 3 && !RPO ? 0 : 7) : "memory");
#define UFLAG() FLAGC(has_unit, WS_FG + ((ufl * 2 + umat) * 8 + ukq) * 128, 1)
      if (REPE == 0) repack();
      STAMP(8);
      WPF();
      if (!has_unit || (REPE == 3 && QEARLY != 3)) WAITVM(0); else WAITVM(9);
      if (UFN < 0) { WAITVM(0); UFLAG(); }
      lds_signal(L, S_WQ, w2);
      project<SLOT - 14336, S_CSQ, 4>(L, L_WQ + wr0, L_WQ + wr1, xf, w2, lane, L_CSP + 512, t, st, [&]() LAM {
        lds_wait(L, S_WQ);
        STAMP(9);
      }, [&](int n, LAS char*, u32) LAM {
        if (n == UFN) { WAITVM(0); UFLAG(); }
      });
      STAMP(10);
      if (REPLATE == 2) repack();
      u32x4 pq[2][2];
      feature_map(t, st, wpf, g, pq, [](int) {});
      asm volatile("" : "+v"(pq[0][0]), "+v"(pq[0][1]), "+v"(pq[1][0]), "+v"(pq[1][1]));
      STAMP(11);
      if (REPLATE == 1) repack();
      if (TD == 13 || TD == 14) {
        lds_wait(L, S_WO);
        if (r2 < 14) WOF_READ(0, TWF); else __builtin_amdgcn_s_sleep(2 * TWF);
        SCHED();
        if (TD == 14) DBITE(); else if (w2 == 0) DTOUCH();
        SCHED();
        if (r2 < 14) WOF_READ(TWF, 8);
      } else if (r2 < 14 || !ISK) { lds_wait(L, S_WO); WOF_READ(0, 8); }
      STAMP(24);
      if (REPLATE == 2) { WAITVM(0); UFLAG(); }
      __builtin_amdgcn_s_setprio(OPRIO);
      lds_wait(L, S_KVA);
      if (REPLATE == 1) { WAITVM(0); UFLAG(); }
      STAMP(27);
      u32x4 ka[4];
#pragma unroll
      for (int i = 0; i < 4; ++i) ka[i] = *(LAS u32x4*)(L + L_KVA + i * 1024 + lane * 16);
      f32x4 o[2][2];
#pragma unroll
      for (int c = 0; c < 4; ++c) o[c >> 1][c & 1] = mfma16(ka[2 * (c & 1) + 1], pq[c >> 1][1], mfma16(ka[2 * (c & 1)], pq[c >> 1][0], z4));
      const f32x2 da = swp16(o[0][1][0], o[0][1][0]), db = swp16(o[1][1][0], o[1][1][0]);
      const float den[2] = {swp32(da.y, da.y).y, swp32(db.y, db.y).y};
#pragma unroll
      for (int c = 0; c < 2; ++c) {
        const float rd = frcp(den[c] + 1e-6f), r3 = g == 3 ? 0.f : rd;
        const u32x4 ob = {pk(o[c][0][0] * rd, o[c][0][1] * rd), pk(o[c][0][2] * rd, o[c][0][3] * rd), pk(o[c][1][0] * r3, o[c][1][1] * r3), filled(pk(o[c][1][2] * r3, o[c][1][3] * r3))};
        ST16(rs, ob, lane * 16, WS_O + ((tile * 8 + 2 * w2 + c) * 16 + head) * 1024);
      }
      STAMP(12);
      if (WOE == 3) lds_wait(L, S_WO);
      *(volatile LAS u32*)(L + L_FLG + S_W2A * 16 + w2 * 4) = (u32)__builtin_amdgcn_s_memrealtime();
      lds_signal(L, S_W2B, w2);
      __builtin_amdgcn_s_setprio(0);
      if (TD == 3 && w2 == 0) DTOUCH();
    }
  }
  STAMP(13);
  u32x4 hres = {0u, 0u, 0u, 0u};
  const int xo = tok * 896 + cg * 128 + kh * 64 + g * 16;
  STAMP(23);
  if (r2 < 14) {
    u32x4 xr, of[8];
    asm volatile("global_load_dwordx4 %0, %1, %2" : "=v"(xr) : "v"(xo), "s"(x) : "memory");
    lds_wait2(L, S_W2B, S_WOFA);
#define WIN2() do { if (A ? W1 < 2 : W1 < 3) GUBLK(A ? 0 : 1); } while (0)
    if (W2F == 1 || W2F == 3) WIN2();
    if (W2F == 4 && !A) { GUBLK(0); GUBLK(1); }
    if (W2F == 5 && !A) GUBLK(1);
    if (T2) {
      const u32x4 ts = *(volatile LAS u32x4*)(L + L_FLG + S_W2A * 16);
      const u32 t0 = __builtin_amdgcn_readfirstlane(ts.x), due = t0 + T2 + __builtin_amdgcn_readfirstlane(max(max(0, (int)(ts.y - ts.x)), max((int)(ts.z - ts.x), (int)(ts.w - ts.x))));
#pragma nounroll
      for (u32 sp = 0; sp < 64 && (int)((u32)__builtin_amdgcn_s_memrealtime() - due) < 0; ++sp) __builtin_amdgcn_s_sleep(1);
    } else __builtin_amdgcn_s_sleep(A ? D2A : D2B);
    STAMP(14);
#define OFA(c_) _Pragma("unroll") for (int i = 0; i < 8; ++i) \
      asm volatile("global_load_dwordx4 %0, %1, %2 offset:%3 sc1" : c_(of[i]) : "v"(lane * 16), "s"(ws + WS_O + ((tile2 * 8 + tg * 4 + tb) * 16 + kh * 8 + 4) * 1024), "n"(i * 1024 - 4096) : "memory")
#define OFW(k_) asm volatile("s_waitcnt vmcnt(%9)" : "+v"(of[0]), "+v"(of[1]), "+v"(of[2]), "+v"(of[3]), "+v"(of[4]), "+v"(of[5]), "+v"(of[6]), "+v"(of[7]), "+v"(xr) : "n"(k_) : "memory")
#define OFBAD() (!__all(max(max(max(of[0].w, of[1].w), max(of[2].w, of[3].w)), max(max(of[4].w, of[5].w), max(of[6].w, of[7].w))) != EMPTY))
    OFA("=v");
    f32x4 a[4] = {};
    u32 mx = 0;
#pragma unroll
    for (int i = 0; i < 8; ++i) {
      asm volatile("s_waitcnt vmcnt(%1)" : "+v"(of[i]) : "n"(7 - i) : "memory");
      if (i == PG - 1) { lds_wait(L, S_WOFB); }
      if (i == 7) GUBLK(A ? 0 : 3);
#pragma unroll
      for (int k = 0; k < 4; ++k) a[k] = mfma16(wof[i][k], of[i], a[k]);
      mx = max(mx, of[i].w);
      SCHED();
    }
    asm volatile("" : "+v"(xr));
    if (__builtin_expect(!__all(mx != EMPTY), 0)) {
#pragma nounroll
      for (u32 sp = 0; sp < SPIN_CAP; ++sp) { __builtin_amdgcn_s_sleep(RSL); OFA("+v"); OFW(0); if (!OFBAD()) break; }
#pragma unroll
      for (int i = 0; i < 8; ++i)
#pragma unroll
        for (int k = 0; k < 4; ++k) a[k] = mfma16(wof[i][k], of[i], i ? a[k] : z4);
    }
    STAMP(25);
    *(LAS f32x4*)(L + L_EX + wave * 2048 + lane * 16) = kh ? a[0] : a[2];
    *(LAS f32x4*)(L + L_EX + wave * 2048 + 1024 + lane * 16) = kh ? a[1] : a[3];
    lds_signal(L, A ? S_EXA : S_EXB, w2);
    lds_wait1(L, A ? S_EXB : S_EXA, w2);
    STAMP(15);
    const f32x4 c0 = (kh ? a[2] : a[0]) + *(LAS f32x4*)(L + L_EX + (wave ^ 4) * 2048 + lane * 16);
    const f32x4 c1 = (kh ? a[3] : a[1]) + *(LAS f32x4*)(L + L_EX + (wave ^ 4) * 2048 + 1024 + lane * 16);
    const f32x4 h0 = c0 + f32x4{blo(xr.x), bhi(xr.x), blo(xr.y), bhi(xr.y)}, h1 = c1 + f32x4{blo(xr.z), bhi(xr.z), blo(xr.w), bhi(xr.w)};
    hres = u32x4{pk(h0[0], h0[1]), pk(h0[2], h0[3]), pk(h1[0], h1[1]), filled(pk(h1[2], h1[3]))};
    ST16(rs, hres, hvo, kh * 2048);
    const f32x4 r0 = {blo(hres.x), bhi(hres.x), blo(hres.y), bhi(hres.y)}, r1 = {blo(hres.z), bhi(hres.z), blo(hres.w), bhi(hres.w)};
    const f32x4 a1 = r0 + r1, a2q = r0 * r0 + r1 * r1;
    float s1 = (a1[0] + a1[1]) + (a1[2] + a1[3]), s2 = (a2q[0] + a2q[1]) + (a2q[2] + a2q[3]);
    { const float t = swadd16(s1, s2); const f32x2 a2 = sw32(t); s1 = a2.x + a2.y; }
    { const u32x2 sv = __builtin_bit_cast(u32x2, sw16(s1)); __builtin_amdgcn_raw_buffer_store_b64(u32x2{sv.x, filled(sv.y)}, rs, fvo, sto + kh * 16384, 16); }
    WAITVM(2);
  } else {
    if (TD == 1) dma4(wd + ((tile2 * 2 + r2 - 14) * 8 + wave) * 49 * 128, min(lane, 48) * 128, L_JUNK);
    lds_wait2(L, S_W2B, S_WOFA);
    if (CF) { HH_CLEAR(A ? NE : 0, 4 * NCL); }
    WIN2();
    if (W2F >= 3) { lds_wait(L, S_WOFB); if (W2F < 5 || A) GUBLK(A ? 3 : 4); }
    if (!CF) { HH_CLEAR(A ? NE : 0, 4 * NCL); }
    DRAIN();
    if (!CE2) HH_FLAG();
    if (CE2 && A) __builtin_amdgcn_s_sleep(DLY5);
  }
  if constexpr (A && CSL) CS_OUT(2);
  lds_signal(L, A ? S_HFA : S_HFB, w2);
  STAMP(16);
  u32x4 wdf[NWD][8];
  const __amdgpu_buffer_rsrc_t rd = __builtin_amdgcn_make_buffer_rsrc((void*)(wd + w2 * 448 * 896 + min(cg, 6) * 128 - 1024), 0, 448 * 896 + 1024, 0x00020000);
  const u32 dvo = (lane >> 3) * 8 * 896 + (((lane & 7) ^ ((lane >> 2) & 4)) * 16);
  u32 da_ = 2 * SLOT + w2 * 7168 + hh * 512 + q4 * 128 + (g & 1) * 32 + p4 * 8 + 64 * (q4 >> 1), db_ = da_ ^ 64, dc_ = da_ + (L_EX - 2 * SLOT - w2 * 6144), dd_ = dc_ ^ 64;
  const u32 po = WS_FHH + ((tile2 * 16 + 4 * w2 + ((lane >> 1) & 3)) * 4 + 2 * tg + (lane & 1)) * 128;
  const u32 ho = WS_HH + ((tile2 * 2 + tg) * 4 + w2) * 57344;
#define WDLOAD(k, bf) dma_wd<k>(rd, dvo, ((bf) + 2) * SLOT + w2 * 7168, L_EX + (bf) * 4096 + w2 * 1024)
#define WDREAD(dst, bf) do { asm volatile("" : "+v"(da_), "+v"(db_), "+v"(dc_), "+v"(dd_)); \
    _Pragma("unroll") for (int i = 0; i < 8; ++i) { LAS char* p_ = L + (i & 1 ? db_ : da_) + (bf) * SLOT + (i >> 1) * 2048; \
      const u32x2 x_ = trread(p_), y_ = trread(i < 6 ? p_ + 1024 : L + (i & 1 ? dd_ : dc_) + (bf) * 4096); dst[i] = u32x4{x_.x, x_.y, y_.x, y_.y}; } } while (0)
#define THR() asm volatile("s_waitcnt vmcnt(%0)" :: "n"(THRN) : "memory")
#define THR0() asm volatile("s_waitcnt vmcnt(%0)" :: "n"(THRN0) : "memory")
  u32 have = 0;
#define PLOAD() peek(ws, po, lane < 8, tag + 7)
#define PMIN(pv) do { const u64 d = (pv) - tag; have = max(have, min8(d <= 8 ? (u32)d : 0u)); } while (0)
#define NEEDP(n) do { _Pragma("nounroll") for (u32 sp = 0; have < (n) && sp < SPIN_CAP; ++sp) { PMIN(PLOAD()); if (PSLP && have < (n)) __builtin_amdgcn_s_sleep(PSLP); } \
    __builtin_amdgcn_fence(__ATOMIC_ACQUIRE, "wavefront"); } while (0)
#define HBASE() u32 hb3 = w2 * 7168 + lane * 16, hb0 = hb3 + 2 * SLOT, hb2 = hb0 + 2 * SLOT, hb7 = L_EX + w2 * 1024 + lane * 16; asm volatile("" : "+v"(hb0), "+v"(hb2), "+v"(hb7))
#define HFRAG(bf, i) (*(LAS u32x4*)(L + ((i) < 7 ? ((bf) == 3 ? hb3 : (bf) == 2 ? hb2 : hb0 + (bf) * SLOT) + (i) * 1024 : hb7 + (bf) * 4096)))
#define HQLOAD(q, j) _Pragma("unroll") for (int i = 0; i < 8; ++i) hq[q][i] = __builtin_bit_cast(u32x4, __builtin_amdgcn_raw_buffer_load_b128(rs, lane * 16 + (i & 3) * 1024, ho + (j) * 8192 + (i & 4) * 1024, 16))
#define HQLOADT(q, j) _Pragma("unroll") for (int i = 0; i < 8; ++i) { THR(); hq[q][i] = __builtin_bit_cast(u32x4, __builtin_amdgcn_raw_buffer_load_b128(rs, lane * 16 + (i & 3) * 1024, ho + (j) * 8192 + (i & 4) * 1024, 16)); }
#define DOWN16(ac, wf, hf_) _Pragma("unroll") for (int m = 0; m < 16; ++m) ac[(m >> 1) & 1][m & 1] = mfma32(wf[2 * (m >> 2) + (m & 1)], hf_[2 * (m >> 2) + ((m >> 1) & 1)], ac[(m >> 1) & 1][m & 1])
#define PARTIAL_OUT(ac, b0, b1) _Pragma("unroll") for (int n = 0; n < 16; ++n) { const int T = n >> 3, k = (n >> 2) & 1, q = n & 3; \
    *(LAS f32x4*)&HFRAG(T ? b1 : b0, n & 7) = f32x4{ac[T][k][4 * q], ac[T][k][4 * q + 1], ac[T][k][4 * q + 2], ac[T][k][4 * q + 3]}; }
#define DOWN_OUT(ac, wf, hf_, b0, b1) _Pragma("unroll") for (int m = 0; m < 16 + OLAG; ++m) { \
    if (m < 16) ac[m >> 3][(m >> 2) & 1] = mfma32(wf[2 * (m & 3) + ((m >> 2) & 1)], hf_[2 * (m & 3) + (m >> 3)], ac[m >> 3][(m >> 2) & 1]); \
    SCHED(); \
    if (m >= OLAG) { const int n = m - OLAG, T = n >> 3, k = (n >> 2) & 1, q = n & 3; \
      *(LAS f32x4*)&HFRAG(T ? b1 : b0, n & 7) = f32x4{ac[T][k][4 * q], ac[T][k][4 * q + 1], ac[T][k][4 * q + 2], ac[T][k][4 * q + 3]}; } \
    SCHED(); }
#define FREE(f) do { _Pragma("nounroll") for (u32 sp = 0; sp < (SPIN_CAP << 4); ++sp) { const u32x4 fr = *(volatile LAS u32x4*)(L + L_FLG + S_DONE4 * 16); \
          if (__builtin_amdgcn_readfirstlane(min(min(fr.x, fr.y), min(fr.z, fr.w))) >= (f)) break; __builtin_amdgcn_s_sleep(BSL); } asm volatile("" ::: "memory"); } while (0)
#define HQBAD(q) (!__all(max(max(max(hq[q][0].w, hq[q][1].w), max(hq[q][2].w, hq[q][3].w)), max(max(hq[q][4].w, hq[q][5].w), max(hq[q][6].w, hq[q][7].w))) != EMPTY))
#define HQCHECK(q, j) do { if (__builtin_expect(HQBAD(q), 0)) { _Pragma("nounroll") for (u32 sp = 0; sp < SPIN_CAP; ++sp) { \
      __builtin_amdgcn_s_sleep(RSL); asm volatile("" ::: "memory"); HQLOAD(q, j); if (!HQBAD(q)) break; } } SCHED(); } while (0)
  if constexpr (A) {
    const int T32 = tile * 4 + wave;
    u32x2 st[7];
    u32x4 hb[28];
    u64 fcl;
    const u32 stv = (7 * hh * 2048 + (lane & 31)) * 8;
#define STA(c_) _Pragma("unroll") for (int i = 0; i < 7; ++i) asm volatile("global_load_dwordx2 %0, %1, %2 sc1" : c_(st[i]) : "v"(stv), "s"(ws + WS_ST + (i * 2048 + T32 * 32) * 8) : "memory")
#define STW(k_) asm volatile("s_waitcnt vmcnt(%7)" : "+v"(st[0]), "+v"(st[1]), "+v"(st[2]), "+v"(st[3]), "+v"(st[4]), "+v"(st[5]), "+v"(st[6]) : "n"(k_) : "memory")
#define STBAD() (!__all(max(max(max(st[0].y, st[1].y), max(st[2].y, st[3].y)), max(max(st[4].y, st[5].y), st[6].y)) != EMPTY))
#define HBA(c_, q) _Pragma("unroll") for (int i = 0; i < 4; ++i) asm volatile("global_load_dwordx4 %0, %1, %2 offset:%3 sc1" : c_(hb[4 * (q) + i]) : "v"(lane * 16), "s"(ws + WS_H + (T32 * 28 + 4 * (q)) * 1024), "n"(i * 1024) : "memory")
    lds_wait2(L, S_HFA, S_HFB);
    if (CLW == 1) {
#pragma unroll
      for (int i = NCL; i < 7; ++i) ST16(rs, empty4, lane * 16, hho + i * 8192);
    }
    __builtin_amdgcn_s_sleep(DLY3);
    if (!SPEC || r2 >= 14) {
#pragma nounroll
      for (u32 sp = 0; sp < SPIN_CAP; ++sp) { STA("=v"); STW(0); if (!STBAD()) break; __builtin_amdgcn_s_sleep(RSL); }
    }
    STA("=v");
    STAMP(17);
#pragma unroll
    for (int q = 0; q < HLA / 4; ++q) { HBA("=v", q); }
    u32 wb_[3] = {(u32)lane * 16, (u32)lane * 16 + 2 * SLOT, (u32)lane * 16 + 4 * SLOT};
    asm volatile("" : "+v"(wb_[1]), "+v"(wb_[2]));
#define WFRAG(sl, s) (*(LAS u32x4*)(L + wb_[(sl) >> 1] + ((sl) & 1) * SLOT + (s) * 1024))
    LAS char* csl = L + L_CS + hh * 16;
    u32x4 ring[7];
    f32x4 cq[4];
    float nmu, ve, k0, k1;
    u32 mx;
    const int pvo = WS_FHH + ((tile * 16 + r) * 4 + wave) * 128;
#define PROG(n) FLAG(pvo + 131072, n)
    f32x16 acc[3];
    float y[8], gu[8];
    u32x4 yb;
    u32x4 cw;
#define LT(i)
#define CQ(j_, k_) do { const f32x4 c_ = *(LAS f32x4*)(csl + (j_) * 128 + (k_) * 32); _Pragma("unroll") for (int i_ = 0; i_ < 4; ++i_) acc[(j_) % 3][4 * (k_) + i_] = c_[i_]; } while (0)
#define HB1(i_) asm volatile("global_load_dwordx4 %0, %1, %2 offset:%3 sc1" : "=v"(hb[i_]) : "v"(lane * 16), "s"(ws + WS_H + (T32 * 28 + ((i_) & ~3)) * 1024), "n"(((i_) & 3) * 1024) : "memory")
#define FCL() asm volatile("global_load_dwordx2 %0, %1, %2 sc1" : "=v"(fcl) : "v"(p4 * 128), "s"(ws + WS_FH + (T32 * 32 + 28) * 128) : "memory")
    auto gu01 = [&](auto LC) LAM {
      constexpr bool LD = LC.value;
      sfor<0, 56>([&](auto NC) LAM {
        constexpr int n = NC.value, s = n >> 1, m = n + 7;
        if constexpr (LD && !(n & 1)) { u32x4 t_ = hb[s]; asm volatile("s_waitcnt vmcnt(%1)" : "+v"(t_) : "n"((HLA + s < 29 ? HLA + s : 29) - s - 1 + (CLW == 2 && n > CN ? (n - CN < 7 - NCL ? n - CN : 7 - NCL) : 0)) : "memory"); hb[s] = t_; }
        acc[n & 1] = mfma32(ring[n % 7], hb[s], acc[n & 1]);
        SCHED();
        if constexpr (LD && (n & 1) && HLA + s < 28) HB1(HLA + s);
        if constexpr (LD && (n & 1) && HLA + s == 28) FCL();
        if constexpr (CLW == 2 && LD && n >= CN && n < CN + 7 - NCL) ST16(rs, empty4, lane * 16, hho + (n - CN + NCL) * 8192);
        if constexpr (n & 1) mx = max(mx, hb[s].w);
        if constexpr (m < 56) ring[n % 7] = WFRAG(m & 1, m >> 1); else ring[n % 7] = WFRAG(2, m - 56);
        if constexpr (n >= 34 && n < 38) CQ(2, n - 34);
        if constexpr (n >= 44 && n < 52) { acc[2][2 * (n - 44)] *= nmu; acc[2][2 * (n - 44) + 1] *= nmu; PIN(acc[2]); }
        SCHED();
      });
      STAMP(18);
    };
    auto gu_block = [&](auto JC) __attribute__((always_inline)) {
      constexpr int j = JC.value, NE_ = j == 2 ? 16 : 8;
      sfor<0, 28>([&](auto SC) LAM {
        constexpr int s = SC.value;
        if constexpr (PACE > 0 && j >= PJ0 && j <= PJ1 && s % PS == 0) __builtin_amdgcn_s_sleep(PACE);
        if constexpr (HW && s % 2 == HW - 1 && (s < 27 || j < 6)) { asm volatile("" :: "v"(ring[(s + 1) % 7])); SCHED(); }
        acc[j % 3] = mfma32(ring[s % 7], hb[s], acc[j % 3]);
        constexpr int sg = j == 3 ? S_WIN3 : j == 4 ? S_REFILL : S_FREE01;
        if constexpr (j >= 3 && j < 6 && s == 12) cw = *(volatile LAS u32x4*)(L + L_FLG + sg * 16);
        if constexpr (j >= 3 && j < 6 && s == 20) { if (__builtin_expect(!__builtin_amdgcn_readfirstlane(cw.x & cw.y & cw.z & cw.w), 0)) lds_wait(L, sg); asm volatile("" ::: "memory"); }
        if constexpr (!CLW && j == 2 && s < 7 - NCL) ST16(rs, (u32x4{EMPTY, EMPTY, EMPTY, EMPTY}), lane * 16, hho + (NCL + s) * 8192);
        if constexpr (s + 7 < 28) ring[s % 7] = WFRAG(gu_slot(j), s + 7);
        else if constexpr (j < 6) ring[s % 7] = WFRAG(gu_slot(j + 1), s - 21);
        if constexpr (s == 0 && j == 2) lds_signal(L, S_WSEEN, wave);
        if constexpr (s == 0 && j >= 3) *(volatile LAS u32*)(L + L_FLG + S_DONE4 * 16 + wave * 4) = j - 2u;
        sfor<0, NE_>([&](auto EC) LAM {
          constexpr int e = EC.value, i = e & 7, k = j == 2 ? s - e - e / 4 : s - 1 - 2 * e, a = j == 2 ? e >> 3 : (j - 1) % 3;
          const float ga = acc[a][i];
          if constexpr (k == 0) y[i] = ga * ga;
          if constexpr (k == 1) y[i] = fmaf(y[i], k1, k0);
          if constexpr (k == 2) y[i] = ga * y[i];
          if constexpr (k == 3) y[i] = fexp2(y[i]);
          if constexpr (k == 4) y[i] = fmaf(y[i], ve, ve);
          if constexpr (k == 5) { gu[i] = ga * acc[a][i + 8]; PIN(gu[i]); }
          if constexpr (k == 6) y[i] = frcp(y[i]);
          if constexpr (k == 7) y[i] = y[i] * gu[i];
          if constexpr (k >= 0 && k < 8 && k != 5) PIN(y[i]);
          if constexpr (j == 2 && k == 8 && (e & 1)) { u32 p_ = pk(y[i - 1], y[i]); if (i == 7) p_ = filled(p_); PIN(p_); yb[i >> 1] = p_; }
          if constexpr (CLW == 2 && j == 2 && k == 8 && e == 7) WAITVM(0);
          if constexpr (j == 2 && k == 8 && i == 7) ST16(rs, yb, lane * 16, hho + a * 8192);
          if constexpr (j == 2 && k == 9 && i == 7 && (a == 0 || !FLG13)) FLAG(pvo, a + 1);
        });
        if constexpr (j > 2) {
          if constexpr (s == 21) { yb[0] = pk(y[0], y[1]); yb[1] = pk(y[2], y[3]); }
          if constexpr (s == 22) yb[2] = pk(y[4], y[5]);
          if constexpr (!CLW && j == 3 && s == 22) { if (FLG13) WAITVM(3); else WAITVM(4); }
          if constexpr (s == 23) { yb[3] = filled(pk(y[6], y[7])); ST16(rs, yb, lane * 16, hho + (j - 1) * 8192); }
          if constexpr (!CLW && s == 24 && (j == 3 || (j == 4 && !FLG13))) FLAG(pvo, j);
        }
        if constexpr (j < 6 && s >= 17 && s < 21) CQ(j + 1, s - 17);
        if constexpr (j < 6 && s >= 22 && s < 27) {
          sfor<0, 16>([&](auto EC) LAM {
            constexpr int e = EC.value;
            if constexpr ((e < 2 ? 22 : e < 4 ? 23 : 23 + (e >> 2)) == s) acc[(j + 1) % 3][e] *= nmu;
          });
          PIN(acc[(j + 1) % 3]);
        }
        SCHED();
      });
      if (j == 3) STAMP(26);
      if (j == 2) STAMP(29);
      if (j == 4) STAMP(30);
      if (j == 5) STAMP(31);
    };
#define HBW(q) asm volatile("s_waitcnt vmcnt(0)" : "+v"(hb[4 * (q)]), "+v"(hb[4 * (q) + 1]), "+v"(hb[4 * (q) + 2]), "+v"(hb[4 * (q) + 3]) :: "memory")
#define HBBAD(q) (!__all(max(max(hb[4 * (q)].w, hb[4 * (q) + 1].w), max(hb[4 * (q) + 2].w, hb[4 * (q) + 3].w)) != EMPTY))
#define HBC(q, c_) asm volatile("s_bitcmp0_b32 %6, %7\n\ts_cbranch_scc1 1f\n\tglobal_load_dwordx4 %0, %4, %5 sc1\n\tglobal_load_dwordx4 %1, %4, %5 offset:1024 sc1\n\t" \
      "global_load_dwordx4 %2, %4, %5 offset:2048 sc1\n\tglobal_load_dwordx4 %3, %4, %5 offset:3072 sc1\n1:" : "+v"(hb[4 * (q)]), "+v"(hb[4 * (q) + 1]), "+v"(hb[4 * (q) + 2]), "+v"(hb[4 * (q) + 3]) \
      : "v"(lane * 16), "s"(ws + WS_H + (T32 * 28 + 4 * (q)) * 1024), "s"(c_), "n"(q) : "memory", "scc")
#define REDO(m_) do { STA("+v"); _Pragma("unroll") for (int q = 0; q < 7; ++q) HBC(q, m_); STW(0); _Pragma("unroll") for (int q = 0; q < 7; ++q) HBW(q); } while (0)
#define FIX() do { _Pragma("nounroll") for (u32 sp = 0; sp < SPIN_CAP; ++sp) { u32 m = 0; \
        _Pragma("unroll") for (int q = 0; q < 7; ++q) m |= HBBAD(q) ? 1u << q : 0u; \
        if (!m && !STBAD()) break; if (sp) __builtin_amdgcn_s_sleep(RSL); m = __builtin_amdgcn_readfirstlane(m); REDO(m); } } while (0)
    auto first = [&](auto LC) LAM {
#pragma unroll
      for (int k = 0; k < 8; ++k) { const f32x4 c_ = *(LAS f32x4*)(csl + k * 32); acc[k >> 2][4 * (k & 3)] = c_[0]; acc[k >> 2][4 * (k & 3) + 1] = c_[1]; acc[k >> 2][4 * (k & 3) + 2] = c_[2]; acc[k >> 2][4 * (k & 3) + 3] = c_[3]; }
#pragma unroll
      for (int s = 0; s < 7; ++s) ring[s] = WFRAG(s & 1, s >> 1);
      f32x2 sf[7];
#pragma unroll
      for (int i = 0; i < 7; ++i) sf[i] = __builtin_bit_cast(f32x2, st[i]);
      float s1 = ((sf[0].x + sf[1].x) + (sf[2].x + sf[3].x)) + ((sf[4].x + sf[5].x) + sf[6].x);
      float s2 = ((sf[0].y + sf[1].y) + (sf[2].y + sf[3].y)) + ((sf[4].y + sf[5].y) + sf[6].y);
      { f32x2 a = sw32(s1); s1 = a.x + a.y; a = sw32(s2); s2 = a.x + a.y; }
      nmu = s1 * (-1.f / 448.f); ve = fmaxf(s2 * (1.f / 448.f) - nmu * nmu, 0.f) + 1e-5f;
      const float rstd = __builtin_amdgcn_rsqf(ve);
      k0 = -2.30220819f * rstd; k1 = -0.10294324f * rstd * rstd * rstd;
#pragma unroll
      for (int i = 0; i < 32; ++i) acc[i >> 4][i & 15] *= nmu;
      mx = 0;
      __builtin_amdgcn_s_setprio(GUPRIO);
      SCHED();
      gu01(LC);
    };
    STW(HLA);
    if (__builtin_expect(STBAD(), 0)) {
#pragma unroll
      for (int q = 0; q < HLA / 4; ++q) HBW(q);
#pragma nounroll
      for (u32 sp = 0; sp < SPIN_CAP; ++sp) {
        u32 m = 0;
#pragma unroll
        for (int i = 0; i < 7; ++i) { const u64 bm = __builtin_amdgcn_ballot_w64(st[i].y == EMPTY); m |= ((u32)bm ? 1u << (i >> 1) : 0u) | ((u32)(bm >> 32) ? 1u << ((7 + i) >> 1) : 0u); }
        m = __builtin_amdgcn_readfirstlane(m);
        if (!m) break;
        if (sp) __builtin_amdgcn_s_sleep(RSL);
        STA("+v");
#pragma unroll
        for (int q = 0; q < HLA / 4; ++q) HBC(q, m);
        STW(0);
#pragma unroll
        for (int q = 0; q < HLA / 4; ++q) HBW(q);
      }
    }
    first(IC<1>{});
    if (__builtin_expect(!__all(mx != EMPTY), 0)) { FIX(); first(IC<0>{}); }
    asm volatile("s_waitcnt vmcnt(%1)" : "+v"(fcl) : "n"(CLW == 2 ? 7 - NCL : 0) : "memory");
    if (__builtin_expect(!__all(fcl == tag + 1), 0)) poll_flags(ws, WS_FH + (T32 * 32 + 28 + p4) * 128, lane < 4, tag);
    gu_block(IC<2>{}); gu_block(IC<3>{}); gu_block(IC<4>{}); gu_block(IC<5>{}); gu_block(IC<6>{});
#pragma unroll
    for (int i = 0; i < 8; ++i) { const float ga = acc[0][i]; y[i] = ga * acc[0][i + 8] * frcp(fmaf(fexp2(ga * fmaf(ga * ga, k1, k0)), ve, ve)); }
    yb = u32x4{pk(y[0], y[1]), pk(y[2], y[3]), pk(y[4], y[5]), filled(pk(y[6], y[7]))};
    ST16(rs, yb, lane * 16, hho + 6 * 8192);
    *(volatile LAS u32*)(L + L_FLG + S_DONE4 * 16 + wave * 4) = 5u;
    __builtin_amdgcn_s_setprio(0);
    STAMP(19);
    if (r2 < 14) {
      f32x16 da[2][2] = {};
      u32x4 hq[2][8], wl[2][8];
      HBASE();
      __builtin_amdgcn_s_setprio(ATP);
      lds_wait1(L, S_SPARE, w2);
      STAMP2(15);
      HQLOAD(0, 4);
      WDREAD(wl[0], 1);
      SCHED();
      if (A6L == 0) { __builtin_amdgcn_s_sleep(ADLY); HQLOAD(1, 6); }
      STAMP2(16);
      HQCHECK(0, 4);
      if (A6L == 1) { __builtin_amdgcn_s_sleep(ADLY); HQLOAD(1, 6); }
      SCHED();
      DOWN16(da, wl[0], hq[0]);
      SCHED();
      if (A6L == 2) { __builtin_amdgcn_s_sleep(ADLY); HQLOAD(1, 6); }
      lds_wait1(L, S_CONS, w2);
      WDREAD(wl[1], 0);
      STAMP2(18);
      HQCHECK(1, 6);
      STAMP2(31);
      DOWN_OUT(da, wl[1], hq[1], 0, 1);
      STAMP(20);
    }
  } else {
    lds_wait2(L, S_HFA, S_HFB);
    if (TD == 6 && w2 == 0) DTOUCH();
    if (TD == 7) dma4(wd + min(b * 49 + w2 * 13, 12544 - 13) * 128, min(lane, 12) * 128, L_JUNK);
    __builtin_amdgcn_s_setprio(BP3);
    if (r2 >= 14) GUBLK(4);
#define REFILL() do { lds_wait<BSL>(L, S_WSEEN); GUBLK(5); GUBLK(6); } while (0)
    if (r2 >= 14) {
      WAITVM(0); lds_signal(L, S_WIN3, w2);
      REFILL();
      WAITVM(0); lds_signal(L, S_REFILL, w2); lds_signal(L, S_FREE01, w2);
    } else {
      u64 pv;
      const char* hs = ws + ho + 4096;
#define PEEK() do { if (AP) { pv = tag + 7; asm volatile("s_mov_b64 exec, 0xff\n\tglobal_atomic_add_x2 %0, %1, %2, %3 sc0\n\ts_mov_b64 exec, -1" : "+v"(pv) : "v"(po), "v"(0ull), "s"(ws) : "memory"); } \
        else asm volatile("global_load_dwordx2 %0, %1, %2 sc1" : "=v"(pv) : "v"(po), "s"(ws) : "memory"); } while (0)
#define PEEKED(k_, n_) do { asm volatile("s_waitcnt vmcnt(%1)" : "+v"(pv) : "n"(k_) : "memory"); PMIN(pv); NEEDP(n_); } while (0)
#define HQA(c_, q, j) _Pragma("unroll") for (int i = 0; i < 8; ++i) asm volatile("global_load_dwordx4 %0, %1, %2 offset:%3 sc1" : c_(hq[q][i]) : "v"(lane * 16), "s"(hs + (j) * 8192), "n"(i * 1024 - 4096) : "memory")
#define HQW(q, k_) asm volatile("s_waitcnt vmcnt(%8)" : "+v"(hq[q][0]), "+v"(hq[q][1]), "+v"(hq[q][2]), "+v"(hq[q][3]), "+v"(hq[q][4]), "+v"(hq[q][5]), "+v"(hq[q][6]), "+v"(hq[q][7]) : "n"(k_) : "memory")
#define HQOK(q, j, k_) do { HQW(q, k_); if (__builtin_expect(HQBAD(q), 0)) { _Pragma("nounroll") for (u32 sp = 0; sp < SPIN_CAP; ++sp) { \
        __builtin_amdgcn_s_sleep(RSL); HQA("+v", q, j); HQW(q, 0); if (!HQBAD(q)) break; } } SCHED(); } while (0)
      {
        u32x2 e[24][2];
        const u32 ea_ = 0, eb_ = 0;
        asm volatile("" : "+v"(da_), "+v"(db_), "+v"(dc_), "+v"(dd_));
#define WBL(N0, N1, NS, SL) sfor<N0, N1>([&](auto NC) LAM { constexpr int n = NC.value, k = n - (N0); \
          if constexpr (k < (NS)) dma_wd1<n, (SL) * SLOT + k * 1024>(rd, dvo, w2 * 7168); else dma_wd1<n, L_EX + (k - (NS)) * 4096>(rd, dvo, w2 * 1024); })
#define WBR(N0, N1, NS, SL) do { sfor<N0, N1>([&](auto NC) LAM { constexpr int n = NC.value, k = n - (N0); \
          if constexpr (k >= (NS)) { e[n][0] = trread(L + dc_ + (k - (NS)) * 4096); e[n][1] = trread(L + dd_ + (k - (NS)) * 4096); } \
          else if constexpr ((SL) == 4) { e[n][0] = trread(L + da_ + 2 * SLOT + k * 1024); e[n][1] = trread(L + db_ + 2 * SLOT + k * 1024); } \
          else { e[n][0] = trread(L + ea_ + k * 1024); e[n][1] = trread(L + eb_ + k * 1024); } }); \
          asm volatile("s_waitcnt lgkmcnt(0)" : "+v"(e[(N1) - 1][1]) :: "memory"); } while (0)
        WBL(0, 11, 7, 4);
        WAITVM(0);
        STAMP2(24);
        WBR(0, 11, 7, 4);
        WBL(11, 15, 0, 0);
        WAITVM(0);
        STAMP2(25);
        WBR(11, 15, 0, 0);
        WBL(15, 19, 0, 0);
        GUBLK(4);
        WAITVM(7);
        WBR(15, 19, 0, 0);
        WBL(19, 23, 0, 0);
        STAMP2(27);
        lds_wait<BSL>(L, S_WSEEN);
        GUBLK(5);
        GUBLK(6);
        WAITVM(14);
        lds_signal(L, S_WIN3, w2);
        STAMP2(26);
        WBR(19, 23, 0, 0);
        dma_wd1<23, L_EX + 3 * 4096>(rd, dvo, w2 * 1024);
        FREE(1);
        PEEK();
        WDLOAD(3, 0);
        WAITVM(17);
        lds_signal(L, S_REFILL, w2);
        STAMP2(5);
        PEEKED(8, 0);
        lds_signal(L, S_FREE01, w2);
        e[23][0] = trread(L + dc_ + 3 * 4096); e[23][1] = trread(L + dd_ + 3 * 4096);
#pragma unroll
        for (int n = 0; n < 24; ++n) wdf[n >> 3][n & 7] = u32x4{e[n & ~1][n & 1].x, e[n & ~1][n & 1].y, e[n | 1][n & 1].x, e[n | 1][n & 1].y};
      }
      STAMP2(23);
      f32x16 acc[2][2];
      u32x4 hq[2][8];
      STAMP2(16);
      HBASE();
#pragma unroll
      for (int k = 0; k < 3; ++k) asm volatile("" : "+v"(wdf[k][0]), "+v"(wdf[k][1]), "+v"(wdf[k][2]), "+v"(wdf[k][3]), "+v"(wdf[k][4]), "+v"(wdf[k][5]), "+v"(wdf[k][6]), "+v"(wdf[k][7]) :: "memory");
      const bool w4e = have < 1;
      if (w4e) { FREE(2); WDLOAD(4, 1); }
      NEEDP(1);
      STAMP2(1);
      HQA("=v", 0, 0);
      if (!w4e && W4O == 0) { FREE(2); WDLOAD(4, 1); }
      STAMP2(2);
      HQA("=v", 1, 1);
      if (W4O == 1) { if (!w4e) { FREE(2); WDLOAD(4, 1); } else WAITVM(8); }
      FREE(3);
      auto round = [&](auto JC) LAM {
        constexpr int J = JC.value, q = J & 1, w = J % 3, nb = 2 - 2 * J;
        __builtin_amdgcn_s_setprio(J < 2 ? DP0 : DNPRIO);
        sfor<0, 18>([&](auto MC) LAM {
          constexpr int m = MC.value, i = m - 8 - (m > 13 ? 2 : 0);
          if constexpr (m < 16) macc<J == 0 && m < 4 ? 0 : 1>(acc[(m >> 1) & 1][m & 1], wdf[w][2 * (m >> 2) + (m & 1)], hq[q][2 * (m >> 2) + ((m >> 1) & 1)]);
          SCHED();
          if constexpr (J < 2 && m < 7) dma_wd1<(J + 5) * 8 + m, (nb + 2) * SLOT + m * 1024>(rd, dvo, w2 * 7168);
          if constexpr (J < 2 && m == 7) dma_wd1<(J + 5) * 8 + 7, L_EX + nb * 4096>(rd, dvo, w2 * 1024);
          if constexpr (!CLW && J == 0 && m == 7) PEEK();
          if constexpr (J == 1 && m == 7) { if (CLW) WAITVM(16); else PEEKED(16, 3); lds_signal(L, S_SPARE, w2); FREE(4); }
          if constexpr (J == 2 && m == 7) { WAITVM(8); lds_signal(L, S_CONS, w2); FREE(5); if (B5D) __builtin_amdgcn_s_sleep(B5D); }
          if constexpr (J < 3 && m >= 8 && m != 14 && m != 15) {
            asm volatile("global_load_dwordx4 %0, %1, %2 offset:%3 sc1" : "=v"(hq[q][i]) : "v"(lane * 16), "s"(hs + (J == 2 ? 5 : J + 2) * 8192), "n"(i * 1024 - 4096) : "memory");
            if constexpr (J < 2) {
              LAS char* p_ = L + (i & 1 ? db_ : da_) + 2 * J * SLOT + (i >> 1) * 2048;
              const u32x2 x_ = trread(p_), y_ = trread(i < 6 ? p_ + 1024 : L + (i & 1 ? dd_ : dc_) + 2 * J * 4096);
              wdf[J][i] = u32x4{x_.x, x_.y, y_.x, y_.y};
            }
          }
          SCHED();
        });
        if (J < 2) __builtin_amdgcn_s_setprio(BP3);
      };
      HQOK(0, 0, W4O ? 16 : 8);
      round(IC<0>{});
      STAMP2(8);
      HQOK(1, 1, CLW ? 16 : 17);
      asm volatile("s_waitcnt lgkmcnt(0)" : "+v"(wdf[0][7]) :: "memory");
      round(IC<1>{});
      STAMP2(9);
      HQOK(0, 2, 16);
      round(IC<2>{});
      STAMP2(10);
      HQOK(1, 3, 8);
      round(IC<3>{});
      STAMP2(11);
      HQOK(0, 5, 0);
      STAMP2(4);
      DOWN_OUT(acc, wdf[1], hq[0], 2, 3);
      STAMP(20);
    }
  }
  if (r2 < 14) {
    BAR();
    STAMP2(7);
    float c[8] = {blo(hres.x), bhi(hres.x), blo(hres.y), bhi(hres.y), blo(hres.z), bhi(hres.z), blo(hres.w), bhi(hres.w)};
    const int rl = ((tb & 1) * 16 + l15) * 16;
    f32x4 v0 = {c[0], c[1], c[2], c[3]}, v1 = {c[4], c[5], c[6], c[7]};
#pragma unroll
    for (int h = 0; h < 2; ++h) {
      const int bf = (tb >> 1) + 2 * h;
      const bool p7 = kh && g == 3;
      u32 rq = (p7 ? L_EX + bf * 4096 : (bf == 3 ? 0 : (bf + 2) * SLOT) + (4 * kh + g) * 1024) + rl;
      const u32 st7 = p7 ? 1024 : 7168;
      asm volatile("" : "+v"(rq));
#pragma unroll
      for (int w = 0; w < 4; ++w) { v0 += *(LAS f32x4*)(L + rq + w * st7); v1 += *(LAS f32x4*)(L + rq + w * st7 + 512); }
    }
#pragma unroll
    for (int i = 0; i < 4; ++i) { c[i] = v0[i]; c[4 + i] = v1[i]; }
    { const u32x4 ov = {pk(c[0], c[1]), pk(c[2], c[3]), pk(c[4], c[5]), pk(c[6], c[7])}; asm volatile("global_store_dwordx4 %0, %1, %2 sc1\n\ts_nop 1" :: "v"(xo), "v"(ov), "s"(out) : "memory"); }
    if (A && b == 0 && tid == 0) __hip_atomic_store((GAS u64*)(ws + WS_E), tag, RLX_AGENT);
  }
  STAMP(21);
  };
  if (wave < 4) role(IC<1>{}); else role(IC<0>{});
}

extern "C" void kernel_launch(void* const* d_in, const int* in_sizes, int n_in, void* d_out, int out_size, void* d_ws, size_t ws_size, hipStream_t stream) {
  (void)hipFuncSetAttribute((const void*)fused_block, hipFuncAttributeMaxDynamicSharedMemorySize, LDS_BYTES);
  fused_block<<<256, 512, LDS_BYTES, stream>>>((const char*)d_in[0], (const char*)d_in[1], (const char*)d_in[2], (const char*)d_in[3], (const char*)d_in[5], (const char*)d_in[6],
                                               (const char*)d_in[7], (char*)d_out, (char*)d_ws);
}
```

```cpp
#include <hip/hip_runtime.h>
#include <hip/hip_bf16.h>
#include <stdint.h>

typedef unsigned u32;
typedef unsigned long long u64;
typedef u32 u32x4 __attribute__((ext_vector_type(4)));
typedef u32 u32x2 __attribute__((ext_vector_type(2)));
typedef float f32x2 __attribute__((ext_vector_type(2)));
typedef float f32x4 __attribute__((ext_vector_type(4)));
typedef float f32x16 __attribute__((ext_vector_type(16)));
typedef short s16x4 __attribute__((ext_vector_type(4)));
typedef __bf16 bf16x2 __attribute__((ext_vector_type(2)));
typedef __bf16 bf16x8 __attribute__((ext_vector_type(8)));
#define LAS __attribute__((address_space(3)))
#define GAS __attribute__((address_space(1)))
#define DI __device__ __forceinline__
#define RLX_AGENT __ATOMIC_RELAXED, __HIP_MEMORY_SCOPE_AGENT

constexpr int WS_E = 0;
constexpr int WS_FKV = 8192;
constexpr int WS_FO = WS_FKV + 131072;
constexpr int WS_FH = WS_FO + 131072;
constexpr int WS_FG = WS_FH + 262144;
constexpr int WS_FD = WS_FG + 114688;
constexpr int WS_FHH = WS_FD + 57344;
constexpr int WS_CS = WS_FHH + 262144;
constexpr int WS_ST = WS_CS + 200704;
constexpr int WS_WGU = WS_ST + 229376;
constexpr int WS_WD = WS_WGU + 3211264;
constexpr int WS_KVP = WS_WD + 1605632;
constexpr int WS_O = WS_KVP + 1048576;
constexpr int WS_H = WS_O + 2097152;
constexpr int WS_HH = WS_H + 1835008;
#define UNITX4
#define NOWD
#define WDOFF(cg, fb, bl) (((((cg) * 4 + (fb) / 28) * 7 + (fb) % 7) * 4 + (fb) / 7 % 4) * 2 + (bl))
#define HHOFF(T32, fb) (((((T32) >> 1) * 4 + (fb) / 28) * 7 + (fb) % 7) * 8 + ((fb) / 7 & 3) * 2 + ((T32) & 1))
constexpr int WS_STAMP = WS_HH + 7340032;
constexpr int WS_LOG = WS_STAMP + 256 * 8 * 32 * 8;
constexpr int WS_USED = WS_LOG + 4096;
constexpr int SLOT = 28672;
constexpr int L_WK = 4 * SLOT, L_WV = 0, L_WQ = 14336;
constexpr int L_PKV = 2 * SLOT;
constexpr int L_WO = 3 * SLOT;
constexpr int L_EX = 5 * SLOT;
constexpr int L_KVA = 5 * SLOT;
constexpr int L_ST1 = 5 * SLOT + 4096;
constexpr int L_WPHI = 5 * SLOT + 5120;
constexpr int L_CSP = 5 * SLOT + 7168;
constexpr int L_CS = 159744;
constexpr int L_FLG = 160768;
constexpr int L_JUNK = 161280;
constexpr int L_STAMP = 161536;
constexpr int L_RED = SLOT;
constexpr int LDS_BYTES = 163840;
constexpr int gu_slot(int j) { return j % 5; }
constexpr int CODE_PF = 48384;
constexpr u32 SPIN_CAP = 1u << 16;
#define LL 1
#define AP 1

#define STAMP(i) do {} while (0)
#define STAMP2(i) do {} while (0)
#define STAMP2X(i) do {} while (0)

DI f32x4 mfma16(u32x4 a, u32x4 b, f32x4 c) { return __builtin_amdgcn_mfma_f32_16x16x32_bf16(__builtin_bit_cast(bf16x8, a), __builtin_bit_cast(bf16x8, b), c, 0, 0, 0); }
DI f32x16 mfma32(u32x4 a, u32x4 b, f32x16 c) { return __builtin_amdgcn_mfma_f32_32x32x16_bf16(__builtin_bit_cast(bf16x8, a), __builtin_bit_cast(bf16x8, b), c, 0, 0, 0); }
DI u32 pk(float lo, float hi) { f32x2 p = {lo, hi}; return __builtin_bit_cast(u32, __builtin_convertvector(p, bf16x2)); }
DI float blo(u32 v) { return __builtin_bit_cast(float, v << 16); }
DI float bhi(u32 v) { return __builtin_bit_cast(float, v & 0xffff0000u); }
DI float dot2(u32 a, u32 b, float c) { return __builtin_amdgcn_fdot2_f32_bf16(__builtin_bit_cast(bf16x2, a), __builtin_bit_cast(bf16x2, b), c, false); }
DI u32x2 trread(LAS char* p) { return __builtin_bit_cast(u32x2, __builtin_amdgcn_ds_read_tr16_b64_v4i16((LAS s16x4*)p)); }
DI u32x4 trfrag(LAS char* p, int step) { u32x2 a = trread(p), b = trread(p + step); return u32x4{a.x, a.y, b.x, b.y}; }
DI float fexp2(float x) { return __builtin_amdgcn_exp2f(x); }
DI float frcp(float x) { return __builtin_amdgcn_rcpf(x); }
template <int C> DI float dppror(float x) { return __builtin_bit_cast(float, __builtin_amdgcn_update_dpp(0, __builtin_bit_cast(int, x), 0x120 + C, 0xf, 0xf, false)); }
DI float row_max(float x) { x = fmaxf(x, dppror<8>(x)); x = fmaxf(x, dppror<4>(x)); x = fmaxf(x, dppror<2>(x)); return fmaxf(x, dppror<1>(x)); }
DI float row_min(float x) { x = fminf(x, dppror<8>(x)); x = fminf(x, dppror<4>(x)); x = fminf(x, dppror<2>(x)); return fminf(x, dppror<1>(x)); }
DI float row_sum(float x) { x += dppror<8>(x); x += dppror<4>(x); x += dppror<2>(x); return x + dppror<1>(x); }
DI f32x2 sw16(float x) { u32 u = __builtin_bit_cast(u32, x); auto r = __builtin_amdgcn_permlane16_swap(u, u, false, false); return f32x2{__builtin_bit_cast(float, (u32)r[0]), __builtin_bit_cast(float, (u32)r[1])}; }
DI f32x2 sw32(float x) { u32 u = __builtin_bit_cast(u32, x); auto r = __builtin_amdgcn_permlane32_swap(u, u, false, false); return f32x2{__builtin_bit_cast(float, (u32)r[0]), __builtin_bit_cast(float, (u32)r[1])}; }
DI float grp_sum(float x) { f32x2 a = sw16(x); x = a.x + a.y; a = sw32(x); return a.x + a.y; }
DI float grp_max(float x) { f32x2 a = sw16(x); x = fmaxf(a.x, a.y); a = sw32(x); return fmaxf(a.x, a.y); }
DI float grp_min(float x) { f32x2 a = sw16(x); x = fminf(a.x, a.y); a = sw32(x); return fminf(a.x, a.y); }

#define BAR() asm volatile("s_waitcnt lgkmcnt(0)\n\ts_barrier" ::: "memory")
#define WAITVM(n) asm volatile("s_waitcnt vmcnt(" #n ")" ::: "memory")
#define DRAIN() do { asm volatile("s_waitcnt vmcnt(0)" ::: "memory"); __builtin_amdgcn_s_waitcnt(0x0F70); } while (0)
#define SCHED() __builtin_amdgcn_sched_barrier(0)
DI void dma16(const void* base, u32 voff, u32 lds) {
  u32 keep;
  asm volatile("s_mov_b32 %0, m0\n\ts_mov_b32 m0, %3\n\ts_nop 0\n\tglobal_load_lds_dwordx4 %1, %2\n\ts_mov_b32 m0, %0" : "=&s"(keep) : "v"(voff), "s"(base), "s"(lds) : "memory");
}
DI void dma16c(const void* base, u32 voff, u32 lds) {
  u32 keep;
  asm volatile("s_mov_b32 %0, m0\n\ts_mov_b32 m0, %3\n\ts_nop 0\n\tglobal_load_lds_dwordx4 %1, %2 sc1\n\ts_mov_b32 m0, %0" : "=&s"(keep) : "v"(voff), "s"(base), "s"(lds) : "memory");
}
DI void dma4(const void* base, u32 voff, u32 lds) {
  u32 keep;
  asm volatile("s_mov_b32 %0, m0\n\ts_mov_b32 m0, %3\n\ts_nop 0\n\tglobal_load_lds_dword %1, %2\n\ts_mov_b32 m0, %0" : "=&s"(keep) : "v"(voff), "s"(base), "s"(lds) : "memory");
}
DI void dma16c_if(u32 go, const void* base, u32 voff, u32 lds) {
  u32 keep;
  asm volatile("s_cmp_eq_u32 %4, 0\n\ts_cbranch_scc1 1f\n\ts_mov_b32 %0, m0\n\ts_mov_b32 m0, %3\n\ts_nop 0\n\tglobal_load_lds_dwordx4 %1, %2 sc1\n\ts_mov_b32 m0, %0\n1:"
               : "=&s"(keep) : "v"(voff), "s"(base), "s"(lds), "s"(go) : "memory", "scc");
}
template <int SI, int LI> DI void dma7(__amdgpu_buffer_rsrc_t rs, u32 voff, u32 so, u32 lds) {
  u32 t;
  asm volatile("s_add_u32 m0, %4, %6\n\ts_add_u32 %0, %3, %5\n\tbuffer_load_dwordx4 %1, %2, %0 offen sc1 lds\n\tbuffer_load_dwordx4 %1, %2, %0 offen offset:1024 sc1 lds\n\t"
               "buffer_load_dwordx4 %1, %2, %0 offen offset:2048 sc1 lds\n\tbuffer_load_dwordx4 %1, %2, %0 offen offset:3072 sc1 lds\n\t"
               "s_add_u32 m0, m0, 4096\n\ts_add_u32 %0, %0, 4096\n\tbuffer_load_dwordx4 %1, %2, %0 offen sc1 lds\n\tbuffer_load_dwordx4 %1, %2, %0 offen offset:1024 sc1 lds\n\t"
               "buffer_load_dwordx4 %1, %2, %0 offen offset:2048 sc1 lds"
               : "=&s"(t) : "v"(voff), "s"(rs), "s"(so), "s"(lds), "n"(SI), "n"(LI) : "memory", "scc");
}
constexpr int wrow(int q, int j, int hf) { return (((7 * q + j) >> 2) * 64 + 2 * ((7 * q + j) & 3) + hf) * 896 + 1024; }
#define DL_(n, o) "s_mov_b32 %0, %" #n "\n\tbuffer_load_dwordx4 %1, %2, %0 offen offset:" #o " lds\n\t"
template <int J> DI void dma_wd(__amdgpu_buffer_rsrc_t rs, u32 voff, u32 lds, u32 lds7) {
  u32 t;
  asm volatile("s_mov_b32 m0, %3\n\t" DL_(5, 0) DL_(6, 1024) DL_(7, 2048) DL_(8, 3072) "s_add_u32 m0, m0, 4096\n\t" DL_(9, 0) DL_(10, 1024) DL_(11, 2048) "s_mov_b32 m0, %4\n\t" DL_(12, 0)
               : "=&s"(t) : "v"(voff), "s"(rs), "s"(lds), "s"(lds7), "n"(wrow(0, J, 0)), "n"(wrow(0, J, 1) - 1024), "n"(wrow(1, J, 0) - 2048), "n"(wrow(1, J, 1) - 3072),
                 "n"(wrow(2, J, 0)), "n"(wrow(2, J, 1) - 1024), "n"(wrow(3, J, 0) - 2048), "n"(wrow(3, J, 1)) : "memory", "scc");
}
template <int N, int LO> DI void dma_wd1(__amdgpu_buffer_rsrc_t rs, u32 voff, u32 lds) {
  u32 t;
  asm volatile("s_add_u32 m0, %3, %4\n\t" DL_(5, 0) : "=&s"(t) : "v"(voff), "s"(rs), "s"(lds), "n"(LO), "n"(wrow(N / 2 % 4, N / 8, N % 2)) : "memory", "scc");
}
DI void touch(const void* base, u32 voff) {
  asm volatile("s_mov_b32 m0, %2\n\ts_nop 0\n\tglobal_load_lds_dword %0, %1" :: "v"(voff), "s"(base), "n"(L_JUNK) : "memory");
}
constexpr int xrow(int n) { return ((n / 14) * 16 + (n & 1) * 8) * 896 + ((n % 14) >> 1) * 128 + 2048 - n % 4 * 1024; }
template <int N> DI void dma_x(__amdgpu_buffer_rsrc_t rs, u32 voff, u32 lds) {
  u32 t;
  asm volatile("s_add_u32 m0, %3, %4\n\t" DL_(5, 0) DL_(6, 1024) DL_(7, 2048) DL_(8, 3072)
               : "=&s"(t) : "v"(voff), "s"(rs), "s"(lds), "n"(N * 1024), "n"(xrow(N)), "n"(xrow(N + 1)), "n"(xrow(N + 2)), "n"(xrow(N + 3)) : "memory", "scc");
}
DI u32 vm_count() {
  u32 t;
  asm volatile("s_getreg_b32 %0, hwreg(HW_REG_IB_STS)" : "=s"(t) :: "memory");
  return (t & 15u) | ((t >> 18) & 48u);
}
DI u32 min8(u32 c) {
  c = min(c, (u32)__builtin_amdgcn_update_dpp(0, (int)c, 0x121, 0xf, 0xf, false)); c = min(c, (u32)__builtin_amdgcn_update_dpp(0, (int)c, 0x122, 0xf, 0xf, false));
  c = min(c, (u32)__builtin_amdgcn_update_dpp(0, (int)c, 0x124, 0xf, 0xf, false));
  return __builtin_amdgcn_readfirstlane(min(c, (u32)__builtin_amdgcn_update_dpp(0, (int)c, 0x128, 0xf, 0xf, false)));
}
DI void dma4c(const void* base, u32 voff, u32 lds) {
  u32 keep;
  asm volatile("s_mov_b32 %0, m0\n\ts_mov_b32 m0, %3\n\ts_nop 0\n\tglobal_load_lds_dword %1, %2 sc1\n\ts_mov_b32 m0, %0" : "=&s"(keep) : "v"(voff), "s"(base), "s"(lds) : "memory");
}
#define LD16(rs, off) __builtin_bit_cast(u32x4, __builtin_amdgcn_raw_buffer_load_b128(rs, off, 0, 16))
#define ST16(rs, v, off, soff) do { const u32x4 d_ = __builtin_bit_cast(u32x4, v); __builtin_amdgcn_raw_buffer_store_b128(d_, rs, off, soff, 16); asm volatile("s_nop 1" :: "v"(d_) : "memory"); } while (0)

#define FLAG(off, n) __builtin_amdgcn_raw_buffer_store_b64(__builtin_bit_cast(u32x2, tag + (n)), rs, fvo, off, 16)
#define FLAGC(c, off, n) __builtin_amdgcn_raw_buffer_store_b64(__builtin_bit_cast(u32x2, tag + (n)), rs, (c) ? fvo : NOWHERE, off, 16)
constexpr int NOWHERE = 0x7ffffff0;
constexpr u32 EMPTY = 0xffffffffu;
DI u32 filled(u32 w) { return min(w, EMPTY - 1u); }
DI u64 peek(const char* ws, u32 off, bool on, u64 dflt) {
  u64 z = 0, v = dflt;
  asm volatile("" : "+v"(z));
  if (on) v = __hip_atomic_fetch_add((GAS u64*)(ws + off), z, __ATOMIC_RELAXED, AP == 2 ? __HIP_MEMORY_SCOPE_SYSTEM : __HIP_MEMORY_SCOPE_AGENT);
  return v;
}
DI void poll_flags(const char* ws, u32 off, bool on, u64 tag) {
#pragma nounroll
  for (u32 sp = 0; sp < SPIN_CAP; ++sp) {
    u64 v = tag + 1;
    if (on || !LL) v = __hip_atomic_load((const GAS u64*)(ws + off), RLX_AGENT);
    if (__all(v == tag + 1)) break;
  }
  __builtin_amdgcn_fence(__ATOMIC_ACQUIRE, "wavefront");
}
template <int SL = 1> DI void lds_wait(LAS char* L, int sig) {
#pragma nounroll
  for (u32 sp = 0; sp < (SPIN_CAP << 4); ++sp) {
    const u32x4 v = *(volatile LAS u32x4*)(L + L_FLG + sig * 16);
    if (__builtin_amdgcn_readfirstlane(v.x & v.y & v.z & v.w)) break;
    __builtin_amdgcn_s_sleep(SL);
  }
  asm volatile("" ::: "memory");
}
DI void lds_wait2(LAS char* L, int sig, int sig2) {
#pragma nounroll
  for (u32 sp = 0; sp < (SPIN_CAP << 4); ++sp) {
    const u32x4 v = *(volatile LAS u32x4*)(L + L_FLG + sig * 16) & *(volatile LAS u32x4*)(L + L_FLG + sig2 * 16);
    if (__builtin_amdgcn_readfirstlane(v.x & v.y & v.z & v.w)) break;
    __builtin_amdgcn_s_sleep(1);
  }
  asm volatile("" ::: "memory");
}
DI void lds_wait1(LAS char* L, int sig, int w) {
#pragma nounroll
  for (u32 sp = 0; sp < (SPIN_CAP << 4); ++sp) {
    if (__builtin_amdgcn_readfirstlane(*(volatile LAS u32*)(L + L_FLG + sig * 16 + w * 4))) break;
    __builtin_amdgcn_s_sleep(1);
  }
  asm volatile("" ::: "memory");
}
DI void lds_signal(LAS char* L, int sig, int w) {
  asm volatile("" ::: "memory");
  *(volatile LAS u32*)(L + L_FLG + sig * 16 + w * 4) = 1u;
}

template <int N> struct IC { static constexpr int value = N; };
template <int I, int N, class F> DI void sfor(F&& f) { if constexpr (I < N) { f(IC<I>{}); sfor<I + 1, N>(f); } }
#define LAM __attribute__((always_inline))
#define PIN(v) asm volatile("" : "+v"(v))
constexpr float LOG2E = 1.4426950408889634f;
template <int K> DI void macc(f32x4& c, u32x4 a, u32x4 b) {
  if (K == 0) asm volatile("v_mfma_f32_16x16x32_bf16 %0, %1, %2, 0" : "=&v"(c) : "v"(a), "v"(b));
  else if (K == 1) asm volatile("v_mfma_f32_16x16x32_bf16 %0, %1, %2, %0" : "+v"(c) : "v"(a), "v"(b));
  else c = mfma16(a, b, c);
}
template <int K> DI void macc(f32x16& c, u32x4 a, u32x4 b) {
  if (K == 0) asm volatile("v_mfma_f32_32x32x16_bf16 %0, %1, %2, 0" : "=&v"(c) : "v"(a), "v"(b));
  else if (K == 1) asm volatile("v_mfma_f32_32x32x16_bf16 %0, %1, %2, %0" : "+v"(c) : "v"(a), "v"(b));
  else c = mfma32(a, b, c);
}
template <int GAP, int SIG, int NW, class E, class F> DI void project(LAS char* L, u32 w0, u32 w1, const u32x4 (&xf)[2][14], int tp, int lane, int csl, f32x4 (&t)[2][2], f32x2 (&st)[2], E&& pre, F&& mid) {
  u32 wq = (tp & 1 ? w1 : w0) + (tp >> 1) * (7 * 2048 + GAP), ca = csl + (lane >> 4) * 16, sa = L_ST1 + (tp * 32 + (lane & 15)) * 8, fa = L_FLG + SIG * 16 + (lane & (NW - 1)) * 4;
  u32 cw = ca + tp * 64, fw = L_FLG + SIG * 16 + tp * 4, one = 1;
  u32x4 ones = {0x3f803f80u, 0x3f803f80u, 0x3f803f80u, 0x3f803f80u};
  LAS char *p0 = L + w0, *p1 = L + w1, *pq = L + wq, *pca = L + ca, *psa = L + sa, *pfa = L + fa, *pcw = L + cw, *pfw = L + fw;
  asm volatile("" : "+v"(p0), "+v"(p1), "+v"(pq), "+v"(pca), "+v"(psa), "+v"(pfa), "+v"(pcw), "+v"(pfw), "+v"(ones), "+v"(one));
  pre();
  u32x4 cf[7];
  u32x2 ra[7], rb[7];
  f32x4 cs, cp[4];
  u32 sw;
#define WAT(n) (((n) & 1 ? p1 : p0) + ((n) >> 1) * 2048 + ((n) >= 14 ? GAP : 0))
#pragma unroll
  for (int i = 0; i < 7; ++i) cf[i] = trfrag(pq + i * 2048, 256);
  SCHED();
  sfor<0, 7>([&](auto IC_) LAM {
    constexpr int i = IC_.value;
    macc<i == 0 ? 0 : i == 6 ? 2 : 1>(cs, cf[i], ones);
    SCHED();
    if (i < 6) { ra[i] = trread(WAT(i)); rb[i] = trread(WAT(i) + 256); }
    SCHED();
  });
  sfor<0, 28>([&](auto NC) LAM {
    constexpr int n = NC.value, K = n < 2 ? 0 : n < 26 ? 1 : 2;
    const u32x4 f = {ra[n % 7].x, ra[n % 7].y, rb[n % 7].x, rb[n % 7].y};
    macc<K>(t[0][n & 1], f, xf[0][n >> 1]);
    SCHED();
    if constexpr (n + 6 < 28) ra[(n + 6) % 7] = trread(WAT(n + 6));
    if constexpr (n == 24) st[0] = *(LAS f32x2*)(psa);
    if constexpr (n == 22 || n == 23) cp[n - 22] = *(LAS f32x4*)(pca + (n - 22) * 64);
    if constexpr (n >= 26) { float a = cp[n - 26][0] + cp[n - 24][0], b = cp[n - 26][1] + cp[n - 24][1]; PIN(a); PIN(b); cp[n - 26][0] = a; cp[n - 26][1] = b; }
    SCHED();
    macc<K>(t[1][n & 1], f, xf[1][n >> 1]);
    SCHED();
    if constexpr (n + 6 < 28) rb[(n + 6) % 7] = trread(WAT(n + 6) + 256);
    if constexpr (n == 24) st[1] = *(LAS f32x2*)(psa + 128);
    if constexpr (n == 22 || n == 23) cp[n - 20] = *(LAS f32x4*)(pca + (n - 20) * 64);
    if constexpr (n >= 26) { float a = cp[n - 26][2] + cp[n - 24][2], b = cp[n - 26][3] + cp[n - 24][3]; PIN(a); PIN(b); cp[n - 26][2] = a; cp[n - 26][3] = b; }
    if constexpr (n == 2) { if ((lane & 15) == 0) *(LAS f32x4*)(pcw) = cs; }
    if constexpr (n == 3) { asm volatile("" ::: "memory"); *(volatile LAS u32*)(pfw) = one; }
    if constexpr (n == 16) sw = *(volatile LAS u32*)(pfa);
    if constexpr (n == 21) { if (__builtin_expect(__builtin_amdgcn_ballot_w64(sw == 0) != 0, 0)) { lds_wait(L, SIG); if (NW > 4) lds_wait(L, SIG + 1); } asm volatile("" ::: "memory"); }
    mid(n, pfw - SIG * 16, one);
    SCHED();
  });
#pragma unroll
  for (int tb = 0; tb < 2; ++tb)
#pragma unroll
    for (int k = 0; k < 2; ++k) t[tb][k] += cp[k] * st[tb].x;
}
DI f32x2 swp16(float a, float b) { auto r = __builtin_amdgcn_permlane16_swap(__builtin_bit_cast(u32, a), __builtin_bit_cast(u32, b), false, false); return f32x2{__builtin_bit_cast(float, (u32)r[0]), __builtin_bit_cast(float, (u32)r[1])}; }
DI f32x2 swp32(float a, float b) { auto r = __builtin_amdgcn_permlane32_swap(__builtin_bit_cast(u32, a), __builtin_bit_cast(u32, b), false, false); return f32x2{__builtin_bit_cast(float, (u32)r[0]), __builtin_bit_cast(float, (u32)r[1])}; }
DI float swadd32(float a, float b) { const f32x2 s = swp32(a, b); return s.x + s.y; }
DI float swadd16(float a, float b) { const f32x2 s = swp16(a, b); return s.x + s.y; }
DI float mx2(float a, float b) { float r; asm("v_max_f32 %0, %1, %2" : "=v"(r) : "v"(a), "v"(b)); return r; }
DI float mx3(float a, float b, float c) { return fmaxf(fmaxf(a, b), c); }
DI float mn3(float a, float b, float c) { return fminf(fminf(a, b), c); }
template <class F> DI void feature_map(const f32x4 (&t)[2][2], const f32x2 (&st)[2], const u32x4 (&wpf)[2], int g, u32x4 (&P)[2][2], F&& mid) {
  const float pad = g < 3 ? 0.f : -3.0e38f;
  const f32x4 z4 = {0.f, 0.f, 0.f, 0.f};
  const float sc[2] = {st[0].y * LOG2E, st[1].y * LOG2E}, scg = g < 2 ? sc[0] : sc[1];
  float p[2][8], m[4], mp[4];
#pragma unroll
  for (int tb = 0; tb < 2; ++tb) {
    const u32x4 tq = {pk(t[tb][0][0], t[tb][0][1]), pk(t[tb][0][2], t[tb][0][3]), pk(t[tb][1][0], t[tb][1][1]), pk(t[tb][1][2], t[tb][1][3])};
    const f32x4 p0 = mfma16(wpf[0], tq, z4), p1 = mfma16(wpf[1], tq, z4);
#pragma unroll
    for (int i = 0; i < 8; ++i) p[tb][i] = i < 4 ? p0[i] : p1[i - 4];
  }
  mid(0);
#pragma unroll
  for (int tb = 0; tb < 2; ++tb) {
    const float* q = p[tb];
    m[2 * tb] = mx3(mx3(q[0], q[1], q[2]), mx3(q[3], q[4], q[5]), mx3(q[5], q[6], q[7]));
    m[2 * tb + 1] = mx3(mx3(-q[0], -q[1], -q[2]), mx3(-q[3], -q[4], -q[5]), mx3(-q[5], -q[6], -q[7]));
  }
  {
    const f32x2 a = swp16(m[0], m[1]), b = swp16(m[2], m[3]);
    const f32x2 c = swp32(mx2(a.x, a.y), mx2(b.x, b.y));
    const float d = mx2(c.x, c.y) * -scg;
    const f32x2 e = swp32(d, d), f0 = swp16(e.x, e.x), f1 = swp16(e.y, e.y);
    m[0] = f0.x; m[1] = f0.y; m[2] = f1.x; m[3] = f1.y;
#pragma unroll
    for (int c2 = 0; c2 < 4; ++c2) mp[c2] = m[c2] + pad;
  }
  float ex[4][8], sm[4];
#pragma unroll
  for (int c = 0; c < 4; ++c) {
    mid(1 + c);
#pragma unroll
    for (int i = 0; i < 8; ++i) {
      ex[c][i] = fexp2(fmaf(c & 1 ? -p[c >> 1][i] : p[c >> 1][i], sc[c >> 1], i & 2 ? mp[c] : m[c]));
    }
    sm[c] = ((ex[c][0] + ex[c][1]) + (ex[c][2] + ex[c][3])) + ((ex[c][4] + ex[c][5]) + (ex[c][6] + ex[c][7]));
  }
  mid(5);
  {
    const float d = frcp(swadd32(swadd16(sm[0], sm[1]), swadd16(sm[2], sm[3])));
    const f32x2 e = swp32(d, d), f0 = swp16(e.x, e.x), f1 = swp16(e.y, e.y);
    sm[0] = f0.x; sm[1] = f0.y; sm[2] = f1.x; sm[3] = f1.y;
  }
#pragma unroll
  for (int c = 0; c < 4; ++c)
#pragma unroll
    for (int e = 0; e < 4; ++e) P[c >> 1][c & 1][e] = pk(ex[c][2 * e] * sm[c], ex[c][2 * e + 1] * sm[c]);
}
DI u32x4 piece(const u32x4 (&rp)[8], int e) {
  u32x4 f;
#pragma unroll
  for (int m = 0; m < 4; ++m) f[m] = __builtin_amdgcn_perm(rp[2 * m + 1][e >> 1], rp[2 * m][e >> 1], (e & 1) ? 0x07060302u : 0x05040100u);
  return f;
}
#define RP_OPS "+v"(rp[0]), "+v"(rp[1]), "+v"(rp[2]), "+v"(rp[3]), "+v"(rp[4]), "+v"(rp[5]), "+v"(rp[6]), "+v"(rp[7])
#define PF1 0
#define PF2 84
#define PF3 120
#define TP0 -1
#define TP1 13
#define TP2 -1
#define TOUCH 5
#define NW3 4
#define THRN0 4
#define DNPRIO 3
#define NWD 3
#define MIDCHK 0
#define HELP 0
#define NHW 7
#define W3LATE 0
#define W3WAIT 1
#define ROT 1
#define EXPT 0
#define PFM 12
#define PSLP 4
#define PLAG 0
#define WTOUCH 0
#define REPLATE 0
#define WB0 8
#define WB1 4
#define ULATE 0
#define VPRIO 3
#define VPRIO0 3
#define VPN 27
#define FPRIO 2
#define CLB 0
#define KAT 99
#define STF 0
#define QN0 0
#define QST 4
#define PB0 3
#define WOT 0
#define WTN 2
#define CLF 0
#define TQ 1
#define TU 0
#define TO 1
#define SN_ 49
#define SS_ 128
#define SD_ 0
#define RPO 0
#define REPE 0
#define UFN 14
#define SBA 0
#define TD 17
#define NR5 16
#define NR6 23
#define G4E 0
#define TS0 2
#define TS1 8
#define TWF 4
#define BP3 0
#define W3J 2
#define TQD -1
#define TOD 0
#define TDD 10
#define CPN 7
#define X0A 5
#define NE 0
#define G6E 0
#define B5E 2
#define B5D 0
#define HW 1
#define PACE 1
#define PS 28
#define PJ0 1
#define PJ1 6
#define ATP 3
#define P1E 0
#define HLA 12
#define FLG13 1
#define H1F 1
#define OLAG 5
#define W4O 0
#define CLW 2
#define CN 50
#define WKF 1
#define WON 7
#define KGO -1
#define QEARLY 1
#define DLY1 4
#define W1 1
#define W2C 0
#define WOE 2
#define W3THR 1
#define HW3 1
#define HW1 1
#define SN 4
#define BATCH 1
#define B23 1
#define WD5L 0
#define A56 1
#define BGO 196
#define BALL 0
#define BSL 4
#define RSL 4
#define H5 1
#define H6 1
#define BORD 1
#define DP0 3
#define AIL 1
#define SEEN 3
#define ADLY 4
#define W6E 0
#define SPLIT 1
#define NCL 4
#define B5L 1
#define A6L 1
#define BO2 2
#define TW 0
#define CL 0
#define CE 1
#define W5D 1
#define W2F 5
#define D2A 12
#define D2B 16
#define SPEC 1
#define CLP 28
#define CLS 3
#define CE2 0
#define DLY5 60
#define CSL 1
#define CF 0
#define OPRIO 3
#define PG 1
#define ISK 1
#define T2 70
#define FXW 8
#define DLY4 16
#define PUBS 5
#define THRN 40
#define DLY2 8
#define DLY3 15
#define GUPRIO 2
enum { S_FREE01 = 0, S_REFILL, S_DONE4, S_WIN3, S_WK, S_WV, S_WQ, S_WSEEN, S_KDONE, S_V, S_PHIK, S_CSV, S_KVA, S_WO, S_CSQ, S_PHI, S_X0, S_X1, S_XA, S_CSK, S_CS, S_WOFA, S_WOFB, S_EXA, S_EXB, S_W2A, S_W2B, S_HFA, S_HFB, S_CONS, S_SPARE, S_XF0 };

__global__ void __launch_bounds__(512, 2) fused_block(const char* __restrict__ x, const char* __restrict__ wqkv, const char* __restrict__ wphi, const char* __restrict__ wo,
                                                      const char* __restrict__ wg, const char* __restrict__ wu, const char* __restrict__ wd, char* __restrict__ out, char* ws) {
  extern __shared__ __attribute__((aligned(16))) char smem[];
  LAS char* L = (LAS char*)smem;
  const int tid = threadIdx.x, lane = tid & 63, wave = __builtin_amdgcn_readfirstlane(tid >> 6), w2 = wave & 3;
  const int b = blockIdx.x, li = b >> 3, xp = (b & 7) >> 1, xh = b & 1;
  const int tile = 4 * xp + (li >> 3), r = 8 * xh + (li & 7);
  const int tile2 = 4 * xp + 2 * xh + (li >> 4), r2 = li & 15;
  const int tg = r2 >= 7 ? 1 : 0, cg = r2 - 7 * tg;
  const int l15 = lane & 15, g = lane >> 4, q4 = l15 >> 2, p4 = lane & 3, hh = lane >> 5;
  const int head = r;
  {
    u64 pc_; asm volatile("s_getpc_b64 %0" : "=s"(pc_));
    touch((const char*)pc_, min((b & ~7) * 256 + (tid >> 5) * 128, CODE_PF));
    *(LAS u32*)(L + L_FLG + (tid & 127) * 4) = 0u;
  }
  int tn = wave < 2 ? 28 : 85 - 24 * wave, ti = (b & 7) * 64 + li * 2 + wave;
  asm volatile("" : "+s"(tn), "+s"(ti));
  ti = wave < 2 ? ti : b;
  u32 tvo = min(ti * tn + min(lane, tn - 1), wave == 3 ? 3135 : wave == 2 ? 9407 : 14335) * 128, xvo = (tile * 128 + w2 * 32 + (lane >> 3)) * 896 + (((lane & 7) ^ (lane >> 3)) * 16);
  asm volatile("s_waitcnt lgkmcnt(0)\n\ts_barrier" : "+v"(tvo), "+v"(xvo) :: "memory");
  asm volatile("" :: "s"(x), "s"(wqkv), "s"(wphi), "s"(wo), "s"(wg), "s"(wu), "s"(wd), "s"(out), "s"(ws));
  STAMP(0);
  u64 epoch;
  if (wave < 4) {
    const char* tb_ = wave < 2 ? x : wave == 2 ? wqkv : wo;
    asm volatile("" : "+s"(tb_));
    touch(tb_, tvo);
  }
  asm volatile("s_load_dwordx2 %0, %1, 0x0" : "=s"(epoch) : "s"(ws) : "memory");
  if (wave >= 4) {
    const __amdgpu_buffer_rsrc_t rx = __builtin_amdgcn_make_buffer_rsrc((void*)(x - 2048), 0, 2048 * 896 + 2048, 0x00020000);
    sfor<0, 7>([&](auto NC) LAM {
      dma_x<4 * NC.value>(rx, xvo, w2 * 28672);
      if (NC.value == X0A) { asm volatile("s_waitcnt vmcnt(%0)" :: "n"(4 * X0A - 10) : "memory"); lds_signal(L, S_X0, w2); }
    });
  }
  asm volatile("s_waitcnt lgkmcnt(0)" : "+s"(epoch) :: "memory");
  const __amdgpu_buffer_rsrc_t rs = __builtin_amdgcn_make_buffer_rsrc((void*)ws, 0, WS_USED, 0x00020000);
  const u32x4 ones = {0x3f803f80u, 0x3f803f80u, 0x3f803f80u, 0x3f803f80u};
  const f32x4 z4 = {0.f, 0.f, 0.f, 0.f};
  auto role = [&](auto AC) __attribute__((always_inline)) {
  constexpr bool A = AC.value;
  if (A) asm volatile("; A" ::: "memory"); else asm volatile("; B" ::: "memory");
  u32x4 rp[8];
  const int fvo = lane < 16 ? lane * 8 : NOWHERE;
  const int uidx = (tile * 10 + __builtin_popcount(~0x2A54u & ((1u << head) - 1u))) * 4 + w2, unit = uidx * 49 / 80;
  const bool has_unit = !((0x2A54 >> head) & 1) && (uidx + 1) * 49 / 80 != unit;
  const int u3 = unit >> 1, umat = unit & 1, ukq = u3 / 28, ufl = u3 - 28 * ukq;
  const char* gusrc = ws + WS_WGU + 7 * r * SLOT;
  const u32 guo = WS_WGU + 7 * r * SLOT + w2 * 7168;
#define GUBLK(j) dma7<(j) * SLOT, gu_slot(j) * SLOT>(rs, lane * 16, guo, w2 * 7168)
  const u32 wvo = (lane >> 2) * 2688 + (((lane & 3) ^ (2 * (lane >> 5))) * 16);
  const char* wbase = wqkv + head * 56 - (head & 1) * 8 + 16 * w2 * 2688;
  const u32 wr0 = (8 * g + q4) * 64 + (((p4 + (head & 1)) ^ (4 * (g & 1))) * 8), wr1 = (8 * g + q4) * 64 + ((((4 + p4 + (head & 1)) & 7) ^ (4 * (g & 1))) * 8);
  const int tb = w2; constexpr int kh = A ? 0 : 1;
  const int hho = WS_HH + (((tile * 2 + (w2 >> 1)) * 4 + (r >> 2)) * 56 + (r & 3) * 2 + (w2 & 1)) * 1024;
  const int tok = tile2 * 128 + tg * 64 + tb * 16 + l15;
  const int hvo = WS_H + ((tok >> 5) * 28 + 4 * cg + (g >> 1)) * 1024 + ((g & 1) * 32 + (tok & 31)) * 16;
  const int sto = WS_ST + (2 * cg * 2048 + tile2 * 128 + tg * 64 + tb * 16) * 8;
  const u32x4 empty4 = {EMPTY, EMPTY, EMPTY, EMPTY};
#define HH_CLEAR(i0, i1) _Pragma("unroll") for (int i = i0; i < i1; ++i) ST16(rs, empty4, lane * 16 + (i & 1) * 2048, WS_HH + (((tile2 * 2 + r2 - 14) * 4 + w2) * 56 + kh + (i >> 1) * 4) * 1024)
#define HH_FLAG() FLAG(WS_FH + ((tile2 * 4 + 2 * (r2 - 14) + kh) * 32 + 28 + w2) * 128, 1)
  u32x4 wof[8][4];
  f32x4 csp[7];
  u64 tag;
#define WOF_READ(i0, i1) do { u32 wa_ = L_WO + (kh * 224 + 4 * g + q4) * 128 + 16 * p4 + 64 * (q4 >> 1), wb_ = wa_ ^ 64, wc_ = wa_ + (g == 3 ? 0 : 2048), wd_ = wc_ ^ 64; \
    asm volatile("" : "+v"(wa_), "+v"(wb_), "+v"(wc_), "+v"(wd_)); \
    _Pragma("unroll") for (int i = i0; i < i1; ++i) _Pragma("unroll") for (int k = 0; k < 4; ++k) { \
      const u32x2 a_ = trread(L + (k >> 1 ? wb_ : wa_) + 28 * i * 128 + (k & 1) * 8), c_ = trread(L + (k >> 1 ? wd_ : wc_) + 28 * i * 128 + (k & 1) * 8); \
      wof[i][k] = u32x4{a_.x, a_.y, c_.x, c_.y}; } } while (0)
#define XF_READ(i) _Pragma("unroll") for (int s = 0; s < 14; ++s) xf[i][s] = *(LAS u32x4*)((s & 1 ? xr1 : xr0) + (i) * 14336 + (s >> 1) * 2048)
#define LN_STATS(kh) do { f32x4 g1 = z4, g2 = z4; \
    _Pragma("unroll") for (int s = 0; s < 14; ++s) { g1 = mfma16(ones, xf[kh][s], g1); g2 = mfma16(xf[kh][s], xf[kh][s], g2); } \
    const float d01 = (p4 & 1) ? g2[1] : g2[0], d23 = (p4 & 1) ? g2[3] : g2[2], s2 = (p4 & 2) ? d23 : d01; \
    const float nmu = g1[0] * (-1.f / 448.f), var = fmaxf(s2 * (1.f / 448.f) - nmu * nmu, 0.f); \
    if (g == q4) *(LAS f32x2*)(L + L_ST1 + (w2 * 32 + kh * 16 + l15) * 8) = f32x2{nmu, __builtin_amdgcn_rsqf(var + 1e-5f)}; } while (0)
#define WPF() do { u32 wp[8]; \
    _Pragma("unroll") for (int j = 0; j < 8; ++j) wp[j] = *(LAS u32*)(L + L_WPHI + 4 * g * 56 + min(l15, 13) * 4 + ((j & 3) + 16 * (j >> 2)) * 56); \
    _Pragma("unroll") for (int e = 0; e < 4; ++e) { const bool z = e >= 2 && g == 3; \
      wpf[0][e] = z ? 0u : __builtin_amdgcn_perm(wp[2 * e + 1], wp[2 * e], 0x05040100u); wpf[1][e] = z ? 0u : __builtin_amdgcn_perm(wp[2 * e + 1], wp[2 * e], 0x07060302u); } \
    asm volatile("" : "+v"(wpf[0]), "+v"(wpf[1])); } while (0)
  {
    u32x4 xf[2][14], wpf[2];
    f32x2 st[2];
    LAS char* xr0 = L + w2 * 28672 + l15 * 128 + ((g ^ (l15 & 7)) * 16);
    LAS char* xr1 = L + w2 * 28672 + l15 * 128 + (((4 + g) ^ (l15 & 7)) * 16);
    LAS char* pw = L + L_PKV + (w2 * 32 + l15) * 224 + g * 8;
    const char* wos = wo + (r2 < 14 ? cg : 0) * 128 + w2 * 56 * 896;
    const u32 wovo = (lane >> 3) * 896 + (((lane & 7) ^ ((lane >> 2) & 4)) * 16);
#define WO_HALF(hf_) _Pragma("unroll") for (int n = 0; n < 7; ++n) dma16(wos + ((hf_) * 224 + n * 8) * 896, wovo, L_WO + ((hf_) * 28 + w2 * 7 + n) * 1024)
    int clo = WS_O + ((tile * 8 + 2 * w2) * 16 + head) * 1024, clh = r2 < 14 ? hvo : NOWHERE, cls = r2 < 14 && lane < 16 ? (lane >> 3) * 16384 + (lane & 7) * 16 : NOWHERE, clt = sto;
    int kvo = WS_KVP + ((head * 16 + tile) * 4 + w2) * 1024, fko = WS_FKV + ((head * 4 + w2) * 16 + tile) * 128;
    u32x4 em4 = empty4;
    u32 wtv = min(lane, 24) * 128;
    const char* wts = wo + min(li * 98 + w2 * 25, 3136 - 25) * 128;
    auto clear = [&](int c) LAM {
      if (c == 0) ST16(rs, em4, lane * 16, clo);
      if (c == 1) ST16(rs, em4, lane * 16, clo + 16384);
      if (c == 2) ST16(rs, em4, clh, 0);
      if (c == 3) ST16(rs, em4, clh, 2048);
      if (c == 4) ST16(rs, em4, cls, clt);
    };
    if constexpr (A) {
      if (wave == 2) { dma16(wphi + head * 1568, lane * 16, L_WPHI); dma16(wphi + head * 1568, min(lane + 64, 97) * 16, L_WPHI + 1024); }
      __builtin_amdgcn_s_waitcnt(0x0F70);
      tag = epoch * 6364136223846793005ull + 1442695040888963407ull;
      STAMP(1);
#define WKLOAD() _Pragma("unroll") for (int i = 0; i < 7; ++i) dma16(wbase + 896 + 64 * i * 2688, wvo, L_WK + (4 * i + w2) * 1024)
      if (KAT == 3) { WKLOAD(); if (wave == 2) { WAITVM(7); lds_signal(L, S_PHI, 0); } }
      else if (wave == 2) { if (CLF == 9) WAITVM(5); else WAITVM(0); lds_signal(L, S_PHI, 0); }
      if (KAT > 3 && KAT < 28) { lds_wait1(L, S_SPARE, w2); WKLOAD(); }
      lds_wait1(L, S_PHI, 0);
      WPF();
#define DBITE() dma4(wd + min(b * 49 + w2 * 13, 12544 - 13) * 128, min(lane, 12) * 128, L_JUNK)
      if (TD == 9) {
        __builtin_amdgcn_s_sleep(TS0);
        if (w2 & 1) __builtin_amdgcn_s_sleep(TS1);
        if (w2 & 2) { __builtin_amdgcn_s_sleep(TS1); __builtin_amdgcn_s_sleep(TS1); }
        DBITE();
      }
      if (TD == 10 && w2 == 0) DBITE();
      if (WKF == 2) {
#pragma nounroll
        for (u32 sp = 0; sp < (SPIN_CAP << 4); ++sp) { const u32x4 v = *(volatile LAS u32x4*)(L + L_FLG + S_X0 * 16); if (__builtin_amdgcn_readfirstlane(v.x | v.y | v.z | v.w)) break; __builtin_amdgcn_s_sleep(1); }
        WKLOAD();
      }
      lds_wait1(L, S_X0, w2);
      if (WKF == 1) { WKLOAD(); }
      XF_READ(0);
      if (w2 < 2) lds_signal(L, S_XF0, w2);
      if (TD == 10 && w2 == 1) DBITE();
      if (WKF == 0) { WKLOAD(); }
      if (TD == 10 && w2 == 2) DBITE();
      if (TD == 11) DBITE();
      STAMP(27);
      LN_STATS(0);
      SCHED();
      STAMP(2);
      lds_wait1(L, S_X1, w2);
      XF_READ(1);
      if (QEARLY == 3 && w2 < 2) lds_signal(L, S_SPARE, w2);
      STAMP(3);
      if (SBA) LN_STATS(1);
      WAITVM(0);
      STAMP(4);
      lds_signal(L, S_WK, w2);
      if (TD == 10 && w2 == 3) DBITE();
      if (TD == 12) DBITE();
      f32x4 t[2][2];
      u32 pr = L_PKV + (4 * g + q4) * 224 + 8 * p4;
      asm volatile("" : "+v"(pr), "+v"(pw));
      asm volatile("" : "+s"(clo), "+v"(clh), "+v"(cls), "+s"(clt), "+s"(kvo), "+s"(fko), "+s"(tag), "+v"(em4));
      if (WOT == 5) asm volatile("" : "+v"(wtv), "+s"(wts));
      project<0, S_CSK, 8>(L, L_WK + wr0, L_WK + wr1, xf, w2, lane, L_CSP, t, st, [&]() LAM {
        lds_wait(L, S_WK);
        STAMP(5);
        __builtin_amdgcn_s_setprio(2);
      }, [&](int n, LAS char* sg, u32 one) LAM {
        if (n == KGO) *(volatile LAS u32*)(sg + S_KDONE * 16) = one;
      });
      STAMP(6);
      u32x4 P[2][2];
      __builtin_amdgcn_s_setprio(FPRIO);
      feature_map(t, st, wpf, g, P, [&](int i) LAM {
        if (WOT == 5 && i == 0) { SCHED(); dma4(wts, wtv, L_JUNK); SCHED(); }
        if (CLF == 1 && i > 0) { SCHED(); clear(i - 1); SCHED(); }
        if (CLF == 2 && i == 5) { SCHED(); clear(0); clear(1); clear(2); clear(3); clear(4); SCHED(); }
        if (CLF == 3 && i >= 3) { SCHED(); clear(2 * i - 6); clear(2 * i - 5); SCHED(); }
        if (CLF == 4 && i == 1) { SCHED(); clear(0); clear(1); clear(2); clear(3); clear(4); SCHED(); }
      });
#pragma unroll
      for (int c = 0; c < 4; ++c) {
        LAS char* p = pw + (c >> 1) * 16 * 224 + (c & 1) * 64;
        *(LAS u32x2*)(p) = u32x2{P[c >> 1][c & 1].x, P[c >> 1][c & 1].y}; *(LAS u32x2*)(p + 32) = u32x2{P[c >> 1][c & 1].z, P[c >> 1][c & 1].w};
      }
      lds_signal(L, S_PHIK, w2);
      STAMP(7);
      if (CLF == 0 || CLF == 8) { clear(0); clear(1); }
      if (CLF == 0 || CLF == 7) { clear(2); clear(3); clear(4); }
      lds_wait2(L, S_V, S_PHIK);
      u32x4 pa[4], pb[8];
#pragma unroll
      for (int m = 0; m < 4; ++m) {
        pa[m] = trfrag(L + pr + w2 * 32 + m * 32 * 224, 16 * 224);
        pb[2 * m] = trfrag(L + pr + m * 32 * 224 + 128, 16 * 224); pb[2 * m + 1] = trfrag(L + pr + m * 32 * 224 + 160, 16 * 224);
      }
      SCHED();
      f32x4 kv[2] = {};
#pragma unroll
      for (int m = 0; m < 8; ++m) kv[m & 1] = mfma16(pa[m >> 1], pb[m], kv[m & 1]);
      const u32x4 kvb = {pk(kv[0][0], kv[0][1]), pk(kv[0][2], kv[0][3]), pk(kv[1][0], kv[1][1]), pk(kv[1][2], kv[1][3])};
      ST16(rs, kvb, lane * 16, kvo);
      STAMP(8);
      if (TD == 17) {
        lds_signal(L, S_KDONE, w2);
        if (wave == 0) { lds_wait(L, S_KDONE); touch(wd + b * 49 * 128, min(lane, 48) * 128); WAITVM(1); } else WAITVM(0);
      } else if (TD == 19) {
        lds_signal(L, S_KDONE, w2);
        lds_wait(L, S_KDONE);
        if (wave == 0) touch(wd + b * 49 * 128, min(lane, 48) * 128);
        if (r2 < 14) {
#pragma unroll
          for (int n = 0; n < WON; ++n) dma16(wos + n * 8 * 896, wovo, L_WO + (w2 * 7 + n) * 1024);
          if (wave == 0) asm volatile("s_waitcnt vmcnt(%0)" :: "n"(WON + 1) : "memory"); else asm volatile("s_waitcnt vmcnt(%0)" :: "n"(WON) : "memory");
        } else if (wave == 0) WAITVM(1); else WAITVM(0);
      } else if (TD == 18) {
        lds_signal(L, S_KDONE, w2);
        lds_wait(L, S_KDONE);
        touch(wd + min(b * 49 + w2 * 13, 12544 - 13) * 128, min(lane, 12) * 128);
        WAITVM(1);
      } else DRAIN();
      FLAG(fko, 1);
      __builtin_amdgcn_s_setprio(0);
      STAMP(9);
      if (HW1) { lds_signal(L, S_XA, w2); lds_wait(L, S_XA); }
#define DTOUCH() dma4(wd + b * 49 * 128, min(lane, 48) * 128, L_JUNK)
      if (TD == 2 && wave == 0) DTOUCH();
      if (r2 < 14 || !ISK) {
#pragma unroll
        for (int n = TD == 19 ? WON : 0; n < 7; ++n) dma16(wos + n * 8 * 896, wovo, L_WO + (w2 * 7 + n) * 1024);
        WO_HALF(1);
      } else { HH_CLEAR(0, NE); if (NE < 8) __builtin_amdgcn_s_sleep(20); }
      poll_flags(ws, WS_FKV + ((head * 4 + w2) * 16 + l15) * 128, lane < 16, tag);
      lds_signal(L, S_WO, w2);
      STAMP(10);
      u32x4 tt[16];
#pragma unroll
      for (int i = 0; i < 16; ++i) tt[i] = __builtin_bit_cast(u32x4, __builtin_amdgcn_raw_buffer_load_b128(rs, lane * 16, WS_KVP + head * 65536 + w2 * 1024 + i * 4096, 16));
      u32 fgo;
      {
        const int pl = (tile & 1) * 16 + min(lane >> 2, 13);
        fgo = wave == 0 ? WS_FKV + ((l15 * 4 + 2 * tg + (g & 1)) * 16 + tile2) * 128
            : wave == 1 ? WS_FKV + (((((tile >> 1) & 1) * 8 + (pl & 7)) * 4 + p4) * 16 + (tile & 12) + (pl >> 3)) * 128
                        : WS_FG + (min(((7 * r) >> 2) + g, (7 * r + 6) >> 2) * 16 + (l15 & 8) + min(lane & 7, 6)) * 128;
      }
      u64 fg = __hip_atomic_load((const GAS u64*)(ws + fgo), RLX_AGENT);
      SCHED();
      const u32 one = g == q4 ? ((p4 & 1) ? 0x3f800000u : 0x3f80u) : 0u, s01 = p4 < 2 ? one : 0u, s23 = p4 < 2 ? 0u : one;
      const u32x4 sel0 = {s01, s23, 0u, 0u}, sel1 = {0u, 0u, s01, s23};
      f32x4 s0 = {}, s1 = {};
#pragma unroll
      for (int i = 0; i < 16; ++i) { s0 = mfma16(sel0, tt[i], s0); s1 = mfma16(sel1, tt[i], s1); }
      LAS char* kw = L + L_KVA + (w2 >> 1) * 1024 + lane * 16 + (w2 & 1) * 8;
      *(LAS u32x2*)(kw) = u32x2{pk(s0[0], s0[1]), pk(s0[2], s0[3])};
      *(LAS u32x2*)(kw + 2048) = u32x2{pk(s1[0], s1[1]), pk(s1[2], s1[3])};
      lds_signal(L, S_KVA, w2);
      if (TD == 4 && wave == 2) DTOUCH();
      STAMP(11);
      if (WOE == 3) { WO_HALF(1); WAITVM(0); lds_signal(L, S_WO, w2); }
      asm volatile("" : "+v"(fg) :: "memory");
      if (!__all(fg == tag + 1)) poll_flags(ws, fgo, true, tag);
      __builtin_amdgcn_fence(__ATOMIC_ACQUIRE, "wavefront");
      lds_signal(L, S_WOFA, w2);
      lds_wait1(L, S_WOFA, 3);
      const int fl = min(((7 * r) >> 2) + w2, (7 * r + 6) >> 2);
#pragma unroll
      for (int i = 0; i < 7; ++i) asm volatile("global_load_dwordx4 %0, %1, %2 offset:%3 sc1" : "=v"(csp[i]) : "v"(((7 * r) >> 2) + w2 == fl ? lane * 16 : 0), "s"(ws + WS_CS + fl * 7168 + 4096), "n"(i * 1024 - 4096) : "memory");
      GUBLK(2);
      if (r2 < 14 || !ISK) { lds_wait(L, S_WO); WOF_READ(0, 8); }
      SCHED();
#define CS_OUT(k_) do { asm volatile("s_waitcnt vmcnt(%7)" : "+v"(csp[0]), "+v"(csp[1]), "+v"(csp[2]), "+v"(csp[3]), "+v"(csp[4]), "+v"(csp[5]), "+v"(csp[6]) : "n"(k_) : "memory"); \
        const f32x4 sm = ((csp[0] + csp[1]) + (csp[2] + csp[3])) + ((csp[4] + csp[5]) + csp[6]); \
        const float e0 = swadd32(sm[0], sm[1]), e1 = swadd32(sm[2], sm[3]);     \
        const int j0 = 4 * (((7 * r) >> 2) + w2) + 2 * (lane & 1) - 7 * r, cr = ((g & 1) * 16 + hh * 8 + (l15 >> 1)) * 4; \
        if (j0 >= 0 && j0 < 7) *(LAS float*)(L + L_CS + j0 * 128 + cr) = e0; \
        if (j0 + 1 >= 0 && j0 + 1 < 7) *(LAS float*)(L + L_CS + (j0 + 1) * 128 + cr) = e1; } while (0)
      lds_signal(L, S_WOFB, w2);
      if (!CSL) CS_OUT(7);
      STAMP(24);
    } else {
      const int rg = lane >> 3;
      const u32 lvo = rg * 8 * 3584 + (lane & 7) * 16;
      const char* us = (umat ? wu : wg) + ukq * 64 * 3584 + ufl * 128;
      asm volatile("" : "+s"(us));
      auto repack = [&]() LAM {
        if (has_unit) {
          const int so = rg * 512 + (lane & 7) * 16;
          const int sb = WS_WGU + 4 * ufl * SLOT + ukq * 4096 + umat * 256;
          const int s1 = SLOT, s2 = 2 * SLOT;
          float c[8];
#pragma unroll
          for (int e = 0; e < 8; ++e) {
            const u32x4 f = piece(rp, e);
            ST16(rs, f, so + (e & 1) * 128, sb + ((e >> 1) & 1) * s1 + (e >> 2) * s2);
            c[e] = mfma16(ones, f, z4)[0];
          }
          const f32x4 cv = g & 1 ? f32x4{c[4], c[5], c[6], c[7]} : f32x4{c[0], c[1], c[2], c[3]};
          ST16(rs, cv, g < 2 ? (l15 >> 3) * 512 + (lane & 7) * 32 + g * 16 : NOWHERE, WS_CS + ufl * 7168 + ukq * 1024 + (umat & 1) * 256);
        }
      };
#define RPLOAD() _Pragma("unroll") for (int j = 0; j < 8; ++j) asm volatile("global_load_dwordx4 %0, %1, %2" : "=v"(rp[j]) : "v"(lvo), "s"(us + j * 3584) : "memory")
      if (RPO == 2 && has_unit) { RPLOAD(); }
      tag = epoch * 6364136223846793005ull + 1442695040888963407ull;
      STAMP(1);
      XF_READ(0);
      if (w2 < 2) lds_signal(L, S_XF0, 2 + w2);
      if (RPO == 2 && has_unit) WAITVM(8); else WAITVM(0);
      lds_signal(L, S_X1, w2);
      if (RPO == 1 && has_unit) { RPLOAD(); }
      STAMP(2);
      if (STF < 7) lds_wait(L, S_XF0);
      XF_READ(1);
      if (QEARLY == 3 && w2 < 2) lds_signal(L, S_SPARE, 2 + w2);
      SCHED();
#define WDST(c) ((c) * 1024 + ((c) >= 14 ? SLOT - 14336 : 0))
#pragma unroll
      for (int i = 0; i < 7 - STF; ++i) dma16(wbase + 1792 + 64 * i * 2688, wvo, L_WV + WDST(4 * i + w2));
      STAMP(3);
      __builtin_amdgcn_s_setprio(PB0);
      if (!SBA) LN_STATS(1);
      lds_signal(L, S_CS, w2);
      if (STF == 7) lds_wait(L, S_XF0);
#pragma unroll
      for (int i = 7 - STF; i < 7; ++i) dma16(wbase + 1792 + 64 * i * 2688, wvo, L_WV + WDST(4 * i + w2));
      STAMP(4);
      f32x4 t[2][2];
      asm volatile("" : "+v"(pw));
      if (WOT == 4) asm volatile("" : "+v"(wtv), "+s"(wts));
      project<SLOT - 14336, S_CSV, 4>(L, L_WV + wr0, L_WV + wr1, xf, w2, lane, L_CSP + 256, t, st, [&]() LAM {
      if (QEARLY == 3) {
        lds_wait(L, S_SPARE);
#pragma unroll
        for (int i = 0; i < 7; ++i) dma16(wbase + 64 * i * 2688, wvo, L_WQ + WDST(4 * i + w2));
      }
      if (REPE == 3) { asm volatile("s_waitcnt vmcnt(%8)" : RP_OPS : "n"(QEARLY == 3 ? 14 : 7) : "memory"); repack(); if (has_unit) { if (QEARLY == 3) WAITVM(16); else WAITVM(9); } }
      if (RPO == 0 && has_unit) { RPLOAD(); } else if (REPE == 3 && has_unit) {} else if (QEARLY == 3) WAITVM(7); else WAITVM(0);
      STAMP(28);
      if (RPO) {} else if (QEARLY == 3) WAITVM(15); else WAITVM(8);
      lds_signal(L, S_WV, w2);
      STAMP(29);
      lds_wait2(L, S_WV, S_WK);
      STAMP(30);
      if (QEARLY == 1) {
#pragma unroll
        for (int i = 0; i < 7; ++i) dma16(wbase + 64 * i * 2688, wvo, L_WQ + WDST(4 * i + w2));
      }
        if (REPE == 2) { asm volatile("s_waitcnt vmcnt(7)" : RP_OPS :: "memory"); repack(); }
        if (TD == 8 && !has_unit && !((0x2A54 >> head) & 1)) dma4(wd + min((uidx - unit) * 51, 12544 - 51) * 128, min(lane, 50) * 128, L_JUNK);
        if (KGO >= 0) lds_wait1(L, S_KDONE, w2);
        STAMP(5);
        __builtin_amdgcn_s_setprio(VPRIO0);
      }, [&](int n, LAS char*, u32) LAM {
        if (n == VPN) __builtin_amdgcn_s_setprio(VPRIO);
        if (WOT == 4 && n == WTN) dma4(wts, wtv, L_JUNK);
        if (QEARLY == 2 && n >= QN0 && (n - QN0) % QST == 0 && (n - QN0) / QST < 7) { const int i = (n - QN0) / QST; dma16(wbase + 64 * i * 2688, wvo, L_WQ + WDST(4 * i + w2)); }
      });
#pragma unroll
      for (int c = 0; c < 4; ++c) t[c >> 1][c & 1] *= st[c >> 1].y;
      STAMP(6);
#pragma unroll
      for (int c = 0; c < 2; ++c) {
        LAS char* p = pw + c * 16 * 224 + 128;
        *(LAS u32x2*)(p) = u32x2{pk(t[c][0][0], t[c][0][1]), pk(t[c][0][2], t[c][0][3])};
        *(LAS u32x2*)(p + 32) = g == 3 ? u32x2{0x3f80u, 0u} : u32x2{pk(t[c][1][0], t[c][1][1]), pk(t[c][1][2], t[c][1][3])};
      }
      lds_signal(L, S_V, w2);
      __builtin_amdgcn_s_setprio(0);
      STAMP(7);
      if (!QEARLY) {
#pragma unroll
        for (int i = 0; i < 7; ++i) dma16(wbase + 64 * i * 2688, wvo, L_WQ + WDST(4 * i + w2));
      }
      if (WOE == 0) WO_HALF(0);
      if (WOE < 2) { lds_wait(L, S_KDONE); WO_HALF(1); }
      if (REPE == 0) asm volatile("s_waitcnt vmcnt(%8)" : RP_OPS : "n"(QEARLY == 3 && !RPO ? 0 : 7) : "memory");
#define UFLAG() FLAGC(has_unit, WS_FG + ((ufl * 2 + umat) * 8 + ukq) * 128, 1)
      if (REPE == 0) repack();
      STAMP(8);
      WPF();
      if (!has_unit || (REPE == 3 && QEARLY != 3)) WAITVM(0); else WAITVM(9);
      if (UFN < 0) { WAITVM(0); UFLAG(); }
      lds_signal(L, S_WQ, w2);
      project<SLOT - 14336, S_CSQ, 4>(L, L_WQ + wr0, L_WQ + wr1, xf, w2, lane, L_CSP + 512, t, st, [&]() LAM {
        lds_wait(L, S_WQ);
        STAMP(9);
      }, [&](int n, LAS char*, u32) LAM {
        if (n == UFN) { WAITVM(0); UFLAG(); }
      });
      STAMP(10);
      if (REPLATE == 2) repack();
      u32x4 pq[2][2];
      feature_map(t, st, wpf, g, pq, [](int) {});
      asm volatile("" : "+v"(pq[0][0]), "+v"(pq[0][1]), "+v"(pq[1][0]), "+v"(pq[1][1]));
      STAMP(11);
      if (REPLATE == 1) repack();
      if (TD == 13 || TD == 14) {
        lds_wait(L, S_WO);
        if (r2 < 14) WOF_READ(0, TWF); else __builtin_amdgcn_s_sleep(2 * TWF);
        SCHED();
        if (TD == 14) DBITE(); else if (w2 == 0) DTOUCH();
        SCHED();
        if (r2 < 14) WOF_READ(TWF, 8);
      } else if (r2 < 14 || !ISK) { lds_wait(L, S_WO); WOF_READ(0, 8); }
      STAMP(24);
      if (REPLATE == 2) { WAITVM(0); UFLAG(); }
      __builtin_amdgcn_s_setprio(OPRIO);
      lds_wait(L, S_KVA);
      if (REPLATE == 1) { WAITVM(0); UFLAG(); }
      STAMP(27);
      u32x4 ka[4];
#pragma unroll
      for (int i = 0; i < 4; ++i) ka[i] = *(LAS u32x4*)(L + L_KVA + i * 1024 + lane * 16);
      f32x4 o[2][2];
#pragma unroll
      for (int c = 0; c < 4; ++c) o[c >> 1][c & 1] = mfma16(ka[2 * (c & 1) + 1], pq[c >> 1][1], mfma16(ka[2 * (c & 1)], pq[c >> 1][0], z4));
      const f32x2 da = swp16(o[0][1][0], o[0][1][0]), db = swp16(o[1][1][0], o[1][1][0]);
      const float den[2] = {swp32(da.y, da.y).y, swp32(db.y, db.y).y};
#pragma unroll
      for (int c = 0; c < 2; ++c) {
        const float rd = frcp(den[c] + 1e-6f), r3 = g == 3 ? 0.f : rd;
        const u32x4 ob = {pk(o[c][0][0] * rd, o[c][0][1] * rd), pk(o[c][0][2] * rd, o[c][0][3] * rd), pk(o[c][1][0] * r3, o[c][1][1] * r3), filled(pk(o[c][1][2] * r3, o[c][1][3] * r3))};
        ST16(rs, ob, lane * 16, WS_O + ((tile * 8 + 2 * w2 + c) * 16 + head) * 1024);
      }
      STAMP(12);
      if (WOE == 3) lds_wait(L, S_WO);
      *(volatile LAS u32*)(L + L_FLG + S_W2A * 16 + w2 * 4) = (u32)__builtin_amdgcn_s_memrealtime();
      lds_signal(L, S_W2B, w2);
      __builtin_amdgcn_s_setprio(0);
      if (TD == 3 && w2 == 0) DTOUCH();
    }
  }
  STAMP(13);
  u32x4 hres = {0u, 0u, 0u, 0u};
  const int xo = tok * 896 + cg * 128 + kh * 64 + g * 16;
  STAMP(23);
  if (r2 < 14) {
    u32x4 xr, of[8];
    asm volatile("global_load_dwordx4 %0, %1, %2" : "=v"(xr) : "v"(xo), "s"(x) : "memory");
    lds_wait2(L, S_W2B, S_WOFA);
#define WIN2() do { if (A ? W1 < 2 : W1 < 3) GUBLK(A ? 0 : 1); } while (0)
    if (W2F == 1 || W2F == 3) WIN2();
    if (W2F == 4 && !A) { GUBLK(0); GUBLK(1); }
    if (W2F == 5 && !A) GUBLK(1);
    if (T2) {
      const u32x4 ts = *(volatile LAS u32x4*)(L + L_FLG + S_W2A * 16);
      const u32 t0 = __builtin_amdgcn_readfirstlane(ts.x), due = t0 + T2 + __builtin_amdgcn_readfirstlane(max(max(0, (int)(ts.y - ts.x)), max((int)(ts.z - ts.x), (int)(ts.w - ts.x))));
#pragma nounroll
      for (u32 sp = 0; sp < 64 && (int)((u32)__builtin_amdgcn_s_memrealtime() - due) < 0; ++sp) __builtin_amdgcn_s_sleep(1);
    } else __builtin_amdgcn_s_sleep(A ? D2A : D2B);
    STAMP(14);
#define OFA(c_) _Pragma("unroll") for (int i = 0; i < 8; ++i) \
      asm volatile("global_load_dwordx4 %0, %1, %2 offset:%3 sc1" : c_(of[i]) : "v"(lane * 16), "s"(ws + WS_O + ((tile2 * 8 + tg * 4 + tb) * 16 + kh * 8 + 4) * 1024), "n"(i * 1024 - 4096) : "memory")
#define OFW(k_) asm volatile("s_waitcnt vmcnt(%9)" : "+v"(of[0]), "+v"(of[1]), "+v"(of[2]), "+v"(of[3]), "+v"(of[4]), "+v"(of[5]), "+v"(of[6]), "+v"(of[7]), "+v"(xr) : "n"(k_) : "memory")
#define OFBAD() (!__all(max(max(max(of[0].w, of[1].w), max(of[2].w, of[3].w)), max(max(of[4].w, of[5].w), max(of[6].w, of[7].w))) != EMPTY))
    OFA("=v");
    f32x4 a[4] = {};
    u32 mx = 0;
#pragma unroll
    for (int i = 0; i < 8; ++i) {
      asm volatile("s_waitcnt vmcnt(%1)" : "+v"(of[i]) : "n"(7 - i) : "memory");
      if (i == PG - 1) { lds_wait(L, S_WOFB); }
      if (i == 7) GUBLK(A ? 0 : 3);
#pragma unroll
      for (int k = 0; k < 4; ++k) a[k] = mfma16(wof[i][k], of[i], a[k]);
      mx = max(mx, of[i].w);
      SCHED();
    }
    asm volatile("" : "+v"(xr));
    if (__builtin_expect(!__all(mx != EMPTY), 0)) {
#pragma nounroll
      for (u32 sp = 0; sp < SPIN_CAP; ++sp) { __builtin_amdgcn_s_sleep(RSL); OFA("+v"); OFW(0); if (!OFBAD()) break; }
#pragma unroll
      for (int i = 0; i < 8; ++i)
#pragma unroll
        for (int k = 0; k < 4; ++k) a[k] = mfma16(wof[i][k], of[i], i ? a[k] : z4);
    }
    STAMP(25);
    *(LAS f32x4*)(L + L_EX + wave * 2048 + lane * 16) = kh ? a[0] : a[2];
    *(LAS f32x4*)(L + L_EX + wave * 2048 + 1024 + lane * 16) = kh ? a[1] : a[3];
    lds_signal(L, A ? S_EXA : S_EXB, w2);
    lds_wait1(L, A ? S_EXB : S_EXA, w2);
    STAMP(15);
    const f32x4 c0 = (kh ? a[2] : a[0]) + *(LAS f32x4*)(L + L_EX + (wave ^ 4) * 2048 + lane * 16);
    const f32x4 c1 = (kh ? a[3] : a[1]) + *(LAS f32x4*)(L + L_EX + (wave ^ 4) * 2048 + 1024 + lane * 16);
    const f32x4 h0 = c0 + f32x4{blo(xr.x), bhi(xr.x), blo(xr.y), bhi(xr.y)}, h1 = c1 + f32x4{blo(xr.z), bhi(xr.z), blo(xr.w), bhi(xr.w)};
    hres = u32x4{pk(h0[0], h0[1]), pk(h0[2], h0[3]), pk(h1[0], h1[1]), filled(pk(h1[2], h1[3]))};
    ST16(rs, hres, hvo, kh * 2048);
    const f32x4 r0 = {blo(hres.x), bhi(hres.x), blo(hres.y), bhi(hres.y)}, r1 = {blo(hres.z), bhi(hres.z), blo(hres.w), bhi(hres.w)};
    const f32x4 a1 = r0 + r1, a2q = r0 * r0 + r1 * r1;
    float s1 = (a1[0] + a1[1]) + (a1[2] + a1[3]), s2 = (a2q[0] + a2q[1]) + (a2q[2] + a2q[3]);
    { const float t = swadd16(s1, s2); const f32x2 a2 = sw32(t); s1 = a2.x + a2.y; }
    { const u32x2 sv = __builtin_bit_cast(u32x2, sw16(s1)); __builtin_amdgcn_raw_buffer_store_b64(u32x2{sv.x, filled(sv.y)}, rs, fvo, sto + kh * 16384, 16); }
    WAITVM(2);
  } else {
    if (TD == 1) dma4(wd + ((tile2 * 2 + r2 - 14) * 8 + wave) * 49 * 128, min(lane, 48) * 128, L_JUNK);
    lds_wait2(L, S_W2B, S_WOFA);
    if (CF) { HH_CLEAR(A ? NE : 0, 4 * NCL); }
    WIN2();
    if (W2F >= 3) { lds_wait(L, S_WOFB); if (W2F < 5 || A) GUBLK(A ? 3 : 4); }
    if (!CF) { HH_CLEAR(A ? NE : 0, 4 * NCL); }
    DRAIN();
    if (!CE2) HH_FLAG();
    if (CE2 && A) __builtin_amdgcn_s_sleep(DLY5);
  }
  if constexpr (A && CSL) CS_OUT(2);
  lds_signal(L, A ? S_HFA : S_HFB, w2);
  STAMP(16);
  u32x4 wdf[NWD][8];
  const __amdgpu_buffer_rsrc_t rd = __builtin_amdgcn_make_buffer_rsrc((void*)(wd + w2 * 448 * 896 + min(cg, 6) * 128 - 1024), 0, 448 * 896 + 1024, 0x00020000);
  const u32 dvo = (lane >> 3) * 8 * 896 + (((lane & 7) ^ ((lane >> 2) & 4)) * 16);
  u32 da_ = 2 * SLOT + w2 * 7168 + hh * 512 + q4 * 128 + (g & 1) * 32 + p4 * 8 + 64 * (q4 >> 1), db_ = da_ ^ 64, dc_ = da_ + (L_EX - 2 * SLOT - w2 * 6144), dd_ = dc_ ^ 64;
  const u32 po = WS_FHH + ((tile2 * 16 + 4 * w2 + ((lane >> 1) & 3)) * 4 + 2 * tg + (lane & 1)) * 128;
  const u32 ho = WS_HH + ((tile2 * 2 + tg) * 4 + w2) * 57344;
#define WDLOAD(k, bf) dma_wd<k>(rd, dvo, ((bf) + 2) * SLOT + w2 * 7168, L_EX + (bf) * 4096 + w2 * 1024)
#define WDREAD(dst, bf) do { asm volatile("" : "+v"(da_), "+v"(db_), "+v"(dc_), "+v"(dd_)); \
    _Pragma("unroll") for (int i = 0; i < 8; ++i) { LAS char* p_ = L + (i & 1 ? db_ : da_) + (bf) * SLOT + (i >> 1) * 2048; \
      const u32x2 x_ = trread(p_), y_ = trread(i < 6 ? p_ + 1024 : L + (i & 1 ? dd_ : dc_) + (bf) * 4096); dst[i] = u32x4{x_.x, x_.y, y_.x, y_.y}; } } while (0)
#define THR() asm volatile("s_waitcnt vmcnt(%0)" :: "n"(THRN) : "memory")
#define THR0() asm volatile("s_waitcnt vmcnt(%0)" :: "n"(THRN0) : "memory")
  u32 have = 0;
#define PLOAD() peek(ws, po, lane < 8, tag + 7)
#define PMIN(pv) do { const u64 d = (pv) - tag; have = max(have, min8(d <= 8 ? (u32)d : 0u)); } while (0)
#define NEEDP(n) do { _Pragma("nounroll") for (u32 sp = 0; have < (n) && sp < SPIN_CAP; ++sp) { PMIN(PLOAD()); if (PSLP && have < (n)) __builtin_amdgcn_s_sleep(PSLP); } \
    __builtin_amdgcn_fence(__ATOMIC_ACQUIRE, "wavefront"); } while (0)
#define HBASE() u32 hb3 = w2 * 7168 + lane * 16, hb0 = hb3 + 2 * SLOT, hb2 = hb0 + 2 * SLOT, hb7 = L_EX + w2 * 1024 + lane * 16; asm volatile("" : "+v"(hb0), "+v"(hb2), "+v"(hb7))
#define HFRAG(bf, i) (*(LAS u32x4*)(L + ((i) < 7 ? ((bf) == 3 ? hb3 : (bf) == 2 ? hb2 : hb0 + (bf) * SLOT) + (i) * 1024 : hb7 + (bf) * 4096)))
#define HQLOAD(q, j) _Pragma("unroll") for (int i = 0; i < 8; ++i) hq[q][i] = __builtin_bit_cast(u32x4, __builtin_amdgcn_raw_buffer_load_b128(rs, lane * 16 + (i & 3) * 1024, ho + (j) * 8192 + (i & 4) * 1024, 16))
#define HQLOADT(q, j) _Pragma("unroll") for (int i = 0; i < 8; ++i) { THR(); hq[q][i] = __builtin_bit_cast(u32x4, __builtin_amdgcn_raw_buffer_load_b128(rs, lane * 16 + (i & 3) * 1024, ho + (j) * 8192 + (i & 4) * 1024, 16)); }
#define DOWN16(ac, wf, hf_) _Pragma("unroll") for (int m = 0; m < 16; ++m) ac[(m >> 1) & 1][m & 1] = mfma32(wf[2 * (m >> 2) + (m & 1)], hf_[2 * (m >> 2) + ((m >> 1) & 1)], ac[(m >> 1) & 1][m & 1])
#define PARTIAL_OUT(ac, b0, b1) _Pragma("unroll") for (int n = 0; n < 16; ++n) { const int T = n >> 3, k = (n >> 2) & 1, q = n & 3; \
    *(LAS f32x4*)&HFRAG(T ? b1 : b0, n & 7) = f32x4{ac[T][k][4 * q], ac[T][k][4 * q + 1], ac[T][k][4 * q + 2], ac[T][k][4 * q + 3]}; }
#define DOWN_OUT(ac, wf, hf_, b0, b1) _Pragma("unroll") for (int m = 0; m < 16 + OLAG; ++m) { \
    if (m < 16) ac[m >> 3][(m >> 2) & 1] = mfma32(wf[2 * (m & 3) + ((m >> 2) & 1)], hf_[2 * (m & 3) + (m >> 3)], ac[m >> 3][(m >> 2) & 1]); \
    SCHED(); \
    if (m >= OLAG) { const int n = m - OLAG, T = n >> 3, k = (n >> 2) & 1, q = n & 3; \
      *(LAS f32x4*)&HFRAG(T ? b1 : b0, n & 7) = f32x4{ac[T][k][4 * q], ac[T][k][4 * q + 1], ac[T][k][4 * q + 2], ac[T][k][4 * q + 3]}; } \
    SCHED(); }
#define FREE(f) do { _Pragma("nounroll") for (u32 sp = 0; sp < (SPIN_CAP << 4); ++sp) { const u32x4 fr = *(volatile LAS u32x4*)(L + L_FLG + S_DONE4 * 16); \
          if (__builtin_amdgcn_readfirstlane(min(min(fr.x, fr.y), min(fr.z, fr.w))) >= (f)) break; __builtin_amdgcn_s_sleep(BSL); } asm volatile("" ::: "memory"); } while (0)
#define HQBAD(q) (!__all(max(max(max(hq[q][0].w, hq[q][1].w), max(hq[q][2].w, hq[q][3].w)), max(max(hq[q][4].w, hq[q][5].w), max(hq[q][6].w, hq[q][7].w))) != EMPTY))
#define HQCHECK(q, j) do { if (__builtin_expect(HQBAD(q), 0)) { _Pragma("nounroll") for (u32 sp = 0; sp < SPIN_CAP; ++sp) { \
      __builtin_amdgcn_s_sleep(RSL); asm volatile("" ::: "memory"); HQLOAD(q, j); if (!HQBAD(q)) break; } } SCHED(); } while (0)
  if constexpr (A) {
    const int T32 = tile * 4 + wave;
    u32x2 st[7];
    u32x4 hb[28];
    u64 fcl;
    const u32 stv = (7 * hh * 2048 + (lane & 31)) * 8;
#define STA(c_) _Pragma("unroll") for (int i = 0; i < 7; ++i) asm volatile("global_load_dwordx2 %0, %1, %2 sc1" : c_(st[i]) : "v"(stv), "s"(ws + WS_ST + (i * 2048 + T32 * 32) * 8) : "memory")
#define STW(k_) asm volatile("s_waitcnt vmcnt(%7)" : "+v"(st[0]), "+v"(st[1]), "+v"(st[2]), "+v"(st[3]), "+v"(st[4]), "+v"(st[5]), "+v"(st[6]) : "n"(k_) : "memory")
#define STBAD() (!__all(max(max(max(st[0].y, st[1].y), max(st[2].y, st[3].y)), max(max(st[4].y, st[5].y), st[6].y)) != EMPTY))
#define HBA(c_, q) _Pragma("unroll") for (int i = 0; i < 4; ++i) asm volatile("global_load_dwordx4 %0, %1, %2 offset:%3 sc1" : c_(hb[4 * (q) + i]) : "v"(lane * 16), "s"(ws + WS_H + (T32 * 28 + 4 * (q)) * 1024), "n"(i * 1024) : "memory")
    lds_wait2(L, S_HFA, S_HFB);
    if (CLW == 1) {
#pragma unroll
      for (int i = NCL; i < 7; ++i) ST16(rs, empty4, lane * 16, hho + i * 8192);
    }
    __builtin_amdgcn_s_sleep(DLY3);
    if (!SPEC || r2 >= 14) {
#pragma nounroll
      for (u32 sp = 0; sp < SPIN_CAP; ++sp) { STA("=v"); STW(0); if (!STBAD()) break; __builtin_amdgcn_s_sleep(RSL); }
    }
    STA("=v");
    STAMP(17);
#pragma unroll
    for (int q = 0; q < HLA / 4; ++q) { HBA("=v", q); }
    u32 wb_[3] = {(u32)lane * 16, (u32)lane * 16 + 2 * SLOT, (u32)lane * 16 + 4 * SLOT};
    asm volatile("" : "+v"(wb_[1]), "+v"(wb_[2]));
#define WFRAG(sl, s) (*(LAS u32x4*)(L + wb_[(sl) >> 1] + ((sl) & 1) * SLOT + (s) * 1024))
    LAS char* csl = L + L_CS + hh * 16;
    u32x4 ring[7];
    f32x4 cq[4];
    float nmu, ve, k0, k1;
    u32 mx;
    const int pvo = WS_FHH + ((tile * 16 + r) * 4 + wave) * 128;
#define PROG(n) FLAG(pvo + 131072, n)
    f32x16 acc[3];
    float y[8], gu[8];
    u32x4 yb;
    u32x4 cw;
#define LT(i)
#define CQ(j_, k_) do { const f32x4 c_ = *(LAS f32x4*)(csl + (j_) * 128 + (k_) * 32); _Pragma("unroll") for (int i_ = 0; i_ < 4; ++i_) acc[(j_) % 3][4 * (k_) + i_] = c_[i_]; } while (0)
#define HB1(i_) asm volatile("global_load_dwordx4 %0, %1, %2 offset:%3 sc1" : "=v"(hb[i_]) : "v"(lane * 16), "s"(ws + WS_H + (T32 * 28 + ((i_) & ~3)) * 1024), "n"(((i_) & 3) * 1024) : "memory")
#define FCL() asm volatile("global_load_dwordx2 %0, %1, %2 sc1" : "=v"(fcl) : "v"(p4 * 128), "s"(ws + WS_FH + (T32 * 32 + 28) * 128) : "memory")
    auto gu01 = [&](auto LC) LAM {
      constexpr bool LD = LC.value;
      sfor<0, 56>([&](auto NC) LAM {
        constexpr int n = NC.value, s = n >> 1, m = n + 7;
        if constexpr (LD && !(n & 1)) { u32x4 t_ = hb[s]; asm volatile("s_waitcnt vmcnt(%1)" : "+v"(t_) : "n"((HLA + s < 29 ? HLA + s : 29) - s - 1 + (CLW == 2 && n > CN ? (n - CN < 7 - NCL ? n - CN : 7 - NCL) : 0)) : "memory"); hb[s] = t_; }
        acc[n & 1] = mfma32(ring[n % 7], hb[s], acc[n & 1]);
        SCHED();
        if constexpr (LD && (n & 1) && HLA + s < 28) HB1(HLA + s);
        if constexpr (LD && (n & 1) && HLA + s == 28) FCL();
        if constexpr (CLW == 2 && LD && n >= CN && n < CN + 7 - NCL) ST16(rs, empty4, lane * 16, hho + (n - CN + NCL) * 8192);
        if constexpr (n & 1) mx = max(mx, hb[s].w);
        if constexpr (m < 56) ring[n % 7] = WFRAG(m & 1, m >> 1); else ring[n % 7] = WFRAG(2, m - 56);
        if constexpr (n >= 34 && n < 38) CQ(2, n - 34);
        if constexpr (n >= 44 && n < 52) { acc[2][2 * (n - 44)] *= nmu; acc[2][2 * (n - 44) + 1] *= nmu; PIN(acc[2]); }
        SCHED();
      });
      STAMP(18);
    };
    auto gu_block = [&](auto JC) __attribute__((always_inline)) {
      constexpr int j = JC.value, NE_ = j == 2 ? 16 : 8;
      sfor<0, 28>([&](auto SC) LAM {
        constexpr int s = SC.value;
        if constexpr (PACE > 0 && j >= PJ0 && j <= PJ1 && s % PS == 0) __builtin_amdgcn_s_sleep(PACE);
        if constexpr (HW && s % 2 == HW - 1 && (s < 27 || j < 6)) { asm volatile("" :: "v"(ring[(s + 1) % 7])); SCHED(); }
        acc[j % 3] = mfma32(ring[s % 7], hb[s], acc[j % 3]);
        constexpr int sg = j == 3 ? S_WIN3 : j == 4 ? S_REFILL : S_FREE01;
        if constexpr (j >= 3 && j < 6 && s == 12) cw = *(volatile LAS u32x4*)(L + L_FLG + sg * 16);
        if constexpr (j >= 3 && j < 6 && s == 20) { if (__builtin_expect(!__builtin_amdgcn_readfirstlane(cw.x & cw.y & cw.z & cw.w), 0)) lds_wait(L, sg); asm volatile("" ::: "memory"); }
        if constexpr (!CLW && j == 2 && s < 7 - NCL) ST16(rs, (u32x4{EMPTY, EMPTY, EMPTY, EMPTY}), lane * 16, hho + (NCL + s) * 8192);
        if constexpr (s + 7 < 28) ring[s % 7] = WFRAG(gu_slot(j), s + 7);
        else if constexpr (j < 6) ring[s % 7] = WFRAG(gu_slot(j + 1), s - 21);
        if constexpr (s == 0 && j == 2) lds_signal(L, S_WSEEN, wave);
        if constexpr (s == 0 && j >= 3) *(volatile LAS u32*)(L + L_FLG + S_DONE4 * 16 + wave * 4) = j - 2u;
        sfor<0, NE_>([&](auto EC) LAM {
          constexpr int e = EC.value, i = e & 7, k = j == 2 ? s - e - e / 4 : s - 1 - 2 * e, a = j == 2 ? e >> 3 : (j - 1) % 3;
          const float ga = acc[a][i];
          if constexpr (k == 0) y[i] = ga * ga;
          if constexpr (k == 1) y[i] = fmaf(y[i], k1, k0);
          if constexpr (k == 2) y[i] = ga * y[i];
          if constexpr (k == 3) y[i] = fexp2(y[i]);
          if constexpr (k == 4) y[i] = fmaf(y[i], ve, ve);
          if constexpr (k == 5) { gu[i] = ga * acc[a][i + 8]; PIN(gu[i]); }
          if constexpr (k == 6) y[i] = frcp(y[i]);
          if constexpr (k == 7) y[i] = y[i] * gu[i];
          if constexpr (k >= 0 && k < 8 && k != 5) PIN(y[i]);
          if constexpr (j == 2 && k == 8 && (e & 1)) { u32 p_ = pk(y[i - 1], y[i]); if (i == 7) p_ = filled(p_); PIN(p_); yb[i >> 1] = p_; }
          if constexpr (CLW == 2 && j == 2 && k == 8 && e == 7) WAITVM(0);
          if constexpr (j == 2 && k == 8 && i == 7) ST16(rs, yb, lane * 16, hho + a * 8192);
          if constexpr (j == 2 && k == 9 && i == 7 && (a == 0 || !FLG13)) FLAG(pvo, a + 1);
        });
        if constexpr (j > 2) {
          if constexpr (s == 21) { yb[0] = pk(y[0], y[1]); yb[1] = pk(y[2], y[3]); }
          if constexpr (s == 22) yb[2] = pk(y[4], y[5]);
          if constexpr (!CLW && j == 3 && s == 22) { if (FLG13) WAITVM(3); else WAITVM(4); }
          if constexpr (s == 23) { yb[3] = filled(pk(y[6], y[7])); ST16(rs, yb, lane * 16, hho + (j - 1) * 8192); }
          if constexpr (!CLW && s == 24 && (j == 3 || (j == 4 && !FLG13))) FLAG(pvo, j);
        }
        if constexpr (j < 6 && s >= 17 && s < 21) CQ(j + 1, s - 17);
        if constexpr (j < 6 && s >= 22 && s < 27) {
          sfor<0, 16>([&](auto EC) LAM {
            constexpr int e = EC.value;
            if constexpr ((e < 2 ? 22 : e < 4 ? 23 : 23 + (e >> 2)) == s) acc[(j + 1) % 3][e] *= nmu;
          });
          PIN(acc[(j + 1) % 3]);
        }
        SCHED();
      });
      if (j == 3) STAMP(26);
      if (j == 2) STAMP(29);
      if (j == 4) STAMP(30);
      if (j == 5) STAMP(31);
    };
#define HBW(q) asm volatile("s_waitcnt vmcnt(0)" : "+v"(hb[4 * (q)]), "+v"(hb[4 * (q) + 1]), "+v"(hb[4 * (q) + 2]), "+v"(hb[4 * (q) + 3]) :: "memory")
#define HBBAD(q) (!__all(max(max(hb[4 * (q)].w, hb[4 * (q) + 1].w), max(hb[4 * (q) + 2].w, hb[4 * (q) + 3].w)) != EMPTY))
#define HBC(q, c_) asm volatile("s_bitcmp0_b32 %6, %7\n\ts_cbranch_scc1 1f\n\tglobal_load_dwordx4 %0, %4, %5 sc1\n\tglobal_load_dwordx4 %1, %4, %5 offset:1024 sc1\n\t" \
      "global_load_dwordx4 %2, %4, %5 offset:2048 sc1\n\tglobal_load_dwordx4 %3, %4, %5 offset:3072 sc1\n1:" : "+v"(hb[4 * (q)]), "+v"(hb[4 * (q) + 1]), "+v"(hb[4 * (q) + 2]), "+v"(hb[4 * (q) + 3]) \
      : "v"(lane * 16), "s"(ws + WS_H + (T32 * 28 + 4 * (q)) * 1024), "s"(c_), "n"(q) : "memory", "scc")
#define REDO(m_) do { STA("+v"); _Pragma("unroll") for (int q = 0; q < 7; ++q) HBC(q, m_); STW(0); _Pragma("unroll") for (int q = 0; q < 7; ++q) HBW(q); } while (0)
#define FIX() do { _Pragma("nounroll") for (u32 sp = 0; sp < SPIN_CAP; ++sp) { u32 m = 0; \
        _Pragma("unroll") for (int q = 0; q < 7; ++q) m |= HBBAD(q) ? 1u << q : 0u; \
        if (!m && !STBAD()) break; if (sp) __builtin_amdgcn_s_sleep(RSL); m = __builtin_amdgcn_readfirstlane(m); REDO(m); } } while (0)
    auto first = [&](auto LC) LAM {
#pragma unroll
      for (int k = 0; k < 8; ++k) { const f32x4 c_ = *(LAS f32x4*)(csl + k * 32); acc[k >> 2][4 * (k & 3)] = c_[0]; acc[k >> 2][4 * (k & 3) + 1] = c_[1]; acc[k >> 2][4 * (k & 3) + 2] = c_[2]; acc[k >> 2][4 * (k & 3) + 3] = c_[3]; }
#pragma unroll
      for (int s = 0; s < 7; ++s) ring[s] = WFRAG(s & 1, s >> 1);
      f32x2 sf[7];
#pragma unroll
      for (int i = 0; i < 7; ++i) sf[i] = __builtin_bit_cast(f32x2, st[i]);
      float s1 = ((sf[0].x + sf[1].x) + (sf[2].x + sf[3].x)) + ((sf[4].x + sf[5].x) + sf[6].x);
      float s2 = ((sf[0].y + sf[1].y) + (sf[2].y + sf[3].y)) + ((sf[4].y + sf[5].y) + sf[6].y);
      { f32x2 a = sw32(s1); s1 = a.x + a.y; a = sw32(s2); s2 = a.x + a.y; }
      nmu = s1 * (-1.f / 448.f); ve = fmaxf(s2 * (1.f / 448.f) - nmu * nmu, 0.f) + 1e-5f;
      const float rstd = __builtin_amdgcn_rsqf(ve);
      k0 = -2.30220819f * rstd; k1 = -0.10294324f * rstd * rstd * rstd;
#pragma unroll
      for (int i = 0; i < 32; ++i) acc[i >> 4][i & 15] *= nmu;
      mx = 0;
      __builtin_amdgcn_s_setprio(GUPRIO);
      SCHED();
      gu01(LC);
    };
    STW(HLA);
    if (__builtin_expect(STBAD(), 0)) {
#pragma unroll
      for (int q = 0; q < HLA / 4; ++q) HBW(q);
#pragma nounroll
      for (u32 sp = 0; sp < SPIN_CAP; ++sp) {
        u32 m = 0;
#pragma unroll
        for (int i = 0; i < 7; ++i) { const u64 bm = __builtin_amdgcn_ballot_w64(st[i].y == EMPTY); m |= ((u32)bm ? 1u << (i >> 1) : 0u) | ((u32)(bm >> 32) ? 1u << ((7 + i) >> 1) : 0u); }
        m = __builtin_amdgcn_readfirstlane(m);
        if (!m) break;
        if (sp) __builtin_amdgcn_s_sleep(RSL);
        STA("+v");
#pragma unroll
        for (int q = 0; q < HLA / 4; ++q) HBC(q, m);
        STW(0);
#pragma unroll
        for (int q = 0; q < HLA / 4; ++q) HBW(q);
      }
    }
    first(IC<1>{});
    if (__builtin_expect(!__all(mx != EMPTY), 0)) { FIX(); first(IC<0>{}); }
    asm volatile("s_waitcnt vmcnt(%1)" : "+v"(fcl) : "n"(CLW == 2 ? 7 - NCL : 0) : "memory");
    if (__builtin_expect(!__all(fcl == tag + 1), 0)) poll_flags(ws, WS_FH + (T32 * 32 + 28 + p4) * 128, lane < 4, tag);
    gu_block(IC<2>{}); gu_block(IC<3>{}); gu_block(IC<4>{}); gu_block(IC<5>{}); gu_block(IC<6>{});
#pragma unroll
    for (int i = 0; i < 8; ++i) { const float ga = acc[0][i]; y[i] = ga * acc[0][i + 8] * frcp(fmaf(fexp2(ga * fmaf(ga * ga, k1, k0)), ve, ve)); }
    yb = u32x4{pk(y[0], y[1]), pk(y[2], y[3]), pk(y[4], y[5]), filled(pk(y[6], y[7]))};
    ST16(rs, yb, lane * 16, hho + 6 * 8192);
    *(volatile LAS u32*)(L + L_FLG + S_DONE4 * 16 + wave * 4) = 5u;
    __builtin_amdgcn_s_setprio(0);
    STAMP(19);
    if (r2 < 14) {
      f32x16 da[2][2] = {};
      u32x4 hq[2][8], wl[2][8];
      HBASE();
      __builtin_amdgcn_s_setprio(ATP);
      lds_wait1(L, S_SPARE, w2);
      STAMP2(15);
      HQLOAD(0, 4);
      WDREAD(wl[0], 1);
      SCHED();
      if (A6L == 0) { __builtin_amdgcn_s_sleep(ADLY); HQLOAD(1, 6); }
      STAMP2(16);
      HQCHECK(0, 4);
      if (A6L == 1) { __builtin_amdgcn_s_sleep(ADLY); HQLOAD(1, 6); }
      SCHED();
      DOWN16(da, wl[0], hq[0]);
      SCHED();
      if (A6L == 2) { __builtin_amdgcn_s_sleep(ADLY); HQLOAD(1, 6); }
      lds_wait1(L, S_CONS, w2);
      WDREAD(wl[1], 0);
      STAMP2(18);
      HQCHECK(1, 6);
      STAMP2(31);
      DOWN_OUT(da, wl[1], hq[1], 0, 1);
      STAMP(20);
    }
  } else {
    lds_wait2(L, S_HFA, S_HFB);
    if (TD == 6 && w2 == 0) DTOUCH();
    if (TD == 7) dma4(wd + min(b * 49 + w2 * 13, 12544 - 13) * 128, min(lane, 12) * 128, L_JUNK);
    __builtin_amdgcn_s_setprio(BP3);
    if (r2 >= 14) GUBLK(4);
#define REFILL() do { lds_wait<BSL>(L, S_WSEEN); GUBLK(5); GUBLK(6); } while (0)
    if (r2 >= 14) {
      WAITVM(0); lds_signal(L, S_WIN3, w2);
      REFILL();
      WAITVM(0); lds_signal(L, S_REFILL, w2); lds_signal(L, S_FREE01, w2);
    } else {
      u64 pv;
      const char* hs = ws + ho + 4096;
#define PEEK() do { if (AP) { pv = tag + 7; asm volatile("s_mov_b64 exec, 0xff\n\tglobal_atomic_add_x2 %0, %1, %2, %3 sc0\n\ts_mov_b64 exec, -1" : "+v"(pv) : "v"(po), "v"(0ull), "s"(ws) : "memory"); } \
        else asm volatile("global_load_dwordx2 %0, %1, %2 sc1" : "=v"(pv) : "v"(po), "s"(ws) : "memory"); } while (0)
#define PEEKED(k_, n_) do { asm volatile("s_waitcnt vmcnt(%1)" : "+v"(pv) : "n"(k_) : "memory"); PMIN(pv); NEEDP(n_); } while (0)
#define HQA(c_, q, j) _Pragma("unroll") for (int i = 0; i < 8; ++i) asm volatile("global_load_dwordx4 %0, %1, %2 offset:%3 sc1" : c_(hq[q][i]) : "v"(lane * 16), "s"(hs + (j) * 8192), "n"(i * 1024 - 4096) : "memory")
#define HQW(q, k_) asm volatile("s_waitcnt vmcnt(%8)" : "+v"(hq[q][0]), "+v"(hq[q][1]), "+v"(hq[q][2]), "+v"(hq[q][3]), "+v"(hq[q][4]), "+v"(hq[q][5]), "+v"(hq[q][6]), "+v"(hq[q][7]) : "n"(k_) : "memory")
#define HQOK(q, j, k_) do { HQW(q, k_); if (__builtin_expect(HQBAD(q), 0)) { _Pragma("nounroll") for (u32 sp = 0; sp < SPIN_CAP; ++sp) { \
        __builtin_amdgcn_s_sleep(RSL); HQA("+v", q, j); HQW(q, 0); if (!HQBAD(q)) break; } } SCHED(); } while (0)
      {
        u32x2 e[24][2];
        const u32 ea_ = 0, eb_ = 0;
        asm volatile("" : "+v"(da_), "+v"(db_), "+v"(dc_), "+v"(dd_));
#define WBL(N0, N1, NS, SL) sfor<N0, N1>([&](auto NC) LAM { constexpr int n = NC.value, k = n - (N0); \
          if constexpr (k < (NS)) dma_wd1<n, (SL) * SLOT + k * 1024>(rd, dvo, w2 * 7168); else dma_wd1<n, L_EX + (k - (NS)) * 4096>(rd, dvo, w2 * 1024); })
#define WBR(N0, N1, NS, SL) do { sfor<N0, N1>([&](auto NC) LAM { constexpr int n = NC.value, k = n - (N0); \
          if constexpr (k >= (NS)) { e[n][0] = trread(L + dc_ + (k - (NS)) * 4096); e[n][1] = trread(L + dd_ + (k - (NS)) * 4096); } \
          else if constexpr ((SL) == 4) { e[n][0] = trread(L + da_ + 2 * SLOT + k * 1024); e[n][1] = trread(L + db_ + 2 * SLOT + k * 1024); } \
          else { e[n][0] = trread(L + ea_ + k * 1024); e[n][1] = trread(L + eb_ + k * 1024); } }); \
          asm volatile("s_waitcnt lgkmcnt(0)" : "+v"(e[(N1) - 1][1]) :: "memory"); } while (0)
        WBL(0, 11, 7, 4);
        WAITVM(0);
        STAMP2(24);
        WBR(0, 11, 7, 4);
        WBL(11, 15, 0, 0);
        WAITVM(0);
        STAMP2(25);
        WBR(11, 15, 0, 0);
        WBL(15, 19, 0, 0);
        GUBLK(4);
        WAITVM(7);
        WBR(15, 19, 0, 0);
        WBL(19, 23, 0, 0);
        STAMP2(27);
        lds_wait<BSL>(L, S_WSEEN);
        GUBLK(5);
        GUBLK(6);
        WAITVM(14);
        lds_signal(L, S_WIN3, w2);
        STAMP2(26);
        WBR(19, 23, 0, 0);
        dma_wd1<23, L_EX + 3 * 4096>(rd, dvo, w2 * 1024);
        FREE(1);
        PEEK();
        WDLOAD(3, 0);
        WAITVM(17);
        lds_signal(L, S_REFILL, w2);
        STAMP2(5);
        PEEKED(8, 0);
        lds_signal(L, S_FREE01, w2);
        e[23][0] = trread(L + dc_ + 3 * 4096); e[23][1] = trread(L + dd_ + 3 * 4096);
#pragma unroll
        for (int n = 0; n < 24; ++n) wdf[n >> 3][n & 7] = u32x4{e[n & ~1][n & 1].x, e[n & ~1][n & 1].y, e[n | 1][n & 1].x, e[n | 1][n & 1].y};
      }
      STAMP2(23);
      f32x16 acc[2][2];
      u32x4 hq[2][8];
      STAMP2(16);
      HBASE();
#pragma unroll
      for (int k = 0; k < 3; ++k) asm volatile("" : "+v"(wdf[k][0]), "+v"(wdf[k][1]), "+v"(wdf[k][2]), "+v"(wdf[k][3]), "+v"(wdf[k][4]), "+v"(wdf[k][5]), "+v"(wdf[k][6]), "+v"(wdf[k][7]) :: "memory");
      const bool w4e = have < 1;
      if (w4e) { FREE(2); WDLOAD(4, 1); }
      NEEDP(1);
      STAMP2(1);
      HQA("=v", 0, 0);
      if (!w4e && W4O == 0) { FREE(2); WDLOAD(4, 1); }
      STAMP2(2);
      HQA("=v", 1, 1);
      if (W4O == 1) { if (!w4e) { FREE(2); WDLOAD(4, 1); } else WAITVM(8); }
      FREE(3);
      auto round = [&](auto JC) LAM {
        constexpr int J = JC.value, q = J & 1, w = J % 3, nb = 2 - 2 * J;
        __builtin_amdgcn_s_setprio(J < 2 ? DP0 : DNPRIO);
        sfor<0, 18>([&](auto MC) LAM {
          constexpr int m = MC.value, i = m - 8 - (m > 13 ? 2 : 0);
          if constexpr (m < 16) macc<J == 0 && m < 4 ? 0 : 1>(acc[(m >> 1) & 1][m & 1], wdf[w][2 * (m >> 2) + (m & 1)], hq[q][2 * (m >> 2) + ((m >> 1) & 1)]);
          SCHED();
          if constexpr (J < 2 && m < 7) dma_wd1<(J + 5) * 8 + m, (nb + 2) * SLOT + m * 1024>(rd, dvo, w2 * 7168);
          if constexpr (J < 2 && m == 7) dma_wd1<(J + 5) * 8 + 7, L_EX + nb * 4096>(rd, dvo, w2 * 1024);
          if constexpr (!CLW && J == 0 && m == 7) PEEK();
          if constexpr (J == 1 && m == 7) { if (CLW) WAITVM(16); else PEEKED(16, 3); lds_signal(L, S_SPARE, w2); FREE(4); }
          if constexpr (J == 2 && m == 7) { WAITVM(8); lds_signal(L, S_CONS, w2); FREE(5); if (B5D) __builtin_amdgcn_s_sleep(B5D); }
          if constexpr (J < 3 && m >= 8 && m != 14 && m != 15) {
            asm volatile("global_load_dwordx4 %0, %1, %2 offset:%3 sc1" : "=v"(hq[q][i]) : "v"(lane * 16), "s"(hs + (J == 2 ? 5 : J + 2) * 8192), "n"(i * 1024 - 4096) : "memory");
            if constexpr (J < 2) {
              LAS char* p_ = L + (i & 1 ? db_ : da_) + 2 * J * SLOT + (i >> 1) * 2048;
              const u32x2 x_ = trread(p_), y_ = trread(i < 6 ? p_ + 1024 : L + (i & 1 ? dd_ : dc_) + 2 * J * 4096);
              wdf[J][i] = u32x4{x_.x, x_.y, y_.x, y_.y};
            }
          }
          SCHED();
        });
        if (J < 2) __builtin_amdgcn_s_setprio(BP3);
      };
      HQOK(0, 0, W4O ? 16 : 8);
      round(IC<0>{});
      STAMP2(8);
      HQOK(1, 1, CLW ? 16 : 17);
      asm volatile("s_waitcnt lgkmcnt(0)" : "+v"(wdf[0][7]) :: "memory");
      round(IC<1>{});
      STAMP2(9);
      HQOK(0, 2, 16);
      round(IC<2>{});
      STAMP2(10);
      HQOK(1, 3, 8);
      round(IC<3>{});
      STAMP2(11);
      HQOK(0, 5, 0);
      STAMP2(4);
      DOWN_OUT(acc, wdf[1], hq[0], 2, 3);
      STAMP(20);
    }
  }
  if (r2 < 14) {
    BAR();
    STAMP2(7);
    float c[8] = {blo(hres.x), bhi(hres.x), blo(hres.y), bhi(hres.y), blo(hres.z), bhi(hres.z), blo(hres.w), bhi(hres.w)};
    const int rl = ((tb & 1) * 16 + l15) * 16;
    f32x4 v0 = {c[0], c[1], c[2], c[3]}, v1 = {c[4], c[5], c[6], c[7]};
#pragma unroll
    for (int h = 0; h < 2; ++h) {
      const int bf = (tb >> 1) + 2 * h;
      const bool p7 = kh && g == 3;
      u32 rq = (p7 ? L_EX + bf * 4096 : (bf == 3 ? 0 : (bf + 2) * SLOT) + (4 * kh + g) * 1024) + rl;
      const u32 st7 = p7 ? 1024 : 7168;
      asm volatile("" : "+v"(rq));
#pragma unroll
      for (int w = 0; w < 4; ++w) { v0 += *(LAS f32x4*)(L + rq + w * st7); v1 += *(LAS f32x4*)(L + rq + w * st7 + 512); }
    }
#pragma unroll
    for (int i = 0; i < 4; ++i) { c[i] = v0[i]; c[4 + i] = v1[i]; }
    { const u32x4 ov = {pk(c[0], c[1]), pk(c[2], c[3]), pk(c[4], c[5]), pk(c[6], c[7])}; asm volatile("global_store_dwordx4 %0, %1, %2 sc1\n\ts_nop 1" :: "v"(xo), "v"(ov), "s"(out) : "memory"); }
    if (A && b == 0 && tid == 0) __hip_atomic_store((GAS u64*)(ws + WS_E), tag, RLX_AGENT);
  }
  STAMP(21);
  };
  if (wave < 4) role(IC<1>{}); else role(IC<0>{});
}

extern "C" void kernel_launch(void* const* d_in, const int* in_sizes, int n_in, void* d_out, int out_size, void* d_ws, size_t ws_size, hipStream_t stream) {
  (void)hipFuncSetAttribute((const void*)fused_block, hipFuncAttributeMaxDynamicSharedMemorySize, LDS_BYTES);
  fused_block<<<256, 512, LDS_BYTES, stream>>>((const char*)d_in[0], (const char*)d_in[1], (const char*)d_in[2], (const char*)d_in[3], (const char*)d_in[5], (const char*)d_in[6],
                                               (const char*)d_in[7], (char*)d_out, (char*)d_ws);
}
```

```cpp
#include <hip/hip_runtime.h>
#include <hip/hip_bf16.h>
#include <stdint.h>

typedef unsigned u32;
typedef unsigned long long u64;
typedef u32 u32x4 __attribute__((ext_vector_type(4)));
typedef u32 u32x2 __attribute__((ext_vector_type(2)));
typedef float f32x2 __attribute__((ext_vector_type(2)));
typedef float f32x4 __attribute__((ext_vector_type(4)));
typedef float f32x16 __attribute__((ext_vector_type(16)));
typedef short s16x4 __attribute__((ext_vector_type(4)));
typedef __bf16 bf16x2 __attribute__((ext_vector_type(2)));
typedef __bf16 bf16x8 __attribute__((ext_vector_type(8)));
#define LAS __attribute__((address_space(3)))
#define GAS __attribute__((address_space(1)))
#define DI __device__ __forceinline__
#define RLX_AGENT __ATOMIC_RELAXED, __HIP_MEMORY_SCOPE_AGENT

constexpr int WS_E = 0;
constexpr int WS_FKV = 8192;
constexpr int WS_FO = WS_FKV + 131072;
constexpr int WS_FH = WS_FO + 131072;
constexpr int WS_FG = WS_FH + 262144;
constexpr int WS_FD = WS_FG + 114688;
constexpr int WS_FHH = WS_FD + 57344;
constexpr int WS_CS = WS_FHH + 262144;
constexpr int WS_ST = WS_CS + 200704;
constexpr int WS_WGU = WS_ST + 229376;
constexpr int WS_WD = WS_WGU + 3211264;
constexpr int WS_KVP = WS_WD + 1605632;
constexpr int WS_O = WS_KVP + 1048576;
constexpr int WS_H = WS_O + 2097152;
constexpr int WS_HH = WS_H + 1835008;
#define UNITX4
#define CS7
#define NOWD
#define WDOFF(cg, fb, bl) (((((cg) * 4 + (fb) / 28) * 7 + (fb) % 7) * 4 + (fb) / 7 % 4) * 2 + (bl))
#define HHOFF(T32, fb) (((((T32) >> 1) * 4 + (fb) / 28) * 7 + (fb) % 7) * 8 + ((fb) / 7 & 3) * 2 + ((T32) & 1))
constexpr int WS_STAMP = WS_HH + 7340032;
constexpr int WS_LOG = WS_STAMP + 256 * 8 * 32 * 8;
constexpr int WS_USED = WS_LOG + 4096;
constexpr int SLOT = 28672;
constexpr int L_WK = 4 * SLOT, L_WV = 0, L_WQ = 14336;
constexpr int L_PKV = 2 * SLOT;
constexpr int L_WO = 3 * SLOT;
constexpr int L_EX = 5 * SLOT;
constexpr int L_KVA = 5 * SLOT;
constexpr int L_ST1 = 5 * SLOT + 4096;
constexpr int L_WPHI = 5 * SLOT + 5120;
constexpr int L_CSP = 5 * SLOT + 7168;
constexpr int L_CS = 159744;
constexpr int L_FLG = 160768;
constexpr int L_JUNK = 161280;
constexpr int L_STAMP = 161536;
constexpr int L_RED = SLOT;
constexpr int LDS_BYTES = 163840;
constexpr int gu_slot(int j) { return j % 5; }
constexpr int CODE_PF = 48384;
constexpr u32 SPIN_CAP = 1u << 16;
#define LL 1
#define AP 1

#define STAMP(i) do {} while (0)
#define STAMP2(i) do {} while (0)
#define STAMP2X(i) do {} while (0)

DI f32x4 mfma16(u32x4 a, u32x4 b, f32x4 c) { return __builtin_amdgcn_mfma_f32_16x16x32_bf16(__builtin_bit_cast(bf16x8, a), __builtin_bit_cast(bf16x8, b), c, 0, 0, 0); }
DI f32x16 mfma32(u32x4 a, u32x4 b, f32x16 c) { return __builtin_amdgcn_mfma_f32_32x32x16_bf16(__builtin_bit_cast(bf16x8, a), __builtin_bit_cast(bf16x8, b), c, 0, 0, 0); }
DI u32 pk(float lo, float hi) { f32x2 p = {lo, hi}; return __builtin_bit_cast(u32, __builtin_convertvector(p, bf16x2)); }
DI float blo(u32 v) { return __builtin_bit_cast(float, v << 16); }
DI float bhi(u32 v) { return __builtin_bit_cast(float, v & 0xffff0000u); }
DI float dot2(u32 a, u32 b, float c) { return __builtin_amdgcn_fdot2_f32_bf16(__builtin_bit_cast(bf16x2, a), __builtin_bit_cast(bf16x2, b), c, false); }
DI u32x2 trread(LAS char* p) { return __builtin_bit_cast(u32x2, __builtin_amdgcn_ds_read_tr16_b64_v4i16((LAS s16x4*)p)); }
DI u32x4 trfrag(LAS char* p, int step) { u32x2 a = trread(p), b = trread(p + step); return u32x4{a.x, a.y, b.x, b.y}; }
DI float fexp2(float x) { return __builtin_amdgcn_exp2f(x); }
DI float frcp(float x) { return __builtin_amdgcn_rcpf(x); }
template <int C> DI float dppror(float x) { return __builtin_bit_cast(float, __builtin_amdgcn_update_dpp(0, __builtin_bit_cast(int, x), 0x120 + C, 0xf, 0xf, false)); }
DI float row_max(float x) { x = fmaxf(x, dppror<8>(x)); x = fmaxf(x, dppror<4>(x)); x = fmaxf(x, dppror<2>(x)); return fmaxf(x, dppror<1>(x)); }
DI float row_min(float x) { x = fminf(x, dppror<8>(x)); x = fminf(x, dppror<4>(x)); x = fminf(x, dppror<2>(x)); return fminf(x, dppror<1>(x)); }
DI float row_sum(float x) { x += dppror<8>(x); x += dppror<4>(x); x += dppror<2>(x); return x + dppror<1>(x); }
DI f32x2 sw16(float x) { u32 u = __builtin_bit_cast(u32, x); auto r = __builtin_amdgcn_permlane16_swap(u, u, false, false); return f32x2{__builtin_bit_cast(float, (u32)r[0]), __builtin_bit_cast(float, (u32)r[1])}; }
DI f32x2 sw32(float x) { u32 u = __builtin_bit_cast(u32, x); auto r = __builtin_amdgcn_permlane32_swap(u, u, false, false); return f32x2{__builtin_bit_cast(float, (u32)r[0]), __builtin_bit_cast(float, (u32)r[1])}; }
DI float grp_sum(float x) { f32x2 a = sw16(x); x = a.x + a.y; a = sw32(x); return a.x + a.y; }
DI float grp_max(float x) { f32x2 a = sw16(x); x = fmaxf(a.x, a.y); a = sw32(x); return fmaxf(a.x, a.y); }
DI float grp_min(float x) { f32x2 a = sw16(x); x = fminf(a.x, a.y); a = sw32(x); return fminf(a.x, a.y); }

#define BAR() asm volatile("s_waitcnt lgkmcnt(0)\n\ts_barrier" ::: "memory")
#define WAITVM(n) asm volatile("s_waitcnt vmcnt(" #n ")" ::: "memory")
#define DRAIN() do { asm volatile("s_waitcnt vmcnt(0)" ::: "memory"); __builtin_amdgcn_s_waitcnt(0x0F70); } while (0)
#define SCHED() __builtin_amdgcn_sched_barrier(0)
DI void dma16(const void* base, u32 voff, u32 lds) {
  u32 keep;
  asm volatile("s_mov_b32 %0, m0\n\ts_mov_b32 m0, %3\n\ts_nop 0\n\tglobal_load_lds_dwordx4 %1, %2\n\ts_mov_b32 m0, %0" : "=&s"(keep) : "v"(voff), "s"(base), "s"(lds) : "memory");
}
DI void dma16c(const void* base, u32 voff, u32 lds) {
  u32 keep;
  asm volatile("s_mov_b32 %0, m0\n\ts_mov_b32 m0, %3\n\ts_nop 0\n\tglobal_load_lds_dwordx4 %1, %2 sc1\n\ts_mov_b32 m0, %0" : "=&s"(keep) : "v"(voff), "s"(base), "s"(lds) : "memory");
}
DI void dma4(const void* base, u32 voff, u32 lds) {
  u32 keep;
  asm volatile("s_mov_b32 %0, m0\n\ts_mov_b32 m0, %3\n\ts_nop 0\n\tglobal_load_lds_dword %1, %2\n\ts_mov_b32 m0, %0" : "=&s"(keep) : "v"(voff), "s"(base), "s"(lds) : "memory");
}
DI void dma16c_if(u32 go, const void* base, u32 voff, u32 lds) {
  u32 keep;
  asm volatile("s_cmp_eq_u32 %4, 0\n\ts_cbranch_scc1 1f\n\ts_mov_b32 %0, m0\n\ts_mov_b32 m0, %3\n\ts_nop 0\n\tglobal_load_lds_dwordx4 %1, %2 sc1\n\ts_mov_b32 m0, %0\n1:"
               : "=&s"(keep) : "v"(voff), "s"(base), "s"(lds), "s"(go) : "memory", "scc");
}
template <int SI, int LI> DI void dma7(__amdgpu_buffer_rsrc_t rs, u32 voff, u32 so, u32 lds) {
  u32 t;
  asm volatile("s_add_u32 m0, %4, %6\n\ts_add_u32 %0, %3, %5\n\tbuffer_load_dwordx4 %1, %2, %0 offen sc1 lds\n\tbuffer_load_dwordx4 %1, %2, %0 offen offset:1024 sc1 lds\n\t"
               "buffer_load_dwordx4 %1, %2, %0 offen offset:2048 sc1 lds\n\tbuffer_load_dwordx4 %1, %2, %0 offen offset:3072 sc1 lds\n\t"
               "s_add_u32 m0, m0, 4096\n\ts_add_u32 %0, %0, 4096\n\tbuffer_load_dwordx4 %1, %2, %0 offen sc1 lds\n\tbuffer_load_dwordx4 %1, %2, %0 offen offset:1024 sc1 lds\n\t"
               "buffer_load_dwordx4 %1, %2, %0 offen offset:2048 sc1 lds"
               : "=&s"(t) : "v"(voff), "s"(rs), "s"(so), "s"(lds), "n"(SI), "n"(LI) : "memory", "scc");
}
constexpr int wrow(int q, int j, int hf) { return (((7 * q + j) >> 2) * 64 + 2 * ((7 * q + j) & 3) + hf) * 896 + 1024; }
#define DL_(n, o) "s_mov_b32 %0, %" #n "\n\tbuffer_load_dwordx4 %1, %2, %0 offen offset:" #o " lds\n\t"
template <int J> DI void dma_wd(__amdgpu_buffer_rsrc_t rs, u32 voff, u32 lds, u32 lds7) {
  u32 t;
  asm volatile("s_mov_b32 m0, %3\n\t" DL_(5, 0) DL_(6, 1024) DL_(7, 2048) DL_(8, 3072) "s_add_u32 m0, m0, 4096\n\t" DL_(9, 0) DL_(10, 1024) DL_(11, 2048) "s_mov_b32 m0, %4\n\t" DL_(12, 0)
               : "=&s"(t) : "v"(voff), "s"(rs), "s"(lds), "s"(lds7), "n"(wrow(0, J, 0)), "n"(wrow(0, J, 1) - 1024), "n"(wrow(1, J, 0) - 2048), "n"(wrow(1, J, 1) - 3072),
                 "n"(wrow(2, J, 0)), "n"(wrow(2, J, 1) - 1024), "n"(wrow(3, J, 0) - 2048), "n"(wrow(3, J, 1)) : "memory", "scc");
}
template <int N, int LO> DI void dma_wd1(__amdgpu_buffer_rsrc_t rs, u32 voff, u32 lds) {
  u32 t;
  asm volatile("s_add_u32 m0, %3, %4\n\t" DL_(5, 0) : "=&s"(t) : "v"(voff), "s"(rs), "s"(lds), "n"(LO), "n"(wrow(N / 2 % 4, N / 8, N % 2)) : "memory", "scc");
}
DI void touch(const void* base, u32 voff) {
  asm volatile("s_mov_b32 m0, %2\n\ts_nop 0\n\tglobal_load_lds_dword %0, %1" :: "v"(voff), "s"(base), "n"(L_JUNK) : "memory");
}
constexpr int xrow(int n) { return ((n / 14) * 16 + (n & 1) * 8) * 896 + ((n % 14) >> 1) * 128 + 2048 - n % 4 * 1024; }
template <int N> DI void dma_x(__amdgpu_buffer_rsrc_t rs, u32 voff, u32 lds) {
  u32 t;
  asm volatile("s_add_u32 m0, %3, %4\n\t" DL_(5, 0) DL_(6, 1024) DL_(7, 2048) DL_(8, 3072)
               : "=&s"(t) : "v"(voff), "s"(rs), "s"(lds), "n"(N * 1024), "n"(xrow(N)), "n"(xrow(N + 1)), "n"(xrow(N + 2)), "n"(xrow(N + 3)) : "memory", "scc");
}
DI u32 vm_count() {
  u32 t;
  asm volatile("s_getreg_b32 %0, hwreg(HW_REG_IB_STS)" : "=s"(t) :: "memory");
  return (t & 15u) | ((t >> 18) & 48u);
}
DI u32 min8(u32 c) {
  c = min(c, (u32)__builtin_amdgcn_update_dpp(0, (int)c, 0x121, 0xf, 0xf, false)); c = min(c, (u32)__builtin_amdgcn_update_dpp(0, (int)c, 0x122, 0xf, 0xf, false));
  c = min(c, (u32)__builtin_amdgcn_update_dpp(0, (int)c, 0x124, 0xf, 0xf, false));
  return __builtin_amdgcn_readfirstlane(min(c, (u32)__builtin_amdgcn_update_dpp(0, (int)c, 0x128, 0xf, 0xf, false)));
}
DI void dma4c(const void* base, u32 voff, u32 lds) {
  u32 keep;
  asm volatile("s_mov_b32 %0, m0\n\ts_mov_b32 m0, %3\n\ts_nop 0\n\tglobal_load_lds_dword %1, %2 sc1\n\ts_mov_b32 m0, %0" : "=&s"(keep) : "v"(voff), "s"(base), "s"(lds) : "memory");
}
#define LD16(rs, off) __builtin_bit_cast(u32x4, __builtin_amdgcn_raw_buffer_load_b128(rs, off, 0, 16))
#define ST16(rs, v, off, soff) do { const u32x4 d_ = __builtin_bit_cast(u32x4, v); __builtin_amdgcn_raw_buffer_store_b128(d_, rs, off, soff, 16); asm volatile("s_nop 1" :: "v"(d_) : "memory"); } while (0)

#define FLAG(off, n) __builtin_amdgcn_raw_buffer_store_b64(__builtin_bit_cast(u32x2, tag + (n)), rs, fvo, off, 16)
#define FLAGC(c, off, n) __builtin_amdgcn_raw_buffer_store_b64(__builtin_bit_cast(u32x2, tag + (n)), rs, (c) ? fvo : NOWHERE, off, 16)
constexpr int NOWHERE = 0x7ffffff0;
constexpr u32 EMPTY = 0xffffffffu;
DI u32 filled(u32 w) { return min(w, EMPTY - 1u); }
DI u64 peek(const char* ws, u32 off, bool on, u64 dflt) {
  u64 z = 0, v = dflt;
  asm volatile("" : "+v"(z));
  if (on) v = __hip_atomic_fetch_add((GAS u64*)(ws + off), z, __ATOMIC_RELAXED, AP == 2 ? __HIP_MEMORY_SCOPE_SYSTEM : __HIP_MEMORY_SCOPE_AGENT);
  return v;
}
DI void poll_flags(const char* ws, u32 off, bool on, u64 tag) {
#pragma nounroll
  for (u32 sp = 0; sp < SPIN_CAP; ++sp) {
    u64 v = tag + 1;
    if (on || !LL) v = __hip_atomic_load((const GAS u64*)(ws + off), RLX_AGENT);
    if (__all(v == tag + 1)) break;
  }
  __builtin_amdgcn_fence(__ATOMIC_ACQUIRE, "wavefront");
}
template <int SL = 1> DI void lds_wait(LAS char* L, int sig) {
#pragma nounroll
  for (u32 sp = 0; sp < (SPIN_CAP << 4); ++sp) {
    const u32x4 v = *(volatile LAS u32x4*)(L + L_FLG + sig * 16);
    if (__builtin_amdgcn_readfirstlane(v.x & v.y & v.z & v.w)) break;
    __builtin_amdgcn_s_sleep(SL);
  }
  asm volatile("" ::: "memory");
}
DI void lds_wait2(LAS char* L, int sig, int sig2) {
#pragma nounroll
  for (u32 sp = 0; sp < (SPIN_CAP << 4); ++sp) {
    const u32x4 v = *(volatile LAS u32x4*)(L + L_FLG + sig * 16) & *(volatile LAS u32x4*)(L + L_FLG + sig2 * 16);
    if (__builtin_amdgcn_readfirstlane(v.x & v.y & v.z & v.w)) break;
    __builtin_amdgcn_s_sleep(1);
  }
  asm volatile("" ::: "memory");
}
DI void lds_wait1(LAS char* L, int sig, int w) {
#pragma nounroll
  for (u32 sp = 0; sp < (SPIN_CAP << 4); ++sp) {
    if (__builtin_amdgcn_readfirstlane(*(volatile LAS u32*)(L + L_FLG + sig * 16 + w * 4))) break;
    __builtin_amdgcn_s_sleep(1);
  }
  asm volatile("" ::: "memory");
}
DI void lds_signal(LAS char* L, int sig, int w) {
  asm volatile("" ::: "memory");
  *(volatile LAS u32*)(L + L_FLG + sig * 16 + w * 4) = 1u;
}

template <int N> struct IC { static constexpr int value = N; };
template <int I, int N, class F> DI void sfor(F&& f) { if constexpr (I < N) { f(IC<I>{}); sfor<I + 1, N>(f); } }
#define LAM __attribute__((always_inline))
#define PIN(v) asm volatile("" : "+v"(v))
constexpr float LOG2E = 1.4426950408889634f;
template <int K> DI void macc(f32x4& c, u32x4 a, u32x4 b) {
  if (K == 0) asm volatile("v_mfma_f32_16x16x32_bf16 %0, %1, %2, 0" : "=&v"(c) : "v"(a), "v"(b));
  else if (K == 1) asm volatile("v_mfma_f32_16x16x32_bf16 %0, %1, %2, %0" : "+v"(c) : "v"(a), "v"(b));
  else c = mfma16(a, b, c);
}
template <int K> DI void macc(f32x16& c, u32x4 a, u32x4 b) {
  if (K == 0) asm volatile("v_mfma_f32_32x32x16_bf16 %0, %1, %2, 0" : "=&v"(c) : "v"(a), "v"(b));
  else if (K == 1) asm volatile("v_mfma_f32_32x32x16_bf16 %0, %1, %2, %0" : "+v"(c) : "v"(a), "v"(b));
  else c = mfma32(a, b, c);
}
template <int GAP, int SIG, int NW, class E, class F> DI void project(LAS char* L, u32 w0, u32 w1, const u32x4 (&xf)[2][14], int tp, int lane, int csl, f32x4 (&t)[2][2], f32x2 (&st)[2], E&& pre, F&& mid) {
  u32 wq = (tp & 1 ? w1 : w0) + (tp >> 1) * (7 * 2048 + GAP), ca = csl + (lane >> 4) * 16, sa = L_ST1 + (tp * 32 + (lane & 15)) * 8, fa = L_FLG + SIG * 16 + (lane & (NW - 1)) * 4;
  u32 cw = ca + tp * 64, fw = L_FLG + SIG * 16 + tp * 4, one = 1;
  u32x4 ones = {0x3f803f80u, 0x3f803f80u, 0x3f803f80u, 0x3f803f80u};
  LAS char *p0 = L + w0, *p1 = L + w1, *pq = L + wq, *pca = L + ca, *psa = L + sa, *pfa = L + fa, *pcw = L + cw, *pfw = L + fw;
  asm volatile("" : "+v"(p0), "+v"(p1), "+v"(pq), "+v"(pca), "+v"(psa), "+v"(pfa), "+v"(pcw), "+v"(pfw), "+v"(ones), "+v"(one));
  pre();
  u32x4 cf[7];
  u32x2 ra[7], rb[7];
  f32x4 cs, cp[4];
  u32 sw;
#define WAT(n) (((n) & 1 ? p1 : p0) + ((n) >> 1) * 2048 + ((n) >= 14 ? GAP : 0))
#pragma unroll
  for (int i = 0; i < 7; ++i) cf[i] = trfrag(pq + i * 2048, 256);
  SCHED();
  sfor<0, 7>([&](auto IC_) LAM {
    constexpr int i = IC_.value;
    macc<i == 0 ? 0 : i == 6 ? 2 : 1>(cs, cf[i], ones);
    SCHED();
    if (i < 6) { ra[i] = trread(WAT(i)); rb[i] = trread(WAT(i) + 256); }
    SCHED();
  });
  sfor<0, 28>([&](auto NC) LAM {
    constexpr int n = NC.value, K = n < 2 ? 0 : n < 26 ? 1 : 2;
    const u32x4 f = {ra[n % 7].x, ra[n % 7].y, rb[n % 7].x, rb[n % 7].y};
    macc<K>(t[0][n & 1], f, xf[0][n >> 1]);
    SCHED();
    if constexpr (n + 6 < 28) ra[(n + 6) % 7] = trread(WAT(n + 6));
    if constexpr (n == 24) st[0] = *(LAS f32x2*)(psa);
    if constexpr (n == 22 || n == 23) cp[n - 22] = *(LAS f32x4*)(pca + (n - 22) * 64);
    if constexpr (n >= 26) { float a = cp[n - 26][0] + cp[n - 24][0], b = cp[n - 26][1] + cp[n - 24][1]; PIN(a); PIN(b); cp[n - 26][0] = a; cp[n - 26][1] = b; }
    SCHED();
    macc<K>(t[1][n & 1], f, xf[1][n >> 1]);
    SCHED();
    if constexpr (n + 6 < 28) rb[(n + 6) % 7] = trread(WAT(n + 6) + 256);
    if constexpr (n == 24) st[1] = *(LAS f32x2*)(psa + 128);
    if constexpr (n == 22 || n == 23) cp[n - 20] = *(LAS f32x4*)(pca + (n - 20) * 64);
    if constexpr (n >= 26) { float a = cp[n - 26][2] + cp[n - 24][2], b = cp[n - 26][3] + cp[n - 24][3]; PIN(a); PIN(b); cp[n - 26][2] = a; cp[n - 26][3] = b; }
    if constexpr (n == 2) { if ((lane & 15) == 0) *(LAS f32x4*)(pcw) = cs; }
    if constexpr (n == 3) { asm volatile("" ::: "memory"); *(volatile LAS u32*)(pfw) = one; }
    if constexpr (n == 16) sw = *(volatile LAS u32*)(pfa);
    if constexpr (n == 21) { if (__builtin_expect(__builtin_amdgcn_ballot_w64(sw == 0) != 0, 0)) { lds_wait(L, SIG); if (NW > 4) lds_wait(L, SIG + 1); } asm volatile("" ::: "memory"); }
    mid(n, pfw - SIG * 16, one);
    SCHED();
  });
#pragma unroll
  for (int tb = 0; tb < 2; ++tb)
#pragma unroll
    for (int k = 0; k < 2; ++k) t[tb][k] += cp[k] * st[tb].x;
}
DI f32x2 swp16(float a, float b) { auto r = __builtin_amdgcn_permlane16_swap(__builtin_bit_cast(u32, a), __builtin_bit_cast(u32, b), false, false); return f32x2{__builtin_bit_cast(float, (u32)r[0]), __builtin_bit_cast(float, (u32)r[1])}; }
DI f32x2 swp32(float a, float b) { auto r = __builtin_amdgcn_permlane32_swap(__builtin_bit_cast(u32, a), __builtin_bit_cast(u32, b), false, false); return f32x2{__builtin_bit_cast(float, (u32)r[0]), __builtin_bit_cast(float, (u32)r[1])}; }
DI float swadd32(float a, float b) { const f32x2 s = swp32(a, b); return s.x + s.y; }
DI float swadd16(float a, float b) { const f32x2 s = swp16(a, b); return s.x + s.y; }
DI float mx2(float a, float b) { float r; asm("v_max_f32 %0, %1, %2" : "=v"(r) : "v"(a), "v"(b)); return r; }
DI float mx3(float a, float b, float c) { return fmaxf(fmaxf(a, b), c); }
DI float mn3(float a, float b, float c) { return fminf(fminf(a, b), c); }
template <class F> DI void feature_map(const f32x4 (&t)[2][2], const f32x2 (&st)[2], const u32x4 (&wpf)[2], int g, u32x4 (&P)[2][2], F&& mid) {
  const float pad = g < 3 ? 0.f : -3.0e38f;
  const f32x4 z4 = {0.f, 0.f, 0.f, 0.f};
  const float sc[2] = {st[0].y * LOG2E, st[1].y * LOG2E}, scg = g < 2 ? sc[0] : sc[1];
  float p[2][8], m[4], mp[4];
#pragma unroll
  for (int tb = 0; tb < 2; ++tb) {
    const u32x4 tq = {pk(t[tb][0][0], t[tb][0][1]), pk(t[tb][0][2], t[tb][0][3]), pk(t[tb][1][0], t[tb][1][1]), pk(t[tb][1][2], t[tb][1][3])};
    const f32x4 p0 = mfma16(wpf[0], tq, z4), p1 = mfma16(wpf[1], tq, z4);
#pragma unroll
    for (int i = 0; i < 8; ++i) p[tb][i] = i < 4 ? p0[i] : p1[i - 4];
  }
  mid(0);
#pragma unroll
  for (int tb = 0; tb < 2; ++tb) {
    const float* q = p[tb];
    m[2 * tb] = mx3(mx3(q[0], q[1], q[2]), mx3(q[3], q[4], q[5]), mx3(q[5], q[6], q[7]));
    m[2 * tb + 1] = mx3(mx3(-q[0], -q[1], -q[2]), mx3(-q[3], -q[4], -q[5]), mx3(-q[5], -q[6], -q[7]));
  }
  {
    const f32x2 a = swp16(m[0], m[1]), b = swp16(m[2], m[3]);
    const f32x2 c = swp32(mx2(a.x, a.y), mx2(b.x, b.y));
    const float d = mx2(c.x, c.y) * -scg;
    const f32x2 e = swp32(d, d), f0 = swp16(e.x, e.x), f1 = swp16(e.y, e.y);
    m[0] = f0.x; m[1] = f0.y; m[2] = f1.x; m[3] = f1.y;
#pragma unroll
    for (int c2 = 0; c2 < 4; ++c2) mp[c2] = m[c2] + pad;
  }
  float ex[4][8], sm[4];
#pragma unroll
  for (int c = 0; c < 4; ++c) {
    mid(1 + c);
#pragma unroll
    for (int i = 0; i < 8; ++i) {
      ex[c][i] = fexp2(fmaf(c & 1 ? -p[c >> 1][i] : p[c >> 1][i], sc[c >> 1], i & 2 ? mp[c] : m[c]));
    }
    sm[c] = ((ex[c][0] + ex[c][1]) + (ex[c][2] + ex[c][3])) + ((ex[c][4] + ex[c][5]) + (ex[c][6] + ex[c][7]));
  }
  mid(5);
  {
    const float d = frcp(swadd32(swadd16(sm[0], sm[1]), swadd16(sm[2], sm[3])));
    const f32x2 e = swp32(d, d), f0 = swp16(e.x, e.x), f1 = swp16(e.y, e.y);
    sm[0] = f0.x; sm[1] = f0.y; sm[2] = f1.x; sm[3] = f1.y;
  }
#pragma unroll
  for (int c = 0; c < 4; ++c)
#pragma unroll
    for (int e = 0; e < 4; ++e) P[c >> 1][c & 1][e] = pk(ex[c][2 * e] * sm[c], ex[c][2 * e + 1] * sm[c]);
}
DI u32x4 piece(const u32x4 (&rp)[8], int e) {
  u32x4 f;
#pragma unroll
  for (int m = 0; m < 4; ++m) f[m] = __builtin_amdgcn_perm(rp[2 * m + 1][e >> 1], rp[2 * m][e >> 1], (e & 1) ? 0x07060302u : 0x05040100u);
  return f;
}
#define RP_OPS "+v"(rp[0]), "+v"(rp[1]), "+v"(rp[2]), "+v"(rp[3]), "+v"(rp[4]), "+v"(rp[5]), "+v"(rp[6]), "+v"(rp[7])
#define PF1 0
#define PF2 84
#define PF3 120
#define TP0 -1
#define TP1 13
#define TP2 -1
#define TOUCH 5
#define NW3 4
#define THRN0 4
#define DNPRIO 3
#define NWD 3
#define MIDCHK 0
#define HELP 0
#define NHW 7
#define W3LATE 0
#define W3WAIT 1
#define ROT 1
#define EXPT 0
#define PFM 12
#define PSLP 4
#define PLAG 0
#define WTOUCH 0
#define REPLATE 0
#define WB0 8
#define WB1 4
#define ULATE 0
#define VPRIO 3
#define VPRIO0 3
#define VPN 27
#define FPRIO 2
#define CLB 0
#define KAT 99
#define STF 0
#define QN0 0
#define QST 4
#define PB0 3
#define WOT 0
#define WTN 2
#define CLF 0
#define TQ 1
#define TU 0
#define TO 1
#define SN_ 49
#define SS_ 128
#define SD_ 0
#define RPO 0
#define REPE 0
#define UFN 14
#define SBA 0
#define TD 17
#define NR5 16
#define NR6 23
#define G4E 0
#define TS0 2
#define TS1 8
#define TWF 4
#define BP3 0
#define W3J 2
#define TQD -1
#define TOD 0
#define TDD 10
#define CPN 7
#define X0A 5
#define NE 0
#define G6E 0
#define B5E 2
#define B5D 0
#define HW 1
#define PACE 1
#define PS 28
#define PJ0 1
#define PJ1 6
#define ATP 3
#define P1E 0
#define HLA 12
#define FLG13 1
#define H1F 1
#define OLAG 5
#define W4O 0
#define CLW 2
#define CN 50
#define WKF 1
#define WON 7
#define PRE 1
#define OPT 3
#define KGO -1
#define QEARLY 1
#define DLY1 4
#define W1 1
#define W2C 0
#define WOE 2
#define W3THR 1
#define HW3 1
#define HW1 1
#define SN 4
#define BATCH 1
#define B23 1
#define WD5L 0
#define A56 1
#define BGO 196
#define BALL 0
#define BSL 4
#define RSL 4
#define H5 1
#define H6 1
#define BORD 1
#define DP0 3
#define AIL 1
#define SEEN 3
#define ADLY 4
#define W6E 0
#define SPLIT 1
#define NCL 4
#define B5L 1
#define A6L 1
#define BO2 2
#define TW 0
#define CL 0
#define CE 1
#define W5D 1
#define W2F 5
#define D2A 12
#define D2B 16
#define SPEC 1
#define CLP 28
#define CLS 3
#define CE2 0
#define DLY5 60
#define CSL 1
#define CF 0
#define OPRIO 3
#define PG 1
#define ISK 1
#define T2 70
#define FXW 8
#define DLY4 16
#define PUBS 5
#define THRN 40
#define DLY2 8
#define DLY3 15
#define GUPRIO 2
enum { S_FREE01 = 0, S_REFILL, S_DONE4, S_WIN3, S_WK, S_WV, S_WQ, S_WSEEN, S_KDONE, S_V, S_PHIK, S_CSV, S_KVA, S_WO, S_CSQ, S_PHI, S_X0, S_X1, S_XA, S_CSK, S_CS, S_WOFA, S_WOFB, S_EXA, S_EXB, S_W2A, S_W2B, S_HFA, S_HFB, S_CONS, S_SPARE, S_XF0 };

__global__ void __launch_bounds__(512, 2) fused_block(const char* __restrict__ x, const char* __restrict__ wqkv, const char* __restrict__ wphi, const char* __restrict__ wo,
                                                      const char* __restrict__ wg, const char* __restrict__ wu, const char* __restrict__ wd, char* __restrict__ out, char* ws) {
  extern __shared__ __attribute__((aligned(16))) char smem[];
  LAS char* L = (LAS char*)smem;
  const int tid = threadIdx.x, lane = tid & 63, wave = __builtin_amdgcn_readfirstlane(tid >> 6), w2 = wave & 3;
  const int b = blockIdx.x, li = b >> 3, xp = (b & 7) >> 1, xh = b & 1;
  const int tile = 4 * xp + (li >> 3), r = 8 * xh + (li & 7);
  const int tile2 = 4 * xp + 2 * xh + (li >> 4), r2 = li & 15;
  const int tg = r2 >= 7 ? 1 : 0, cg = r2 - 7 * tg;
  const int l15 = lane & 15, g = lane >> 4, q4 = l15 >> 2, p4 = lane & 3, hh = lane >> 5;
  const int head = r;
  {
    u64 pc_; asm volatile("s_getpc_b64 %0" : "=s"(pc_));
    touch((const char*)pc_, min((b & ~7) * 256 + (tid >> 5) * 128, CODE_PF));
    *(LAS u32*)(L + L_FLG + (tid & 127) * 4) = 0u;
  }
  int tn = wave < 2 ? 28 : 85 - 24 * wave, ti = (b & 7) * 64 + li * 2 + wave;
  asm volatile("" : "+s"(tn), "+s"(ti));
  ti = wave < 2 ? ti : b;
  u32 tvo = min(ti * tn + min(lane, tn - 1), wave == 3 ? 3135 : wave == 2 ? 9407 : 14335) * 128, xvo = (tile * 128 + w2 * 32 + (lane >> 3)) * 896 + (((lane & 7) ^ (lane >> 3)) * 16);
  asm volatile("s_waitcnt lgkmcnt(0)\n\ts_barrier" : "+v"(tvo), "+v"(xvo) :: "memory");
  asm volatile("" :: "s"(x), "s"(wqkv), "s"(wphi), "s"(wo), "s"(wg), "s"(wu), "s"(wd), "s"(out), "s"(ws));
  STAMP(0);
  u64 epoch;
  if (wave < 4) {
    const char* tb_ = wave < 2 ? x : wave == 2 ? wqkv : wo;
    asm volatile("" : "+s"(tb_));
    touch(tb_, tvo);
  }
  asm volatile("s_load_dwordx2 %0, %1, 0x0" : "=s"(epoch) : "s"(ws) : "memory");
  if (wave >= 4) {
    const __amdgpu_buffer_rsrc_t rx = __builtin_amdgcn_make_buffer_rsrc((void*)(x - 2048), 0, 2048 * 896 + 2048, 0x00020000);
    sfor<0, 7>([&](auto NC) LAM {
      dma_x<4 * NC.value>(rx, xvo, w2 * 28672);
      if (NC.value == X0A) { asm volatile("s_waitcnt vmcnt(%0)" :: "n"(4 * X0A - 10) : "memory"); lds_signal(L, S_X0, w2); }
    });
  }
  asm volatile("s_waitcnt lgkmcnt(0)" : "+s"(epoch) :: "memory");
  const __amdgpu_buffer_rsrc_t rs = __builtin_amdgcn_make_buffer_rsrc((void*)ws, 0, WS_USED, 0x00020000);
  const u32x4 ones = {0x3f803f80u, 0x3f803f80u, 0x3f803f80u, 0x3f803f80u};
  const f32x4 z4 = {0.f, 0.f, 0.f, 0.f};
  auto role = [&](auto AC) __attribute__((always_inline)) {
  constexpr bool A = AC.value;
  if (A) asm volatile("; A" ::: "memory"); else asm volatile("; B" ::: "memory");
  u32x4 rp[8];
  const int fvo = lane < 16 ? lane * 8 : NOWHERE;
  const int uidx = (tile * 10 + __builtin_popcount(~0x2A54u & ((1u << head) - 1u))) * 4 + w2, unit = uidx * 49 / 80;
  const bool has_unit = !((0x2A54 >> head) & 1) && (uidx + 1) * 49 / 80 != unit;
  const int u3 = unit >> 1, umat = unit & 1, ukq = u3 / 28, ufl = u3 - 28 * ukq;
  const char* gusrc = ws + WS_WGU + 7 * r * SLOT;
  const u32 guo = WS_WGU + 7 * r * SLOT + w2 * 7168;
#define GUBLK(j) dma7<(j) * SLOT, gu_slot(j) * SLOT>(rs, lane * 16, guo, w2 * 7168)
  const u32 wvo = (lane >> 2) * 2688 + (((lane & 3) ^ (2 * (lane >> 5))) * 16);
  const char* wbase = wqkv + head * 56 - (head & 1) * 8 + 16 * w2 * 2688;
  const u32 wr0 = (8 * g + q4) * 64 + (((p4 + (head & 1)) ^ (4 * (g & 1))) * 8), wr1 = (8 * g + q4) * 64 + ((((4 + p4 + (head & 1)) & 7) ^ (4 * (g & 1))) * 8);
  const int tb = w2; constexpr int kh = A ? 0 : 1;
  const int hho = WS_HH + (((tile * 2 + (w2 >> 1)) * 4 + (r >> 2)) * 56 + (r & 3) * 2 + (w2 & 1)) * 1024;
  const int tok = tile2 * 128 + tg * 64 + tb * 16 + l15;
  const int hvo = WS_H + ((tok >> 5) * 28 + 4 * cg + (g >> 1)) * 1024 + ((g & 1) * 32 + (tok & 31)) * 16;
  const int sto = WS_ST + (2 * cg * 2048 + tile2 * 128 + tg * 64 + tb * 16) * 8;
  const u32x4 empty4 = {EMPTY, EMPTY, EMPTY, EMPTY};
#define HH_CLEAR(i0, i1) _Pragma("unroll") for (int i = i0; i < i1; ++i) ST16(rs, empty4, lane * 16 + (i & 1) * 2048, WS_HH + (((tile2 * 2 + r2 - 14) * 4 + w2) * 56 + kh + (i >> 1) * 4) * 1024)
#define HH_FLAG() FLAG(WS_FH + ((tile2 * 4 + 2 * (r2 - 14) + kh) * 32 + 28 + w2) * 128, 1)
  u32x4 wof[8][4];
  f32x4 csp[4];
  u64 tag;
  u32x4 xr;
  const int xo = tok * 896 + cg * 128 + kh * 64 + g * 16;
#define XR() asm volatile("global_load_dwordx4 %0, %1, %2" : "=v"(xr) : "v"(r2 < 14 ? xo : 0), "s"(x) : "memory")
#define WOF_READ(i0, i1) do { u32 wa_ = L_WO + (kh * 224 + 4 * g + q4) * 128 + 16 * p4 + 64 * (q4 >> 1), wb_ = wa_ ^ 64, wc_ = wa_ + (g == 3 ? 0 : 2048), wd_ = wc_ ^ 64; \
    asm volatile("" : "+v"(wa_), "+v"(wb_), "+v"(wc_), "+v"(wd_)); \
    _Pragma("unroll") for (int i = i0; i < i1; ++i) _Pragma("unroll") for (int k = 0; k < 4; ++k) { \
      const u32x2 a_ = trread(L + (k >> 1 ? wb_ : wa_) + 28 * i * 128 + (k & 1) * 8), c_ = trread(L + (k >> 1 ? wd_ : wc_) + 28 * i * 128 + (k & 1) * 8); \
      wof[i][k] = u32x4{a_.x, a_.y, c_.x, c_.y}; } } while (0)
#define XF_READ(i) _Pragma("unroll") for (int s = 0; s < 14; ++s) xf[i][s] = *(LAS u32x4*)((s & 1 ? xr1 : xr0) + (i) * 14336 + (s >> 1) * 2048)
#define LN_STATS(kh) do { f32x4 g1 = z4, g2 = z4; \
    _Pragma("unroll") for (int s = 0; s < 14; ++s) { g1 = mfma16(ones, xf[kh][s], g1); g2 = mfma16(xf[kh][s], xf[kh][s], g2); } \
    const float d01 = (p4 & 1) ? g2[1] : g2[0], d23 = (p4 & 1) ? g2[3] : g2[2], s2 = (p4 & 2) ? d23 : d01; \
    const float nmu = g1[0] * (-1.f / 448.f), var = fmaxf(s2 * (1.f / 448.f) - nmu * nmu, 0.f); \
    if (g == q4) *(LAS f32x2*)(L + L_ST1 + (w2 * 32 + kh * 16 + l15) * 8) = f32x2{nmu, __builtin_amdgcn_rsqf(var + 1e-5f)}; } while (0)
#define WPF() do { u32 wp[8]; \
    _Pragma("unroll") for (int j = 0; j < 8; ++j) wp[j] = *(LAS u32*)(L + L_WPHI + 4 * g * 56 + min(l15, 13) * 4 + ((j & 3) + 16 * (j >> 2)) * 56); \
    _Pragma("unroll") for (int e = 0; e < 4; ++e) { const bool z = e >= 2 && g == 3; \
      wpf[0][e] = z ? 0u : __builtin_amdgcn_perm(wp[2 * e + 1], wp[2 * e], 0x05040100u); wpf[1][e] = z ? 0u : __builtin_amdgcn_perm(wp[2 * e + 1], wp[2 * e], 0x07060302u); } \
    asm volatile("" : "+v"(wpf[0]), "+v"(wpf[1])); } while (0)
  {
    u32x4 xf[2][14], wpf[2];
    f32x2 st[2];
    LAS char* xr0 = L + w2 * 28672 + l15 * 128 + ((g ^ (l15 & 7)) * 16);
    LAS char* xr1 = L + w2 * 28672 + l15 * 128 + (((4 + g) ^ (l15 & 7)) * 16);
    LAS char* pw = L + L_PKV + (w2 * 32 + l15) * 224 + g * 8;
    const char* wos = wo + (r2 < 14 ? cg : 0) * 128 + w2 * 56 * 896;
    const u32 wovo = (lane >> 3) * 896 + (((lane & 7) ^ ((lane >> 2) & 4)) * 16);
#define WO_HALF(hf_) _Pragma("unroll") for (int n = 0; n < 7; ++n) dma16(wos + ((hf_) * 224 + n * 8) * 896, wovo, L_WO + ((hf_) * 28 + w2 * 7 + n) * 1024)
    int clo = WS_O + ((tile * 8 + 2 * w2) * 16 + head) * 1024, clh = r2 < 14 ? hvo : NOWHERE, cls = r2 < 14 && lane < 16 ? (lane >> 3) * 16384 + (lane & 7) * 16 : NOWHERE, clt = sto;
    int kvo = WS_KVP + ((head * 16 + tile) * 4 + w2) * 1024, fko = WS_FKV + ((head * 4 + w2) * 16 + tile) * 128;
    u32x4 em4 = empty4;
    u32 wtv = min(lane, 24) * 128;
    const char* wts = wo + min(li * 98 + w2 * 25, 3136 - 25) * 128;
    auto clear = [&](int c) LAM {
      if (c == 0) ST16(rs, em4, lane * 16, clo);
      if (c == 1) ST16(rs, em4, lane * 16, clo + 16384);
      if (c == 2) ST16(rs, em4, clh, 0);
      if (c == 3) ST16(rs, em4, clh, 2048);
      if (c == 4) ST16(rs, em4, cls, clt);
    };
    if constexpr (A) {
      if (wave == 2) { dma16(wphi + head * 1568, lane * 16, L_WPHI); dma16(wphi + head * 1568, min(lane + 64, 97) * 16, L_WPHI + 1024); }
      __builtin_amdgcn_s_waitcnt(0x0F70);
      tag = epoch * 6364136223846793005ull + 1442695040888963407ull;
      STAMP(1);
#define WKLOAD() _Pragma("unroll") for (int i = 0; i < 7; ++i) dma16(wbase + 896 + 64 * i * 2688, wvo, L_WK + (4 * i + w2) * 1024)
      if (KAT == 3) { WKLOAD(); if (wave == 2) { WAITVM(7); lds_signal(L, S_PHI, 0); } }
      else if (wave == 2) { if (CLF == 9) WAITVM(5); else WAITVM(0); lds_signal(L, S_PHI, 0); }
      if (KAT > 3 && KAT < 28) { lds_wait1(L, S_SPARE, w2); WKLOAD(); }
      lds_wait1(L, S_PHI, 0);
      WPF();
#define DBITE() dma4(wd + min(b * 49 + w2 * 13, 12544 - 13) * 128, min(lane, 12) * 128, L_JUNK)
      if (TD == 9) {
        __builtin_amdgcn_s_sleep(TS0);
        if (w2 & 1) __builtin_amdgcn_s_sleep(TS1);
        if (w2 & 2) { __builtin_amdgcn_s_sleep(TS1); __builtin_amdgcn_s_sleep(TS1); }
        DBITE();
      }
      if (TD == 10 && w2 == 0) DBITE();
      if (WKF == 2) {
#pragma nounroll
        for (u32 sp = 0; sp < (SPIN_CAP << 4); ++sp) { const u32x4 v = *(volatile LAS u32x4*)(L + L_FLG + S_X0 * 16); if (__builtin_amdgcn_readfirstlane(v.x | v.y | v.z | v.w)) break; __builtin_amdgcn_s_sleep(1); }
        WKLOAD();
      }
      lds_wait1(L, S_X0, w2);
      if (WKF == 1) { WKLOAD(); }
      XF_READ(0);
      if (w2 < 2) lds_signal(L, S_XF0, w2);
      if (TD == 10 && w2 == 1) DBITE();
      if (WKF == 0) { WKLOAD(); }
      if (TD == 10 && w2 == 2) DBITE();
      if (TD == 11) DBITE();
      STAMP(27);
      LN_STATS(0);
      SCHED();
      STAMP(2);
      lds_wait1(L, S_X1, w2);
      XF_READ(1);
      if (QEARLY == 3 && w2 < 2) lds_signal(L, S_SPARE, w2);
      STAMP(3);
      if (SBA) LN_STATS(1);
      WAITVM(0);
      STAMP(4);
      lds_signal(L, S_WK, w2);
      if (TD == 10 && w2 == 3) DBITE();
      if (TD == 12) DBITE();
      f32x4 t[2][2];
      u32 pr = L_PKV + (4 * g + q4) * 224 + 8 * p4;
      asm volatile("" : "+v"(pr), "+v"(pw));
      asm volatile("" : "+s"(clo), "+v"(clh), "+v"(cls), "+s"(clt), "+s"(kvo), "+s"(fko), "+s"(tag), "+v"(em4));
      if (WOT == 5) asm volatile("" : "+v"(wtv), "+s"(wts));
      project<0, S_CSK, 8>(L, L_WK + wr0, L_WK + wr1, xf, w2, lane, L_CSP, t, st, [&]() LAM {
        lds_wait(L, S_WK);
        STAMP(5);
        __builtin_amdgcn_s_setprio(2);
      }, [&](int n, LAS char* sg, u32 one) LAM {
        if (n == KGO) *(volatile LAS u32*)(sg + S_KDONE * 16) = one;
      });
      STAMP(6);
      u32x4 P[2][2];
      __builtin_amdgcn_s_setprio(FPRIO);
      feature_map(t, st, wpf, g, P, [&](int i) LAM {
        if (WOT == 5 && i == 0) { SCHED(); dma4(wts, wtv, L_JUNK); SCHED(); }
        if (CLF == 1 && i > 0) { SCHED(); clear(i - 1); SCHED(); }
        if (CLF == 2 && i == 5) { SCHED(); clear(0); clear(1); clear(2); clear(3); clear(4); SCHED(); }
        if (CLF == 3 && i >= 3) { SCHED(); clear(2 * i - 6); clear(2 * i - 5); SCHED(); }
        if (CLF == 4 && i == 1) { SCHED(); clear(0); clear(1); clear(2); clear(3); clear(4); SCHED(); }
      });
#pragma unroll
      for (int c = 0; c < 4; ++c) {
        LAS char* p = pw + (c >> 1) * 16 * 224 + (c & 1) * 64;
        *(LAS u32x2*)(p) = u32x2{P[c >> 1][c & 1].x, P[c >> 1][c & 1].y}; *(LAS u32x2*)(p + 32) = u32x2{P[c >> 1][c & 1].z, P[c >> 1][c & 1].w};
      }
      lds_signal(L, S_PHIK, w2);
      STAMP(7);
      if (OPT & 4) XR();
      if (CLF == 0 || CLF == 8) { clear(0); clear(1); }
      if (CLF == 0 || CLF == 7) { clear(2); clear(3); clear(4); }
      lds_wait2(L, S_V, S_PHIK);
      u32x4 pa[4], pb[8];
#pragma unroll
      for (int m = 0; m < 4; ++m) {
        pa[m] = trfrag(L + pr + w2 * 32 + m * 32 * 224, 16 * 224);
        pb[2 * m] = trfrag(L + pr + m * 32 * 224 + 128, 16 * 224); pb[2 * m + 1] = trfrag(L + pr + m * 32 * 224 + 160, 16 * 224);
      }
      SCHED();
      f32x4 kv[2] = {};
#pragma unroll
      for (int m = 0; m < 8; ++m) kv[m & 1] = mfma16(pa[m >> 1], pb[m], kv[m & 1]);
      const u32x4 kvb = {pk(kv[0][0], kv[0][1]), pk(kv[0][2], kv[0][3]), pk(kv[1][0], kv[1][1]), pk(kv[1][2], kv[1][3])};
      ST16(rs, kvb, lane * 16, kvo);
      STAMP(8);
      if (TD == 17) {
        lds_signal(L, S_KDONE, w2);
        if (wave == 0) { lds_wait(L, S_KDONE); touch(wd + b * 49 * 128, min(lane, 48) * 128); WAITVM(1); } else WAITVM(0);
      } else if (TD == 19) {
        lds_signal(L, S_KDONE, w2);
        lds_wait(L, S_KDONE);
        if (wave == 0) touch(wd + b * 49 * 128, min(lane, 48) * 128);
        if (r2 < 14) {
#pragma unroll
          for (int n = 0; n < WON; ++n) dma16(wos + n * 8 * 896, wovo, L_WO + (w2 * 7 + n) * 1024);
          if (wave == 0) asm volatile("s_waitcnt vmcnt(%0)" :: "n"(WON + 1) : "memory"); else asm volatile("s_waitcnt vmcnt(%0)" :: "n"(WON) : "memory");
        } else if (wave == 0) WAITVM(1); else WAITVM(0);
      } else if (TD == 18) {
        lds_signal(L, S_KDONE, w2);
        lds_wait(L, S_KDONE);
        touch(wd + min(b * 49 + w2 * 13, 12544 - 13) * 128, min(lane, 12) * 128);
        WAITVM(1);
      } else DRAIN();
      if (OPT & 4) asm volatile("" : "+v"(xr));
      FLAG(fko, 1);
      __builtin_amdgcn_s_setprio(0);
      STAMP(9);
      lds_signal(L, S_XA, w2); lds_wait(L, S_XA);
      if ((OPT & 2) && wave == 1) FLAG(WS_FO + (head * 16 + tile) * 128, 1);
#define DTOUCH() dma4(wd + b * 49 * 128, min(lane, 48) * 128, L_JUNK)
      if (TD == 2 && wave == 0) DTOUCH();
      if (r2 < 14 || !ISK) {
#pragma unroll
        for (int n = TD == 19 ? WON : 0; n < 7; ++n) dma16(wos + n * 8 * 896, wovo, L_WO + (w2 * 7 + n) * 1024);
        WO_HALF(1);
      } else { HH_CLEAR(0, NE); if (NE < 8) __builtin_amdgcn_s_sleep(20); }
      if (OPT & 8) XR();
      poll_flags(ws, WS_FKV + ((head * 4 + w2) * 16 + l15) * 128, lane < 16, tag);
      if (OPT & 8) asm volatile("s_waitcnt vmcnt(0)" : "+v"(xr) :: "memory");
      lds_signal(L, S_WO, w2);
      STAMP(10);
      u32x4 tt[16];
#pragma unroll
      for (int i = 0; i < 16; ++i) tt[i] = __builtin_bit_cast(u32x4, __builtin_amdgcn_raw_buffer_load_b128(rs, lane * 16, WS_KVP + head * 65536 + w2 * 1024 + i * 4096, 16));
      u32 fgo;
      {
        const int pl = (tile & 1) * 16 + min(OPT & 2 ? l15 : lane >> 2, 13);
        fgo = wave == 0 ? (OPT & 2 ? WS_FO + (l15 * 16 + tile2) * 128 : WS_FKV + ((l15 * 4 + 2 * tg + (g & 1)) * 16 + tile2) * 128)
            : wave == 1 ? (OPT & 2 ? WS_FO + ((((tile >> 1) & 1) * 8 + (pl & 7)) * 16 + (tile & 12) + (pl >> 3)) * 128 : WS_FKV + (((((tile >> 1) & 1) * 8 + (pl & 7)) * 4 + p4) * 16 + (tile & 12) + (pl >> 3)) * 128)
            : wave == 2 && (OPT & 1) ? fko
                        : WS_FG + (min(((7 * r) >> 2) + g, (7 * r + 6) >> 2) * 16 + (l15 & 8) + min(lane & 7, 6)) * 128;
      }
      u64 fg = __hip_atomic_load((const GAS u64*)(ws + fgo), RLX_AGENT);
      SCHED();
      const u32 one = g == q4 ? ((p4 & 1) ? 0x3f800000u : 0x3f80u) : 0u, s01 = p4 < 2 ? one : 0u, s23 = p4 < 2 ? 0u : one;
      const u32x4 sel0 = {s01, s23, 0u, 0u}, sel1 = {0u, 0u, s01, s23};
      f32x4 s0 = {}, s1 = {};
#pragma unroll
      for (int i = 0; i < 16; ++i) { s0 = mfma16(sel0, tt[i], s0); s1 = mfma16(sel1, tt[i], s1); }
      LAS char* kw = L + L_KVA + (w2 >> 1) * 1024 + lane * 16 + (w2 & 1) * 8;
      *(LAS u32x2*)(kw) = u32x2{pk(s0[0], s0[1]), pk(s0[2], s0[3])};
      *(LAS u32x2*)(kw + 2048) = u32x2{pk(s1[0], s1[1]), pk(s1[2], s1[3])};
      lds_signal(L, S_KVA, w2);
      if (TD == 4 && wave == 2) DTOUCH();
      STAMP(11);
      if (WOE == 3) { WO_HALF(1); WAITVM(0); lds_signal(L, S_WO, w2); }
      asm volatile("" : "+v"(fg) :: "memory");
      if (!__all(fg == tag + 1)) poll_flags(ws, fgo, true, tag);
      __builtin_amdgcn_fence(__ATOMIC_ACQUIRE, "wavefront");
      lds_signal(L, S_WOFA, w2);
      lds_wait1(L, S_WOFA, 3);
      const int fl = min(((7 * r) >> 2) + w2, (7 * r + 6) >> 2);
#pragma unroll
      for (int i = 0; i < 4; ++i) asm volatile("global_load_dwordx4 %0, %1, %2 offset:%3 sc1" : "=v"(csp[i]) : "v"(((7 * r) >> 2) + w2 == fl ? (i < 3 ? lane : lane & 31) * 16 : 0), "s"(ws + WS_CS + fl * 4096), "n"(i * 1024) : "memory");
      GUBLK(2);
      if (r2 < 14 || !ISK) { lds_wait(L, S_WO); WOF_READ(0, 8); }
      SCHED();
#define CS_OUT(k_) do { asm volatile("s_waitcnt vmcnt(%4)" : "+v"(csp[0]), "+v"(csp[1]), "+v"(csp[2]), "+v"(csp[3]) : "n"(k_) : "memory"); \
        const f32x4 sm = (csp[0] + csp[1]) + (csp[2] + 0.5f * csp[3]);     \
        const float e0 = swadd32(sm[0], sm[1]), e1 = swadd32(sm[2], sm[3]);     \
        const int j0 = 4 * (((7 * r) >> 2) + w2) + 2 * (lane & 1) - 7 * r, cr = ((g & 1) * 16 + hh * 8 + (l15 >> 1)) * 4; \
        if (j0 >= 0 && j0 < 7) *(LAS float*)(L + L_CS + j0 * 128 + cr) = e0; \
        if (j0 + 1 >= 0 && j0 + 1 < 7) *(LAS float*)(L + L_CS + (j0 + 1) * 128 + cr) = e1; } while (0)
      lds_signal(L, S_WOFB, w2);
      if (!CSL) CS_OUT(7);
      STAMP(24);
    } else {
      const int rg = lane >> 3;
      const u32 lvo = rg * 8 * 3584 + (lane & 7) * 16;
      const char* us = (umat ? wu : wg) + ukq * 64 * 3584 + ufl * 128;
      asm volatile("" : "+s"(us));
      auto repack = [&]() LAM {
        if (has_unit) {
          const int so = rg * 512 + (lane & 7) * 16;
          const int sb = WS_WGU + 4 * ufl * SLOT + ukq * 4096 + umat * 256;
          const int s1 = SLOT, s2 = 2 * SLOT;
          float c[8];
#pragma unroll
          for (int e = 0; e < 8; ++e) {
            const u32x4 f = piece(rp, e);
            ST16(rs, f, so + (e & 1) * 128, sb + ((e >> 1) & 1) * s1 + (e >> 2) * s2);
            c[e] = mfma16(ones, f, z4)[0];
          }
          f32x4 cv = g & 1 ? f32x4{c[4], c[5], c[6], c[7]} : f32x4{c[0], c[1], c[2], c[3]};
#pragma unroll
          for (int e = 0; e < 4; ++e) cv[e] += dppror<8>(cv[e]);
          ST16(rs, cv, g < 2 && l15 < 8 ? l15 * 32 + g * 16 : NOWHERE, WS_CS + ufl * 4096 + ukq * 512 + umat * 256);
        }
      };
#define RPLOAD() _Pragma("unroll") for (int j = 0; j < 8; ++j) asm volatile("global_load_dwordx4 %0, %1, %2" : "=v"(rp[j]) : "v"(lvo), "s"(us + j * 3584) : "memory")
      if (RPO == 2 && has_unit) { RPLOAD(); }
      tag = epoch * 6364136223846793005ull + 1442695040888963407ull;
      STAMP(1);
      XF_READ(0);
      if (w2 < 2) lds_signal(L, S_XF0, 2 + w2);
      if (RPO == 2 && has_unit) WAITVM(8); else WAITVM(0);
      lds_signal(L, S_X1, w2);
      if (RPO == 1 && has_unit) { RPLOAD(); }
      STAMP(2);
      if (STF < 7) lds_wait(L, S_XF0);
      XF_READ(1);
      if (QEARLY == 3 && w2 < 2) lds_signal(L, S_SPARE, 2 + w2);
      SCHED();
#define WDST(c) ((c) * 1024 + ((c) >= 14 ? SLOT - 14336 : 0))
#pragma unroll
      for (int i = 0; i < 7 - STF; ++i) dma16(wbase + 1792 + 64 * i * 2688, wvo, L_WV + WDST(4 * i + w2));
      STAMP(3);
      __builtin_amdgcn_s_setprio(PB0);
      if (!SBA) LN_STATS(1);
      lds_signal(L, S_CS, w2);
      if (STF == 7) lds_wait(L, S_XF0);
#pragma unroll
      for (int i = 7 - STF; i < 7; ++i) dma16(wbase + 1792 + 64 * i * 2688, wvo, L_WV + WDST(4 * i + w2));
      STAMP(4);
      f32x4 t[2][2];
      asm volatile("" : "+v"(pw));
      if (WOT == 4) asm volatile("" : "+v"(wtv), "+s"(wts));
      project<SLOT - 14336, S_CSV, 4>(L, L_WV + wr0, L_WV + wr1, xf, w2, lane, L_CSP + 256, t, st, [&]() LAM {
      if (QEARLY == 3) {
        lds_wait(L, S_SPARE);
#pragma unroll
        for (int i = 0; i < 7; ++i) dma16(wbase + 64 * i * 2688, wvo, L_WQ + WDST(4 * i + w2));
      }
      if (REPE == 3) { asm volatile("s_waitcnt vmcnt(%8)" : RP_OPS : "n"(QEARLY == 3 ? 14 : 7) : "memory"); repack(); if (has_unit) { if (QEARLY == 3) WAITVM(16); else WAITVM(9); } }
      if (RPO == 0 && has_unit) { RPLOAD(); } else if (REPE == 3 && has_unit) {} else if (QEARLY == 3) WAITVM(7); else WAITVM(0);
      STAMP(28);
      if (RPO) {} else if (QEARLY == 3) WAITVM(15); else WAITVM(8);
      lds_signal(L, S_WV, w2);
      STAMP(29);
      lds_wait2(L, S_WV, S_WK);
      STAMP(30);
      if (QEARLY == 1) {
#pragma unroll
        for (int i = 0; i < 7; ++i) dma16(wbase + 64 * i * 2688, wvo, L_WQ + WDST(4 * i + w2));
      }
        if (REPE == 2) { asm volatile("s_waitcnt vmcnt(7)" : RP_OPS :: "memory"); repack(); }
        if (TD == 8 && !has_unit && !((0x2A54 >> head) & 1)) dma4(wd + min((uidx - unit) * 51, 12544 - 51) * 128, min(lane, 50) * 128, L_JUNK);
        if (KGO >= 0) lds_wait1(L, S_KDONE, w2);
        STAMP(5);
        __builtin_amdgcn_s_setprio(VPRIO0);
      }, [&](int n, LAS char*, u32) LAM {
        if (n == VPN) __builtin_amdgcn_s_setprio(VPRIO);
        if (WOT == 4 && n == WTN) dma4(wts, wtv, L_JUNK);
        if (QEARLY == 2 && n >= QN0 && (n - QN0) % QST == 0 && (n - QN0) / QST < 7) { const int i = (n - QN0) / QST; dma16(wbase + 64 * i * 2688, wvo, L_WQ + WDST(4 * i + w2)); }
      });
#pragma unroll
      for (int c = 0; c < 4; ++c) t[c >> 1][c & 1] *= st[c >> 1].y;
      STAMP(6);
#pragma unroll
      for (int c = 0; c < 2; ++c) {
        LAS char* p = pw + c * 16 * 224 + 128;
        *(LAS u32x2*)(p) = u32x2{pk(t[c][0][0], t[c][0][1]), pk(t[c][0][2], t[c][0][3])};
        *(LAS u32x2*)(p + 32) = g == 3 ? u32x2{0x3f80u, 0u} : u32x2{pk(t[c][1][0], t[c][1][1]), pk(t[c][1][2], t[c][1][3])};
      }
      lds_signal(L, S_V, w2);
      if (OPT & 4) XR();
      __builtin_amdgcn_s_setprio(0);
      STAMP(7);
      if (!QEARLY) {
#pragma unroll
        for (int i = 0; i < 7; ++i) dma16(wbase + 64 * i * 2688, wvo, L_WQ + WDST(4 * i + w2));
      }
      if (WOE == 0) WO_HALF(0);
      if (WOE < 2) { lds_wait(L, S_KDONE); WO_HALF(1); }
      if (REPE == 0) asm volatile("s_waitcnt vmcnt(%8)" : RP_OPS : "n"(OPT & 4 ? 8 : 7) : "memory");
#define UFLAG() FLAGC(has_unit, WS_FG + ((ufl * 2 + umat) * 8 + ukq) * 128, 1)
      if (REPE == 0) repack();
      STAMP(8);
      WPF();
      if (!has_unit || (REPE == 3 && QEARLY != 3)) WAITVM(0); else WAITVM(9);
      if (OPT & 4) asm volatile("" : "+v"(xr));
      lds_signal(L, S_WQ, w2);
      project<SLOT - 14336, S_CSQ, 4>(L, L_WQ + wr0, L_WQ + wr1, xf, w2, lane, L_CSP + 512, t, st, [&]() LAM {
        lds_wait(L, S_WQ);
        STAMP(9);
      }, [&](int n, LAS char*, u32) LAM {
        if (n == UFN) { WAITVM(0); UFLAG(); if (OPT & 8) XR(); }
      });
      STAMP(10);
      if (REPLATE == 2) repack();
      u32x4 pq[2][2];
      feature_map(t, st, wpf, g, pq, [](int) {});
      asm volatile("" : "+v"(pq[0][0]), "+v"(pq[0][1]), "+v"(pq[1][0]), "+v"(pq[1][1]));
      STAMP(11);
      if (REPLATE == 1) repack();
      if (TD == 13 || TD == 14) {
        lds_wait(L, S_WO);
        if (r2 < 14) WOF_READ(0, TWF); else __builtin_amdgcn_s_sleep(2 * TWF);
        SCHED();
        if (TD == 14) DBITE(); else if (w2 == 0) DTOUCH();
        SCHED();
        if (r2 < 14) WOF_READ(TWF, 8);
      } else if (r2 < 14 || !ISK) { lds_wait(L, S_WO); WOF_READ(0, 8); }
      STAMP(24);
      if (REPLATE == 2) { WAITVM(0); UFLAG(); }
      if (OPT & 8) asm volatile("s_waitcnt vmcnt(0)" : "+v"(xr) :: "memory");
      __builtin_amdgcn_s_setprio(OPRIO);
      lds_wait(L, S_KVA);
      if (REPLATE == 1) { WAITVM(0); UFLAG(); }
      STAMP(27);
      u32x4 ka[4];
#pragma unroll
      for (int i = 0; i < 4; ++i) ka[i] = *(LAS u32x4*)(L + L_KVA + i * 1024 + lane * 16);
      f32x4 o[2][2];
#pragma unroll
      for (int c = 0; c < 4; ++c) o[c >> 1][c & 1] = mfma16(ka[2 * (c & 1) + 1], pq[c >> 1][1], mfma16(ka[2 * (c & 1)], pq[c >> 1][0], z4));
      const f32x2 da = swp16(o[0][1][0], o[0][1][0]), db = swp16(o[1][1][0], o[1][1][0]);
      const float den[2] = {swp32(da.y, da.y).y, swp32(db.y, db.y).y};
#pragma unroll
      for (int c = 0; c < 2; ++c) {
        const float rd = frcp(den[c] + 1e-6f), r3 = g == 3 ? 0.f : rd;
        const u32x4 ob = {pk(o[c][0][0] * rd, o[c][0][1] * rd), pk(o[c][0][2] * rd, o[c][0][3] * rd), pk(o[c][1][0] * r3, o[c][1][1] * r3), filled(pk(o[c][1][2] * r3, o[c][1][3] * r3))};
        ST16(rs, ob, lane * 16, WS_O + ((tile * 8 + 2 * w2 + c) * 16 + head) * 1024);
      }
      STAMP(12);
      if (WOE == 3) lds_wait(L, S_WO);
      *(volatile LAS u32*)(L + L_FLG + S_W2A * 16 + w2 * 4) = (u32)__builtin_amdgcn_s_memrealtime();
      lds_signal(L, S_W2B, w2);
      __builtin_amdgcn_s_setprio(0);
      if (TD == 3 && w2 == 0) DTOUCH();
    }
  }
  STAMP(13);
  u32x4 hres = {0u, 0u, 0u, 0u};
  STAMP(23);
  if (r2 < 14) {
    u32x4 of[8];
    if (!(OPT & 12)) XR();
    lds_wait2(L, S_W2B, S_WOFA);
#define WIN2() do { if (A ? W1 < 2 : W1 < 3) GUBLK(A ? 0 : 1); } while (0)
    if (W2F == 1 || W2F == 3) WIN2();
    if (W2F == 4 && !A) { GUBLK(0); GUBLK(1); }
    if (W2F == 5 && !A) GUBLK(1);
    if (T2) {
      const u32x4 ts = *(volatile LAS u32x4*)(L + L_FLG + S_W2A * 16);
      const u32 t0 = __builtin_amdgcn_readfirstlane(ts.x), due = t0 + T2 + __builtin_amdgcn_readfirstlane(max(max(0, (int)(ts.y - ts.x)), max((int)(ts.z - ts.x), (int)(ts.w - ts.x))));
#pragma nounroll
      for (u32 sp = 0; sp < 64 && (int)((u32)__builtin_amdgcn_s_memrealtime() - due) < 0; ++sp) __builtin_amdgcn_s_sleep(1);
    } else __builtin_amdgcn_s_sleep(A ? D2A : D2B);
    STAMP(14);
#define OFA(c_) _Pragma("unroll") for (int i = 0; i < 8; ++i) \
      asm volatile("global_load_dwordx4 %0, %1, %2 offset:%3 sc1" : c_(of[i]) : "v"(lane * 16), "s"(ws + WS_O + ((tile2 * 8 + tg * 4 + tb) * 16 + kh * 8 + 4) * 1024), "n"(i * 1024 - 4096) : "memory")
#define OFW(k_) asm volatile("s_waitcnt vmcnt(%9)" : "+v"(of[0]), "+v"(of[1]), "+v"(of[2]), "+v"(of[3]), "+v"(of[4]), "+v"(of[5]), "+v"(of[6]), "+v"(of[7]), "+v"(xr) : "n"(k_) : "memory")
#define OFBAD() (!__all(max(max(max(of[0].w, of[1].w), max(of[2].w, of[3].w)), max(max(of[4].w, of[5].w), max(of[6].w, of[7].w))) != EMPTY))
    OFA("=v");
    f32x4 a[4] = {};
    u32 mx = 0;
#pragma unroll
    for (int i = 0; i < 8; ++i) {
      asm volatile("s_waitcnt vmcnt(%1)" : "+v"(of[i]) : "n"(7 - i) : "memory");
      if (i == PG - 1) { lds_wait(L, S_WOFB); }
      if (i == 7) GUBLK(A ? 0 : 3);
#pragma unroll
      for (int k = 0; k < 4; ++k) a[k] = mfma16(wof[i][k], of[i], a[k]);
      mx = max(mx, of[i].w);
      SCHED();
    }
    asm volatile("" : "+v"(xr));
    if (__builtin_expect(!__all(mx != EMPTY), 0)) {
#pragma nounroll
      for (u32 sp = 0; sp < SPIN_CAP; ++sp) { __builtin_amdgcn_s_sleep(RSL); OFA("+v"); OFW(0); if (!OFBAD()) break; }
#pragma unroll
      for (int i = 0; i < 8; ++i)
#pragma unroll
        for (int k = 0; k < 4; ++k) a[k] = mfma16(wof[i][k], of[i], i ? a[k] : z4);
    }
    STAMP(25);
    *(LAS f32x4*)(L + L_EX + wave * 2048 + lane * 16) = kh ? a[0] : a[2];
    *(LAS f32x4*)(L + L_EX + wave * 2048 + 1024 + lane * 16) = kh ? a[1] : a[3];
    lds_signal(L, A ? S_EXA : S_EXB, w2);
    lds_wait1(L, A ? S_EXB : S_EXA, w2);
    STAMP(15);
    const f32x4 c0 = (kh ? a[2] : a[0]) + *(LAS f32x4*)(L + L_EX + (wave ^ 4) * 2048 + lane * 16);
    const f32x4 c1 = (kh ? a[3] : a[1]) + *(LAS f32x4*)(L + L_EX + (wave ^ 4) * 2048 + 1024 + lane * 16);
    const f32x4 h0 = c0 + f32x4{blo(xr.x), bhi(xr.x), blo(xr.y), bhi(xr.y)}, h1 = c1 + f32x4{blo(xr.z), bhi(xr.z), blo(xr.w), bhi(xr.w)};
    hres = u32x4{pk(h0[0], h0[1]), pk(h0[2], h0[3]), pk(h1[0], h1[1]), filled(pk(h1[2], h1[3]))};
    ST16(rs, hres, hvo, kh * 2048);
    const f32x4 r0 = {blo(hres.x), bhi(hres.x), blo(hres.y), bhi(hres.y)}, r1 = {blo(hres.z), bhi(hres.z), blo(hres.w), bhi(hres.w)};
    const f32x4 a1 = r0 + r1, a2q = r0 * r0 + r1 * r1;
    float s1 = (a1[0] + a1[1]) + (a1[2] + a1[3]), s2 = (a2q[0] + a2q[1]) + (a2q[2] + a2q[3]);
    { const float t = swadd16(s1, s2); const f32x2 a2 = sw32(t); s1 = a2.x + a2.y; }
    { const u32x2 sv = __builtin_bit_cast(u32x2, sw16(s1)); __builtin_amdgcn_raw_buffer_store_b64(u32x2{sv.x, filled(sv.y)}, rs, fvo, sto + kh * 16384, 16); }
    WAITVM(2);
  } else {
    if (TD == 1) dma4(wd + ((tile2 * 2 + r2 - 14) * 8 + wave) * 49 * 128, min(lane, 48) * 128, L_JUNK);
    lds_wait2(L, S_W2B, S_WOFA);
    if (CF) { HH_CLEAR(A ? NE : 0, 4 * NCL); }
    WIN2();
    if (W2F >= 3) { lds_wait(L, S_WOFB); if (W2F < 5 || A) GUBLK(A ? 3 : 4); }
    if (!CF) { HH_CLEAR(A ? NE : 0, 4 * NCL); }
    DRAIN();
    if (!CE2) HH_FLAG();
    if (CE2 && A) __builtin_amdgcn_s_sleep(DLY5);
  }
  if constexpr (A && CSL) CS_OUT(2);
  lds_signal(L, A ? S_HFA : S_HFB, w2);
  STAMP(16);
  u32x4 wdf[NWD][8];
  const __amdgpu_buffer_rsrc_t rd = __builtin_amdgcn_make_buffer_rsrc((void*)(wd + w2 * 448 * 896 + min(cg, 6) * 128 - 1024), 0, 448 * 896 + 1024, 0x00020000);
  const u32 dvo = (lane >> 3) * 8 * 896 + (((lane & 7) ^ ((lane >> 2) & 4)) * 16);
  u32 da_ = 2 * SLOT + w2 * 7168 + hh * 512 + q4 * 128 + (g & 1) * 32 + p4 * 8 + 64 * (q4 >> 1), db_ = da_ ^ 64, dc_ = da_ + (L_EX - 2 * SLOT - w2 * 6144), dd_ = dc_ ^ 64;
  const u32 po = WS_FHH + ((tile2 * 16 + 4 * w2 + ((lane >> 1) & 3)) * 4 + 2 * tg + (lane & 1)) * 128;
  const u32 ho = WS_HH + ((tile2 * 2 + tg) * 4 + w2) * 57344;
#define WDLOAD(k, bf) dma_wd<k>(rd, dvo, ((bf) + 2) * SLOT + w2 * 7168, L_EX + (bf) * 4096 + w2 * 1024)
#define WDREAD(dst, bf) do { asm volatile("" : "+v"(da_), "+v"(db_), "+v"(dc_), "+v"(dd_)); \
    _Pragma("unroll") for (int i = 0; i < 8; ++i) { LAS char* p_ = L + (i & 1 ? db_ : da_) + (bf) * SLOT + (i >> 1) * 2048; \
      const u32x2 x_ = trread(p_), y_ = trread(i < 6 ? p_ + 1024 : L + (i & 1 ? dd_ : dc_) + (bf) * 4096); dst[i] = u32x4{x_.x, x_.y, y_.x, y_.y}; } } while (0)
#define THR() asm volatile("s_waitcnt vmcnt(%0)" :: "n"(THRN) : "memory")
#define THR0() asm volatile("s_waitcnt vmcnt(%0)" :: "n"(THRN0) : "memory")
  u32 have = 0;
#define PLOAD() peek(ws, po, lane < 8, tag + 7)
#define PMIN(pv) do { const u64 d = (pv) - tag; have = max(have, min8(d <= 8 ? (u32)d : 0u)); } while (0)
#define NEEDP(n) do { _Pragma("nounroll") for (u32 sp = 0; have < (n) && sp < SPIN_CAP; ++sp) { PMIN(PLOAD()); if (PSLP && have < (n)) __builtin_amdgcn_s_sleep(PSLP); } \
    __builtin_amdgcn_fence(__ATOMIC_ACQUIRE, "wavefront"); } while (0)
#define HBASE() u32 hb3 = w2 * 7168 + lane * 16, hb0 = hb3 + 2 * SLOT, hb2 = hb0 + 2 * SLOT, hb7 = L_EX + w2 * 1024 + lane * 16; asm volatile("" : "+v"(hb0), "+v"(hb2), "+v"(hb7))
#define HFRAG(bf, i) (*(LAS u32x4*)(L + ((i) < 7 ? ((bf) == 3 ? hb3 : (bf) == 2 ? hb2 : hb0 + (bf) * SLOT) + (i) * 1024 : hb7 + (bf) * 4096)))
#define HQLOAD(q, j) _Pragma("unroll") for (int i = 0; i < 8; ++i) hq[q][i] = __builtin_bit_cast(u32x4, __builtin_amdgcn_raw_buffer_load_b128(rs, lane * 16 + (i & 3) * 1024, ho + (j) * 8192 + (i & 4) * 1024, 16))
#define HQLOADT(q, j) _Pragma("unroll") for (int i = 0; i < 8; ++i) { THR(); hq[q][i] = __builtin_bit_cast(u32x4, __builtin_amdgcn_raw_buffer_load_b128(rs, lane * 16 + (i & 3) * 1024, ho + (j) * 8192 + (i & 4) * 1024, 16)); }
#define DOWN16(ac, wf, hf_) _Pragma("unroll") for (int m = 0; m < 16; ++m) ac[(m >> 1) & 1][m & 1] = mfma32(wf[2 * (m >> 2) + (m & 1)], hf_[2 * (m >> 2) + ((m >> 1) & 1)], ac[(m >> 1) & 1][m & 1])
#define PARTIAL_OUT(ac, b0, b1) _Pragma("unroll") for (int n = 0; n < 16; ++n) { const int T = n >> 3, k = (n >> 2) & 1, q = n & 3; \
    *(LAS f32x4*)&HFRAG(T ? b1 : b0, n & 7) = f32x4{ac[T][k][4 * q], ac[T][k][4 * q + 1], ac[T][k][4 * q + 2], ac[T][k][4 * q + 3]}; }
#define DOWN_OUT(ac, wf, hf_, b0, b1) _Pragma("unroll") for (int m = 0; m < 16 + OLAG; ++m) { \
    if (m < 16) ac[m >> 3][(m >> 2) & 1] = mfma32(wf[2 * (m & 3) + ((m >> 2) & 1)], hf_[2 * (m & 3) + (m >> 3)], ac[m >> 3][(m >> 2) & 1]); \
    SCHED(); \
    if (m >= OLAG) { const int n = m - OLAG, T = n >> 3, k = (n >> 2) & 1, q = n & 3; \
      *(LAS f32x4*)&HFRAG(T ? b1 : b0, n & 7) = f32x4{ac[T][k][4 * q], ac[T][k][4 * q + 1], ac[T][k][4 * q + 2], ac[T][k][4 * q + 3]}; } \
    SCHED(); }
#define FREE(f) do { _Pragma("nounroll") for (u32 sp = 0; sp < (SPIN_CAP << 4); ++sp) { const u32x4 fr = *(volatile LAS u32x4*)(L + L_FLG + S_DONE4 * 16); \
          if (__builtin_amdgcn_readfirstlane(min(min(fr.x, fr.y), min(fr.z, fr.w))) >= (f)) break; __builtin_amdgcn_s_sleep(BSL); } asm volatile("" ::: "memory"); } while (0)
#define HQBAD(q) (!__all(max(max(max(hq[q][0].w, hq[q][1].w), max(hq[q][2].w, hq[q][3].w)), max(max(hq[q][4].w, hq[q][5].w), max(hq[q][6].w, hq[q][7].w))) != EMPTY))
#define HQCHECK(q, j) do { if (__builtin_expect(HQBAD(q), 0)) { _Pragma("nounroll") for (u32 sp = 0; sp < SPIN_CAP; ++sp) { \
      __builtin_amdgcn_s_sleep(RSL); asm volatile("" ::: "memory"); HQLOAD(q, j); if (!HQBAD(q)) break; } } SCHED(); } while (0)
  if constexpr (A) {
    const int T32 = tile * 4 + wave;
    u32x2 st[7];
    u32x4 hb[28];
    u64 fcl;
    const u32 stv = (7 * hh * 2048 + (lane & 31)) * 8;
#define STA(c_) _Pragma("unroll") for (int i = 0; i < 7; ++i) asm volatile("global_load_dwordx2 %0, %1, %2 sc1" : c_(st[i]) : "v"(stv), "s"(ws + WS_ST + (i * 2048 + T32 * 32) * 8) : "memory")
#define STW(k_) asm volatile("s_waitcnt vmcnt(%7)" : "+v"(st[0]), "+v"(st[1]), "+v"(st[2]), "+v"(st[3]), "+v"(st[4]), "+v"(st[5]), "+v"(st[6]) : "n"(k_) : "memory")
#define STBAD() (!__all(max(max(max(st[0].y, st[1].y), max(st[2].y, st[3].y)), max(max(st[4].y, st[5].y), st[6].y)) != EMPTY))
#define HBA(c_, q) _Pragma("unroll") for (int i = 0; i < 4; ++i) asm volatile("global_load_dwordx4 %0, %1, %2 offset:%3 sc1" : c_(hb[4 * (q) + i]) : "v"(lane * 16), "s"(ws + WS_H + (T32 * 28 + 4 * (q)) * 1024), "n"(i * 1024) : "memory")
    lds_wait2(L, S_HFA, S_HFB);
    if (CLW == 1) {
#pragma unroll
      for (int i = NCL; i < 7; ++i) ST16(rs, empty4, lane * 16, hho + i * 8192);
    }
    __builtin_amdgcn_s_sleep(DLY3);
    if (!SPEC || r2 >= 14) {
#pragma nounroll
      for (u32 sp = 0; sp < SPIN_CAP; ++sp) { STA("=v"); STW(0); if (!STBAD()) break; __builtin_amdgcn_s_sleep(RSL); }
    }
    STA("=v");
    STAMP(17);
#pragma unroll
    for (int q = 0; q < HLA / 4; ++q) { HBA("=v", q); }
    u32 wb_[3] = {(u32)lane * 16, (u32)lane * 16 + 2 * SLOT, (u32)lane * 16 + 4 * SLOT};
    asm volatile("" : "+v"(wb_[1]), "+v"(wb_[2]));
#define WFRAG(sl, s) (*(LAS u32x4*)(L + wb_[(sl) >> 1] + ((sl) & 1) * SLOT + (s) * 1024))
    LAS char* csl = L + L_CS + hh * 16;
    u32x4 ring[7];
    f32x4 cq[4];
    float nmu, ve, k0, k1;
    u32 mx;
    const int pvo = WS_FHH + ((tile * 16 + r) * 4 + wave) * 128;
#define PROG(n) FLAG(pvo + 131072, n)
    f32x16 acc[3];
    float y[8], gu[8];
    u32x4 yb;
    u32x4 cw;
#define LT(i)
#define CQ(j_, k_) do { const f32x4 c_ = *(LAS f32x4*)(csl + (j_) * 128 + (k_) * 32); _Pragma("unroll") for (int i_ = 0; i_ < 4; ++i_) acc[(j_) % 3][4 * (k_) + i_] = c_[i_]; } while (0)
#define HB1(i_) asm volatile("global_load_dwordx4 %0, %1, %2 offset:%3 sc1" : "=v"(hb[i_]) : "v"(lane * 16), "s"(ws + WS_H + (T32 * 28 + ((i_) & ~3)) * 1024), "n"(((i_) & 3) * 1024) : "memory")
#define FCL() asm volatile("global_load_dwordx2 %0, %1, %2 sc1" : "=v"(fcl) : "v"(p4 * 128), "s"(ws + WS_FH + (T32 * 32 + 28) * 128) : "memory")
    auto gu01 = [&](auto LC) LAM {
      constexpr bool LD = LC.value;
      sfor<0, 56>([&](auto NC) LAM {
        constexpr int n = NC.value, s = n >> 1, m = n + 7;
        if constexpr (LD && !(n & 1)) { u32x4 t_ = hb[s]; asm volatile("s_waitcnt vmcnt(%1)" : "+v"(t_) : "n"((HLA + s < 29 ? HLA + s : 29) - s - 1 + (CLW == 2 && n > CN ? (n - CN < 7 - NCL ? n - CN : 7 - NCL) : 0)) : "memory"); hb[s] = t_; }
        acc[n & 1] = mfma32(ring[n % 7], hb[s], acc[n & 1]);
        SCHED();
        if constexpr (LD && (n & 1) && HLA + s < 28) HB1(HLA + s);
        if constexpr (LD && (n & 1) && HLA + s == 28) FCL();
        if constexpr (CLW == 2 && LD && n >= CN && n < CN + 7 - NCL) ST16(rs, empty4, lane * 16, hho + (n - CN + NCL) * 8192);
        if constexpr (n & 1) mx = max(mx, hb[s].w);
        if constexpr (m < 56) ring[n % 7] = WFRAG(m & 1, m >> 1); else ring[n % 7] = WFRAG(2, m - 56);
        if constexpr (n >= 34 && n < 38) CQ(2, n - 34);
        if constexpr (n >= 44 && n < 52) { acc[2][2 * (n - 44)] *= nmu; acc[2][2 * (n - 44) + 1] *= nmu; PIN(acc[2]); }
        SCHED();
      });
      STAMP(18);
    };
    auto gu_block = [&](auto JC) __attribute__((always_inline)) {
      constexpr int j = JC.value, NE_ = j == 2 ? 16 : 8;
      sfor<0, 28>([&](auto SC) LAM {
        constexpr int s = SC.value;
        if constexpr (PACE > 0 && j >= PJ0 && j <= PJ1 && s % PS == 0) __builtin_amdgcn_s_sleep(PACE);
        if constexpr (HW && s % 2 == HW - 1 && (s < 27 || j < 6)) { asm volatile("" :: "v"(ring[(s + 1) % 7])); SCHED(); }
        acc[j % 3] = mfma32(ring[s % 7], hb[s], acc[j % 3]);
        constexpr int sg = j == 3 ? S_WIN3 : j == 4 ? S_REFILL : S_FREE01;
        if constexpr (j >= 3 && j < 6 && s == 12) cw = *(volatile LAS u32x4*)(L + L_FLG + sg * 16);
        if constexpr (j >= 3 && j < 6 && s == 20) { if (__builtin_expect(!__builtin_amdgcn_readfirstlane(cw.x & cw.y & cw.z & cw.w), 0)) lds_wait(L, sg); asm volatile("" ::: "memory"); }
        if constexpr (!CLW && j == 2 && s < 7 - NCL) ST16(rs, (u32x4{EMPTY, EMPTY, EMPTY, EMPTY}), lane * 16, hho + (NCL + s) * 8192);
        if constexpr (s + 7 < 28) ring[s % 7] = WFRAG(gu_slot(j), s + 7);
        else if constexpr (j < 6) ring[s % 7] = WFRAG(gu_slot(j + 1), s - 21);
        if constexpr (s == 0 && j == 2) lds_signal(L, S_WSEEN, wave);
        if constexpr (s == 0 && j >= 3) *(volatile LAS u32*)(L + L_FLG + S_DONE4 * 16 + wave * 4) = j - 2u;
        sfor<0, NE_>([&](auto EC) LAM {
          constexpr int e = EC.value, i = e & 7, k = j == 2 ? s - e - e / 4 : s - 1 - 2 * e, a = j == 2 ? e >> 3 : (j - 1) % 3;
          const float ga = acc[a][i];
          if constexpr (k == 0) y[i] = ga * ga;
          if constexpr (k == 1) y[i] = fmaf(y[i], k1, k0);
          if constexpr (k == 2) y[i] = ga * y[i];
          if constexpr (k == 3) y[i] = fexp2(y[i]);
          if constexpr (k == 4) y[i] = fmaf(y[i], ve, ve);
          if constexpr (k == 5) { gu[i] = ga * acc[a][i + 8]; PIN(gu[i]); }
          if constexpr (k == 6) y[i] = frcp(y[i]);
          if constexpr (k == 7) y[i] = y[i] * gu[i];
          if constexpr (k >= 0 && k < 8 && k != 5) PIN(y[i]);
          if constexpr (j == 2 && k == 8 && (e & 1)) { u32 p_ = pk(y[i - 1], y[i]); if (i == 7) p_ = filled(p_); PIN(p_); yb[i >> 1] = p_; }
          if constexpr (CLW == 2 && j == 2 && k == 8 && e == 7) WAITVM(0);
          if constexpr (j == 2 && k == 8 && i == 7) ST16(rs, yb, lane * 16, hho + a * 8192);
          if constexpr (j == 2 && k == 9 && i == 7 && (a == 0 || !FLG13)) FLAG(pvo, a + 1);
        });
        if constexpr (j > 2) {
          if constexpr (s == 21) { yb[0] = pk(y[0], y[1]); yb[1] = pk(y[2], y[3]); }
          if constexpr (s == 22) yb[2] = pk(y[4], y[5]);
          if constexpr (!CLW && j == 3 && s == 22) { if (FLG13) WAITVM(3); else WAITVM(4); }
          if constexpr (s == 23) { yb[3] = filled(pk(y[6], y[7])); ST16(rs, yb, lane * 16, hho + (j - 1) * 8192); }
          if constexpr (!CLW && s == 24 && (j == 3 || (j == 4 && !FLG13))) FLAG(pvo, j);
        }
        if constexpr (j < 6 && s >= 17 && s < 21) CQ(j + 1, s - 17);
        if constexpr (j < 6 && s >= 22 && s < 27) {
          sfor<0, 16>([&](auto EC) LAM {
            constexpr int e = EC.value;
            if constexpr ((e < 2 ? 22 : e < 4 ? 23 : 23 + (e >> 2)) == s) acc[(j + 1) % 3][e] *= nmu;
          });
          PIN(acc[(j + 1) % 3]);
        }
        SCHED();
      });
      if (j == 3) STAMP(26);
      if (j == 2) STAMP(29);
      if (j == 4) STAMP(30);
      if (j == 5) STAMP(31);
    };
#define HBW(q) asm volatile("s_waitcnt vmcnt(0)" : "+v"(hb[4 * (q)]), "+v"(hb[4 * (q) + 1]), "+v"(hb[4 * (q) + 2]), "+v"(hb[4 * (q) + 3]) :: "memory")
#define HBBAD(q) (!__all(max(max(hb[4 * (q)].w, hb[4 * (q) + 1].w), max(hb[4 * (q) + 2].w, hb[4 * (q) + 3].w)) != EMPTY))
#define HBC(q, c_) asm volatile("s_bitcmp0_b32 %6, %7\n\ts_cbranch_scc1 1f\n\tglobal_load_dwordx4 %0, %4, %5 sc1\n\tglobal_load_dwordx4 %1, %4, %5 offset:1024 sc1\n\t" \
      "global_load_dwordx4 %2, %4, %5 offset:2048 sc1\n\tglobal_load_dwordx4 %3, %4, %5 offset:3072 sc1\n1:" : "+v"(hb[4 * (q)]), "+v"(hb[4 * (q) + 1]), "+v"(hb[4 * (q) + 2]), "+v"(hb[4 * (q) + 3]) \
      : "v"(lane * 16), "s"(ws + WS_H + (T32 * 28 + 4 * (q)) * 1024), "s"(c_), "n"(q) : "memory", "scc")
#define REDO(m_) do { STA("+v"); _Pragma("unroll") for (int q = 0; q < 7; ++q) HBC(q, m_); STW(0); _Pragma("unroll") for (int q = 0; q < 7; ++q) HBW(q); } while (0)
#define FIX() do { _Pragma("nounroll") for (u32 sp = 0; sp < SPIN_CAP; ++sp) { u32 m = 0; \
        _Pragma("unroll") for (int q = 0; q < 7; ++q) m |= HBBAD(q) ? 1u << q : 0u; \
        if (!m && !STBAD()) break; if (sp) __builtin_amdgcn_s_sleep(RSL); m = __builtin_amdgcn_readfirstlane(m); REDO(m); } } while (0)
    auto first0 = [&]() LAM {
#pragma unroll
      for (int k = 0; k < 8; ++k) { const f32x4 c_ = *(LAS f32x4*)(csl + k * 32); acc[k >> 2][4 * (k & 3)] = c_[0]; acc[k >> 2][4 * (k & 3) + 1] = c_[1]; acc[k >> 2][4 * (k & 3) + 2] = c_[2]; acc[k >> 2][4 * (k & 3) + 3] = c_[3]; }
#pragma unroll
      for (int s = 0; s < 7; ++s) ring[s] = WFRAG(s & 1, s >> 1);
    };
    auto first = [&](auto LC) LAM {
      if (!LC.value || !PRE) first0();
      f32x2 sf[7];
#pragma unroll
      for (int i = 0; i < 7; ++i) sf[i] = __builtin_bit_cast(f32x2, st[i]);
      float s1 = ((sf[0].x + sf[1].x) + (sf[2].x + sf[3].x)) + ((sf[4].x + sf[5].x) + sf[6].x);
      float s2 = ((sf[0].y + sf[1].y) + (sf[2].y + sf[3].y)) + ((sf[4].y + sf[5].y) + sf[6].y);
      { f32x2 a = sw32(s1); s1 = a.x + a.y; a = sw32(s2); s2 = a.x + a.y; }
      nmu = s1 * (-1.f / 448.f); ve = fmaxf(s2 * (1.f / 448.f) - nmu * nmu, 0.f) + 1e-5f;
      const float rstd = __builtin_amdgcn_rsqf(ve);
      k0 = -2.30220819f * rstd; k1 = -0.10294324f * rstd * rstd * rstd;
#pragma unroll
      for (int i = 0; i < 32; ++i) acc[i >> 4][i & 15] *= nmu;
      mx = 0;
      __builtin_amdgcn_s_setprio(GUPRIO);
      SCHED();
      gu01(LC);
    };
    if (PRE == 1) { first0(); SCHED(); }
    STW(HLA);
    if (__builtin_expect(STBAD(), 0)) {
#pragma unroll
      for (int q = 0; q < HLA / 4; ++q) HBW(q);
#pragma nounroll
      for (u32 sp = 0; sp < SPIN_CAP; ++sp) {
        u32 m = 0;
#pragma unroll
        for (int i = 0; i < 7; ++i) { const u64 bm = __builtin_amdgcn_ballot_w64(st[i].y == EMPTY); m |= ((u32)bm ? 1u << (i >> 1) : 0u) | ((u32)(bm >> 32) ? 1u << ((7 + i) >> 1) : 0u); }
        m = __builtin_amdgcn_readfirstlane(m);
        if (!m) break;
        if (sp) __builtin_amdgcn_s_sleep(RSL);
        STA("+v");
#pragma unroll
        for (int q = 0; q < HLA / 4; ++q) HBC(q, m);
        STW(0);
#pragma unroll
        for (int q = 0; q < HLA / 4; ++q) HBW(q);
      }
    }
    first(IC<1>{});
    if (__builtin_expect(!__all(mx != EMPTY), 0)) { FIX(); first(IC<0>{}); }
    asm volatile("s_waitcnt vmcnt(%1)" : "+v"(fcl) : "n"(CLW == 2 ? 7 - NCL : 0) : "memory");
    if (__builtin_expect(!__all(fcl == tag + 1), 0)) poll_flags(ws, WS_FH + (T32 * 32 + 28 + p4) * 128, lane < 4, tag);
    gu_block(IC<2>{}); gu_block(IC<3>{}); gu_block(IC<4>{}); gu_block(IC<5>{}); gu_block(IC<6>{});
#pragma unroll
    for (int i = 0; i < 8; ++i) { const float ga = acc[0][i]; y[i] = ga * acc[0][i + 8] * frcp(fmaf(fexp2(ga * fmaf(ga * ga, k1, k0)), ve, ve)); }
    yb = u32x4{pk(y[0], y[1]), pk(y[2], y[3]), pk(y[4], y[5]), filled(pk(y[6], y[7]))};
    ST16(rs, yb, lane * 16, hho + 6 * 8192);
    *(volatile LAS u32*)(L + L_FLG + S_DONE4 * 16 + wave * 4) = 5u;
    __builtin_amdgcn_s_setprio(0);
    STAMP(19);
    if (r2 < 14) {
      f32x16 da[2][2] = {};
      u32x4 hq[2][8], wl[2][8];
      HBASE();
      __builtin_amdgcn_s_setprio(ATP);
      lds_wait1(L, S_SPARE, w2);
      STAMP2(15);
      HQLOAD(0, 4);
      WDREAD(wl[0], 1);
      SCHED();
      if (A6L == 0) { __builtin_amdgcn_s_sleep(ADLY); HQLOAD(1, 6); }
      STAMP2(16);
      HQCHECK(0, 4);
      if (A6L == 1) { __builtin_amdgcn_s_sleep(ADLY); HQLOAD(1, 6); }
      SCHED();
      DOWN16(da, wl[0], hq[0]);
      SCHED();
      if (A6L == 2) { __builtin_amdgcn_s_sleep(ADLY); HQLOAD(1, 6); }
      lds_wait1(L, S_CONS, w2);
      WDREAD(wl[1], 0);
      STAMP2(18);
      HQCHECK(1, 6);
      STAMP2(31);
      DOWN_OUT(da, wl[1], hq[1], 0, 1);
      STAMP(20);
    }
  } else {
    lds_wait2(L, S_HFA, S_HFB);
    if (TD == 6 && w2 == 0) DTOUCH();
    if (TD == 7) dma4(wd + min(b * 49 + w2 * 13, 12544 - 13) * 128, min(lane, 12) * 128, L_JUNK);
    __builtin_amdgcn_s_setprio(BP3);
    if (r2 >= 14) GUBLK(4);
#define REFILL() do { lds_wait<BSL>(L, S_WSEEN); GUBLK(5); GUBLK(6); } while (0)
    if (r2 >= 14) {
      WAITVM(0); lds_signal(L, S_WIN3, w2);
      REFILL();
      WAITVM(0); lds_signal(L, S_REFILL, w2); lds_signal(L, S_FREE01, w2);
    } else {
      u64 pv;
      const char* hs = ws + ho + 4096;
#define PEEK() do { if (AP) { pv = tag + 7; asm volatile("s_mov_b64 exec, 0xff\n\tglobal_atomic_add_x2 %0, %1, %2, %3 sc0\n\ts_mov_b64 exec, -1" : "+v"(pv) : "v"(po), "v"(0ull), "s"(ws) : "memory"); } \
        else asm volatile("global_load_dwordx2 %0, %1, %2 sc1" : "=v"(pv) : "v"(po), "s"(ws) : "memory"); } while (0)
#define PEEKED(k_, n_) do { asm volatile("s_waitcnt vmcnt(%1)" : "+v"(pv) : "n"(k_) : "memory"); PMIN(pv); NEEDP(n_); } while (0)
#define HQA(c_, q, j) _Pragma("unroll") for (int i = 0; i < 8; ++i) asm volatile("global_load_dwordx4 %0, %1, %2 offset:%3 sc1" : c_(hq[q][i]) : "v"(lane * 16), "s"(hs + (j) * 8192), "n"(i * 1024 - 4096) : "memory")
#define HQW(q, k_) asm volatile("s_waitcnt vmcnt(%8)" : "+v"(hq[q][0]), "+v"(hq[q][1]), "+v"(hq[q][2]), "+v"(hq[q][3]), "+v"(hq[q][4]), "+v"(hq[q][5]), "+v"(hq[q][6]), "+v"(hq[q][7]) : "n"(k_) : "memory")
#define HQOK(q, j, k_) do { HQW(q, k_); if (__builtin_expect(HQBAD(q), 0)) { _Pragma("nounroll") for (u32 sp = 0; sp < SPIN_CAP; ++sp) { \
        __builtin_amdgcn_s_sleep(RSL); HQA("+v", q, j); HQW(q, 0); if (!HQBAD(q)) break; } } SCHED(); } while (0)
      {
        u32x2 e[24][2];
        const u32 ea_ = 0, eb_ = 0;
        asm volatile("" : "+v"(da_), "+v"(db_), "+v"(dc_), "+v"(dd_));
#define WBL(N0, N1, NS, SL) sfor<N0, N1>([&](auto NC) LAM { constexpr int n = NC.value, k = n - (N0); \
          if constexpr (k < (NS)) dma_wd1<n, (SL) * SLOT + k * 1024>(rd, dvo, w2 * 7168); else dma_wd1<n, L_EX + (k - (NS)) * 4096>(rd, dvo, w2 * 1024); })
#define WBR(N0, N1, NS, SL) do { sfor<N0, N1>([&](auto NC) LAM { constexpr int n = NC.value, k = n - (N0); \
          if constexpr (k >= (NS)) { e[n][0] = trread(L + dc_ + (k - (NS)) * 4096); e[n][1] = trread(L + dd_ + (k - (NS)) * 4096); } \
          else if constexpr ((SL) == 4) { e[n][0] = trread(L + da_ + 2 * SLOT + k * 1024); e[n][1] = trread(L + db_ + 2 * SLOT + k * 1024); } \
          else { e[n][0] = trread(L + ea_ + k * 1024); e[n][1] = trread(L + eb_ + k * 1024); } }); \
          asm volatile("s_waitcnt lgkmcnt(0)" : "+v"(e[(N1) - 1][1]) :: "memory"); } while (0)
        WBL(0, 11, 7, 4);
        WAITVM(0);
        STAMP2(24);
        WBR(0, 11, 7, 4);
        WBL(11, 15, 0, 0);
        WAITVM(0);
        STAMP2(25);
        WBR(11, 15, 0, 0);
        WBL(15, 19, 0, 0);
        GUBLK(4);
        WAITVM(7);
        WBR(15, 19, 0, 0);
        WBL(19, 23, 0, 0);
        STAMP2(27);
        lds_wait<BSL>(L, S_WSEEN);
        GUBLK(5);
        GUBLK(6);
        WAITVM(14);
        lds_signal(L, S_WIN3, w2);
        STAMP2(26);
        WBR(19, 23, 0, 0);
        dma_wd1<23, L_EX + 3 * 4096>(rd, dvo, w2 * 1024);
        FREE(1);
        PEEK();
        WDLOAD(3, 0);
        WAITVM(17);
        lds_signal(L, S_REFILL, w2);
        STAMP2(5);
        PEEKED(8, 0);
        lds_signal(L, S_FREE01, w2);
        e[23][0] = trread(L + dc_ + 3 * 4096); e[23][1] = trread(L + dd_ + 3 * 4096);
#pragma unroll
        for (int n = 0; n < 24; ++n) wdf[n >> 3][n & 7] = u32x4{e[n & ~1][n & 1].x, e[n & ~1][n & 1].y, e[n | 1][n & 1].x, e[n | 1][n & 1].y};
      }
      STAMP2(23);
      f32x16 acc[2][2];
      u32x4 hq[2][8];
      STAMP2(16);
      HBASE();
#pragma unroll
      for (int k = 0; k < 3; ++k) asm volatile("" : "+v"(wdf[k][0]), "+v"(wdf[k][1]), "+v"(wdf[k][2]), "+v"(wdf[k][3]), "+v"(wdf[k][4]), "+v"(wdf[k][5]), "+v"(wdf[k][6]), "+v"(wdf[k][7]) :: "memory");
      const bool w4e = have < 1;
      if (w4e) { FREE(2); WDLOAD(4, 1); }
      NEEDP(1);
      STAMP2(1);
      HQA("=v", 0, 0);
      if (!w4e && W4O == 0) { FREE(2); WDLOAD(4, 1); }
      STAMP2(2);
      HQA("=v", 1, 1);
      if (W4O == 1) { if (!w4e) { FREE(2); WDLOAD(4, 1); } else WAITVM(8); }
      FREE(3);
      auto round = [&](auto JC) LAM {
        constexpr int J = JC.value, q = J & 1, w = J % 3, nb = 2 - 2 * J;
        __builtin_amdgcn_s_setprio(J < 2 ? DP0 : DNPRIO);
        sfor<0, 18>([&](auto MC) LAM {
          constexpr int m = MC.value, i = m - 8 - (m > 13 ? 2 : 0);
          if constexpr (m < 16) macc<J == 0 && m < 4 ? 0 : 1>(acc[(m >> 1) & 1][m & 1], wdf[w][2 * (m >> 2) + (m & 1)], hq[q][2 * (m >> 2) + ((m >> 1) & 1)]);
          SCHED();
          if constexpr (J < 2 && m < 7) dma_wd1<(J + 5) * 8 + m, (nb + 2) * SLOT + m * 1024>(rd, dvo, w2 * 7168);
          if constexpr (J < 2 && m == 7) dma_wd1<(J + 5) * 8 + 7, L_EX + nb * 4096>(rd, dvo, w2 * 1024);
          if constexpr (!CLW && J == 0 && m == 7) PEEK();
          if constexpr (J == 1 && m == 7) { if (CLW) WAITVM(16); else PEEKED(16, 3); lds_signal(L, S_SPARE, w2); FREE(4); }
          if constexpr (J == 2 && m == 7) { WAITVM(8); lds_signal(L, S_CONS, w2); FREE(5); if (B5D) __builtin_amdgcn_s_sleep(B5D); }
          if constexpr (J < 3 && m >= 8 && m != 14 && m != 15) {
            asm volatile("global_load_dwordx4 %0, %1, %2 offset:%3 sc1" : "=v"(hq[q][i]) : "v"(lane * 16), "s"(hs + (J == 2 ? 5 : J + 2) * 8192), "n"(i * 1024 - 4096) : "memory");
            if constexpr (J < 2) {
              LAS char* p_ = L + (i & 1 ? db_ : da_) + 2 * J * SLOT + (i >> 1) * 2048;
              const u32x2 x_ = trread(p_), y_ = trread(i < 6 ? p_ + 1024 : L + (i & 1 ? dd_ : dc_) + 2 * J * 4096);
              wdf[J][i] = u32x4{x_.x, x_.y, y_.x, y_.y};
            }
          }
          SCHED();
        });
        if (J < 2) __builtin_amdgcn_s_setprio(BP3);
      };
      HQOK(0, 0, W4O ? 16 : 8);
      round(IC<0>{});
      STAMP2(8);
      HQOK(1, 1, CLW ? 16 : 17);
      asm volatile("s_waitcnt lgkmcnt(0)" : "+v"(wdf[0][7]) :: "memory");
      round(IC<1>{});
      STAMP2(9);
      HQOK(0, 2, 16);
      round(IC<2>{});
      STAMP2(10);
      HQOK(1, 3, 8);
      round(IC<3>{});
      STAMP2(11);
      HQOK(0, 5, 0);
      STAMP2(4);
      DOWN_OUT(acc, wdf[1], hq[0], 2, 3);
      STAMP(20);
    }
  }
  if (r2 < 14) {
    BAR();
    STAMP2(7);
    float c[8] = {blo(hres.x), bhi(hres.x), blo(hres.y), bhi(hres.y), blo(hres.z), bhi(hres.z), blo(hres.w), bhi(hres.w)};
    const int rl = ((tb & 1) * 16 + l15) * 16;
    f32x4 v0 = {c[0], c[1], c[2], c[3]}, v1 = {c[4], c[5], c[6], c[7]};
#pragma unroll
    for (int h = 0; h < 2; ++h) {
      const int bf = (tb >> 1) + 2 * h;
      const bool p7 = kh && g == 3;
      u32 rq = (p7 ? L_EX + bf * 4096 : (bf == 3 ? 0 : (bf + 2) * SLOT) + (4 * kh + g) * 1024) + rl;
      const u32 st7 = p7 ? 1024 : 7168;
      asm volatile("" : "+v"(rq));
#pragma unroll
      for (int w = 0; w < 4; ++w) { v0 += *(LAS f32x4*)(L + rq + w * st7); v1 += *(LAS f32x4*)(L + rq + w * st7 + 512); }
    }
#pragma unroll
    for (int i = 0; i < 4; ++i) { c[i] = v0[i]; c[4 + i] = v1[i]; }
    { const u32x4 ov = {pk(c[0], c[1]), pk(c[2], c[3]), pk(c[4], c[5]), pk(c[6], c[7])}; asm volatile("global_store_dwordx4 %0, %1, %2 sc1\n\ts_nop 1" :: "v"(xo), "v"(ov), "s"(out) : "memory"); }
    if (A && b == 0 && tid == 0) __hip_atomic_store((GAS u64*)(ws + WS_E), tag, RLX_AGENT);
  }
  STAMP(21);
  };
  if (wave < 4) role(IC<1>{}); else role(IC<0>{});
}

extern "C" void kernel_launch(void* const* d_in, const int* in_sizes, int n_in, void* d_out, int out_size, void* d_ws, size_t ws_size, hipStream_t stream) {
  (void)hipFuncSetAttribute((const void*)fused_block, hipFuncAttributeMaxDynamicSharedMemorySize, LDS_BYTES);
  fused_block<<<256, 512, LDS_BYTES, stream>>>((const char*)d_in[0], (const char*)d_in[1], (const char*)d_in[2], (const char*)d_in[3], (const char*)d_in[5], (const char*)d_in[6],
                                               (const char*)d_in[7], (char*)d_out, (char*)d_ws);
}
```

```cpp
#include <hip/hip_runtime.h>
#include <hip/hip_bf16.h>
#include <stdint.h>

typedef unsigned u32;
typedef unsigned long long u64;
typedef u32 u32x4 __attribute__((ext_vector_type(4)));
typedef u32 u32x2 __attribute__((ext_vector_type(2)));
typedef float f32x2 __attribute__((ext_vector_type(2)));
typedef float f32x4 __attribute__((ext_vector_type(4)));
typedef float f32x16 __attribute__((ext_vector_type(16)));
typedef short s16x4 __attribute__((ext_vector_type(4)));
typedef __bf16 bf16x2 __attribute__((ext_vector_type(2)));
typedef __bf16 bf16x8 __attribute__((ext_vector_type(8)));
#define LAS __attribute__((address_space(3)))
#define GAS __attribute__((address_space(1)))
#define DI __device__ __forceinline__
#define RLX_AGENT __ATOMIC_RELAXED, __HIP_MEMORY_SCOPE_AGENT

constexpr int WS_E = 0;
constexpr int WS_FKV = 8192;
constexpr int WS_FO = WS_FKV + 131072;
constexpr int WS_FH = WS_FO + 131072;
constexpr int WS_FG = WS_FH + 262144;
constexpr int WS_FD = WS_FG + 114688;
constexpr int WS_FHH = WS_FD + 57344;
constexpr int WS_CS = WS_FHH + 262144;
constexpr int WS_ST = WS_CS + 200704;
constexpr int WS_WGU = WS_ST + 229376;
constexpr int WS_WD = WS_WGU + 3211264;
constexpr int WS_KVP = WS_WD + 1605632;
constexpr int WS_O = WS_KVP + 1048576;
constexpr int WS_H = WS_O + 2097152;
constexpr int WS_HH = WS_H + 1835008;
#define UNITX4
#define CS7
#define NOWD
#define WDOFF(cg, fb, bl) (((((cg) * 4 + (fb) / 28) * 7 + (fb) % 7) * 4 + (fb) / 7 % 4) * 2 + (bl))
#define HHOFF(T32, fb) (((((T32) >> 1) * 4 + (fb) / 28) * 7 + (fb) % 7) * 8 + ((fb) / 7 & 3) * 2 + ((T32) & 1))
constexpr int WS_STAMP = WS_HH + 7340032;
constexpr int WS_LOG = WS_STAMP + 256 * 8 * 32 * 8;
constexpr int WS_USED = WS_LOG + 4096;
constexpr int SLOT = 28672;
constexpr int L_WK = 4 * SLOT, L_WV = 0, L_WQ = 14336;
constexpr int L_PKV = 2 * SLOT;
constexpr int L_WO = 3 * SLOT;
constexpr int L_EX = 5 * SLOT;
constexpr int L_KVA = 5 * SLOT;
constexpr int L_ST1 = 5 * SLOT + 4096;
constexpr int L_WPHI = 5 * SLOT + 5120;
constexpr int L_CSP = 5 * SLOT + 7168;
constexpr int L_CS = 159744;
constexpr int L_FLG = 160768;
constexpr int L_JUNK = 161280;
constexpr int L_STAMP = 161536;
constexpr int L_RED = SLOT;
constexpr int LDS_BYTES = 163840;
constexpr int gu_slot(int j) { return j % 5; }
constexpr int CODE_PF = 48384;
constexpr u32 SPIN_CAP = 1u << 16;
#define LL 1
#define AP 1

#define STAMP(i) do {} while (0)
#define STAMP2(i) do {} while (0)
#define STAMP2X(i) do {} while (0)

DI f32x4 mfma16(u32x4 a, u32x4 b, f32x4 c) { return __builtin_amdgcn_mfma_f32_16x16x32_bf16(__builtin_bit_cast(bf16x8, a), __builtin_bit_cast(bf16x8, b), c, 0, 0, 0); }
DI f32x16 mfma32(u32x4 a, u32x4 b, f32x16 c) { return __builtin_amdgcn_mfma_f32_32x32x16_bf16(__builtin_bit_cast(bf16x8, a), __builtin_bit_cast(bf16x8, b), c, 0, 0, 0); }
DI u32 pk(float lo, float hi) { f32x2 p = {lo, hi}; return __builtin_bit_cast(u32, __builtin_convertvector(p, bf16x2)); }
DI float blo(u32 v) { return __builtin_bit_cast(float, v << 16); }
DI float bhi(u32 v) { return __builtin_bit_cast(float, v & 0xffff0000u); }
DI float dot2(u32 a, u32 b, float c) { return __builtin_amdgcn_fdot2_f32_bf16(__builtin_bit_cast(bf16x2, a), __builtin_bit_cast(bf16x2, b), c, false); }
DI u32x2 trread(LAS char* p) { return __builtin_bit_cast(u32x2, __builtin_amdgcn_ds_read_tr16_b64_v4i16((LAS s16x4*)p)); }
DI u32x4 trfrag(LAS char* p, int step) { u32x2 a = trread(p), b = trread(p + step); return u32x4{a.x, a.y, b.x, b.y}; }
DI float fexp2(float x) { return __builtin_amdgcn_exp2f(x); }
DI float frcp(float x) { return __builtin_amdgcn_rcpf(x); }
template <int C> DI float dppror(float x) { return __builtin_bit_cast(float, __builtin_amdgcn_update_dpp(0, __builtin_bit_cast(int, x), 0x120 + C, 0xf, 0xf, false)); }
DI float row_max(float x) { x = fmaxf(x, dppror<8>(x)); x = fmaxf(x, dppror<4>(x)); x = fmaxf(x, dppror<2>(x)); return fmaxf(x, dppror<1>(x)); }
DI float row_min(float x) { x = fminf(x, dppror<8>(x)); x = fminf(x, dppror<4>(x)); x = fminf(x, dppror<2>(x)); return fminf(x, dppror<1>(x)); }
DI float row_sum(float x) { x += dppror<8>(x); x += dppror<4>(x); x += dppror<2>(x); return x + dppror<1>(x); }
DI f32x2 sw16(float x) { u32 u = __builtin_bit_cast(u32, x); auto r = __builtin_amdgcn_permlane16_swap(u, u, false, false); return f32x2{__builtin_bit_cast(float, (u32)r[0]), __builtin_bit_cast(float, (u32)r[1])}; }
DI f32x2 sw32(float x) { u32 u = __builtin_bit_cast(u32, x); auto r = __builtin_amdgcn_permlane32_swap(u, u, false, false); return f32x2{__builtin_bit_cast(float, (u32)r[0]), __builtin_bit_cast(float, (u32)r[1])}; }
DI float grp_sum(float x) { f32x2 a = sw16(x); x = a.x + a.y; a = sw32(x); return a.x + a.y; }
DI float grp_max(float x) { f32x2 a = sw16(x); x = fmaxf(a.x, a.y); a = sw32(x); return fmaxf(a.x, a.y); }
DI float grp_min(float x) { f32x2 a = sw16(x); x = fminf(a.x, a.y); a = sw32(x); return fminf(a.x, a.y); }

#define BAR() asm volatile("s_waitcnt lgkmcnt(0)\n\ts_barrier" ::: "memory")
#define WAITVM(n) asm volatile("s_waitcnt vmcnt(" #n ")" ::: "memory")
#define DRAIN() do { asm volatile("s_waitcnt vmcnt(0)" ::: "memory"); __builtin_amdgcn_s_waitcnt(0x0F70); } while (0)
#define SCHED() __builtin_amdgcn_sched_barrier(0)
DI void dma16(const void* base, u32 voff, u32 lds) {
  u32 keep;
  asm volatile("s_mov_b32 %0, m0\n\ts_mov_b32 m0, %3\n\ts_nop 0\n\tglobal_load_lds_dwordx4 %1, %2\n\ts_mov_b32 m0, %0" : "=&s"(keep) : "v"(voff), "s"(base), "s"(lds) : "memory");
}
DI void dma16c(const void* base, u32 voff, u32 lds) {
  u32 keep;
  asm volatile("s_mov_b32 %0, m0\n\ts_mov_b32 m0, %3\n\ts_nop 0\n\tglobal_load_lds_dwordx4 %1, %2 sc1\n\ts_mov_b32 m0, %0" : "=&s"(keep) : "v"(voff), "s"(base), "s"(lds) : "memory");
}
DI void dma4(const void* base, u32 voff, u32 lds) {
  u32 keep;
  asm volatile("s_mov_b32 %0, m0\n\ts_mov_b32 m0, %3\n\ts_nop 0\n\tglobal_load_lds_dword %1, %2\n\ts_mov_b32 m0, %0" : "=&s"(keep) : "v"(voff), "s"(base), "s"(lds) : "memory");
}
DI void dma16c_if(u32 go, const void* base, u32 voff, u32 lds) {
  u32 keep;
  asm volatile("s_cmp_eq_u32 %4, 0\n\ts_cbranch_scc1 1f\n\ts_mov_b32 %0, m0\n\ts_mov_b32 m0, %3\n\ts_nop 0\n\tglobal_load_lds_dwordx4 %1, %2 sc1\n\ts_mov_b32 m0, %0\n1:"
               : "=&s"(keep) : "v"(voff), "s"(base), "s"(lds), "s"(go) : "memory", "scc");
}
template <int SI, int LI> DI void dma7(__amdgpu_buffer_rsrc_t rs, u32 voff, u32 so, u32 lds) {
  u32 t;
  asm volatile("s_add_u32 m0, %4, %6\n\ts_add_u32 %0, %3, %5\n\tbuffer_load_dwordx4 %1, %2, %0 offen sc1 lds\n\tbuffer_load_dwordx4 %1, %2, %0 offen offset:1024 sc1 lds\n\t"
               "buffer_load_dwordx4 %1, %2, %0 offen offset:2048 sc1 lds\n\tbuffer_load_dwordx4 %1, %2, %0 offen offset:3072 sc1 lds\n\t"
               "s_add_u32 m0, m0, 4096\n\ts_add_u32 %0, %0, 4096\n\tbuffer_load_dwordx4 %1, %2, %0 offen sc1 lds\n\tbuffer_load_dwordx4 %1, %2, %0 offen offset:1024 sc1 lds\n\t"
               "buffer_load_dwordx4 %1, %2, %0 offen offset:2048 sc1 lds"
               : "=&s"(t) : "v"(voff), "s"(rs), "s"(so), "s"(lds), "n"(SI), "n"(LI) : "memory", "scc");
}
constexpr int wrow(int q, int j, int hf) { return (((7 * q + j) >> 2) * 64 + 2 * ((7 * q + j) & 3) + hf) * 896 + 1024; }
#define DL_(n, o) "s_mov_b32 %0, %" #n "\n\tbuffer_load_dwordx4 %1, %2, %0 offen offset:" #o " lds\n\t"
template <int J> DI void dma_wd(__amdgpu_buffer_rsrc_t rs, u32 voff, u32 lds, u32 lds7) {
  u32 t;
  asm volatile("s_mov_b32 m0, %3\n\t" DL_(5, 0) DL_(6, 1024) DL_(7, 2048) DL_(8, 3072) "s_add_u32 m0, m0, 4096\n\t" DL_(9, 0) DL_(10, 1024) DL_(11, 2048) "s_mov_b32 m0, %4\n\t" DL_(12, 0)
               : "=&s"(t) : "v"(voff), "s"(rs), "s"(lds), "s"(lds7), "n"(wrow(0, J, 0)), "n"(wrow(0, J, 1) - 1024), "n"(wrow(1, J, 0) - 2048), "n"(wrow(1, J, 1) - 3072),
                 "n"(wrow(2, J, 0)), "n"(wrow(2, J, 1) - 1024), "n"(wrow(3, J, 0) - 2048), "n"(wrow(3, J, 1)) : "memory", "scc");
}
template <int N, int LO> DI void dma_wd1(__amdgpu_buffer_rsrc_t rs, u32 voff, u32 lds) {
  u32 t;
  asm volatile("s_add_u32 m0, %3, %4\n\t" DL_(5, 0) : "=&s"(t) : "v"(voff), "s"(rs), "s"(lds), "n"(LO), "n"(wrow(N / 2 % 4, N / 8, N % 2)) : "memory", "scc");
}
DI void touch(const void* base, u32 voff) {
  asm volatile("s_mov_b32 m0, %2\n\ts_nop 0\n\tglobal_load_lds_dword %0, %1" :: "v"(voff), "s"(base), "n"(L_JUNK) : "memory");
}
constexpr int xrow(int n) { return ((n / 14) * 16 + (n & 1) * 8) * 896 + ((n % 14) >> 1) * 128 + 2048 - n % 4 * 1024; }
template <int N> DI void dma_x(__amdgpu_buffer_rsrc_t rs, u32 voff, u32 lds) {
  u32 t;
  asm volatile("s_add_u32 m0, %3, %4\n\t" DL_(5, 0) DL_(6, 1024) DL_(7, 2048) DL_(8, 3072)
               : "=&s"(t) : "v"(voff), "s"(rs), "s"(lds), "n"(N * 1024), "n"(xrow(N)), "n"(xrow(N + 1)), "n"(xrow(N + 2)), "n"(xrow(N + 3)) : "memory", "scc");
}
DI u32 vm_count() {
  u32 t;
  asm volatile("s_getreg_b32 %0, hwreg(HW_REG_IB_STS)" : "=s"(t) :: "memory");
  return (t & 15u) | ((t >> 18) & 48u);
}
DI u32 min8(u32 c) {
  c = min(c, (u32)__builtin_amdgcn_update_dpp(0, (int)c, 0x121, 0xf, 0xf, false)); c = min(c, (u32)__builtin_amdgcn_update_dpp(0, (int)c, 0x122, 0xf, 0xf, false));
  c = min(c, (u32)__builtin_amdgcn_update_dpp(0, (int)c, 0x124, 0xf, 0xf, false));
  return __builtin_amdgcn_readfirstlane(min(c, (u32)__builtin_amdgcn_update_dpp(0, (int)c, 0x128, 0xf, 0xf, false)));
}
DI void dma4c(const void* base, u32 voff, u32 lds) {
  u32 keep;
  asm volatile("s_mov_b32 %0, m0\n\ts_mov_b32 m0, %3\n\ts_nop 0\n\tglobal_load_lds_dword %1, %2 sc1\n\ts_mov_b32 m0, %0" : "=&s"(keep) : "v"(voff), "s"(base), "s"(lds) : "memory");
}
#define LD16(rs, off) __builtin_bit_cast(u32x4, __builtin_amdgcn_raw_buffer_load_b128(rs, off, 0, 16))
#define ST16(rs, v, off, soff) do { const u32x4 d_ = __builtin_bit_cast(u32x4, v); __builtin_amdgcn_raw_buffer_store_b128(d_, rs, off, soff, 16); asm volatile("s_nop 1" :: "v"(d_) : "memory"); } while (0)

#define FLAG(off, n) __builtin_amdgcn_raw_buffer_store_b64(__builtin_bit_cast(u32x2, tag + (n)), rs, fvo, off, 16)
#define FLAGC(c, off, n) __builtin_amdgcn_raw_buffer_store_b64(__builtin_bit_cast(u32x2, tag + (n)), rs, (c) ? fvo : NOWHERE, off, 16)
constexpr int NOWHERE = 0x7ffffff0;
constexpr u32 EMPTY = 0xffffffffu;
DI u32 filled(u32 w) { return min(w, EMPTY - 1u); }
DI u64 peek(const char* ws, u32 off, bool on, u64 dflt) {
  u64 z = 0, v = dflt;
  asm volatile("" : "+v"(z));
  if (on) v = __hip_atomic_fetch_add((GAS u64*)(ws + off), z, __ATOMIC_RELAXED, AP == 2 ? __HIP_MEMORY_SCOPE_SYSTEM : __HIP_MEMORY_SCOPE_AGENT);
  return v;
}
DI void poll_flags(const char* ws, u32 off, bool on, u64 tag) {
#pragma nounroll
  for (u32 sp = 0; sp < SPIN_CAP; ++sp) {
    u64 v = tag + 1;
    if (on || !LL) v = __hip_atomic_load((const GAS u64*)(ws + off), RLX_AGENT);
    if (__all(v == tag + 1)) break;
  }
  __builtin_amdgcn_fence(__ATOMIC_ACQUIRE, "wavefront");
}
template <int SL = 1> DI void lds_wait(LAS char* L, int sig) {
#pragma nounroll
  for (u32 sp = 0; sp < (SPIN_CAP << 4); ++sp) {
    const u32x4 v = *(volatile LAS u32x4*)(L + L_FLG + sig * 16);
    if (__builtin_amdgcn_readfirstlane(v.x & v.y & v.z & v.w)) break;
    __builtin_amdgcn_s_sleep(SL);
  }
  asm volatile("" ::: "memory");
}
DI void lds_wait2(LAS char* L, int sig, int sig2) {
#pragma nounroll
  for (u32 sp = 0; sp < (SPIN_CAP << 4); ++sp) {
    const u32x4 v = *(volatile LAS u32x4*)(L + L_FLG + sig * 16) & *(volatile LAS u32x4*)(L + L_FLG + sig2 * 16);
    if (__builtin_amdgcn_readfirstlane(v.x & v.y & v.z & v.w)) break;
    __builtin_amdgcn_s_sleep(1);
  }
  asm volatile("" ::: "memory");
}
DI void lds_wait1(LAS char* L, int sig, int w) {
#pragma nounroll
  for (u32 sp = 0; sp < (SPIN_CAP << 4); ++sp) {
    if (__builtin_amdgcn_readfirstlane(*(volatile LAS u32*)(L + L_FLG + sig * 16 + w * 4))) break;
    __builtin_amdgcn_s_sleep(1);
  }
  asm volatile("" ::: "memory");
}
DI void lds_signal(LAS char* L, int sig, int w) {
  asm volatile("" ::: "memory");
  *(volatile LAS u32*)(L + L_FLG + sig * 16 + w * 4) = 1u;
}

template <int N> struct IC { static constexpr int value = N; };
template <int I, int N, class F> DI void sfor(F&& f) { if constexpr (I < N) { f(IC<I>{}); sfor<I + 1, N>(f); } }
#define LAM __attribute__((always_inline))
#define PIN(v) asm volatile("" : "+v"(v))
constexpr float LOG2E = 1.4426950408889634f;
template <int K> DI void macc(f32x4& c, u32x4 a, u32x4 b) {
  if (K == 0) asm volatile("v_mfma_f32_16x16x32_bf16 %0, %1, %2, 0" : "=&v"(c) : "v"(a), "v"(b));
  else if (K == 1) asm volatile("v_mfma_f32_16x16x32_bf16 %0, %1, %2, %0" : "+v"(c) : "v"(a), "v"(b));
  else c = mfma16(a, b, c);
}
template <int K> DI void macc(f32x16& c, u32x4 a, u32x4 b) {
  if (K == 0) asm volatile("v_mfma_f32_32x32x16_bf16 %0, %1, %2, 0" : "=&v"(c) : "v"(a), "v"(b));
  else if (K == 1) asm volatile("v_mfma_f32_32x32x16_bf16 %0, %1, %2, %0" : "+v"(c) : "v"(a), "v"(b));
  else c = mfma32(a, b, c);
}
template <int GAP, int SIG, int NW, class E, class F> DI void project(LAS char* L, u32 w0, u32 w1, const u32x4 (&xf)[2][14], int tp, int lane, int csl, f32x4 (&t)[2][2], f32x2 (&st)[2], E&& pre, F&& mid) {
  u32 wq = (tp & 1 ? w1 : w0) + (tp >> 1) * (7 * 2048 + GAP), ca = csl + (lane >> 4) * 16, sa = L_ST1 + (tp * 32 + (lane & 15)) * 8, fa = L_FLG + SIG * 16 + (lane & (NW - 1)) * 4;
  u32 cw = ca + tp * 64, fw = L_FLG + SIG * 16 + tp * 4, one = 1;
  u32x4 ones = {0x3f803f80u, 0x3f803f80u, 0x3f803f80u, 0x3f803f80u};
  LAS char *p0 = L + w0, *p1 = L + w1, *pq = L + wq, *pca = L + ca, *psa = L + sa, *pfa = L + fa, *pcw = L + cw, *pfw = L + fw;
  asm volatile("" : "+v"(p0), "+v"(p1), "+v"(pq), "+v"(pca), "+v"(psa), "+v"(pfa), "+v"(pcw), "+v"(pfw), "+v"(ones), "+v"(one));
  pre();
  u32x4 cf[7];
  u32x2 ra[7], rb[7];
  f32x4 cs, cp[4];
  u32 sw;
#define WAT(n) (((n) & 1 ? p1 : p0) + ((n) >> 1) * 2048 + ((n) >= 14 ? GAP : 0))
#pragma unroll
  for (int i = 0; i < 7; ++i) cf[i] = trfrag(pq + i * 2048, 256);
  SCHED();
  sfor<0, 7>([&](auto IC_) LAM {
    constexpr int i = IC_.value;
    macc<i == 0 ? 0 : i == 6 ? 2 : 1>(cs, cf[i], ones);
    SCHED();
    if (i < 6) { ra[i] = trread(WAT(i)); rb[i] = trread(WAT(i) + 256); }
    SCHED();
  });
  sfor<0, 28>([&](auto NC) LAM {
    constexpr int n = NC.value, K = n < 2 ? 0 : n < 26 ? 1 : 2;
    const u32x4 f = {ra[n % 7].x, ra[n % 7].y, rb[n % 7].x, rb[n % 7].y};
    macc<K>(t[0][n & 1], f, xf[0][n >> 1]);
    SCHED();
    if constexpr (n + 6 < 28) ra[(n + 6) % 7] = trread(WAT(n + 6));
    if constexpr (n == 24) st[0] = *(LAS f32x2*)(psa);
    if constexpr (n == 22 || n == 23) cp[n - 22] = *(LAS f32x4*)(pca + (n - 22) * 64);
    if constexpr (n >= 26) { float a = cp[n - 26][0] + cp[n - 24][0], b = cp[n - 26][1] + cp[n - 24][1]; PIN(a); PIN(b); cp[n - 26][0] = a; cp[n - 26][1] = b; }
    SCHED();
    macc<K>(t[1][n & 1], f, xf[1][n >> 1]);
    SCHED();
    if constexpr (n + 6 < 28) rb[(n + 6) % 7] = trread(WAT(n + 6) + 256);
    if constexpr (n == 24) st[1] = *(LAS f32x2*)(psa + 128);
    if constexpr (n == 22 || n == 23) cp[n - 20] = *(LAS f32x4*)(pca + (n - 20) * 64);
    if constexpr (n >= 26) { float a = cp[n - 26][2] + cp[n - 24][2], b = cp[n - 26][3] + cp[n - 24][3]; PIN(a); PIN(b); cp[n - 26][2] = a; cp[n - 26][3] = b; }
    if constexpr (n == 2) { if ((lane & 15) == 0) *(LAS f32x4*)(pcw) = cs; }
    if constexpr (n == 3) { asm volatile("" ::: "memory"); *(volatile LAS u32*)(pfw) = one; }
    if constexpr (n == 16) sw = *(volatile LAS u32*)(pfa);
    if constexpr (n == 21) { if (__builtin_expect(__builtin_amdgcn_ballot_w64(sw == 0) != 0, 0)) { lds_wait(L, SIG); if (NW > 4) lds_wait(L, SIG + 1); } asm volatile("" ::: "memory"); }
    mid(n, pfw - SIG * 16, one);
    SCHED();
  });
#pragma unroll
  for (int tb = 0; tb < 2; ++tb)
#pragma unroll
    for (int k = 0; k < 2; ++k) t[tb][k] += cp[k] * st[tb].x;
}
DI f32x2 swp16(float a, float b) { auto r = __builtin_amdgcn_permlane16_swap(__builtin_bit_cast(u32, a), __builtin_bit_cast(u32, b), false, false); return f32x2{__builtin_bit_cast(float, (u32)r[0]), __builtin_bit_cast(float, (u32)r[1])}; }
DI f32x2 swp32(float a, float b) { auto r = __builtin_amdgcn_permlane32_swap(__builtin_bit_cast(u32, a), __builtin_bit_cast(u32, b), false, false); return f32x2{__builtin_bit_cast(float, (u32)r[0]), __builtin_bit_cast(float, (u32)r[1])}; }
DI float swadd32(float a, float b) { const f32x2 s = swp32(a, b); return s.x + s.y; }
DI float swadd16(float a, float b) { const f32x2 s = swp16(a, b); return s.x + s.y; }
DI float mx2(float a, float b) { float r; asm("v_max_f32 %0, %1, %2" : "=v"(r) : "v"(a), "v"(b)); return r; }
DI float mx3(float a, float b, float c) { return fmaxf(fmaxf(a, b), c); }
DI float mn3(float a, float b, float c) { return fminf(fminf(a, b), c); }
template <class F> DI void feature_map(const f32x4 (&t)[2][2], const f32x2 (&st)[2], const u32x4 (&wpf)[2], int g, u32x4 (&P)[2][2], F&& mid) {
  const float pad = g < 3 ? 0.f : -3.0e38f;
  const f32x4 z4 = {0.f, 0.f, 0.f, 0.f};
  const float sc[2] = {st[0].y * LOG2E, st[1].y * LOG2E}, scg = g < 2 ? sc[0] : sc[1];
  float p[2][8], m[4], mp[4];
#pragma unroll
  for (int tb = 0; tb < 2; ++tb) {
    const u32x4 tq = {pk(t[tb][0][0], t[tb][0][1]), pk(t[tb][0][2], t[tb][0][3]), pk(t[tb][1][0], t[tb][1][1]), pk(t[tb][1][2], t[tb][1][3])};
    const f32x4 p0 = mfma16(wpf[0], tq, z4), p1 = mfma16(wpf[1], tq, z4);
#pragma unroll
    for (int i = 0; i < 8; ++i) p[tb][i] = i < 4 ? p0[i] : p1[i - 4];
  }
  mid(0);
#pragma unroll
  for (int tb = 0; tb < 2; ++tb) {
    const float* q = p[tb];
    m[2 * tb] = mx3(mx3(q[0], q[1], q[2]), mx3(q[3], q[4], q[5]), mx3(q[5], q[6], q[7]));
    m[2 * tb + 1] = mx3(mx3(-q[0], -q[1], -q[2]), mx3(-q[3], -q[4], -q[5]), mx3(-q[5], -q[6], -q[7]));
  }
  {
    const f32x2 a = swp16(m[0], m[1]), b = swp16(m[2], m[3]);
    const f32x2 c = swp32(mx2(a.x, a.y), mx2(b.x, b.y));
    const float d = mx2(c.x, c.y) * -scg;
    const f32x2 e = swp32(d, d), f0 = swp16(e.x, e.x), f1 = swp16(e.y, e.y);
    m[0] = f0.x; m[1] = f0.y; m[2] = f1.x; m[3] = f1.y;
#pragma unroll
    for (int c2 = 0; c2 < 4; ++c2) mp[c2] = m[c2] + pad;
  }
  float ex[4][8], sm[4];
#pragma unroll
  for (int c = 0; c < 4; ++c) {
    mid(1 + c);
#pragma unroll
    for (int i = 0; i < 8; ++i) {
      ex[c][i] = fexp2(fmaf(c & 1 ? -p[c >> 1][i] : p[c >> 1][i], sc[c >> 1], i & 2 ? mp[c] : m[c]));
    }
    sm[c] = ((ex[c][0] + ex[c][1]) + (ex[c][2] + ex[c][3])) + ((ex[c][4] + ex[c][5]) + (ex[c][6] + ex[c][7]));
  }
  mid(5);
  {
    const float d = frcp(swadd32(swadd16(sm[0], sm[1]), swadd16(sm[2], sm[3])));
    const f32x2 e = swp32(d, d), f0 = swp16(e.x, e.x), f1 = swp16(e.y, e.y);
    sm[0] = f0.x; sm[1] = f0.y; sm[2] = f1.x; sm[3] = f1.y;
  }
#pragma unroll
  for (int c = 0; c < 4; ++c)
#pragma unroll
    for (int e = 0; e < 4; ++e) P[c >> 1][c & 1][e] = pk(ex[c][2 * e] * sm[c], ex[c][2 * e + 1] * sm[c]);
}
DI u32x4 piece(const u32x4 (&rp)[8], int e) {
  u32x4 f;
#pragma unroll
  for (int m = 0; m < 4; ++m) f[m] = __builtin_amdgcn_perm(rp[2 * m + 1][e >> 1], rp[2 * m][e >> 1], (e & 1) ? 0x07060302u : 0x05040100u);
  return f;
}
#define RP_OPS "+v"(rp[0]), "+v"(rp[1]), "+v"(rp[2]), "+v"(rp[3]), "+v"(rp[4]), "+v"(rp[5]), "+v"(rp[6]), "+v"(rp[7])
#define PF1 0
#define PF2 84
#define PF3 120
#define TP0 -1
#define TP1 13
#define TP2 -1
#define TOUCH 5
#define NW3 4
#define THRN0 4
#define DNPRIO 3
#define NWD 3
#define MIDCHK 0
#define HELP 0
#define NHW 7
#define W3LATE 0
#define W3WAIT 1
#define ROT 1
#define EXPT 0
#define PFM 12
#define PSLP 4
#define PLAG 0
#define WTOUCH 0
#define REPLATE 0
#define WB0 8
#define WB1 4
#define ULATE 0
#define VPRIO 3
#define VPRIO0 3
#define VPN 27
#define FPRIO 2
#define CLB 0
#define KAT 99
#define STF 0
#define QN0 0
#define QST 4
#define PB0 3
#define WOT 0
#define WTN 2
#define CLF 0
#define TQ 1
#define TU 0
#define TO 1
#define SN_ 49
#define SS_ 128
#define SD_ 0
#define RPO 0
#define REPE 0
#define UFN 14
#define SBA 0
#define TD 17
#define NR5 16
#define NR6 23
#define G4E 0
#define TS0 2
#define TS1 8
#define TWF 4
#define BP3 0
#define W3J 2
#define TQD -1
#define TOD 0
#define TDD 10
#define CPN 7
#define X0A 5
#define NE 0
#define G6E 0
#define B5E 2
#define B5D 0
#define HW 1
#define PACE 1
#define PS 28
#define PJ0 1
#define PJ1 6
#define ATP 3
#define P1E 0
#define HLA 12
#define FLG13 1
#define H1F 1
#define OLAG 5
#define W4O 0
#define CLW 2
#define CN 50
#define WKF 1
#define WON 7
#define PRE 1
#define OPT 3
#define KGO -1
#define QEARLY 1
#define DLY1 4
#define W1 1
#define W2C 0
#define WOE 2
#define W3THR 1
#define HW3 1
#define HW1 1
#define SN 4
#define BATCH 1
#define B23 1
#define WD5L 0
#define A56 1
#define BGO 196
#define BALL 0
#define BSL 4
#define RSL 4
#define H5 1
#define H6 1
#define BORD 1
#define DP0 3
#define AIL 1
#define SEEN 3
#define ADLY 4
#define W6E 0
#define SPLIT 1
#define NCL 4
#define B5L 1
#define A6L 1
#define BO2 2
#define TW 0
#define CL 0
#define CE 1
#define W5D 1
#define W2F 5
#define D2A 12
#define D2B 16
#define SPEC 1
#define CLP 28
#define CLS 3
#define CE2 0
#define DLY5 60
#define CSL 1
#define CF 0
#define OPRIO 3
#define PG 1
#define ISK 1
#define T2 70
#define FXW 8
#define DLY4 16
#define PUBS 5
#define THRN 40
#define DLY2 8
#define DLY3 15
#define GUPRIO 2
#define SCH 1
#define GUK 6
#define RCK 5
#define CQ2 17
#define CQ0 13
constexpr int act0(int e) {
  return SCH == 0 ? (e < 16 ? e + e / 4 : 28 * (e / 8 - 1) + 1 + 2 * (e & 7))
       : SCH == 1 ? (e < 8 ? e * 3 / 2 : e < 40 ? 3 * e - 11 : 113 + 2 * (e - 40))
                  : (e < 8 ? e * 3 / 2 : e < 24 ? 2 * e - 3 : 28 * (e / 8 - 1) + 1 + (e < 40 ? 5 * (e & 7) / 2 : 2 * (e & 7)));
}
enum { S_FREE01 = 0, S_REFILL, S_DONE4, S_WIN3, S_WK, S_WV, S_WQ, S_WSEEN, S_KDONE, S_V, S_PHIK, S_CSV, S_KVA, S_WO, S_CSQ, S_PHI, S_X0, S_X1, S_XA, S_CSK, S_CS, S_WOFA, S_WOFB, S_EXA, S_EXB, S_W2A, S_W2B, S_HFA, S_HFB, S_CONS, S_SPARE, S_XF0 };

__global__ void __launch_bounds__(512, 2) fused_block(const char* __restrict__ x, const char* __restrict__ wqkv, const char* __restrict__ wphi, const char* __restrict__ wo,
                                                      const char* __restrict__ wg, const char* __restrict__ wu, const char* __restrict__ wd, char* __restrict__ out, char* ws) {
  extern __shared__ __attribute__((aligned(16))) char smem[];
  LAS char* L = (LAS char*)smem;
  const int tid = threadIdx.x, lane = tid & 63, wave = __builtin_amdgcn_readfirstlane(tid >> 6), w2 = wave & 3;
  const int b = blockIdx.x, li = b >> 3, xp = (b & 7) >> 1, xh = b & 1;
  const int tile = 4 * xp + (li >> 3), r = 8 * xh + (li & 7);
  const int tile2 = 4 * xp + 2 * xh + (li >> 4), r2 = li & 15;
  const int tg = r2 >= 7 ? 1 : 0, cg = r2 - 7 * tg;
  const int l15 = lane & 15, g = lane >> 4, q4 = l15 >> 2, p4 = lane & 3, hh = lane >> 5;
  const int head = r;
  {
    u64 pc_; asm volatile("s_getpc_b64 %0" : "=s"(pc_));
    touch((const char*)pc_, min((b & ~7) * 256 + (tid >> 5) * 128, CODE_PF));
    *(LAS u32*)(L + L_FLG + (tid & 127) * 4) = 0u;
  }
  int tn = wave < 2 ? 28 : 85 - 24 * wave, ti = (b & 7) * 64 + li * 2 + wave;
  asm volatile("" : "+s"(tn), "+s"(ti));
  ti = wave < 2 ? ti : b;
  u32 tvo = min(ti * tn + min(lane, tn - 1), wave == 3 ? 3135 : wave == 2 ? 9407 : 14335) * 128, xvo = (tile * 128 + w2 * 32 + (lane >> 3)) * 896 + (((lane & 7) ^ (lane >> 3)) * 16);
  asm volatile("s_waitcnt lgkmcnt(0)\n\ts_barrier" : "+v"(tvo), "+v"(xvo) :: "memory");
  asm volatile("" :: "s"(x), "s"(wqkv), "s"(wphi), "s"(wo), "s"(wg), "s"(wu), "s"(wd), "s"(out), "s"(ws));
  STAMP(0);
  u64 epoch;
  if (wave < 4) {
    const char* tb_ = wave < 2 ? x : wave == 2 ? wqkv : wo;
    asm volatile("" : "+s"(tb_));
    touch(tb_, tvo);
  }
  asm volatile("s_load_dwordx2 %0, %1, 0x0" : "=s"(epoch) : "s"(ws) : "memory");
  if (wave >= 4) {
    const __amdgpu_buffer_rsrc_t rx = __builtin_amdgcn_make_buffer_rsrc((void*)(x - 2048), 0, 2048 * 896 + 2048, 0x00020000);
    sfor<0, 7>([&](auto NC) LAM {
      dma_x<4 * NC.value>(rx, xvo, w2 * 28672);
      if (NC.value == X0A) { asm volatile("s_waitcnt vmcnt(%0)" :: "n"(4 * X0A - 10) : "memory"); lds_signal(L, S_X0, w2); }
    });
  }
  asm volatile("s_waitcnt lgkmcnt(0)" : "+s"(epoch) :: "memory");
  const __amdgpu_buffer_rsrc_t rs = __builtin_amdgcn_make_buffer_rsrc((void*)ws, 0, WS_USED, 0x00020000);
  const u32x4 ones = {0x3f803f80u, 0x3f803f80u, 0x3f803f80u, 0x3f803f80u};
  const f32x4 z4 = {0.f, 0.f, 0.f, 0.f};
  auto role = [&](auto AC) __attribute__((always_inline)) {
  constexpr bool A = AC.value;
  if (A) asm volatile("; A" ::: "memory"); else asm volatile("; B" ::: "memory");
  u32x4 rp[8];
  const int fvo = lane < 16 ? lane * 8 : NOWHERE;
  const int uidx = (tile * 10 + __builtin_popcount(~0x2A54u & ((1u << head) - 1u))) * 4 + w2, unit = uidx * 49 / 80;
  const bool has_unit = !((0x2A54 >> head) & 1) && (uidx + 1) * 49 / 80 != unit;
  const int u3 = unit >> 1, umat = unit & 1, ukq = u3 / 28, ufl = u3 - 28 * ukq;
  const char* gusrc = ws + WS_WGU + 7 * r * SLOT;
  const u32 guo = WS_WGU + 7 * r * SLOT + w2 * 7168;
#define GUBLK(j) dma7<(j) * SLOT, gu_slot(j) * SLOT>(rs, lane * 16, guo, w2 * 7168)
  const u32 wvo = (lane >> 2) * 2688 + (((lane & 3) ^ (2 * (lane >> 5))) * 16);
  const char* wbase = wqkv + head * 56 - (head & 1) * 8 + 16 * w2 * 2688;
  const u32 wr0 = (8 * g + q4) * 64 + (((p4 + (head & 1)) ^ (4 * (g & 1))) * 8), wr1 = (8 * g + q4) * 64 + ((((4 + p4 + (head & 1)) & 7) ^ (4 * (g & 1))) * 8);
  const int tb = w2; constexpr int kh = A ? 0 : 1;
  const int hho = WS_HH + (((tile * 2 + (w2 >> 1)) * 4 + (r >> 2)) * 56 + (r & 3) * 2 + (w2 & 1)) * 1024;
  const int tok = tile2 * 128 + tg * 64 + tb * 16 + l15;
  const int hvo = WS_H + ((tok >> 5) * 28 + 4 * cg + (g >> 1)) * 1024 + ((g & 1) * 32 + (tok & 31)) * 16;
  const int sto = WS_ST + (2 * cg * 2048 + tile2 * 128 + tg * 64 + tb * 16) * 8;
  const u32x4 empty4 = {EMPTY, EMPTY, EMPTY, EMPTY};
#define HH_CLEAR(i0, i1) _Pragma("unroll") for (int i = i0; i < i1; ++i) ST16(rs, empty4, lane * 16 + (i & 1) * 2048, WS_HH + (((tile2 * 2 + r2 - 14) * 4 + w2) * 56 + kh + (i >> 1) * 4) * 1024)
#define HH_FLAG() FLAG(WS_FH + ((tile2 * 4 + 2 * (r2 - 14) + kh) * 32 + 28 + w2) * 128, 1)
  u32x4 wof[8][4];
  f32x4 csp[4];
  u64 tag;
  u32x4 xr;
  const int xo = tok * 896 + cg * 128 + kh * 64 + g * 16;
#define XR() asm volatile("global_load_dwordx4 %0, %1, %2" : "=v"(xr) : "v"(r2 < 14 ? xo : 0), "s"(x) : "memory")
#define WOF_READ(i0, i1) do { u32 wa_ = L_WO + (kh * 224 + 4 * g + q4) * 128 + 16 * p4 + 64 * (q4 >> 1), wb_ = wa_ ^ 64, wc_ = wa_ + (g == 3 ? 0 : 2048), wd_ = wc_ ^ 64; \
    asm volatile("" : "+v"(wa_), "+v"(wb_), "+v"(wc_), "+v"(wd_)); \
    _Pragma("unroll") for (int i = i0; i < i1; ++i) _Pragma("unroll") for (int k = 0; k < 4; ++k) { \
      const u32x2 a_ = trread(L + (k >> 1 ? wb_ : wa_) + 28 * i * 128 + (k & 1) * 8), c_ = trread(L + (k >> 1 ? wd_ : wc_) + 28 * i * 128 + (k & 1) * 8); \
      wof[i][k] = u32x4{a_.x, a_.y, c_.x, c_.y}; } } while (0)
#define XF_READ(i) _Pragma("unroll") for (int s = 0; s < 14; ++s) xf[i][s] = *(LAS u32x4*)((s & 1 ? xr1 : xr0) + (i) * 14336 + (s >> 1) * 2048)
#define LN_STATS(kh) do { f32x4 g1 = z4, g2 = z4; \
    _Pragma("unroll") for (int s = 0; s < 14; ++s) { g1 = mfma16(ones, xf[kh][s], g1); g2 = mfma16(xf[kh][s], xf[kh][s], g2); } \
    const float d01 = (p4 & 1) ? g2[1] : g2[0], d23 = (p4 & 1) ? g2[3] : g2[2], s2 = (p4 & 2) ? d23 : d01; \
    const float nmu = g1[0] * (-1.f / 448.f), var = fmaxf(s2 * (1.f / 448.f) - nmu * nmu, 0.f); \
    if (g == q4) *(LAS f32x2*)(L + L_ST1 + (w2 * 32 + kh * 16 + l15) * 8) = f32x2{nmu, __builtin_amdgcn_rsqf(var + 1e-5f)}; } while (0)
#define WPF() do { u32 wp[8]; \
    _Pragma("unroll") for (int j = 0; j < 8; ++j) wp[j] = *(LAS u32*)(L + L_WPHI + 4 * g * 56 + min(l15, 13) * 4 + ((j & 3) + 16 * (j >> 2)) * 56); \
    _Pragma("unroll") for (int e = 0; e < 4; ++e) { const bool z = e >= 2 && g == 3; \
      wpf[0][e] = z ? 0u : __builtin_amdgcn_perm(wp[2 * e + 1], wp[2 * e], 0x05040100u); wpf[1][e] = z ? 0u : __builtin_amdgcn_perm(wp[2 * e + 1], wp[2 * e], 0x07060302u); } \
    asm volatile("" : "+v"(wpf[0]), "+v"(wpf[1])); } while (0)
  {
    u32x4 xf[2][14], wpf[2];
    f32x2 st[2];
    LAS char* xr0 = L + w2 * 28672 + l15 * 128 + ((g ^ (l15 & 7)) * 16);
    LAS char* xr1 = L + w2 * 28672 + l15 * 128 + (((4 + g) ^ (l15 & 7)) * 16);
    LAS char* pw = L + L_PKV + (w2 * 32 + l15) * 224 + g * 8;
    const char* wos = wo + (r2 < 14 ? cg : 0) * 128 + w2 * 56 * 896;
    const u32 wovo = (lane >> 3) * 896 + (((lane & 7) ^ ((lane >> 2) & 4)) * 16);
#define WO_HALF(hf_) _Pragma("unroll") for (int n = 0; n < 7; ++n) dma16(wos + ((hf_) * 224 + n * 8) * 896, wovo, L_WO + ((hf_) * 28 + w2 * 7 + n) * 1024)
    int clo = WS_O + ((tile * 8 + 2 * w2) * 16 + head) * 1024, clh = r2 < 14 ? hvo : NOWHERE, cls = r2 < 14 && lane < 16 ? (lane >> 3) * 16384 + (lane & 7) * 16 : NOWHERE, clt = sto;
    int kvo = WS_KVP + ((head * 16 + tile) * 4 + w2) * 1024, fko = WS_FKV + ((head * 4 + w2) * 16 + tile) * 128;
    u32x4 em4 = empty4;
    u32 wtv = min(lane, 24) * 128;
    const char* wts = wo + min(li * 98 + w2 * 25, 3136 - 25) * 128;
    auto clear = [&](int c) LAM {
      if (c == 0) ST16(rs, em4, lane * 16, clo);
      if (c == 1) ST16(rs, em4, lane * 16, clo + 16384);
      if (c == 2) ST16(rs, em4, clh, 0);
      if (c == 3) ST16(rs, em4, clh, 2048);
      if (c == 4) ST16(rs, em4, cls, clt);
    };
    if constexpr (A) {
      if (wave == 2) { dma16(wphi + head * 1568, lane * 16, L_WPHI); dma16(wphi + head * 1568, min(lane + 64, 97) * 16, L_WPHI + 1024); }
      __builtin_amdgcn_s_waitcnt(0x0F70);
      tag = epoch * 6364136223846793005ull + 1442695040888963407ull;
      STAMP(1);
#define WKLOAD() _Pragma("unroll") for (int i = 0; i < 7; ++i) dma16(wbase + 896 + 64 * i * 2688, wvo, L_WK + (4 * i + w2) * 1024)
      if (KAT == 3) { WKLOAD(); if (wave == 2) { WAITVM(7); lds_signal(L, S_PHI, 0); } }
      else if (wave == 2) { if (CLF == 9) WAITVM(5); else WAITVM(0); lds_signal(L, S_PHI, 0); }
      if (KAT > 3 && KAT < 28) { lds_wait1(L, S_SPARE, w2); WKLOAD(); }
      lds_wait1(L, S_PHI, 0);
      WPF();
#define DBITE() dma4(wd + min(b * 49 + w2 * 13, 12544 - 13) * 128, min(lane, 12) * 128, L_JUNK)
      if (TD == 9) {
        __builtin_amdgcn_s_sleep(TS0);
        if (w2 & 1) __builtin_amdgcn_s_sleep(TS1);
        if (w2 & 2) { __builtin_amdgcn_s_sleep(TS1); __builtin_amdgcn_s_sleep(TS1); }
        DBITE();
      }
      if (TD == 10 && w2 == 0) DBITE();
      if (WKF == 2) {
#pragma nounroll
        for (u32 sp = 0; sp < (SPIN_CAP << 4); ++sp) { const u32x4 v = *(volatile LAS u32x4*)(L + L_FLG + S_X0 * 16); if (__builtin_amdgcn_readfirstlane(v.x | v.y | v.z | v.w)) break; __builtin_amdgcn_s_sleep(1); }
        WKLOAD();
      }
      lds_wait1(L, S_X0, w2);
      if (WKF == 1) { WKLOAD(); }
      XF_READ(0);
      if (w2 < 2) lds_signal(L, S_XF0, w2);
      if (TD == 10 && w2 == 1) DBITE();
      if (WKF == 0) { WKLOAD(); }
      if (TD == 10 && w2 == 2) DBITE();
      if (TD == 11) DBITE();
      STAMP(27);
      LN_STATS(0);
      SCHED();
      STAMP(2);
      lds_wait1(L, S_X1, w2);
      XF_READ(1);
      if (QEARLY == 3 && w2 < 2) lds_signal(L, S_SPARE, w2);
      STAMP(3);
      if (SBA) LN_STATS(1);
      WAITVM(0);
      STAMP(4);
      lds_signal(L, S_WK, w2);
      if (TD == 10 && w2 == 3) DBITE();
      if (TD == 12) DBITE();
      f32x4 t[2][2];
      u32 pr = L_PKV + (4 * g + q4) * 224 + 8 * p4;
      asm volatile("" : "+v"(pr), "+v"(pw));
      asm volatile("" : "+s"(clo), "+v"(clh), "+v"(cls), "+s"(clt), "+s"(kvo), "+s"(fko), "+s"(tag), "+v"(em4));
      if (WOT == 5) asm volatile("" : "+v"(wtv), "+s"(wts));
      project<0, S_CSK, 8>(L, L_WK + wr0, L_WK + wr1, xf, w2, lane, L_CSP, t, st, [&]() LAM {
        lds_wait(L, S_WK);
        STAMP(5);
        __builtin_amdgcn_s_setprio(2);
      }, [&](int n, LAS char* sg, u32 one) LAM {
        if (n == KGO) *(volatile LAS u32*)(sg + S_KDONE * 16) = one;
      });
      STAMP(6);
      u32x4 P[2][2];
      __builtin_amdgcn_s_setprio(FPRIO);
      feature_map(t, st, wpf, g, P, [&](int i) LAM {
        if (WOT == 5 && i == 0) { SCHED(); dma4(wts, wtv, L_JUNK); SCHED(); }
        if (CLF == 1 && i > 0) { SCHED(); clear(i - 1); SCHED(); }
        if (CLF == 2 && i == 5) { SCHED(); clear(0); clear(1); clear(2); clear(3); clear(4); SCHED(); }
        if (CLF == 3 && i >= 3) { SCHED(); clear(2 * i - 6); clear(2 * i - 5); SCHED(); }
        if (CLF == 4 && i == 1) { SCHED(); clear(0); clear(1); clear(2); clear(3); clear(4); SCHED(); }
      });
#pragma unroll
      for (int c = 0; c < 4; ++c) {
        LAS char* p = pw + (c >> 1) * 16 * 224 + (c & 1) * 64;
        *(LAS u32x2*)(p) = u32x2{P[c >> 1][c & 1].x, P[c >> 1][c & 1].y}; *(LAS u32x2*)(p + 32) = u32x2{P[c >> 1][c & 1].z, P[c >> 1][c & 1].w};
      }
      lds_signal(L, S_PHIK, w2);
      STAMP(7);
      if (OPT & 4) XR();
      if (CLF == 0 || CLF == 8) { clear(0); clear(1); }
      if (CLF == 0 || CLF == 7) { clear(2); clear(3); clear(4); }
      lds_wait2(L, S_V, S_PHIK);
      u32x4 pa[4], pb[8];
#pragma unroll
      for (int m = 0; m < 4; ++m) {
        pa[m] = trfrag(L + pr + w2 * 32 + m * 32 * 224, 16 * 224);
        pb[2 * m] = trfrag(L + pr + m * 32 * 224 + 128, 16 * 224); pb[2 * m + 1] = trfrag(L + pr + m * 32 * 224 + 160, 16 * 224);
      }
      SCHED();
      f32x4 kv[2] = {};
#pragma unroll
      for (int m = 0; m < 8; ++m) kv[m & 1] = mfma16(pa[m >> 1], pb[m], kv[m & 1]);
      const u32x4 kvb = {pk(kv[0][0], kv[0][1]), pk(kv[0][2], kv[0][3]), pk(kv[1][0], kv[1][1]), pk(kv[1][2], kv[1][3])};
      ST16(rs, kvb, lane * 16, kvo);
      STAMP(8);
      if (TD == 17) {
        lds_signal(L, S_KDONE, w2);
        if (wave == 0) { lds_wait(L, S_KDONE); touch(wd + b * 49 * 128, min(lane, 48) * 128); WAITVM(1); } else WAITVM(0);
      } else if (TD == 19) {
        lds_signal(L, S_KDONE, w2);
        lds_wait(L, S_KDONE);
        if (wave == 0) touch(wd + b * 49 * 128, min(lane, 48) * 128);
        if (r2 < 14) {
#pragma unroll
          for (int n = 0; n < WON; ++n) dma16(wos + n * 8 * 896, wovo, L_WO + (w2 * 7 + n) * 1024);
          if (wave == 0) asm volatile("s_waitcnt vmcnt(%0)" :: "n"(WON + 1) : "memory"); else asm volatile("s_waitcnt vmcnt(%0)" :: "n"(WON) : "memory");
        } else if (wave == 0) WAITVM(1); else WAITVM(0);
      } else if (TD == 18) {
        lds_signal(L, S_KDONE, w2);
        lds_wait(L, S_KDONE);
        touch(wd + min(b * 49 + w2 * 13, 12544 - 13) * 128, min(lane, 12) * 128);
        WAITVM(1);
      } else DRAIN();
      if (OPT & 4) asm volatile("" : "+v"(xr));
      FLAG(fko, 1);
      __builtin_amdgcn_s_setprio(0);
      STAMP(9);
      lds_signal(L, S_XA, w2); lds_wait(L, S_XA);
      if ((OPT & 2) && wave == 1) FLAG(WS_FO + (head * 16 + tile) * 128, 1);
#define DTOUCH() dma4(wd + b * 49 * 128, min(lane, 48) * 128, L_JUNK)
      if (TD == 2 && wave == 0) DTOUCH();
      if (r2 < 14 || !ISK) {
#pragma unroll
        for (int n = TD == 19 ? WON : 0; n < 7; ++n) dma16(wos + n * 8 * 896, wovo, L_WO + (w2 * 7 + n) * 1024);
        WO_HALF(1);
      } else { HH_CLEAR(0, NE); if (NE < 8) __builtin_amdgcn_s_sleep(20); }
      if (OPT & 8) XR();
      poll_flags(ws, WS_FKV + ((head * 4 + w2) * 16 + l15) * 128, lane < 16, tag);
      if (OPT & 8) asm volatile("s_waitcnt vmcnt(0)" : "+v"(xr) :: "memory");
      lds_signal(L, S_WO, w2);
      STAMP(10);
      u32x4 tt[16];
#pragma unroll
      for (int i = 0; i < 16; ++i) tt[i] = __builtin_bit_cast(u32x4, __builtin_amdgcn_raw_buffer_load_b128(rs, lane * 16, WS_KVP + head * 65536 + w2 * 1024 + i * 4096, 16));
      u32 fgo;
      {
        const int pl = (tile & 1) * 16 + min(OPT & 2 ? l15 : lane >> 2, 13);
        fgo = wave == 0 ? (OPT & 2 ? WS_FO + (l15 * 16 + tile2) * 128 : WS_FKV + ((l15 * 4 + 2 * tg + (g & 1)) * 16 + tile2) * 128)
            : wave == 1 ? (OPT & 2 ? WS_FO + ((((tile >> 1) & 1) * 8 + (pl & 7)) * 16 + (tile & 12) + (pl >> 3)) * 128 : WS_FKV + (((((tile >> 1) & 1) * 8 + (pl & 7)) * 4 + p4) * 16 + (tile & 12) + (pl >> 3)) * 128)
            : wave == 2 && (OPT & 1) ? fko
                        : WS_FG + (min(((7 * r) >> 2) + g, (7 * r + 6) >> 2) * 16 + (l15 & 8) + min(lane & 7, 6)) * 128;
      }
      u64 fg = __hip_atomic_load((const GAS u64*)(ws + fgo), RLX_AGENT);
      SCHED();
      const u32 one = g == q4 ? ((p4 & 1) ? 0x3f800000u : 0x3f80u) : 0u, s01 = p4 < 2 ? one : 0u, s23 = p4 < 2 ? 0u : one;
      const u32x4 sel0 = {s01, s23, 0u, 0u}, sel1 = {0u, 0u, s01, s23};
      f32x4 s0 = {}, s1 = {};
#pragma unroll
      for (int i = 0; i < 16; ++i) { s0 = mfma16(sel0, tt[i], s0); s1 = mfma16(sel1, tt[i], s1); }
      LAS char* kw = L + L_KVA + (w2 >> 1) * 1024 + lane * 16 + (w2 & 1) * 8;
      *(LAS u32x2*)(kw) = u32x2{pk(s0[0], s0[1]), pk(s0[2], s0[3])};
      *(LAS u32x2*)(kw + 2048) = u32x2{pk(s1[0], s1[1]), pk(s1[2], s1[3])};
      lds_signal(L, S_KVA, w2);
      if (TD == 4 && wave == 2) DTOUCH();
      STAMP(11);
      if (WOE == 3) { WO_HALF(1); WAITVM(0); lds_signal(L, S_WO, w2); }
      asm volatile("" : "+v"(fg) :: "memory");
      if (!__all(fg == tag + 1)) poll_flags(ws, fgo, true, tag);
      __builtin_amdgcn_fence(__ATOMIC_ACQUIRE, "wavefront");
      lds_signal(L, S_WOFA, w2);
      lds_wait1(L, S_WOFA, 3);
      const int fl = min(((7 * r) >> 2) + w2, (7 * r + 6) >> 2);
#pragma unroll
      for (int i = 0; i < 4; ++i) asm volatile("global_load_dwordx4 %0, %1, %2 offset:%3 sc1" : "=v"(csp[i]) : "v"(((7 * r) >> 2) + w2 == fl ? (i < 3 ? lane : lane & 31) * 16 : 0), "s"(ws + WS_CS + fl * 4096), "n"(i * 1024) : "memory");
      GUBLK(2);
      if (r2 < 14 || !ISK) { lds_wait(L, S_WO); WOF_READ(0, 8); }
      SCHED();
#define CS_OUT(k_) do { asm volatile("s_waitcnt vmcnt(%4)" : "+v"(csp[0]), "+v"(csp[1]), "+v"(csp[2]), "+v"(csp[3]) : "n"(k_) : "memory"); \
        const f32x4 sm = (csp[0] + csp[1]) + (csp[2] + 0.5f * csp[3]);     \
        const float e0 = swadd32(sm[0], sm[1]), e1 = swadd32(sm[2], sm[3]);     \
        const int j0 = 4 * (((7 * r) >> 2) + w2) + 2 * (lane & 1) - 7 * r, cr = ((g & 1) * 16 + hh * 8 + (l15 >> 1)) * 4; \
        if (j0 >= 0 && j0 < 7) *(LAS float*)(L + L_CS + j0 * 128 + cr) = e0; \
        if (j0 + 1 >= 0 && j0 + 1 < 7) *(LAS float*)(L + L_CS + (j0 + 1) * 128 + cr) = e1; } while (0)
      lds_signal(L, S_WOFB, w2);
      if (!CSL) CS_OUT(7);
      STAMP(24);
    } else {
      const int rg = lane >> 3;
      const u32 lvo = rg * 8 * 3584 + (lane & 7) * 16;
      const char* us = (umat ? wu : wg) + ukq * 64 * 3584 + ufl * 128;
      asm volatile("" : "+s"(us));
      auto repack = [&]() LAM {
        if (has_unit) {
          const int so = rg * 512 + (lane & 7) * 16;
          const int sb = WS_WGU + 4 * ufl * SLOT + ukq * 4096 + umat * 256;
          const int s1 = SLOT, s2 = 2 * SLOT;
          float c[8];
#pragma unroll
          for (int e = 0; e < 8; ++e) {
            const u32x4 f = piece(rp, e);
            ST16(rs, f, so + (e & 1) * 128, sb + ((e >> 1) & 1) * s1 + (e >> 2) * s2);
            c[e] = mfma16(ones, f, z4)[0];
          }
          f32x4 cv = g & 1 ? f32x4{c[4], c[5], c[6], c[7]} : f32x4{c[0], c[1], c[2], c[3]};
#pragma unroll
          for (int e = 0; e < 4; ++e) cv[e] += dppror<8>(cv[e]);
          ST16(rs, cv, g < 2 && l15 < 8 ? l15 * 32 + g * 16 : NOWHERE, WS_CS + ufl * 4096 + ukq * 512 + umat * 256);
        }
      };
#define RPLOAD() _Pragma("unroll") for (int j = 0; j < 8; ++j) asm volatile("global_load_dwordx4 %0, %1, %2" : "=v"(rp[j]) : "v"(lvo), "s"(us + j * 3584) : "memory")
      if (RPO == 2 && has_unit) { RPLOAD(); }
      tag = epoch * 6364136223846793005ull + 1442695040888963407ull;
      STAMP(1);
      XF_READ(0);
      if (w2 < 2) lds_signal(L, S_XF0, 2 + w2);
      if (RPO == 2 && has_unit) WAITVM(8); else WAITVM(0);
      lds_signal(L, S_X1, w2);
      if (RPO == 1 && has_unit) { RPLOAD(); }
      STAMP(2);
      if (STF < 7) lds_wait(L, S_XF0);
      XF_READ(1);
      if (QEARLY == 3 && w2 < 2) lds_signal(L, S_SPARE, 2 + w2);
      SCHED();
#define WDST(c) ((c) * 1024 + ((c) >= 14 ? SLOT - 14336 : 0))
#pragma unroll
      for (int i = 0; i < 7 - STF; ++i) dma16(wbase + 1792 + 64 * i * 2688, wvo, L_WV + WDST(4 * i + w2));
      STAMP(3);
      __builtin_amdgcn_s_setprio(PB0);
      if (!SBA) LN_STATS(1);
      lds_signal(L, S_CS, w2);
      if (STF == 7) lds_wait(L, S_XF0);
#pragma unroll
      for (int i = 7 - STF; i < 7; ++i) dma16(wbase + 1792 + 64 * i * 2688, wvo, L_WV + WDST(4 * i + w2));
      STAMP(4);
      f32x4 t[2][2];
      asm volatile("" : "+v"(pw));
      if (WOT == 4) asm volatile("" : "+v"(wtv), "+s"(wts));
      project<SLOT - 14336, S_CSV, 4>(L, L_WV + wr0, L_WV + wr1, xf, w2, lane, L_CSP + 256, t, st, [&]() LAM {
      if (QEARLY == 3) {
        lds_wait(L, S_SPARE);
#pragma unroll
        for (int i = 0; i < 7; ++i) dma16(wbase + 64 * i * 2688, wvo, L_WQ + WDST(4 * i + w2));
      }
      if (REPE == 3) { asm volatile("s_waitcnt vmcnt(%8)" : RP_OPS : "n"(QEARLY == 3 ? 14 : 7) : "memory"); repack(); if (has_unit) { if (QEARLY == 3) WAITVM(16); else WAITVM(9); } }
      if (RPO == 0 && has_unit) { RPLOAD(); } else if (REPE == 3 && has_unit) {} else if (QEARLY == 3) WAITVM(7); else WAITVM(0);
      STAMP(28);
      if (RPO) {} else if (QEARLY == 3) WAITVM(15); else WAITVM(8);
      lds_signal(L, S_WV, w2);
      STAMP(29);
      lds_wait2(L, S_WV, S_WK);
      STAMP(30);
      if (QEARLY == 1) {
#pragma unroll
        for (int i = 0; i < 7; ++i) dma16(wbase + 64 * i * 2688, wvo, L_WQ + WDST(4 * i + w2));
      }
        if (REPE == 2) { asm volatile("s_waitcnt vmcnt(7)" : RP_OPS :: "memory"); repack(); }
        if (TD == 8 && !has_unit && !((0x2A54 >> head) & 1)) dma4(wd + min((uidx - unit) * 51, 12544 - 51) * 128, min(lane, 50) * 128, L_JUNK);
        if (KGO >= 0) lds_wait1(L, S_KDONE, w2);
        STAMP(5);
        __builtin_amdgcn_s_setprio(VPRIO0);
      }, [&](int n, LAS char*, u32) LAM {
        if (n == VPN) __builtin_amdgcn_s_setprio(VPRIO);
        if (WOT == 4 && n == WTN) dma4(wts, wtv, L_JUNK);
        if (QEARLY == 2 && n >= QN0 && (n - QN0) % QST == 0 && (n - QN0) / QST < 7) { const int i = (n - QN0) / QST; dma16(wbase + 64 * i * 2688, wvo, L_WQ + WDST(4 * i + w2)); }
      });
#pragma unroll
      for (int c = 0; c < 4; ++c) t[c >> 1][c & 1] *= st[c >> 1].y;
      STAMP(6);
#pragma unroll
      for (int c = 0; c < 2; ++c) {
        LAS char* p = pw + c * 16 * 224 + 128;
        *(LAS u32x2*)(p) = u32x2{pk(t[c][0][0], t[c][0][1]), pk(t[c][0][2], t[c][0][3])};
        *(LAS u32x2*)(p + 32) = g == 3 ? u32x2{0x3f80u, 0u} : u32x2{pk(t[c][1][0], t[c][1][1]), pk(t[c][1][2], t[c][1][3])};
      }
      lds_signal(L, S_V, w2);
      if (OPT & 4) XR();
      __builtin_amdgcn_s_setprio(0);
      STAMP(7);
      if (!QEARLY) {
#pragma unroll
        for (int i = 0; i < 7; ++i) dma16(wbase + 64 * i * 2688, wvo, L_WQ + WDST(4 * i + w2));
      }
      if (WOE == 0) WO_HALF(0);
      if (WOE < 2) { lds_wait(L, S_KDONE); WO_HALF(1); }
      if (REPE == 0) asm volatile("s_waitcnt vmcnt(%8)" : RP_OPS : "n"(OPT & 4 ? 8 : 7) : "memory");
#define UFLAG() FLAGC(has_unit, WS_FG + ((ufl * 2 + umat) * 8 + ukq) * 128, 1)
      if (REPE == 0) repack();
      STAMP(8);
      WPF();
      if (!has_unit || (REPE == 3 && QEARLY != 3)) WAITVM(0); else WAITVM(9);
      if (OPT & 4) asm volatile("" : "+v"(xr));
      lds_signal(L, S_WQ, w2);
      project<SLOT - 14336, S_CSQ, 4>(L, L_WQ + wr0, L_WQ + wr1, xf, w2, lane, L_CSP + 512, t, st, [&]() LAM {
        lds_wait(L, S_WQ);
        STAMP(9);
      }, [&](int n, LAS char*, u32) LAM {
        if (n == UFN) { WAITVM(0); UFLAG(); if (OPT & 8) XR(); }
      });
      STAMP(10);
      if (REPLATE == 2) repack();
      u32x4 pq[2][2];
      feature_map(t, st, wpf, g, pq, [](int) {});
      asm volatile("" : "+v"(pq[0][0]), "+v"(pq[0][1]), "+v"(pq[1][0]), "+v"(pq[1][1]));
      STAMP(11);
      if (REPLATE == 1) repack();
      if (TD == 13 || TD == 14) {
        lds_wait(L, S_WO);
        if (r2 < 14) WOF_READ(0, TWF); else __builtin_amdgcn_s_sleep(2 * TWF);
        SCHED();
        if (TD == 14) DBITE(); else if (w2 == 0) DTOUCH();
        SCHED();
        if (r2 < 14) WOF_READ(TWF, 8);
      } else if (r2 < 14 || !ISK) { lds_wait(L, S_WO); WOF_READ(0, 8); }
      STAMP(24);
      if (REPLATE == 2) { WAITVM(0); UFLAG(); }
      if (OPT & 8) asm volatile("s_waitcnt vmcnt(0)" : "+v"(xr) :: "memory");
      __builtin_amdgcn_s_setprio(OPRIO);
      lds_wait(L, S_KVA);
      if (REPLATE == 1) { WAITVM(0); UFLAG(); }
      STAMP(27);
      u32x4 ka[4];
#pragma unroll
      for (int i = 0; i < 4; ++i) ka[i] = *(LAS u32x4*)(L + L_KVA + i * 1024 + lane * 16);
      f32x4 o[2][2];
#pragma unroll
      for (int c = 0; c < 4; ++c) o[c >> 1][c & 1] = mfma16(ka[2 * (c & 1) + 1], pq[c >> 1][1], mfma16(ka[2 * (c & 1)], pq[c >> 1][0], z4));
      const f32x2 da = swp16(o[0][1][0], o[0][1][0]), db = swp16(o[1][1][0], o[1][1][0]);
      const float den[2] = {swp32(da.y, da.y).y, swp32(db.y, db.y).y};
#pragma unroll
      for (int c = 0; c < 2; ++c) {
        const float rd = frcp(den[c] + 1e-6f), r3 = g == 3 ? 0.f : rd;
        const u32x4 ob = {pk(o[c][0][0] * rd, o[c][0][1] * rd), pk(o[c][0][2] * rd, o[c][0][3] * rd), pk(o[c][1][0] * r3, o[c][1][1] * r3), filled(pk(o[c][1][2] * r3, o[c][1][3] * r3))};
        ST16(rs, ob, lane * 16, WS_O + ((tile * 8 + 2 * w2 + c) * 16 + head) * 1024);
      }
      STAMP(12);
      if (WOE == 3) lds_wait(L, S_WO);
      *(volatile LAS u32*)(L + L_FLG + S_W2A * 16 + w2 * 4) = (u32)__builtin_amdgcn_s_memrealtime();
      lds_signal(L, S_W2B, w2);
      __builtin_amdgcn_s_setprio(0);
      if (TD == 3 && w2 == 0) DTOUCH();
    }
  }
  STAMP(13);
  u32x4 hres = {0u, 0u, 0u, 0u};
  STAMP(23);
  if (r2 < 14) {
    u32x4 of[8];
    if (!(OPT & 12)) XR();
    lds_wait2(L, S_W2B, S_WOFA);
#define WIN2() do { if (A ? W1 < 2 : W1 < 3) GUBLK(A ? 0 : 1); } while (0)
    if (W2F == 1 || W2F == 3) WIN2();
    if (W2F == 4 && !A) { GUBLK(0); GUBLK(1); }
    if (W2F == 5 && !A) GUBLK(1);
    if (T2) {
      const u32x4 ts = *(volatile LAS u32x4*)(L + L_FLG + S_W2A * 16);
      const u32 t0 = __builtin_amdgcn_readfirstlane(ts.x), due = t0 + T2 + __builtin_amdgcn_readfirstlane(max(max(0, (int)(ts.y - ts.x)), max((int)(ts.z - ts.x), (int)(ts.w - ts.x))));
#pragma nounroll
      for (u32 sp = 0; sp < 64 && (int)((u32)__builtin_amdgcn_s_memrealtime() - due) < 0; ++sp) __builtin_amdgcn_s_sleep(1);
    } else __builtin_amdgcn_s_sleep(A ? D2A : D2B);
    STAMP(14);
#define OFA(c_) _Pragma("unroll") for (int i = 0; i < 8; ++i) \
      asm volatile("global_load_dwordx4 %0, %1, %2 offset:%3 sc1" : c_(of[i]) : "v"(lane * 16), "s"(ws + WS_O + ((tile2 * 8 + tg * 4 + tb) * 16 + kh * 8 + 4) * 1024), "n"(i * 1024 - 4096) : "memory")
#define OFW(k_) asm volatile("s_waitcnt vmcnt(%9)" : "+v"(of[0]), "+v"(of[1]), "+v"(of[2]), "+v"(of[3]), "+v"(of[4]), "+v"(of[5]), "+v"(of[6]), "+v"(of[7]), "+v"(xr) : "n"(k_) : "memory")
#define OFBAD() (!__all(max(max(max(of[0].w, of[1].w), max(of[2].w, of[3].w)), max(max(of[4].w, of[5].w), max(of[6].w, of[7].w))) != EMPTY))
    OFA("=v");
    f32x4 a[4] = {};
    u32 mx = 0;
#pragma unroll
    for (int i = 0; i < 8; ++i) {
      asm volatile("s_waitcnt vmcnt(%1)" : "+v"(of[i]) : "n"(7 - i) : "memory");
      if (i == PG - 1) { lds_wait(L, S_WOFB); }
      if (i == 7) GUBLK(A ? 0 : 3);
#pragma unroll
      for (int k = 0; k < 4; ++k) a[k] = mfma16(wof[i][k], of[i], a[k]);
      mx = max(mx, of[i].w);
      SCHED();
    }
    asm volatile("" : "+v"(xr));
    if (__builtin_expect(!__all(mx != EMPTY), 0)) {
#pragma nounroll
      for (u32 sp = 0; sp < SPIN_CAP; ++sp) { __builtin_amdgcn_s_sleep(RSL); OFA("+v"); OFW(0); if (!OFBAD()) break; }
#pragma unroll
      for (int i = 0; i < 8; ++i)
#pragma unroll
        for (int k = 0; k < 4; ++k) a[k] = mfma16(wof[i][k], of[i], i ? a[k] : z4);
    }
    STAMP(25);
    *(LAS f32x4*)(L + L_EX + wave * 2048 + lane * 16) = kh ? a[0] : a[2];
    *(LAS f32x4*)(L + L_EX + wave * 2048 + 1024 + lane * 16) = kh ? a[1] : a[3];
    lds_signal(L, A ? S_EXA : S_EXB, w2);
    lds_wait1(L, A ? S_EXB : S_EXA, w2);
    STAMP(15);
    const f32x4 c0 = (kh ? a[2] : a[0]) + *(LAS f32x4*)(L + L_EX + (wave ^ 4) * 2048 + lane * 16);
    const f32x4 c1 = (kh ? a[3] : a[1]) + *(LAS f32x4*)(L + L_EX + (wave ^ 4) * 2048 + 1024 + lane * 16);
    const f32x4 h0 = c0 + f32x4{blo(xr.x), bhi(xr.x), blo(xr.y), bhi(xr.y)}, h1 = c1 + f32x4{blo(xr.z), bhi(xr.z), blo(xr.w), bhi(xr.w)};
    hres = u32x4{pk(h0[0], h0[1]), pk(h0[2], h0[3]), pk(h1[0], h1[1]), filled(pk(h1[2], h1[3]))};
    ST16(rs, hres, hvo, kh * 2048);
    const f32x4 r0 = {blo(hres.x), bhi(hres.x), blo(hres.y), bhi(hres.y)}, r1 = {blo(hres.z), bhi(hres.z), blo(hres.w), bhi(hres.w)};
    const f32x4 a1 = r0 + r1, a2q = r0 * r0 + r1 * r1;
    float s1 = (a1[0] + a1[1]) + (a1[2] + a1[3]), s2 = (a2q[0] + a2q[1]) + (a2q[2] + a2q[3]);
    { const float t = swadd16(s1, s2); const f32x2 a2 = sw32(t); s1 = a2.x + a2.y; }
    { const u32x2 sv = __builtin_bit_cast(u32x2, sw16(s1)); __builtin_amdgcn_raw_buffer_store_b64(u32x2{sv.x, filled(sv.y)}, rs, fvo, sto + kh * 16384, 16); }
    WAITVM(2);
  } else {
    if (TD == 1) dma4(wd + ((tile2 * 2 + r2 - 14) * 8 + wave) * 49 * 128, min(lane, 48) * 128, L_JUNK);
    lds_wait2(L, S_W2B, S_WOFA);
    if (CF) { HH_CLEAR(A ? NE : 0, 4 * NCL); }
    WIN2();
    if (W2F >= 3) { lds_wait(L, S_WOFB); if (W2F < 5 || A) GUBLK(A ? 3 : 4); }
    if (!CF) { HH_CLEAR(A ? NE : 0, 4 * NCL); }
    DRAIN();
    if (!CE2) HH_FLAG();
    if (CE2 && A) __builtin_amdgcn_s_sleep(DLY5);
  }
  if constexpr (A && CSL) CS_OUT(2);
  lds_signal(L, A ? S_HFA : S_HFB, w2);
  STAMP(16);
  u32x4 wdf[NWD][8];
  const __amdgpu_buffer_rsrc_t rd = __builtin_amdgcn_make_buffer_rsrc((void*)(wd + w2 * 448 * 896 + min(cg, 6) * 128 - 1024), 0, 448 * 896 + 1024, 0x00020000);
  const u32 dvo = (lane >> 3) * 8 * 896 + (((lane & 7) ^ ((lane >> 2) & 4)) * 16);
  u32 da_ = 2 * SLOT + w2 * 7168 + hh * 512 + q4 * 128 + (g & 1) * 32 + p4 * 8 + 64 * (q4 >> 1), db_ = da_ ^ 64, dc_ = da_ + (L_EX - 2 * SLOT - w2 * 6144), dd_ = dc_ ^ 64;
  const u32 po = WS_FHH + ((tile2 * 16 + 4 * w2 + ((lane >> 1) & 3)) * 4 + 2 * tg + (lane & 1)) * 128;
  const u32 ho = WS_HH + ((tile2 * 2 + tg) * 4 + w2) * 57344;
#define WDLOAD(k, bf) dma_wd<k>(rd, dvo, ((bf) + 2) * SLOT + w2 * 7168, L_EX + (bf) * 4096 + w2 * 1024)
#define WDREAD(dst, bf) do { asm volatile("" : "+v"(da_), "+v"(db_), "+v"(dc_), "+v"(dd_)); \
    _Pragma("unroll") for (int i = 0; i < 8; ++i) { LAS char* p_ = L + (i & 1 ? db_ : da_) + (bf) * SLOT + (i >> 1) * 2048; \
      const u32x2 x_ = trread(p_), y_ = trread(i < 6 ? p_ + 1024 : L + (i & 1 ? dd_ : dc_) + (bf) * 4096); dst[i] = u32x4{x_.x, x_.y, y_.x, y_.y}; } } while (0)
#define THR() asm volatile("s_waitcnt vmcnt(%0)" :: "n"(THRN) : "memory")
#define THR0() asm volatile("s_waitcnt vmcnt(%0)" :: "n"(THRN0) : "memory")
  u32 have = 0;
#define PLOAD() peek(ws, po, lane < 8, tag + 7)
#define PMIN(pv) do { const u64 d = (pv) - tag; have = max(have, min8(d <= 8 ? (u32)d : 0u)); } while (0)
#define NEEDP(n) do { _Pragma("nounroll") for (u32 sp = 0; have < (n) && sp < SPIN_CAP; ++sp) { PMIN(PLOAD()); if (PSLP && have < (n)) __builtin_amdgcn_s_sleep(PSLP); } \
    __builtin_amdgcn_fence(__ATOMIC_ACQUIRE, "wavefront"); } while (0)
#define HBASE() u32 hb3 = w2 * 7168 + lane * 16, hb0 = hb3 + 2 * SLOT, hb2 = hb0 + 2 * SLOT, hb7 = L_EX + w2 * 1024 + lane * 16; asm volatile("" : "+v"(hb0), "+v"(hb2), "+v"(hb7))
#define HFRAG(bf, i) (*(LAS u32x4*)(L + ((i) < 7 ? ((bf) == 3 ? hb3 : (bf) == 2 ? hb2 : hb0 + (bf) * SLOT) + (i) * 1024 : hb7 + (bf) * 4096)))
#define HQLOAD(q, j) _Pragma("unroll") for (int i = 0; i < 8; ++i) hq[q][i] = __builtin_bit_cast(u32x4, __builtin_amdgcn_raw_buffer_load_b128(rs, lane * 16 + (i & 3) * 1024, ho + (j) * 8192 + (i & 4) * 1024, 16))
#define HQLOADT(q, j) _Pragma("unroll") for (int i = 0; i < 8; ++i) { THR(); hq[q][i] = __builtin_bit_cast(u32x4, __builtin_amdgcn_raw_buffer_load_b128(rs, lane * 16 + (i & 3) * 1024, ho + (j) * 8192 + (i & 4) * 1024, 16)); }
#define DOWN16(ac, wf, hf_) _Pragma("unroll") for (int m = 0; m < 16; ++m) ac[(m >> 1) & 1][m & 1] = mfma32(wf[2 * (m >> 2) + (m & 1)], hf_[2 * (m >> 2) + ((m >> 1) & 1)], ac[(m >> 1) & 1][m & 1])
#define PARTIAL_OUT(ac, b0, b1) _Pragma("unroll") for (int n = 0; n < 16; ++n) { const int T = n >> 3, k = (n >> 2) & 1, q = n & 3; \
    *(LAS f32x4*)&HFRAG(T ? b1 : b0, n & 7) = f32x4{ac[T][k][4 * q], ac[T][k][4 * q + 1], ac[T][k][4 * q + 2], ac[T][k][4 * q + 3]}; }
#define DOWN_OUT(ac, wf, hf_, b0, b1) _Pragma("unroll") for (int m = 0; m < 16 + OLAG; ++m) { \
    if (m < 16) ac[m >> 3][(m >> 2) & 1] = mfma32(wf[2 * (m & 3) + ((m >> 2) & 1)], hf_[2 * (m & 3) + (m >> 3)], ac[m >> 3][(m >> 2) & 1]); \
    SCHED(); \
    if (m >= OLAG) { const int n = m - OLAG, T = n >> 3, k = (n >> 2) & 1, q = n & 3; \
      *(LAS f32x4*)&HFRAG(T ? b1 : b0, n & 7) = f32x4{ac[T][k][4 * q], ac[T][k][4 * q + 1], ac[T][k][4 * q + 2], ac[T][k][4 * q + 3]}; } \
    SCHED(); }
#define FREE(f) do { _Pragma("nounroll") for (u32 sp = 0; sp < (SPIN_CAP << 4); ++sp) { const u32x4 fr = *(volatile LAS u32x4*)(L + L_FLG + S_DONE4 * 16); \
          if (__builtin_amdgcn_readfirstlane(min(min(fr.x, fr.y), min(fr.z, fr.w))) >= (f)) break; __builtin_amdgcn_s_sleep(BSL); } asm volatile("" ::: "memory"); } while (0)
#define HQBAD(q) (!__all(max(max(max(hq[q][0].w, hq[q][1].w), max(hq[q][2].w, hq[q][3].w)), max(max(hq[q][4].w, hq[q][5].w), max(hq[q][6].w, hq[q][7].w))) != EMPTY))
#define HQCHECK(q, j) do { if (__builtin_expect(HQBAD(q), 0)) { _Pragma("nounroll") for (u32 sp = 0; sp < SPIN_CAP; ++sp) { \
      __builtin_amdgcn_s_sleep(RSL); asm volatile("" ::: "memory"); HQLOAD(q, j); if (!HQBAD(q)) break; } } SCHED(); } while (0)
  if constexpr (A) {
    const int T32 = tile * 4 + wave;
    u32x2 st[7];
    u32x4 hb[28];
    u64 fcl;
    const u32 stv = (7 * hh * 2048 + (lane & 31)) * 8;
#define STA(c_) _Pragma("unroll") for (int i = 0; i < 7; ++i) asm volatile("global_load_dwordx2 %0, %1, %2 sc1" : c_(st[i]) : "v"(stv), "s"(ws + WS_ST + (i * 2048 + T32 * 32) * 8) : "memory")
#define STW(k_) asm volatile("s_waitcnt vmcnt(%7)" : "+v"(st[0]), "+v"(st[1]), "+v"(st[2]), "+v"(st[3]), "+v"(st[4]), "+v"(st[5]), "+v"(st[6]) : "n"(k_) : "memory")
#define STBAD() (!__all(max(max(max(st[0].y, st[1].y), max(st[2].y, st[3].y)), max(max(st[4].y, st[5].y), st[6].y)) != EMPTY))
#define HBA(c_, q) _Pragma("unroll") for (int i = 0; i < 4; ++i) asm volatile("global_load_dwordx4 %0, %1, %2 offset:%3 sc1" : c_(hb[4 * (q) + i]) : "v"(lane * 16), "s"(ws + WS_H + (T32 * 28 + 4 * (q)) * 1024), "n"(i * 1024) : "memory")
    lds_wait2(L, S_HFA, S_HFB);
    if (CLW == 1) {
#pragma unroll
      for (int i = NCL; i < 7; ++i) ST16(rs, empty4, lane * 16, hho + i * 8192);
    }
    __builtin_amdgcn_s_sleep(DLY3);
    if (!SPEC || r2 >= 14) {
#pragma nounroll
      for (u32 sp = 0; sp < SPIN_CAP; ++sp) { STA("=v"); STW(0); if (!STBAD()) break; __builtin_amdgcn_s_sleep(RSL); }
    }
    STA("=v");
    STAMP(17);
#pragma unroll
    for (int q = 0; q < HLA / 4; ++q) { HBA("=v", q); }
    u32 wb_[3] = {(u32)lane * 16, (u32)lane * 16 + 2 * SLOT, (u32)lane * 16 + 4 * SLOT};
    asm volatile("" : "+v"(wb_[1]), "+v"(wb_[2]));
#define WFRAG(sl, s) (*(LAS u32x4*)(L + wb_[(sl) >> 1] + ((sl) & 1) * SLOT + (s) * 1024))
    LAS char* csl = L + L_CS + hh * 16;
    u32x4 ring[7];
    f32x4 cq[4];
    float nmu, ve, k0, k1;
    u32 mx;
    const int pvo = WS_FHH + ((tile * 16 + r) * 4 + wave) * 128;
#define PROG(n) FLAG(pvo + 131072, n)
    f32x16 acc[3];
    float y[8], gu[8];
    u32x4 yb;
    u32x4 cw;
#define LT(i)
#define CQ(j_, k_) do { const f32x4 c_ = *(LAS f32x4*)(csl + (j_) * 128 + (k_) * 32); _Pragma("unroll") for (int i_ = 0; i_ < 4; ++i_) acc[(j_) % 3][4 * (k_) + i_] = c_[i_]; } while (0)
#define HB1(i_) asm volatile("global_load_dwordx4 %0, %1, %2 offset:%3 sc1" : "=v"(hb[i_]) : "v"(lane * 16), "s"(ws + WS_H + (T32 * 28 + ((i_) & ~3)) * 1024), "n"(((i_) & 3) * 1024) : "memory")
#define FCL() asm volatile("global_load_dwordx2 %0, %1, %2 sc1" : "=v"(fcl) : "v"(p4 * 128), "s"(ws + WS_FH + (T32 * 32 + 28) * 128) : "memory")
    auto gu01 = [&](auto LC) LAM {
      constexpr bool LD = LC.value;
      sfor<0, 56>([&](auto NC) LAM {
        constexpr int n = NC.value, s = n >> 1, m = n + 7;
        if constexpr (LD && !(n & 1)) { u32x4 t_ = hb[s]; asm volatile("s_waitcnt vmcnt(%1)" : "+v"(t_) : "n"((HLA + s < 29 ? HLA + s : 29) - s - 1 + (CLW == 2 && n > CN ? (n - CN < 7 - NCL ? n - CN : 7 - NCL) : 0)) : "memory"); hb[s] = t_; }
        acc[n & 1] = mfma32(ring[n % 7], hb[s], acc[n & 1]);
        SCHED();
        if constexpr (LD && (n & 1) && HLA + s < 28) HB1(HLA + s);
        if constexpr (LD && (n & 1) && HLA + s == 28) FCL();
        if constexpr (CLW == 2 && LD && n >= CN && n < CN + 7 - NCL) ST16(rs, empty4, lane * 16, hho + (n - CN + NCL) * 8192);
        if constexpr (n & 1) mx = max(mx, hb[s].w);
        if constexpr (m < 56) ring[n % 7] = WFRAG(m & 1, m >> 1); else ring[n % 7] = WFRAG(2, m - 56);
        if constexpr (n >= 34 && n < 38) CQ(2, n - 34);
        if constexpr (n >= 44 && n < 52) { acc[2][2 * (n - 44)] *= nmu; acc[2][2 * (n - 44) + 1] *= nmu; PIN(acc[2]); }
        SCHED();
      });
      STAMP(18);
    };
    auto gu_block = [&](auto JC) __attribute__((always_inline)) {
      constexpr int j = JC.value, NE_ = j == 2 ? 16 : 8;
      sfor<0, 28>([&](auto SC) LAM {
        constexpr int s = SC.value;
        if constexpr (PACE > 0 && j >= PJ0 && j <= PJ1 && s % PS == 0) __builtin_amdgcn_s_sleep(PACE);
        if constexpr (HW && s % 2 == HW - 1 && (s < 27 || j < 6)) { asm volatile("" :: "v"(ring[(s + 1) % 7])); SCHED(); }
        acc[j % 3] = mfma32(ring[s % 7], hb[s], acc[j % 3]);
        constexpr int sg = j == 3 ? S_WIN3 : j == 4 ? S_REFILL : S_FREE01;
        if constexpr (j >= 3 && j < 6 && s == 12) cw = *(volatile LAS u32x4*)(L + L_FLG + sg * 16);
        if constexpr (j >= 3 && j < 6 && s == 20) { if (__builtin_expect(!__builtin_amdgcn_readfirstlane(cw.x & cw.y & cw.z & cw.w), 0)) lds_wait(L, sg); asm volatile("" ::: "memory"); }
        if constexpr (!CLW && j == 2 && s < 7 - NCL) ST16(rs, (u32x4{EMPTY, EMPTY, EMPTY, EMPTY}), lane * 16, hho + (NCL + s) * 8192);
        if constexpr (s + 7 < 28) ring[s % 7] = WFRAG(gu_slot(j), s + 7);
        else if constexpr (j < 6) ring[s % 7] = WFRAG(gu_slot(j + 1), s - 21);
        if constexpr (s == 0 && j == 2) lds_signal(L, S_WSEEN, wave);
        if constexpr (s == 0 && j >= 3) *(volatile LAS u32*)(L + L_FLG + S_DONE4 * 16 + wave * 4) = j - 2u;
        sfor<0, 48>([&](auto EC) LAM {
          constexpr int e = EC.value, i = e & 7, a = e >> 3, k = 28 * (j - 2) + s - act0(e);
          if constexpr (k >= 0 && k < 10) {
            const float ga = acc[a % 3][i];
            if constexpr (k == 0) y[i] = ga * ga;
            if constexpr (k == 1) y[i] = fmaf(y[i], k1, k0);
            if constexpr (k == 2) y[i] = ga * y[i];
            if constexpr (k == 3) y[i] = fexp2(y[i]);
            if constexpr (k == 4) y[i] = fmaf(y[i], ve, ve);
            if constexpr (k == GUK) { gu[i] = ga * acc[a % 3][i + 8]; PIN(gu[i]); }
            if constexpr (k == RCK) y[i] = frcp(y[i]);
            if constexpr (k == 7) y[i] = y[i] * gu[i];
            if constexpr (k < 5 || k == RCK || k == 7) PIN(y[i]);
            if constexpr (k == 8 && (e & 1)) { u32 p_ = pk(y[i - 1], y[i]); if (i == 7) p_ = filled(p_); PIN(p_); yb[i >> 1] = p_; }
            if constexpr (k == 8 && e == 7) WAITVM(0);
            if constexpr (k == 8 && i == 7) ST16(rs, yb, lane * 16, hho + a * 8192);
            if constexpr (k == 9 && e == 7) FLAG(pvo, 1);
          }
        });
        constexpr int cq0 = j == 2 ? CQ2 : CQ0;
        if constexpr (j < 6 && s >= cq0 && s < cq0 + 4) CQ(j + 1, s - cq0);
        if constexpr (j < 6 && s >= cq0 + 6 && s < 27) {
          sfor<0, 16>([&](auto EC) LAM {
            constexpr int e = EC.value;
            if constexpr (cq0 + 6 + e * (21 - cq0) / 16 == s) { float v_ = acc[(j + 1) % 3][e] * nmu; PIN(v_); acc[(j + 1) % 3][e] = v_; }
          });
        }
        SCHED();
      });
      if (j == 3) STAMP(26);
      if (j == 2) STAMP(29);
      if (j == 4) STAMP(30);
      if (j == 5) STAMP(31);
    };
#define HBW(q) asm volatile("s_waitcnt vmcnt(0)" : "+v"(hb[4 * (q)]), "+v"(hb[4 * (q) + 1]), "+v"(hb[4 * (q) + 2]), "+v"(hb[4 * (q) + 3]) :: "memory")
#define HBBAD(q) (!__all(max(max(hb[4 * (q)].w, hb[4 * (q) + 1].w), max(hb[4 * (q) + 2].w, hb[4 * (q) + 3].w)) != EMPTY))
#define HBC(q, c_) asm volatile("s_bitcmp0_b32 %6, %7\n\ts_cbranch_scc1 1f\n\tglobal_load_dwordx4 %0, %4, %5 sc1\n\tglobal_load_dwordx4 %1, %4, %5 offset:1024 sc1\n\t" \
      "global_load_dwordx4 %2, %4, %5 offset:2048 sc1\n\tglobal_load_dwordx4 %3, %4, %5 offset:3072 sc1\n1:" : "+v"(hb[4 * (q)]), "+v"(hb[4 * (q) + 1]), "+v"(hb[4 * (q) + 2]), "+v"(hb[4 * (q) + 3]) \
      : "v"(lane * 16), "s"(ws + WS_H + (T32 * 28 + 4 * (q)) * 1024), "s"(c_), "n"(q) : "memory", "scc")
#define REDO(m_) do { STA("+v"); _Pragma("unroll") for (int q = 0; q < 7; ++q) HBC(q, m_); STW(0); _Pragma("unroll") for (int q = 0; q < 7; ++q) HBW(q); } while (0)
#define FIX() do { _Pragma("nounroll") for (u32 sp = 0; sp < SPIN_CAP; ++sp) { u32 m = 0; \
        _Pragma("unroll") for (int q = 0; q < 7; ++q) m |= HBBAD(q) ? 1u << q : 0u; \
        if (!m && !STBAD()) break; if (sp) __builtin_amdgcn_s_sleep(RSL); m = __builtin_amdgcn_readfirstlane(m); REDO(m); } } while (0)
    auto first0 = [&]() LAM {
#pragma unroll
      for (int k = 0; k < 8; ++k) { const f32x4 c_ = *(LAS f32x4*)(csl + k * 32); acc[k >> 2][4 * (k & 3)] = c_[0]; acc[k >> 2][4 * (k & 3) + 1] = c_[1]; acc[k >> 2][4 * (k & 3) + 2] = c_[2]; acc[k >> 2][4 * (k & 3) + 3] = c_[3]; }
#pragma unroll
      for (int s = 0; s < 7; ++s) ring[s] = WFRAG(s & 1, s >> 1);
    };
    auto first = [&](auto LC) LAM {
      if (!LC.value || !PRE) first0();
      f32x2 sf[7];
#pragma unroll
      for (int i = 0; i < 7; ++i) sf[i] = __builtin_bit_cast(f32x2, st[i]);
      float s1 = ((sf[0].x + sf[1].x) + (sf[2].x + sf[3].x)) + ((sf[4].x + sf[5].x) + sf[6].x);
      float s2 = ((sf[0].y + sf[1].y) + (sf[2].y + sf[3].y)) + ((sf[4].y + sf[5].y) + sf[6].y);
      { f32x2 a = sw32(s1); s1 = a.x + a.y; a = sw32(s2); s2 = a.x + a.y; }
      nmu = s1 * (-1.f / 448.f); ve = fmaxf(fmaf(-nmu, nmu, s2 * (1.f / 448.f)), 0.f) + 1e-5f;
      const float rstd = __builtin_amdgcn_rsqf(ve);
      k0 = -2.30220819f * rstd; k1 = -0.10294324f * rstd * rstd * rstd;
#pragma unroll
      for (int i = 0; i < 32; ++i) acc[i >> 4][i & 15] *= nmu;
      mx = 0;
      __builtin_amdgcn_s_setprio(GUPRIO);
      SCHED();
      gu01(LC);
    };
    if (PRE == 1) { first0(); SCHED(); }
    STW(HLA);
    if (__builtin_expect(STBAD(), 0)) {
#pragma unroll
      for (int q = 0; q < HLA / 4; ++q) HBW(q);
#pragma nounroll
      for (u32 sp = 0; sp < SPIN_CAP; ++sp) {
        u32 m = 0;
#pragma unroll
        for (int i = 0; i < 7; ++i) { const u64 bm = __builtin_amdgcn_ballot_w64(st[i].y == EMPTY); m |= ((u32)bm ? 1u << (i >> 1) : 0u) | ((u32)(bm >> 32) ? 1u << ((7 + i) >> 1) : 0u); }
        m = __builtin_amdgcn_readfirstlane(m);
        if (!m) break;
        if (sp) __builtin_amdgcn_s_sleep(RSL);
        STA("+v");
#pragma unroll
        for (int q = 0; q < HLA / 4; ++q) HBC(q, m);
        STW(0);
#pragma unroll
        for (int q = 0; q < HLA / 4; ++q) HBW(q);
      }
    }
    first(IC<1>{});
    if (__builtin_expect(!__all(mx != EMPTY), 0)) { FIX(); first(IC<0>{}); }
    asm volatile("s_waitcnt vmcnt(%1)" : "+v"(fcl) : "n"(CLW == 2 ? 7 - NCL : 0) : "memory");
    if (__builtin_expect(!__all(fcl == tag + 1), 0)) poll_flags(ws, WS_FH + (T32 * 32 + 28 + p4) * 128, lane < 4, tag);
    gu_block(IC<2>{}); gu_block(IC<3>{}); gu_block(IC<4>{}); gu_block(IC<5>{}); gu_block(IC<6>{});
#pragma unroll
    for (int i = 0; i < 8; ++i) { const float ga = acc[0][i]; y[i] = ga * acc[0][i + 8] * frcp(fmaf(fexp2(ga * fmaf(ga * ga, k1, k0)), ve, ve)); }
    yb = u32x4{pk(y[0], y[1]), pk(y[2], y[3]), pk(y[4], y[5]), filled(pk(y[6], y[7]))};
    ST16(rs, yb, lane * 16, hho + 6 * 8192);
    *(volatile LAS u32*)(L + L_FLG + S_DONE4 * 16 + wave * 4) = 5u;
    __builtin_amdgcn_s_setprio(0);
    STAMP(19);
    if (r2 < 14) {
      f32x16 da[2][2] = {};
      u32x4 hq[2][8], wl[2][8];
      HBASE();
      __builtin_amdgcn_s_setprio(ATP);
      lds_wait1(L, S_SPARE, w2);
      STAMP2(15);
      HQLOAD(0, 4);
      WDREAD(wl[0], 1);
      SCHED();
      if (A6L == 0) { __builtin_amdgcn_s_sleep(ADLY); HQLOAD(1, 6); }
      STAMP2(16);
      HQCHECK(0, 4);
      if (A6L == 1) { __builtin_amdgcn_s_sleep(ADLY); HQLOAD(1, 6); }
      SCHED();
      DOWN16(da, wl[0], hq[0]);
      SCHED();
      if (A6L == 2) { __builtin_amdgcn_s_sleep(ADLY); HQLOAD(1, 6); }
      lds_wait1(L, S_CONS, w2);
      WDREAD(wl[1], 0);
      STAMP2(18);
      HQCHECK(1, 6);
      STAMP2(31);
      DOWN_OUT(da, wl[1], hq[1], 0, 1);
      STAMP(20);
    }
  } else {
    lds_wait2(L, S_HFA, S_HFB);
    if (TD == 6 && w2 == 0) DTOUCH();
    if (TD == 7) dma4(wd + min(b * 49 + w2 * 13, 12544 - 13) * 128, min(lane, 12) * 128, L_JUNK);
    __builtin_amdgcn_s_setprio(BP3);
    if (r2 >= 14) GUBLK(4);
#define REFILL() do { lds_wait<BSL>(L, S_WSEEN); GUBLK(5); GUBLK(6); } while (0)
    if (r2 >= 14) {
      WAITVM(0); lds_signal(L, S_WIN3, w2);
      REFILL();
      WAITVM(0); lds_signal(L, S_REFILL, w2); lds_signal(L, S_FREE01, w2);
    } else {
      u64 pv;
      const char* hs = ws + ho + 4096;
#define PEEK() do { if (AP) { pv = tag + 7; asm volatile("s_mov_b64 exec, 0xff\n\tglobal_atomic_add_x2 %0, %1, %2, %3 sc0\n\ts_mov_b64 exec, -1" : "+v"(pv) : "v"(po), "v"(0ull), "s"(ws) : "memory"); } \
        else asm volatile("global_load_dwordx2 %0, %1, %2 sc1" : "=v"(pv) : "v"(po), "s"(ws) : "memory"); } while (0)
#define PEEKED(k_, n_) do { asm volatile("s_waitcnt vmcnt(%1)" : "+v"(pv) : "n"(k_) : "memory"); PMIN(pv); NEEDP(n_); } while (0)
#define HQA(c_, q, j) _Pragma("unroll") for (int i = 0; i < 8; ++i) asm volatile("global_load_dwordx4 %0, %1, %2 offset:%3 sc1" : c_(hq[q][i]) : "v"(lane * 16), "s"(hs + (j) * 8192), "n"(i * 1024 - 4096) : "memory")
#define HQW(q, k_) asm volatile("s_waitcnt vmcnt(%8)" : "+v"(hq[q][0]), "+v"(hq[q][1]), "+v"(hq[q][2]), "+v"(hq[q][3]), "+v"(hq[q][4]), "+v"(hq[q][5]), "+v"(hq[q][6]), "+v"(hq[q][7]) : "n"(k_) : "memory")
#define HQOK(q, j, k_) do { HQW(q, k_); if (__builtin_expect(HQBAD(q), 0)) { _Pragma("nounroll") for (u32 sp = 0; sp < SPIN_CAP; ++sp) { \
        __builtin_amdgcn_s_sleep(RSL); HQA("+v", q, j); HQW(q, 0); if (!HQBAD(q)) break; } } SCHED(); } while (0)
      {
        u32x2 e[24][2];
        const u32 ea_ = 0, eb_ = 0;
        asm volatile("" : "+v"(da_), "+v"(db_), "+v"(dc_), "+v"(dd_));
#define WBL(N0, N1, NS, SL) sfor<N0, N1>([&](auto NC) LAM { constexpr int n = NC.value, k = n - (N0); \
          if constexpr (k < (NS)) dma_wd1<n, (SL) * SLOT + k * 1024>(rd, dvo, w2 * 7168); else dma_wd1<n, L_EX + (k - (NS)) * 4096>(rd, dvo, w2 * 1024); })
#define WBR(N0, N1, NS, SL) do { sfor<N0, N1>([&](auto NC) LAM { constexpr int n = NC.value, k = n - (N0); \
          if constexpr (k >= (NS)) { e[n][0] = trread(L + dc_ + (k - (NS)) * 4096); e[n][1] = trread(L + dd_ + (k - (NS)) * 4096); } \
          else if constexpr ((SL) == 4) { e[n][0] = trread(L + da_ + 2 * SLOT + k * 1024); e[n][1] = trread(L + db_ + 2 * SLOT + k * 1024); } \
          else { e[n][0] = trread(L + ea_ + k * 1024); e[n][1] = trread(L + eb_ + k * 1024); } }); \
          asm volatile("s_waitcnt lgkmcnt(0)" : "+v"(e[(N1) - 1][1]) :: "memory"); } while (0)
        WBL(0, 11, 7, 4);
        WAITVM(0);
        STAMP2(24);
        WBR(0, 11, 7, 4);
        WBL(11, 15, 0, 0);
        WAITVM(0);
        STAMP2(25);
        WBR(11, 15, 0, 0);
        WBL(15, 19, 0, 0);
        GUBLK(4);
        WAITVM(7);
        WBR(15, 19, 0, 0);
        WBL(19, 23, 0, 0);
        STAMP2(27);
        lds_wait<BSL>(L, S_WSEEN);
        GUBLK(5);
        GUBLK(6);
        WAITVM(14);
        lds_signal(L, S_WIN3, w2);
        STAMP2(26);
        WBR(19, 23, 0, 0);
        dma_wd1<23, L_EX + 3 * 4096>(rd, dvo, w2 * 1024);
        FREE(1);
        PEEK();
        WDLOAD(3, 0);
        WAITVM(17);
        lds_signal(L, S_REFILL, w2);
        STAMP2(5);
        PEEKED(8, 0);
        lds_signal(L, S_FREE01, w2);
        e[23][0] = trread(L + dc_ + 3 * 4096); e[23][1] = trread(L + dd_ + 3 * 4096);
#pragma unroll
        for (int n = 0; n < 24; ++n) wdf[n >> 3][n & 7] = u32x4{e[n & ~1][n & 1].x, e[n & ~1][n & 1].y, e[n | 1][n & 1].x, e[n | 1][n & 1].y};
      }
      STAMP2(23);
      f32x16 acc[2][2];
      u32x4 hq[2][8];
      STAMP2(16);
      HBASE();
#pragma unroll
      for (int k = 0; k < 3; ++k) asm volatile("" : "+v"(wdf[k][0]), "+v"(wdf[k][1]), "+v"(wdf[k][2]), "+v"(wdf[k][3]), "+v"(wdf[k][4]), "+v"(wdf[k][5]), "+v"(wdf[k][6]), "+v"(wdf[k][7]) :: "memory");
      const bool w4e = have < 1;
      if (w4e) { FREE(2); WDLOAD(4, 1); }
      NEEDP(1);
      STAMP2(1);
      HQA("=v", 0, 0);
      if (!w4e && W4O == 0) { FREE(2); WDLOAD(4, 1); }
      STAMP2(2);
      HQA("=v", 1, 1);
      if (W4O == 1) { if (!w4e) { FREE(2); WDLOAD(4, 1); } else WAITVM(8); }
      FREE(3);
      auto round = [&](auto JC) LAM {
        constexpr int J = JC.value, q = J & 1, w = J % 3, nb = 2 - 2 * J;
        __builtin_amdgcn_s_setprio(J < 2 ? DP0 : DNPRIO);
        sfor<0, 18>([&](auto MC) LAM {
          constexpr int m = MC.value, i = m - 8 - (m > 13 ? 2 : 0);
          if constexpr (m < 16) macc<J == 0 && m < 4 ? 0 : 1>(acc[(m >> 1) & 1][m & 1], wdf[w][2 * (m >> 2) + (m & 1)], hq[q][2 * (m >> 2) + ((m >> 1) & 1)]);
          SCHED();
          if constexpr (J < 2 && m < 7) dma_wd1<(J + 5) * 8 + m, (nb + 2) * SLOT + m * 1024>(rd, dvo, w2 * 7168);
          if constexpr (J < 2 && m == 7) dma_wd1<(J + 5) * 8 + 7, L_EX + nb * 4096>(rd, dvo, w2 * 1024);
          if constexpr (!CLW && J == 0 && m == 7) PEEK();
          if constexpr (J == 1 && m == 7) { if (CLW) WAITVM(16); else PEEKED(16, 3); lds_signal(L, S_SPARE, w2); FREE(4); }
          if constexpr (J == 2 && m == 7) { WAITVM(8); lds_signal(L, S_CONS, w2); FREE(5); if (B5D) __builtin_amdgcn_s_sleep(B5D); }
          if constexpr (J < 3 && m >= 8 && m != 14 && m != 15) {
            asm volatile("global_load_dwordx4 %0, %1, %2 offset:%3 sc1" : "=v"(hq[q][i]) : "v"(lane * 16), "s"(hs + (J == 2 ? 5 : J + 2) * 8192), "n"(i * 1024 - 4096) : "memory");
            if constexpr (J < 2) {
              LAS char* p_ = L + (i & 1 ? db_ : da_) + 2 * J * SLOT + (i >> 1) * 2048;
              const u32x2 x_ = trread(p_), y_ = trread(i < 6 ? p_ + 1024 : L + (i & 1 ? dd_ : dc_) + 2 * J * 4096);
              wdf[J][i] = u32x4{x_.x, x_.y, y_.x, y_.y};
            }
          }
          SCHED();
        });
        if (J < 2) __builtin_amdgcn_s_setprio(BP3);
      };
      HQOK(0, 0, W4O ? 16 : 8);
      round(IC<0>{});
      STAMP2(8);
      HQOK(1, 1, CLW ? 16 : 17);
      asm volatile("s_waitcnt lgkmcnt(0)" : "+v"(wdf[0][7]) :: "memory");
      round(IC<1>{});
      STAMP2(9);
      HQOK(0, 2, 16);
      round(IC<2>{});
      STAMP2(10);
      HQOK(1, 3, 8);
      round(IC<3>{});
      STAMP2(11);
      HQOK(0, 5, 0);
      STAMP2(4);
      DOWN_OUT(acc, wdf[1], hq[0], 2, 3);
      STAMP(20);
    }
  }
  if (r2 < 14) {
    BAR();
    STAMP2(7);
    float c[8] = {blo(hres.x), bhi(hres.x), blo(hres.y), bhi(hres.y), blo(hres.z), bhi(hres.z), blo(hres.w), bhi(hres.w)};
    const int rl = ((tb & 1) * 16 + l15) * 16;
    f32x4 v0 = {c[0], c[1], c[2], c[3]}, v1 = {c[4], c[5], c[6], c[7]};
#pragma unroll
    for (int h = 0; h < 2; ++h) {
      const int bf = (tb >> 1) + 2 * h;
      const bool p7 = kh && g == 3;
      u32 rq = (p7 ? L_EX + bf * 4096 : (bf == 3 ? 0 : (bf + 2) * SLOT) + (4 * kh + g) * 1024) + rl;
      const u32 st7 = p7 ? 1024 : 7168;
      asm volatile("" : "+v"(rq));
#pragma unroll
      for (int w = 0; w < 4; ++w) { v0 += *(LAS f32x4*)(L + rq + w * st7); v1 += *(LAS f32x4*)(L + rq + w * st7 + 512); }
    }
#pragma unroll
    for (int i = 0; i < 4; ++i) { c[i] = v0[i]; c[4 + i] = v1[i]; }
    { const u32x4 ov = {pk(c[0], c[1]), pk(c[2], c[3]), pk(c[4], c[5]), pk(c[6], c[7])}; asm volatile("global_store_dwordx4 %0, %1, %2 sc1\n\ts_nop 1" :: "v"(xo), "v"(ov), "s"(out) : "memory"); }
    if (A && b == 0 && tid == 0) __hip_atomic_store((GAS u64*)(ws + WS_E), tag, RLX_AGENT);
  }
  STAMP(21);
  };
  if (wave < 4) role(IC<1>{}); else role(IC<0>{});
}

extern "C" void kernel_launch(void* const* d_in, const int* in_sizes, int n_in, void* d_out, int out_size, void* d_ws, size_t ws_size, hipStream_t stream) {
  (void)hipFuncSetAttribute((const void*)fused_block, hipFuncAttributeMaxDynamicSharedMemorySize, LDS_BYTES);
  fused_block<<<256, 512, LDS_BYTES, stream>>>((const char*)d_in[0], (const char*)d_in[1], (const char*)d_in[2], (const char*)d_in[3], (const char*)d_in[5], (const char*)d_in[6],
                                               (const char*)d_in[7], (char*)d_out, (char*)d_ws);
}
```

```cpp
#include <hip/hip_runtime.h>
#include <hip/hip_bf16.h>
#include <stdint.h>

typedef unsigned u32;
typedef unsigned long long u64;
typedef u32 u32x4 __attribute__((ext_vector_type(4)));
typedef u32 u32x2 __attribute__((ext_vector_type(2)));
typedef float f32x2 __attribute__((ext_vector_type(2)));
typedef float f32x4 __attribute__((ext_vector_type(4)));
typedef float f32x16 __attribute__((ext_vector_type(16)));
typedef short s16x4 __attribute__((ext_vector_type(4)));
typedef __bf16 bf16x2 __attribute__((ext_vector_type(2)));
typedef __bf16 bf16x8 __attribute__((ext_vector_type(8)));
#define LAS __attribute__((address_space(3)))
#define GAS __attribute__((address_space(1)))
#define DI __device__ __forceinline__
#define RLX_AGENT __ATOMIC_RELAXED, __HIP_MEMORY_SCOPE_AGENT

constexpr int WS_E = 0;
constexpr int WS_FKV = 8192;
constexpr int WS_FO = WS_FKV + 131072;
constexpr int WS_FH = WS_FO + 131072;
constexpr int WS_FG = WS_FH + 262144;
constexpr int WS_FHH = WS_FG + 172032;
constexpr int WS_CS = WS_FHH + 262144;
constexpr int WS_ST = WS_CS + 200704;
constexpr int WS_WGU = WS_ST + 229376;
constexpr int WS_WD = WS_WGU + 3211264;
constexpr int WS_KVP = WS_WD + 1605632;
constexpr int WS_O = WS_KVP + 1048576;
constexpr int WS_H = WS_O + 2097152;
constexpr int WS_HH = WS_H + 1835008;
#define UNITX4
#define CS7
#define NOWD
#define HHOFF(T32, fb) (((((T32) >> 1) * 4 + (fb) / 28) * 7 + (fb) % 7) * 8 + ((fb) / 7 & 3) * 2 + ((T32) & 1))
constexpr int WS_STAMP = WS_HH + 7340032;
constexpr int WS_LOG = WS_STAMP + 256 * 8 * 32 * 8;
constexpr int WS_USED = WS_LOG + 4096;
constexpr int SLOT = 28672;
constexpr int L_WK = 4 * SLOT, L_WV = 0, L_WQ = 14336;
constexpr int L_PKV = 2 * SLOT;
constexpr int L_WO = 3 * SLOT;
constexpr int L_EX = 5 * SLOT;
constexpr int L_KVA = 5 * SLOT;
constexpr int L_ST1 = 5 * SLOT + 4096;
constexpr int L_WPHI = 5 * SLOT + 5120;
constexpr int L_CSP = 5 * SLOT + 7168;
constexpr int L_CS = 159744;
constexpr int L_FLG = 160768;
constexpr int L_JUNK = 161280;
constexpr int L_STAMP = 161536;
constexpr int LDS_BYTES = 163840;
constexpr int gu_slot(int j) { return j % 5; }
constexpr int CODE_PF = 45056;
constexpr u32 SPIN_CAP = 1u << 16;
constexpr int T2 = 70;
constexpr int DLY3 = 15;
constexpr int BSL = 4, RSL = 4, PSLP = 4;
constexpr int HLA = 12;
constexpr int NCL = 4;
constexpr int CN = 50, OLAG = 5, X0A = 6;

#define STAMP(i) do {} while (0)
#define STAMP2(i) do {} while (0)

DI f32x4 mfma16(u32x4 a, u32x4 b, f32x4 c) { return __builtin_amdgcn_mfma_f32_16x16x32_bf16(__builtin_bit_cast(bf16x8, a), __builtin_bit_cast(bf16x8, b), c, 0, 0, 0); }
DI f32x16 mfma32(u32x4 a, u32x4 b, f32x16 c) { return __builtin_amdgcn_mfma_f32_32x32x16_bf16(__builtin_bit_cast(bf16x8, a), __builtin_bit_cast(bf16x8, b), c, 0, 0, 0); }
DI u32 pk(float lo, float hi) { f32x2 p = {lo, hi}; return __builtin_bit_cast(u32, __builtin_convertvector(p, bf16x2)); }
DI float blo(u32 v) { return __builtin_bit_cast(float, v << 16); }
DI float bhi(u32 v) { return __builtin_bit_cast(float, v & 0xffff0000u); }
DI u32x2 trread(LAS char* p) { return __builtin_bit_cast(u32x2, __builtin_amdgcn_ds_read_tr16_b64_v4i16((LAS s16x4*)p)); }
DI u32x4 trfrag(LAS char* p, int step) { u32x2 a = trread(p), b = trread(p + step); return u32x4{a.x, a.y, b.x, b.y}; }
DI float fexp2(float x) { return __builtin_amdgcn_exp2f(x); }
DI float frcp(float x) { return __builtin_amdgcn_rcpf(x); }
template <int C> DI float dppror(float x) { return __builtin_bit_cast(float, __builtin_amdgcn_update_dpp(0, __builtin_bit_cast(int, x), 0x120 + C, 0xf, 0xf, false)); }
DI f32x2 sw16(float x) { u32 u = __builtin_bit_cast(u32, x); auto r = __builtin_amdgcn_permlane16_swap(u, u, false, false); return f32x2{__builtin_bit_cast(float, (u32)r[0]), __builtin_bit_cast(float, (u32)r[1])}; }
DI f32x2 sw32(float x) { u32 u = __builtin_bit_cast(u32, x); auto r = __builtin_amdgcn_permlane32_swap(u, u, false, false); return f32x2{__builtin_bit_cast(float, (u32)r[0]), __builtin_bit_cast(float, (u32)r[1])}; }

#define BAR() asm volatile("s_waitcnt lgkmcnt(0)\n\ts_barrier" ::: "memory")
#define WAITVM(n) asm volatile("s_waitcnt vmcnt(" #n ")" ::: "memory")
#define DRAIN() do { asm volatile("s_waitcnt vmcnt(0)" ::: "memory"); __builtin_amdgcn_s_waitcnt(0x0F70); } while (0)
#define SCHED() __builtin_amdgcn_sched_barrier(0)
DI void dma16(const void* base, u32 voff, u32 lds) {
  u32 keep;
  asm volatile("s_mov_b32 %0, m0\n\ts_mov_b32 m0, %3\n\ts_nop 0\n\tglobal_load_lds_dwordx4 %1, %2\n\ts_mov_b32 m0, %0" : "=&s"(keep) : "v"(voff), "s"(base), "s"(lds) : "memory");
}
template <int SI, int LI> DI void dma7(__amdgpu_buffer_rsrc_t rs, u32 voff, u32 so, u32 lds) {
  u32 t;
  asm volatile("s_add_u32 m0, %4, %6\n\ts_add_u32 %0, %3, %5\n\tbuffer_load_dwordx4 %1, %2, %0 offen sc1 lds\n\tbuffer_load_dwordx4 %1, %2, %0 offen offset:1024 sc1 lds\n\t"
               "buffer_load_dwordx4 %1, %2, %0 offen offset:2048 sc1 lds\n\tbuffer_load_dwordx4 %1, %2, %0 offen offset:3072 sc1 lds\n\t"
               "s_add_u32 m0, m0, 4096\n\ts_add_u32 %0, %0, 4096\n\tbuffer_load_dwordx4 %1, %2, %0 offen sc1 lds\n\tbuffer_load_dwordx4 %1, %2, %0 offen offset:1024 sc1 lds\n\t"
               "buffer_load_dwordx4 %1, %2, %0 offen offset:2048 sc1 lds"
               : "=&s"(t) : "v"(voff), "s"(rs), "s"(so), "s"(lds), "n"(SI), "n"(LI) : "memory", "scc");
}
constexpr int wrow(int q, int j, int hf) { return (((7 * q + j) >> 2) * 64 + 2 * ((7 * q + j) & 3) + hf) * 896 + 1024; }
#define DL_(n, o) "s_mov_b32 %0, %" #n "\n\tbuffer_load_dwordx4 %1, %2, %0 offen offset:" #o " lds\n\t"
template <int J> DI void dma_wd(__amdgpu_buffer_rsrc_t rs, u32 voff, u32 lds, u32 lds7) {
  u32 t;
  asm volatile("s_mov_b32 m0, %3\n\t" DL_(5, 0) DL_(6, 1024) DL_(7, 2048) DL_(8, 3072) "s_add_u32 m0, m0, 4096\n\t" DL_(9, 0) DL_(10, 1024) DL_(11, 2048) "s_mov_b32 m0, %4\n\t" DL_(12, 0)
               : "=&s"(t) : "v"(voff), "s"(rs), "s"(lds), "s"(lds7), "n"(wrow(0, J, 0)), "n"(wrow(0, J, 1) - 1024), "n"(wrow(1, J, 0) - 2048), "n"(wrow(1, J, 1) - 3072),
                 "n"(wrow(2, J, 0)), "n"(wrow(2, J, 1) - 1024), "n"(wrow(3, J, 0) - 2048), "n"(wrow(3, J, 1)) : "memory", "scc");
}
DI void dmab(__amdgpu_buffer_rsrc_t rs, u32 voff, u32 lds, int lo, int so) {
  u32 t;
  asm volatile("s_add_u32 m0, %3, %4\n\t" DL_(5, 0) : "=&s"(t) : "v"(voff), "s"(rs), "s"(lds), "n"(lo), "n"(so) : "memory", "scc");
}
template <int N, int LO> DI void dma_wd1(__amdgpu_buffer_rsrc_t rs, u32 voff, u32 lds) {
  u32 t;
  asm volatile("s_add_u32 m0, %3, %4\n\t" DL_(5, 0) : "=&s"(t) : "v"(voff), "s"(rs), "s"(lds), "n"(LO), "n"(wrow(N / 2 % 4, N / 8, N % 2)) : "memory", "scc");
}
DI void touch(const void* base, u32 voff) {
  asm volatile("s_mov_b32 m0, %2\n\ts_nop 0\n\tglobal_load_lds_dword %0, %1" :: "v"(voff), "s"(base), "n"(L_JUNK) : "memory");
}
constexpr int xrow(int n) { return ((n / 14) * 16 + (n & 1) * 8) * 896 + ((n % 14) >> 1) * 128 + 2048 - n % 4 * 1024; }
template <int N> DI void dma_x(__amdgpu_buffer_rsrc_t rs, u32 voff, u32 lds) {
  u32 t;
  asm volatile("s_add_u32 m0, %3, %4\n\t" DL_(5, 0) DL_(6, 1024) DL_(7, 2048) DL_(8, 3072)
               : "=&s"(t) : "v"(voff), "s"(rs), "s"(lds), "n"(N * 1024), "n"(xrow(N)), "n"(xrow(N + 1)), "n"(xrow(N + 2)), "n"(xrow(N + 3)) : "memory", "scc");
}
DI u32 min8(u32 c) {
  c = min(c, (u32)__builtin_amdgcn_update_dpp(0, (int)c, 0x121, 0xf, 0xf, false)); c = min(c, (u32)__builtin_amdgcn_update_dpp(0, (int)c, 0x122, 0xf, 0xf, false));
  c = min(c, (u32)__builtin_amdgcn_update_dpp(0, (int)c, 0x124, 0xf, 0xf, false));
  return __builtin_amdgcn_readfirstlane(min(c, (u32)__builtin_amdgcn_update_dpp(0, (int)c, 0x128, 0xf, 0xf, false)));
}
#define ST16(rs, v, off, soff) do { const u32x4 d_ = __builtin_bit_cast(u32x4, v); __builtin_amdgcn_raw_buffer_store_b128(d_, rs, off, soff, 16); asm volatile("s_nop 1" :: "v"(d_) : "memory"); } while (0)

#define FLAG(off, n) __builtin_amdgcn_raw_buffer_store_b64(__builtin_bit_cast(u32x2, tag + (n)), rs, fvo, off, 16)
#define FLAGC(c, off, n) __builtin_amdgcn_raw_buffer_store_b64(__builtin_bit_cast(u32x2, tag + (n)), rs, (c) ? fvo : NOWHERE, off, 16)
constexpr int NOWHERE = 0x7ffffff0;
constexpr u32 EMPTY = 0xffffffffu;
DI u32 filled(u32 w) { return min(w, EMPTY - 1u); }
DI u64 peek(const char* ws, u32 off, bool on, u64 dflt) {
  u64 z = 0, v = dflt;
  asm volatile("" : "+v"(z));
  if (on) v = __hip_atomic_fetch_add((GAS u64*)(ws + off), z, __ATOMIC_RELAXED, __HIP_MEMORY_SCOPE_AGENT);
  return v;
}
DI void poll_flags(const char* ws, u32 off, bool on, u64 tag) {
#pragma nounroll
  for (u32 sp = 0; sp < SPIN_CAP; ++sp) {
    u64 v = tag + 1;
    if (on) v = __hip_atomic_load((const GAS u64*)(ws + off), RLX_AGENT);
    if (__all(v == tag + 1)) break;
  }
  __builtin_amdgcn_fence(__ATOMIC_ACQUIRE, "wavefront");
}
#define POLL_(rd, cmp, sl) "1:\n\t" rd "\n\ts_waitcnt lgkmcnt(0)\n\t" cmp "\n\ts_cbranch_vccz 2f\n\ts_sleep " sl "\n\ts_sub_u32 %1, %1, 1\n\ts_cbranch_scc0 1b\n2:"
template <int SL> DI void lds_poll(u32 at, int off, int least) {
  u32 v, c = SPIN_CAP << 4;
  asm volatile(POLL_("ds_read_b32 %0, %2 offset:%3", "v_cmp_gt_u32 vcc, %5, %0", "%4") : "=&v"(v), "+s"(c) : "v"(at), "n"(off), "n"(SL), "n"(least) : "memory", "scc", "vcc");
}
template <int SL = 2> DI void lds_wait(LAS char*, int sig) { lds_poll<SL>(L_FLG + (threadIdx.x & 3) * 4, sig * 16, 1); }
DI void lds_wait2(LAS char*, int sig, int sig2) { lds_poll<2>(L_FLG + (threadIdx.x & 3) * 4 + (threadIdx.x & 4 ? (sig > sig2 ? sig - sig2 : sig2 - sig) * 16 : 0), (sig < sig2 ? sig : sig2) * 16, 1); }
template <int SL = 2> DI void lds_wait1(LAS char*, int sig, int w) { lds_poll<SL>(L_FLG + w * 4, sig * 16, 1); }
DI void lds_signal(LAS char* L, int sig, int w) {
  asm volatile("" ::: "memory");
  *(volatile LAS u32*)(L + L_FLG + sig * 16 + w * 4) = 1u;
}

template <int N> struct IC { static constexpr int value = N; };
template <int I, int N, class F> DI void sfor(F&& f) { if constexpr (I < N) { f(IC<I>{}); sfor<I + 1, N>(f); } }
#define LAM __attribute__((always_inline))
#define PIN(v) asm volatile("" : "+v"(v))
constexpr float LOG2E = 1.4426950408889634f;
template <int K> DI void macc(f32x4& c, u32x4 a, u32x4 b) {
  if (K == 0) asm volatile("v_mfma_f32_16x16x32_bf16 %0, %1, %2, 0" : "=&v"(c) : "v"(a), "v"(b));
  else if (K == 1) asm volatile("v_mfma_f32_16x16x32_bf16 %0, %1, %2, %0" : "+v"(c) : "v"(a), "v"(b));
  else c = mfma16(a, b, c);
}
template <int K> DI void macc(f32x16& c, u32x4 a, u32x4 b) {
  if (K == 0) asm volatile("v_mfma_f32_32x32x16_bf16 %0, %1, %2, 0" : "=&v"(c) : "v"(a), "v"(b));
  else if (K == 1) asm volatile("v_mfma_f32_32x32x16_bf16 %0, %1, %2, %0" : "+v"(c) : "v"(a), "v"(b));
  else c = mfma32(a, b, c);
}
template <int GAP, int SIG, int NW, class E, class F> DI void project(LAS char* L, u32 w0, u32 w1, const u32x4 (&xf)[2][14], int tp, int lane, int csl, f32x4 (&t)[2][2], f32x2 (&st)[2], E&& pre, F&& mid) {
  u32 wq = (tp & 1 ? w1 : w0) + (tp >> 1) * (7 * 2048 + GAP), ca = csl + (lane >> 4) * 16, sa = L_ST1 + (tp * 32 + (lane & 15)) * 8, fa = L_FLG + SIG * 16 + (lane & (NW - 1)) * 4;
  u32 cw = ca + tp * 64, fw = L_FLG + SIG * 16 + tp * 4, one = 1;
  u32x4 ones = {0x3f803f80u, 0x3f803f80u, 0x3f803f80u, 0x3f803f80u};
  LAS char *p0 = L + w0, *p1 = L + w1, *pq = L + wq, *pca = L + ca, *psa = L + sa, *pfa = L + fa, *pcw = L + cw, *pfw = L + fw;
  asm volatile("" : "+v"(p0), "+v"(p1), "+v"(pq), "+v"(pca), "+v"(psa), "+v"(pfa), "+v"(pcw), "+v"(pfw), "+v"(ones), "+v"(one));
  pre();
  u32x4 cf[7];
  u32x2 ra[7], rb[7];
  f32x4 cs, cp[4];
  u32 sw;
#define WAT(n) (((n) & 1 ? p1 : p0) + ((n) >> 1) * 2048 + ((n) >= 14 ? GAP : 0))
#pragma unroll
  for (int i = 0; i < 7; ++i) cf[i] = trfrag(pq + i * 2048, 256);
  SCHED();
  sfor<0, 7>([&](auto IC_) LAM {
    constexpr int i = IC_.value;
    macc<i == 0 ? 0 : i == 6 ? 2 : 1>(cs, cf[i], ones);
    SCHED();
    if (i < 6) { ra[i] = trread(WAT(i)); rb[i] = trread(WAT(i) + 256); }
    SCHED();
  });
  sfor<0, 28>([&](auto NC) LAM {
    constexpr int n = NC.value, K = n < 2 ? 0 : n < 26 ? 1 : 2;
    const u32x4 f = {ra[n % 7].x, ra[n % 7].y, rb[n % 7].x, rb[n % 7].y};
    macc<K>(t[0][n & 1], f, xf[0][n >> 1]);
    SCHED();
    if constexpr (n + 6 < 28) ra[(n + 6) % 7] = trread(WAT(n + 6));
    if constexpr (n == 24) st[0] = *(LAS f32x2*)(psa);
    if constexpr (n == 22 || n == 23) cp[n - 22] = *(LAS f32x4*)(pca + (n - 22) * 64);
    if constexpr (n >= 26) { float a = cp[n - 26][0] + cp[n - 24][0], b = cp[n - 26][1] + cp[n - 24][1]; PIN(a); PIN(b); cp[n - 26][0] = a; cp[n - 26][1] = b; }
    SCHED();
    macc<K>(t[1][n & 1], f, xf[1][n >> 1]);
    SCHED();
    if constexpr (n + 6 < 28) rb[(n + 6) % 7] = trread(WAT(n + 6) + 256);
    if constexpr (n == 24) st[1] = *(LAS f32x2*)(psa + 128);
    if constexpr (n == 22 || n == 23) cp[n - 20] = *(LAS f32x4*)(pca + (n - 20) * 64);
    if constexpr (n >= 26) { float a = cp[n - 26][2] + cp[n - 24][2], b = cp[n - 26][3] + cp[n - 24][3]; PIN(a); PIN(b); cp[n - 26][2] = a; cp[n - 26][3] = b; }
    if constexpr (n == 2) { if ((lane & 15) == 0) *(LAS f32x4*)(pcw) = cs; }
    if constexpr (n == 3) { asm volatile("" ::: "memory"); *(volatile LAS u32*)(pfw) = one; }
    if constexpr (n == 16) sw = *(volatile LAS u32*)(pfa);
    if constexpr (n == 21) { if (__builtin_expect(__builtin_amdgcn_ballot_w64(sw == 0) != 0, 0)) { lds_wait(L, SIG); if (NW > 4) lds_wait(L, SIG + 1); } asm volatile("" ::: "memory"); }
    mid(n);
    SCHED();
  });
#pragma unroll
  for (int tb = 0; tb < 2; ++tb)
#pragma unroll
    for (int k = 0; k < 2; ++k) t[tb][k] += cp[k] * st[tb].x;
}
DI f32x2 swp16(float a, float b) { auto r = __builtin_amdgcn_permlane16_swap(__builtin_bit_cast(u32, a), __builtin_bit_cast(u32, b), false, false); return f32x2{__builtin_bit_cast(float, (u32)r[0]), __builtin_bit_cast(float, (u32)r[1])}; }
DI f32x2 swp32(float a, float b) { auto r = __builtin_amdgcn_permlane32_swap(__builtin_bit_cast(u32, a), __builtin_bit_cast(u32, b), false, false); return f32x2{__builtin_bit_cast(float, (u32)r[0]), __builtin_bit_cast(float, (u32)r[1])}; }
DI float swadd32(float a, float b) { const f32x2 s = swp32(a, b); return s.x + s.y; }
DI float swadd16(float a, float b) { const f32x2 s = swp16(a, b); return s.x + s.y; }
DI float mx2(float a, float b) { float r; asm("v_max_f32 %0, %1, %2" : "=v"(r) : "v"(a), "v"(b)); return r; }
DI float mx3(float a, float b, float c) { return fmaxf(fmaxf(a, b), c); }
DI void feature_map(const f32x4 (&t)[2][2], const f32x2 (&st)[2], const u32x4 (&wpf)[2], int g, u32x4 (&P)[2][2]) {
  const float pad = g < 3 ? 0.f : -3.0e38f;
  const f32x4 z4 = {0.f, 0.f, 0.f, 0.f};
  const float sc[2] = {st[0].y * LOG2E, st[1].y * LOG2E}, scg = g < 2 ? sc[0] : sc[1];
  float p[2][8], m[4], mp[4];
#pragma unroll
  for (int tb = 0; tb < 2; ++tb) {
    const u32x4 tq = {pk(t[tb][0][0], t[tb][0][1]), pk(t[tb][0][2], t[tb][0][3]), pk(t[tb][1][0], t[tb][1][1]), pk(t[tb][1][2], t[tb][1][3])};
    const f32x4 p0 = mfma16(wpf[0], tq, z4), p1 = mfma16(wpf[1], tq, z4);
#pragma unroll
    for (int i = 0; i < 8; ++i) p[tb][i] = i < 4 ? p0[i] : p1[i - 4];
  }
#pragma unroll
  for (int tb = 0; tb < 2; ++tb) {
    const float* q = p[tb];
    m[2 * tb] = mx3(mx3(q[0], q[1], q[2]), mx3(q[3], q[4], q[5]), mx3(q[5], q[6], q[7]));
    m[2 * tb + 1] = mx3(mx3(-q[0], -q[1], -q[2]), mx3(-q[3], -q[4], -q[5]), mx3(-q[5], -q[6], -q[7]));
  }
  {
    const f32x2 a = swp16(m[0], m[1]), b = swp16(m[2], m[3]);
    const f32x2 c = swp32(mx2(a.x, a.y), mx2(b.x, b.y));
    const float d = mx2(c.x, c.y) * -scg;
    const f32x2 e = swp32(d, d), f0 = swp16(e.x, e.x), f1 = swp16(e.y, e.y);
    m[0] = f0.x; m[1] = f0.y; m[2] = f1.x; m[3] = f1.y;
#pragma unroll
    for (int c2 = 0; c2 < 4; ++c2) mp[c2] = m[c2] + pad;
  }
  float ex[4][8], sm[4];
#pragma unroll
  for (int c = 0; c < 4; ++c) {
#pragma unroll
    for (int i = 0; i < 8; ++i) {
      ex[c][i] = fexp2(fmaf(c & 1 ? -p[c >> 1][i] : p[c >> 1][i], sc[c >> 1], i & 2 ? mp[c] : m[c]));
    }
    sm[c] = ((ex[c][0] + ex[c][1]) + (ex[c][2] + ex[c][3])) + ((ex[c][4] + ex[c][5]) + (ex[c][6] + ex[c][7]));
  }
  {
    const float d = frcp(swadd32(swadd16(sm[0], sm[1]), swadd16(sm[2], sm[3])));
    const f32x2 e = swp32(d, d), f0 = swp16(e.x, e.x), f1 = swp16(e.y, e.y);
    sm[0] = f0.x; sm[1] = f0.y; sm[2] = f1.x; sm[3] = f1.y;
  }
#pragma unroll
  for (int c = 0; c < 4; ++c)
#pragma unroll
    for (int e = 0; e < 4; ++e) P[c >> 1][c & 1][e] = pk(ex[c][2 * e] * sm[c], ex[c][2 * e + 1] * sm[c]);
}
DI u32x4 piece(const u32x4 (&rp)[8], int e) {
  u32x4 f;
#pragma unroll
  for (int m = 0; m < 4; ++m) f[m] = __builtin_amdgcn_perm(rp[2 * m + 1][e >> 1], rp[2 * m][e >> 1], (e & 1) ? 0x07060302u : 0x05040100u);
  return f;
}
#define RP_OPS "+v"(rp[0]), "+v"(rp[1]), "+v"(rp[2]), "+v"(rp[3]), "+v"(rp[4]), "+v"(rp[5]), "+v"(rp[6]), "+v"(rp[7])
constexpr int act0(int e) {
  return e < 8 ? e * 3 / 2 : e < 40 ? 3 * e - 11 : 113 + 2 * (e - 40);
}
enum { S_FREE01 = 0, S_REFILL, S_DONE4, S_WIN3, S_WK, S_WV, S_WQ, S_WSEEN, S_KDONE, S_V, S_PHIK, S_CSV, S_KVA, S_WO, S_CSQ, S_PHI, S_X0, S_X1, S_XA, S_CSK, S_CS, S_WOFA, S_WOFB, S_EXA, S_EXB, S_W2A, S_W2B, S_HFA, S_HFB, S_CONS, S_SPARE, S_XF0 };

__global__ void __launch_bounds__(512, 2) fused_block(const char* __restrict__ x, const char* __restrict__ wqkv, const char* __restrict__ wphi, const char* __restrict__ wo,
                                                      const char* __restrict__ wg, const char* __restrict__ wu, const char* __restrict__ wd, char* __restrict__ out, char* ws) {
  extern __shared__ __attribute__((aligned(16))) char smem[];
  LAS char* L = (LAS char*)smem;
  const int tid = threadIdx.x, lane = tid & 63, wave = __builtin_amdgcn_readfirstlane(tid >> 6), w2 = wave & 3;
  const int b = blockIdx.x, li = b >> 3, xp = (b & 7) >> 1, xh = b & 1;
  const int tile = 4 * xp + (li >> 3), r = 8 * xh + (li & 7);
  const int tile2 = 4 * xp + 2 * xh + (li >> 4), r2 = li & 15;
  const int tg = r2 >= 7 ? 1 : 0, cg = r2 - 7 * tg;
  const int l15 = lane & 15, g = lane >> 4, q4 = l15 >> 2, p4 = lane & 3, hh = lane >> 5;
  const int head = r;
  {
    u64 pc_; asm volatile("s_getpc_b64 %0" : "=s"(pc_));
    touch((const char*)pc_, min((b & ~7) * 256 + (tid >> 5) * 128, CODE_PF));
    *(LAS u32*)(L + L_FLG + (tid & 127) * 4) = 0u;
  }
  int tn = wave < 2 ? 28 : 85 - 24 * wave, ti = (b & 7) * 64 + li * 2 + wave;
  asm volatile("" : "+s"(tn), "+s"(ti));
  ti = wave < 2 ? ti : b;
  u32 tvo = min(ti * tn + min(lane, tn - 1), wave == 3 ? 3135 : wave == 2 ? 9407 : 14335) * 128, xvo = (tile * 128 + w2 * 32 + (lane >> 3)) * 896 + (((lane & 7) ^ (lane >> 3)) * 16);
  asm volatile("s_waitcnt lgkmcnt(0)\n\ts_barrier" : "+v"(tvo), "+v"(xvo) :: "memory");
  asm volatile("" :: "s"(x), "s"(wqkv), "s"(wphi), "s"(wo), "s"(wg), "s"(wu), "s"(wd), "s"(out), "s"(ws));
  STAMP(0);
  u64 epoch;
  if (wave < 4) {
    const char* tb_ = wave < 2 ? x : wave == 2 ? wqkv : wo;
    asm volatile("" : "+s"(tb_));
    touch(tb_, tvo);
  }
  asm volatile("s_load_dwordx2 %0, %1, 0x0" : "=s"(epoch) : "s"(ws) : "memory");
  if (wave >= 4) {
    const __amdgpu_buffer_rsrc_t rx = __builtin_amdgcn_make_buffer_rsrc((void*)(x - 2048), 0, 2048 * 896 + 2048, 0x00020000);
    sfor<0, 7>([&](auto NC) LAM {
      dma_x<4 * NC.value>(rx, xvo, w2 * 28672);
      if (NC.value == X0A) { asm volatile("s_waitcnt vmcnt(%0)" :: "n"(4 * X0A - 10) : "memory"); lds_signal(L, S_X0, w2); }
    });
  }
  asm volatile("s_waitcnt lgkmcnt(0)" : "+s"(epoch) :: "memory");
  const __amdgpu_buffer_rsrc_t rs = __builtin_amdgcn_make_buffer_rsrc((void*)ws, 0, WS_USED, 0x00020000);
  const u32x4 ones = {0x3f803f80u, 0x3f803f80u, 0x3f803f80u, 0x3f803f80u};
  const f32x4 z4 = {0.f, 0.f, 0.f, 0.f};
  auto role = [&](auto AC) __attribute__((always_inline)) {
  constexpr bool A = AC.value;
  if (A) asm volatile("; A" ::: "memory"); else asm volatile("; B" ::: "memory");
  u32x4 rp[8];
  const int fvo = lane < 16 ? lane * 8 : NOWHERE;
  const int uidx = (tile * 10 + __builtin_popcount(~0x2A54u & ((1u << head) - 1u))) * 4 + w2, unit = uidx * 49 / 80;
  const bool has_unit = !((0x2A54 >> head) & 1) && (uidx + 1) * 49 / 80 != unit;
  const int u3 = unit >> 1, umat = unit & 1, ukq = u3 / 28, ufl = u3 - 28 * ukq;
  const u32 guo = WS_WGU + 7 * r * SLOT + w2 * 7168;
#define GUBLK(j) dma7<(j) * SLOT, gu_slot(j) * SLOT>(rs, lane * 16, guo, w2 * 7168)
  const u32 wvo = (lane >> 2) * 2688 + (((lane & 3) ^ (2 * (lane >> 5))) * 16);
  const __amdgpu_buffer_rsrc_t rw = __builtin_amdgcn_make_buffer_rsrc((void*)(wqkv + head * 56 - (head & 1) * 8 + 16 * w2 * 2688), 0, 400 * 2688, 0x00020000);
  const u32 wl0 = w2 * 1024, wl3 = wl0 + (w2 >= 2 ? SLOT - 14336 : 0);
#define WDMA(i, sec, dst, gap) dmab(rw, wvo, (gap) && (i) == 3 ? wl3 : wl0, (dst) + 4096 * (i) + ((gap) && (i) > 3 ? SLOT - 14336 : 0), (sec) + 64 * (i) * 2688)
  const u32 wr0 = (8 * g + q4) * 64 + (((p4 + (head & 1)) ^ (4 * (g & 1))) * 8), wr1 = (8 * g + q4) * 64 + ((((4 + p4 + (head & 1)) & 7) ^ (4 * (g & 1))) * 8);
  const int tb = w2; constexpr int kh = A ? 0 : 1;
  const int hho = WS_HH + (((tile * 2 + (w2 >> 1)) * 4 + (r >> 2)) * 56 + (r & 3) * 2 + (w2 & 1)) * 1024;
  const int tok = tile2 * 128 + tg * 64 + tb * 16 + l15;
  const int hvo = WS_H + ((tok >> 5) * 28 + 4 * cg + (g >> 1)) * 1024 + ((g & 1) * 32 + (tok & 31)) * 16;
  const int sto = WS_ST + (2 * cg * 2048 + tile2 * 128 + tg * 64 + tb * 16) * 8;
  const u32x4 empty4 = {EMPTY, EMPTY, EMPTY, EMPTY};
#define HH_CLEAR() _Pragma("unroll") for (int i = 0; i < 4 * NCL; ++i) ST16(rs, empty4, lane * 16 + (i & 1) * 2048, WS_HH + (((tile2 * 2 + r2 - 14) * 4 + w2) * 56 + kh + (i >> 1) * 4) * 1024)
#define HH_FLAG() FLAG(WS_FH + ((tile2 * 4 + 2 * (r2 - 14) + kh) * 32 + 28 + w2) * 128, 1)
  u32x4 wof[8][4];
  f32x4 csp[4];
  u64 tag;
  const int xo = tok * 896 + cg * 128 + kh * 64 + g * 16;
#define WOF_READ(i0, i1) do { u32 wa_ = L_WO + (kh * 224 + 4 * g + q4) * 128 + 16 * p4 + 64 * (q4 >> 1), wb_ = wa_ ^ 64, wc_ = wa_ + (g == 3 ? 0 : 2048), wd_ = wc_ ^ 64; \
    asm volatile("" : "+v"(wa_), "+v"(wb_), "+v"(wc_), "+v"(wd_)); \
    _Pragma("unroll") for (int i = i0; i < i1; ++i) _Pragma("unroll") for (int k = 0; k < 4; ++k) { \
      const u32x2 a_ = trread(L + (k >> 1 ? wb_ : wa_) + 28 * i * 128 + (k & 1) * 8), c_ = trread(L + (k >> 1 ? wd_ : wc_) + 28 * i * 128 + (k & 1) * 8); \
      wof[i][k] = u32x4{a_.x, a_.y, c_.x, c_.y}; } } while (0)
#define XF_READ(i) _Pragma("unroll") for (int s = 0; s < 14; ++s) xf[i][s] = *(LAS u32x4*)((s & 1 ? xr1 : xr0) + (i) * 14336 + (s >> 1) * 2048)
#define LN_STATS(kh) do { f32x4 g1 = z4, g2 = z4; \
    _Pragma("unroll") for (int s = 0; s < 14; ++s) { g1 = mfma16(ones, xf[kh][s], g1); g2 = mfma16(xf[kh][s], xf[kh][s], g2); } \
    const float d01 = (p4 & 1) ? g2[1] : g2[0], d23 = (p4 & 1) ? g2[3] : g2[2], s2 = (p4 & 2) ? d23 : d01; \
    const float nmu = g1[0] * (-1.f / 448.f), var = fmaxf(s2 * (1.f / 448.f) - nmu * nmu, 0.f); \
    if (g == q4) *(LAS f32x2*)(L + L_ST1 + (w2 * 32 + kh * 16 + l15) * 8) = f32x2{nmu, __builtin_amdgcn_rsqf(var + 1e-5f)}; } while (0)
#define WPF() do { u32 wp[8]; \
    _Pragma("unroll") for (int j = 0; j < 8; ++j) wp[j] = *(LAS u32*)(L + L_WPHI + 4 * g * 56 + min(l15, 13) * 4 + ((j & 3) + 16 * (j >> 2)) * 56); \
    _Pragma("unroll") for (int e = 0; e < 4; ++e) { const bool z = e >= 2 && g == 3; \
      wpf[0][e] = z ? 0u : __builtin_amdgcn_perm(wp[2 * e + 1], wp[2 * e], 0x05040100u); wpf[1][e] = z ? 0u : __builtin_amdgcn_perm(wp[2 * e + 1], wp[2 * e], 0x07060302u); } \
    asm volatile("" : "+v"(wpf[0]), "+v"(wpf[1])); } while (0)
  {
    u32x4 xf[2][14], wpf[2];
    f32x2 st[2];
    LAS char* xr0 = L + w2 * 28672 + l15 * 128 + ((g ^ (l15 & 7)) * 16);
    LAS char* xr1 = L + w2 * 28672 + l15 * 128 + (((4 + g) ^ (l15 & 7)) * 16);
    LAS char* pw = L + L_PKV + (w2 * 32 + l15) * 224 + g * 8;
    const __amdgpu_buffer_rsrc_t ro = __builtin_amdgcn_make_buffer_rsrc((void*)(wo + (r2 < 14 ? cg : 0) * 128 + w2 * 56 * 896), 0, 280 * 896 - 768, 0x00020000);
    const u32 wovo = (lane >> 3) * 896 + (((lane & 7) ^ ((lane >> 2) & 4)) * 16);
#define WO_HALF(hf_) _Pragma("unroll") for (int n = 0; n < 7; ++n) dmab(ro, wovo, w2 * 7168, L_WO + ((hf_) * 28 + n) * 1024, ((hf_) * 224 + n * 8) * 896)
    int clo = WS_O + ((tile * 8 + 2 * w2) * 16 + head) * 1024, clh = r2 < 14 ? hvo : NOWHERE, cls = r2 < 14 && lane < 16 ? (lane >> 3) * 16384 + (lane & 7) * 16 : NOWHERE, clt = sto;
    int kvo = WS_KVP + ((head * 16 + tile) * 4 + w2) * 1024, fko = WS_FKV + ((head * 4 + w2) * 16 + tile) * 128;
    u32x4 em4 = empty4;
    if constexpr (A) {
      if (wave == 2) { dma16(wphi + head * 1568, lane * 16, L_WPHI); dma16(wphi + head * 1568, min(lane + 64, 97) * 16, L_WPHI + 1024); }
      tag = epoch * 6364136223846793005ull + 1442695040888963407ull;
      f32x4 t[2][2];
      u32 pr = L_PKV + (4 * g + q4) * 224 + 8 * p4;
      asm volatile("" : "+v"(pr), "+v"(pw));
      int clo2 = clo + 16384;
      asm volatile("" : "+s"(clo2));
      asm volatile("" : "+s"(clo), "+v"(clh), "+v"(cls), "+s"(clt), "+s"(kvo), "+s"(fko), "+s"(tag), "+v"(em4));
      asm volatile("" : "+v"(xr0), "+v"(xr1));
      project<0, S_CSK, 8>(L, L_WK + wr0, L_WK + wr1, xf, w2, lane, L_CSP, t, st, [&]() LAM {
      __builtin_amdgcn_s_waitcnt(0x0F70);
      STAMP(1);
      if (wave == 2) { WAITVM(0); lds_signal(L, S_PHI, 0); }
      lds_wait1<3>(L, S_PHI, 0);
      WPF();
      lds_wait1<3>(L, S_X0, w2);
#pragma unroll
      for (int i = 0; i < 7; ++i) WDMA(i, 896, L_WK, 0);
      XF_READ(0);
      if (w2 < 2) lds_signal(L, S_XF0, w2);
      STAMP(27);
      LN_STATS(0);
      SCHED();
      STAMP(2);
      lds_wait1(L, S_X1, w2);
      XF_READ(1);
      STAMP(3);
      WAITVM(0);
      STAMP(4);
      lds_signal(L, S_WK, w2);
        lds_wait(L, S_WK);
        STAMP(5);
        __builtin_amdgcn_s_setprio(2);
      }, [](int) {});
      STAMP(6);
      u32x4 P[2][2];
      feature_map(t, st, wpf, g, P);
#pragma unroll
      for (int c = 0; c < 4; ++c) {
        LAS char* p = pw + (c >> 1) * 16 * 224 + (c & 1) * 64;
        *(LAS u32x2*)(p) = u32x2{P[c >> 1][c & 1].x, P[c >> 1][c & 1].y}; *(LAS u32x2*)(p + 32) = u32x2{P[c >> 1][c & 1].z, P[c >> 1][c & 1].w};
      }
      lds_signal(L, S_PHIK, w2);
      STAMP(7);
      ST16(rs, em4, lane * 16, clo); ST16(rs, em4, lane * 16, clo2);
      ST16(rs, em4, clh, 0); ST16(rs, em4, clh, 2048);
      ST16(rs, em4, cls, clt);
      lds_wait2(L, S_V, S_PHIK);
      u32x4 pa[4], pb[8];
#pragma unroll
      for (int m = 0; m < 4; ++m) {
        pa[m] = trfrag(L + pr + w2 * 32 + m * 32 * 224, 16 * 224);
        pb[2 * m] = trfrag(L + pr + m * 32 * 224 + 128, 16 * 224); pb[2 * m + 1] = trfrag(L + pr + m * 32 * 224 + 160, 16 * 224);
      }
      SCHED();
      f32x4 kv[2] = {};
#pragma unroll
      for (int m = 0; m < 8; ++m) kv[m & 1] = mfma16(pa[m >> 1], pb[m], kv[m & 1]);
      SCHED();
      const u32x4 kvb = {pk(kv[0][0], kv[0][1]), pk(kv[0][2], kv[0][3]), pk(kv[1][0], kv[1][1]), pk(kv[1][2], kv[1][3])};
      ST16(rs, kvb, lane * 16, kvo);
      STAMP(8);
      lds_signal(L, S_KDONE, w2);
      if (wave == 0) { lds_wait(L, S_KDONE); touch(wd + b * 49 * 128, min(lane, 48) * 128); WAITVM(1); } else WAITVM(0);
      FLAG(fko, 1);
      __builtin_amdgcn_s_setprio(0);
      STAMP(9);
      lds_signal(L, S_XA, w2); lds_wait(L, S_XA);
      if (wave == 1) FLAG(WS_FO + (head * 16 + tile) * 128, 1);
      if (r2 < 14) { WO_HALF(0); WO_HALF(1); } else __builtin_amdgcn_s_sleep(20);
      {
        u64 v_; u32 c_ = SPIN_CAP;
        asm volatile("s_mov_b64 exec, 0xffff\n1:\n\tglobal_load_dwordx2 %0, %2, %3 sc1\n\ts_waitcnt vmcnt(0)\n\tv_cmp_ne_u64 vcc, %0, %4\n\ts_cbranch_vccz 2f\n\ts_sub_u32 %1, %1, 1\n\ts_cbranch_scc0 1b\n2:\n\ts_mov_b64 exec, -1"
          : "=&v"(v_), "+s"(c_) : "v"(l15 * 128), "s"(ws + WS_FKV + (head * 4 + w2) * 2048), "v"(tag + 1) : "memory", "scc", "vcc");
        __builtin_amdgcn_s_waitcnt(0x0F70);
      }
      lds_signal(L, S_WO, w2);
      STAMP(10);
      u32x4 tt[16];
#pragma unroll
      for (int i = 0; i < 16; ++i) tt[i] = __builtin_bit_cast(u32x4, __builtin_amdgcn_raw_buffer_load_b128(rs, lane * 16, WS_KVP + head * 65536 + w2 * 1024 + i * 4096, 16));
      u32 fgo;
      {
        const int pl = (tile & 1) * 16 + min(l15, 13);
        fgo = wave == 0 ? WS_FO + (l15 * 16 + tile2) * 128
            : wave == 1 ? WS_FO + ((((tile >> 1) & 1) * 8 + (pl & 7)) * 16 + (tile & 12) + (pl >> 3)) * 128
            : wave == 2 ? fko
                        : WS_FG + (min(((7 * r) >> 2) + g, (7 * r + 6) >> 2) * 16 + (l15 & 8) + min(lane & 7, 6)) * 128;
      }
      u64 fg = __hip_atomic_load((const GAS u64*)(ws + fgo), RLX_AGENT);
      SCHED();
      const u32 one = g == q4 ? ((p4 & 1) ? 0x3f800000u : 0x3f80u) : 0u, s01 = p4 < 2 ? one : 0u, s23 = p4 < 2 ? 0u : one;
      const u32x4 sel0 = {s01, s23, 0u, 0u}, sel1 = {0u, 0u, s01, s23};
      f32x4 s0 = {}, s1 = {};
#pragma unroll
      for (int i = 0; i < 16; ++i) { s0 = mfma16(sel0, tt[i], s0); s1 = mfma16(sel1, tt[i], s1); }
      LAS char* kw = L + L_KVA + (w2 >> 1) * 1024 + lane * 16 + (w2 & 1) * 8;
      *(LAS u32x2*)(kw) = u32x2{pk(s0[0], s0[1]), pk(s0[2], s0[3])};
      *(LAS u32x2*)(kw + 2048) = u32x2{pk(s1[0], s1[1]), pk(s1[2], s1[3])};
      lds_signal(L, S_KVA, w2);
      STAMP(11);
      asm volatile("" : "+v"(fg) :: "memory");
      if (!__all(fg == tag + 1)) poll_flags(ws, fgo, true, tag);
      __builtin_amdgcn_fence(__ATOMIC_ACQUIRE, "wavefront");
      lds_signal(L, S_WOFA, w2);
      lds_wait1(L, S_WOFA, 3);
      const int fl = min(((7 * r) >> 2) + w2, (7 * r + 6) >> 2);
#pragma unroll
      for (int i = 0; i < 4; ++i) asm volatile("global_load_dwordx4 %0, %1, %2 offset:%3 sc1" : "=v"(csp[i]) : "v"(((7 * r) >> 2) + w2 == fl ? (i < 3 ? lane : lane & 31) * 16 : 0), "s"(ws + WS_CS + fl * 4096), "n"(i * 1024) : "memory");
      GUBLK(2);
      if (r2 < 14) { lds_wait(L, S_WO); WOF_READ(0, 8); }
      SCHED();
#define CS_OUT(k_) do { asm volatile("s_waitcnt vmcnt(%4)" : "+v"(csp[0]), "+v"(csp[1]), "+v"(csp[2]), "+v"(csp[3]) : "n"(k_) : "memory"); \
        const f32x4 sm = (csp[0] + csp[1]) + (csp[2] + 0.5f * csp[3]);     \
        const float e0 = swadd32(sm[0], sm[1]), e1 = swadd32(sm[2], sm[3]);     \
        const int j0 = 4 * (((7 * r) >> 2) + w2) + 2 * (lane & 1) - 7 * r, cr = ((g & 1) * 16 + hh * 8 + (l15 >> 1)) * 4; \
        if (j0 >= 0 && j0 < 7) *(LAS float*)(L + L_CS + j0 * 128 + cr) = e0; \
        if (j0 + 1 >= 0 && j0 + 1 < 7) *(LAS float*)(L + L_CS + (j0 + 1) * 128 + cr) = e1; } while (0)
      lds_signal(L, S_WOFB, w2);
      STAMP(24);
    } else {
      const int rg = lane >> 3;
      const u32 lvo = rg * 8 * 3584 + (lane & 7) * 16;
      const char* us = (umat ? wu : wg) + ukq * 64 * 3584 + ufl * 128;
      asm volatile("" : "+s"(us));
      int so = rg * 512 + (lane & 7) * 16, sb = WS_WGU + 4 * ufl * SLOT + ukq * 4096 + umat * 256;
      auto repack = [&]() LAM {
        if (has_unit) {
          const int s1 = SLOT, s2 = 2 * SLOT;
          float c[8];
          u32x4 f[8];
#pragma unroll
          for (int e = 0; e < 8; ++e) {
            f[e] = piece(rp, e);
            ST16(rs, f[e], so + (e & 1) * 128, sb + ((e >> 1) & 1) * s1 + (e >> 2) * s2);
            SCHED();
          }
#pragma unroll
          for (int e = 0; e < 8; ++e) c[e] = mfma16(ones, f[e], z4)[0];
          f32x4 cv = g & 1 ? f32x4{c[4], c[5], c[6], c[7]} : f32x4{c[0], c[1], c[2], c[3]};
#pragma unroll
          for (int e = 0; e < 4; ++e) cv[e] += dppror<8>(cv[e]);
          ST16(rs, cv, g < 2 && l15 < 8 ? l15 * 32 + g * 16 : NOWHERE, WS_CS + ufl * 4096 + ukq * 512 + umat * 256);
        }
      };
#define RPLOAD() _Pragma("unroll") for (int j = 0; j < 8; ++j) asm volatile("global_load_dwordx4 %0, %1, %2" : "=v"(rp[j]) : "v"(lvo), "s"(us + j * 3584) : "memory")
      tag = epoch * 6364136223846793005ull + 1442695040888963407ull;
      STAMP(1);
      XF_READ(0);
      if (w2 < 2) lds_signal(L, S_XF0, 2 + w2);
      f32x4 t[2][2];
      asm volatile("" : "+v"(pw));
      project<SLOT - 14336, S_CSV, 4>(L, L_WV + wr0, L_WV + wr1, xf, w2, lane, L_CSP + 256, t, st, [&]() LAM {
      WAITVM(0);
      lds_signal(L, S_X1, w2);
      STAMP(2);
      lds_wait(L, S_XF0);
      XF_READ(1);
      SCHED();
#pragma unroll
      for (int i = 0; i < 7; ++i) WDMA(i, 1792, L_WV, 1);
      STAMP(3);
      __builtin_amdgcn_s_setprio(3);
      LN_STATS(1);
      lds_signal(L, S_CS, w2);
      STAMP(4);
      if (has_unit) { RPLOAD(); } else WAITVM(0);
      STAMP(28);
      asm volatile("" : "+s"(sb));
      PIN(so);
      WAITVM(8);
      lds_signal(L, S_WV, w2);
      STAMP(29);
      lds_wait2(L, S_WV, S_WK);
      STAMP(30);
        STAMP(5);
      }, [&](int n) LAM { if (n < 14 && !(n & 1)) WDMA(n >> 1, 0, L_WQ, 1); });
#pragma unroll
      for (int c = 0; c < 4; ++c) t[c >> 1][c & 1] *= st[c >> 1].y;
      STAMP(6);
#pragma unroll
      for (int c = 0; c < 2; ++c) {
        LAS char* p = pw + c * 16 * 224 + 128;
        *(LAS u32x2*)(p) = u32x2{pk(t[c][0][0], t[c][0][1]), pk(t[c][0][2], t[c][0][3])};
        u32x2 up = {pk(t[c][1][0], t[c][1][1]), pk(t[c][1][2], t[c][1][3])};
        PIN(up);
        *(LAS u32x2*)(p + 32) = g == 3 ? u32x2{0x3f80u, 0u} : up;
      }
      lds_signal(L, S_V, w2);
      __builtin_amdgcn_s_setprio(0);
      STAMP(7);
      asm volatile("s_waitcnt vmcnt(7)" : RP_OPS :: "memory");
#define UFLAG() FLAGC(has_unit, WS_FG + ((ufl * 2 + umat) * 8 + ukq) * 128, 1)
      repack();
      STAMP(8);
      WPF();
      if (!has_unit) WAITVM(0); else WAITVM(9);
      lds_signal(L, S_WQ, w2);
      project<SLOT - 14336, S_CSQ, 4>(L, L_WQ + wr0, L_WQ + wr1, xf, w2, lane, L_CSP + 512, t, st, [&]() LAM {
        lds_wait(L, S_WQ);
        STAMP(9);
      }, [&](int n) LAM {
        if (n == 14) { WAITVM(0); UFLAG(); }
      });
      STAMP(10);
      u32x4 pq[2][2];
      feature_map(t, st, wpf, g, pq);
      asm volatile("" : "+v"(pq[0][0]), "+v"(pq[0][1]), "+v"(pq[1][0]), "+v"(pq[1][1]));
      STAMP(11);
      if (r2 < 14) { lds_wait<1>(L, S_WO); WOF_READ(0, 8); }
      STAMP(24);
      __builtin_amdgcn_s_setprio(3);
      u32x4 ka[4];
      {
        u32 v_, c_ = SPIN_CAP << 4;
        asm volatile(POLL_("ds_read_b32 %0, %6 offset:%8\n\tds_read_b128 %2, %7\n\tds_read_b128 %3, %7 offset:1024\n\tds_read_b128 %4, %7 offset:2048\n\tds_read_b128 %5, %7 offset:3072", "v_cmp_eq_u32 vcc, 0, %0", "1")
          : "=&v"(v_), "+s"(c_), "=&v"(ka[0]), "=&v"(ka[1]), "=&v"(ka[2]), "=&v"(ka[3]) : "v"(L_FLG + (threadIdx.x & 3) * 4), "v"(L_KVA + lane * 16), "n"(S_KVA * 16) : "memory", "scc", "vcc");
      }
      STAMP(27);
      f32x4 o[2][2];
#pragma unroll
      for (int c = 0; c < 4; ++c) o[c >> 1][c & 1] = mfma16(ka[2 * (c & 1) + 1], pq[c >> 1][1], mfma16(ka[2 * (c & 1)], pq[c >> 1][0], z4));
      const float dn = swp32(o[0][1][0], o[1][1][0]).y;
      const float rc = frcp(swp16(dn, dn).y + 1e-6f);
      const f32x2 rden = swp32(rc, rc);
#pragma unroll
      for (int c = 0; c < 2; ++c) {
        const float rd = rden[c], r3 = g == 3 ? 0.f : rd;
        const u32x4 ob = {pk(o[c][0][0] * rd, o[c][0][1] * rd), pk(o[c][0][2] * rd, o[c][0][3] * rd), pk(o[c][1][0] * r3, o[c][1][1] * r3), filled(pk(o[c][1][2] * r3, o[c][1][3] * r3))};
        ST16(rs, ob, lane * 16, WS_O + ((tile * 8 + 2 * w2 + c) * 16 + head) * 1024);
      }
      STAMP(12);
      *(volatile LAS u32*)(L + L_FLG + S_W2A * 16 + w2 * 4) = (u32)__builtin_amdgcn_s_memrealtime();
      lds_signal(L, S_W2B, w2);
      __builtin_amdgcn_s_setprio(0);
    }
  }
  STAMP(13);
  u32x4 hres = {0u, 0u, 0u, 0u};
  STAMP(23);
  if (r2 < 14) {
    u32x4 xr, of[8];
    const char* ob = ws + WS_O + ((tile2 * 8 + tg * 4 + tb) * 16 + kh * 8 + 4) * 1024;
    asm volatile("global_load_dwordx4 %0, %1, %2" : "=v"(xr) : "v"(xo), "s"(x) : "memory");
    lds_wait2(L, S_W2B, S_WOFA);
    if (!A) GUBLK(1);
    {
      const u32x4 ts = *(volatile LAS u32x4*)(L + L_FLG + S_W2A * 16);
      const u32 t0 = __builtin_amdgcn_readfirstlane(ts.x), due = t0 + T2 + __builtin_amdgcn_readfirstlane(max(max(0, (int)(ts.y - ts.x)), max((int)(ts.z - ts.x), (int)(ts.w - ts.x))));
      asm volatile("" : "+s"(ob));
      u64 n_; u32 c_ = 256;
      asm volatile("1:\n\ts_memrealtime %0\n\ts_waitcnt lgkmcnt(0)\n\ts_mov_b64 vcc, %0\n\ts_sub_i32 vcc_lo, vcc_lo, %2\n\ts_cmp_ge_i32 vcc_lo, 0\n\ts_cbranch_scc1 2f\n\ts_sub_u32 %1, %1, 1\n\ts_cbranch_scc0 1b\n2:"
        : "=&s"(n_), "+s"(c_) : "s"(due) : "memory", "scc", "vcc");
    }
    STAMP(14);
#define OFA(c_) _Pragma("unroll") for (int i = 0; i < 8; ++i) \
      asm volatile("global_load_dwordx4 %0, %1, %2 offset:%3 sc1" : c_(of[i]) : "v"(lane * 16), "s"(ob), "n"(i * 1024 - 4096) : "memory")
#define OFW(k_) asm volatile("s_waitcnt vmcnt(%9)" : "+v"(of[0]), "+v"(of[1]), "+v"(of[2]), "+v"(of[3]), "+v"(of[4]), "+v"(of[5]), "+v"(of[6]), "+v"(of[7]), "+v"(xr) : "n"(k_) : "memory")
#define OFBAD() (!__all(max(max(max(of[0].w, of[1].w), max(of[2].w, of[3].w)), max(max(of[4].w, of[5].w), max(of[6].w, of[7].w))) != EMPTY))
    OFA("=v");
    f32x4 a[4] = {}, a0[4] = {};
    u32 mx = 0, exw = L_EX + wave * 2048 + lane * 16, exr = L_EX + (wave ^ 4) * 2048 + lane * 16;
    asm volatile("s_waitcnt vmcnt(8)" : "+v"(xr) :: "memory");
    a0[2 * kh] = f32x4{blo(xr.x), bhi(xr.x), blo(xr.y), bhi(xr.y)}; a0[2 * kh + 1] = f32x4{blo(xr.z), bhi(xr.z), blo(xr.w), bhi(xr.w)};
    a[2 * kh] = a0[2 * kh]; a[2 * kh + 1] = a0[2 * kh + 1];
    asm volatile("" : "+v"(a[2 * kh]), "+v"(a[2 * kh + 1]), "+v"(exw), "+v"(exr));
    lds_wait(L, S_WOFB);
    if constexpr (A) CS_OUT(8);
    SCHED();
#pragma unroll
    for (int i = 0; i < 8; ++i) {
      asm volatile("s_waitcnt vmcnt(%1)" : "+v"(of[i]) : "n"(7 - i) : "memory");
#pragma unroll
      for (int k = 0; k < 4; ++k) a[k ^ (2 - 2 * kh)] = mfma16(wof[i][k ^ (2 - 2 * kh)], of[i], a[k ^ (2 - 2 * kh)]);
      mx = max(mx, of[i].w);
      if (!(i & 1)) PIN(mx);
      SCHED();
    }
    asm volatile("" : "+v"(xr));
    if (__builtin_expect(!__all(mx != EMPTY), 0)) {
#pragma nounroll
      for (u32 sp = 0; sp < SPIN_CAP; ++sp) { __builtin_amdgcn_s_sleep(RSL); OFA("+v"); OFW(0); if (!OFBAD()) break; }
#pragma unroll
      for (int i = 0; i < 8; ++i)
#pragma unroll
        for (int k = 0; k < 4; ++k) a[k] = mfma16(wof[i][k], of[i], i ? a[k] : a0[k]);
    }
    STAMP(25);
    *(LAS f32x4*)(L + exw) = kh ? a[0] : a[2];
    *(LAS f32x4*)(L + exw + 1024) = kh ? a[1] : a[3];
    lds_signal(L, A ? S_EXA : S_EXB, w2);
    GUBLK(A ? 0 : 3);
    f32x4 h0, h1;
    {
      u32 v_, c_ = SPIN_CAP << 4;
      asm volatile(POLL_("ds_read_b32 %0, %4 offset:%6\n\tds_read_b128 %2, %5\n\tds_read_b128 %3, %5 offset:1024", "v_cmp_eq_u32 vcc, 0, %0", "1")
        : "=&v"(v_), "+s"(c_), "=&v"(h0), "=&v"(h1) : "v"(L_FLG + w2 * 4), "v"(exr), "n"((A ? S_EXB : S_EXA) * 16) : "memory", "scc", "vcc");
    }
    STAMP(15);
    h0 = a[2 * kh] + h0; h1 = a[2 * kh + 1] + h1;
    hres = u32x4{pk(h0[0], h0[1]), pk(h0[2], h0[3]), pk(h1[0], h1[1]), filled(pk(h1[2], h1[3]))};
    SCHED();
    ST16(rs, hres, hvo, kh * 2048);
    SCHED();
    const f32x4 r0 = {blo(hres.x), bhi(hres.x), blo(hres.y), bhi(hres.y)}, r1 = {blo(hres.z), bhi(hres.z), blo(hres.w), bhi(hres.w)};
    const f32x4 a1 = r0 + r1, a2q = r0 * r0 + r1 * r1;
    float s1 = (a1[0] + a1[1]) + (a1[2] + a1[3]), s2 = (a2q[0] + a2q[1]) + (a2q[2] + a2q[3]);
    { const float t = swadd16(s1, s2); const f32x2 a2 = sw32(t); s1 = a2.x + a2.y; }
    { const u32x2 sv = __builtin_bit_cast(u32x2, sw16(s1)); __builtin_amdgcn_raw_buffer_store_b64(u32x2{sv.x, filled(sv.y)}, rs, fvo, sto + kh * 16384, 16); }
    WAITVM(2);
  } else {
    lds_wait2(L, S_W2B, S_WOFA);
    GUBLK(A ? 0 : 1);
    lds_wait(L, S_WOFB);
    if (A) GUBLK(3);
    HH_CLEAR();
    DRAIN();
    HH_FLAG();
  }
  if constexpr (A) { if (r2 >= 14) CS_OUT(2); }
  lds_signal(L, A ? S_HFA : S_HFB, w2);
  STAMP(16);
  u32x4 wdf[3][8];
  const __amdgpu_buffer_rsrc_t rd = __builtin_amdgcn_make_buffer_rsrc((void*)(wd + w2 * 448 * 896 + min(cg, 6) * 128 - 1024), 0, 448 * 896 + 1024, 0x00020000);
  const u32 dvo = (lane >> 3) * 8 * 896 + (((lane & 7) ^ ((lane >> 2) & 4)) * 16);
  u32 da_ = 2 * SLOT + w2 * 7168 + hh * 512 + q4 * 128 + (g & 1) * 32 + p4 * 8 + 64 * (q4 >> 1), db_ = da_ ^ 64, dc_ = da_ + (L_EX - 2 * SLOT - w2 * 6144), dd_ = dc_ ^ 64;
  const u32 po = WS_FHH + ((tile2 * 16 + 4 * w2 + ((lane >> 1) & 3)) * 4 + 2 * tg + (lane & 1)) * 128;
  const u32 ho = WS_HH + ((tile2 * 2 + tg) * 4 + w2) * 57344;
#define WDLOAD(k, bf) dma_wd<k>(rd, dvo, ((bf) + 2) * SLOT + w2 * 7168, L_EX + (bf) * 4096 + w2 * 1024)
  u32 have = 0;
#define PLOAD() peek(ws, po, lane < 8, tag + 7)
#define PMIN(pv) do { const u64 d = (pv) - tag; have = max(have, min8(d <= 8 ? (u32)d : 0u)); } while (0)
#define NEEDP(n) do { _Pragma("nounroll") for (u32 sp = 0; have < (n) && sp < SPIN_CAP; ++sp) { PMIN(PLOAD()); if (have < (n)) __builtin_amdgcn_s_sleep(PSLP); } \
    __builtin_amdgcn_fence(__ATOMIC_ACQUIRE, "wavefront"); } while (0)
#define FREE(f) lds_poll<BSL>(L_FLG + (threadIdx.x & 3) * 4, S_DONE4 * 16, f)
#define HQBAD(q) (!__all(max(max(max(hq[q][0].w, hq[q][1].w), max(hq[q][2].w, hq[q][3].w)), max(max(hq[q][4].w, hq[q][5].w), max(hq[q][6].w, hq[q][7].w))) != EMPTY))
  const char* hs = ws + ho + 4096;
#define HQA(c_, q, j) _Pragma("unroll") for (int i = 0; i < 8; ++i) asm volatile("global_load_dwordx4 %0, %1, %2 offset:%3 sc1" : c_(hq[q][i]) : "v"(lane * 16), "s"(hs + (j) * 8192), "n"(i * 1024 - 4096) : "memory")
#define HQW(q, k_) asm volatile("s_waitcnt vmcnt(%8)" : "+v"(hq[q][0]), "+v"(hq[q][1]), "+v"(hq[q][2]), "+v"(hq[q][3]), "+v"(hq[q][4]), "+v"(hq[q][5]), "+v"(hq[q][6]), "+v"(hq[q][7]) : "n"(k_) : "memory")
#define HQOK(q, j, k_) do { HQW(q, k_); if (__builtin_expect(HQBAD(q), 0)) { _Pragma("nounroll") for (u32 sp = 0; sp < SPIN_CAP; ++sp) { \
        __builtin_amdgcn_s_sleep(RSL); HQA("+v", q, j); HQW(q, 0); if (!HQBAD(q)) break; } } SCHED(); } while (0)
  f32x4 t6[4];
  if constexpr (A) {
    const int T32 = tile * 4 + wave;
    u32x2 st[7];
    u32x4 hb[28];
    u64 fcl;
    const u32 stv = (7 * hh * 2048 + (lane & 31)) * 8;
#define STA(c_) _Pragma("unroll") for (int i = 0; i < 7; ++i) asm volatile("global_load_dwordx2 %0, %1, %2 sc1" : c_(st[i]) : "v"(stv), "s"(ws + WS_ST + (i * 2048 + T32 * 32) * 8) : "memory")
#define STW(k_) asm volatile("s_waitcnt vmcnt(%7)" : "+v"(st[0]), "+v"(st[1]), "+v"(st[2]), "+v"(st[3]), "+v"(st[4]), "+v"(st[5]), "+v"(st[6]) : "n"(k_) : "memory")
#define STBAD() (!__all(max(max(max(st[0].y, st[1].y), max(st[2].y, st[3].y)), max(max(st[4].y, st[5].y), st[6].y)) != EMPTY))
#define HBA(c_, q) _Pragma("unroll") for (int i = 0; i < 4; ++i) asm volatile("global_load_dwordx4 %0, %1, %2 offset:%3 sc1" : c_(hb[4 * (q) + i]) : "v"(lane * 16), "s"(ws + WS_H + (T32 * 28 + 4 * (q)) * 1024), "n"(i * 1024) : "memory")
    lds_wait2(L, S_HFA, S_HFB);
    __builtin_amdgcn_s_sleep(DLY3);
    STA("=v");
    STAMP(17);
#pragma unroll
    for (int q = 0; q < HLA / 4; ++q) { HBA("=v", q); }
    u32 wb_[3] = {(u32)lane * 16, (u32)lane * 16 + 2 * SLOT, (u32)lane * 16 + 4 * SLOT};
    asm volatile("" : "+v"(wb_[1]), "+v"(wb_[2]));
#define WFRAG(sl, s) (*(LAS u32x4*)(L + wb_[(sl) >> 1] + ((sl) & 1) * SLOT + (s) * 1024))
    LAS char* csl = L + L_CS + hh * 16;
    u32x4 ring[7];
    float nmu, ve, k0, k1;
    u32 mx;
    const int pvo = WS_FHH + ((tile * 16 + r) * 4 + wave) * 128;
    f32x16 acc[3];
    float y[8], gu[8];
    u32x4 yb;
    u32x4 cw;
#define CQ(j_, k_) do { const f32x4 c_ = *(LAS f32x4*)(csl + (j_) * 128 + (k_) * 32); _Pragma("unroll") for (int i_ = 0; i_ < 4; ++i_) acc[(j_) % 3][4 * (k_) + i_] = c_[i_]; } while (0)
#define HB1(i_) asm volatile("global_load_dwordx4 %0, %1, %2 offset:%3 sc1" : "=v"(hb[i_]) : "v"(lane * 16), "s"(ws + WS_H + (T32 * 28 + ((i_) & ~3)) * 1024), "n"(((i_) & 3) * 1024) : "memory")
#define FCL() asm volatile("global_load_dwordx2 %0, %1, %2 sc1" : "=v"(fcl) : "v"(p4 * 128), "s"(ws + WS_FH + (T32 * 32 + 28) * 128) : "memory")
    auto gu01 = [&](auto LC) LAM {
      constexpr bool LD = LC.value;
      sfor<0, 56>([&](auto NC) LAM {
        constexpr int n = NC.value, s = n >> 1, m = n + 7;
        if constexpr (LD && !(n & 1)) { u32x4 t_ = hb[s]; asm volatile("s_waitcnt vmcnt(%1)" : "+v"(t_) : "n"((HLA + s < 29 ? HLA + s : 29) - s - 1 + (n > CN ? (n - CN < 7 - NCL ? n - CN : 7 - NCL) : 0)) : "memory"); hb[s] = t_; }
        acc[n & 1] = mfma32(ring[n % 7], hb[s], acc[n & 1]);
        SCHED();
        if constexpr (LD && (n & 1) && HLA + s < 28) HB1(HLA + s);
        if constexpr (LD && (n & 1) && HLA + s == 28) FCL();
        if constexpr (LD && n >= CN && n < CN + 7 - NCL) ST16(rs, empty4, lane * 16, hho + (n - CN + NCL) * 8192);
        if constexpr (n & 1) mx = max(mx, hb[s].w);
        if constexpr (m < 56) ring[n % 7] = WFRAG(m & 1, m >> 1); else ring[n % 7] = WFRAG(2, m - 56);
        if constexpr (n >= 34 && n < 38) CQ(2, n - 34);
        if constexpr (n >= 44 && n < 52) { acc[2][2 * (n - 44)] *= nmu; acc[2][2 * (n - 44) + 1] *= nmu; PIN(acc[2]); }
        SCHED();
      });
      STAMP(18);
    };
    auto gu_block = [&](auto JC) __attribute__((always_inline)) {
      constexpr int j = JC.value;
      sfor<0, 28>([&](auto SC) LAM {
        constexpr int s = SC.value;
        if constexpr (s == 0) __builtin_amdgcn_s_sleep(1);
        if constexpr (s % 2 == 0) { asm volatile("" :: "v"(ring[(s + 1) % 7])); SCHED(); }
        acc[j % 3] = mfma32(ring[s % 7], hb[s], acc[j % 3]);
        constexpr int sg = j == 3 ? S_WIN3 : j == 4 ? S_REFILL : S_FREE01;
        if constexpr (j >= 3 && j < 6 && s == 12) cw = *(volatile LAS u32x4*)(L + L_FLG + sg * 16);
        if constexpr (j >= 3 && j < 6 && s == 20) { if (__builtin_expect(!__builtin_amdgcn_readfirstlane(cw.x & cw.y & cw.z & cw.w), 0)) lds_wait(L, sg); asm volatile("" ::: "memory"); }
        if constexpr (s + 7 < 28) ring[s % 7] = WFRAG(gu_slot(j), s + 7);
        else if constexpr (j < 6) ring[s % 7] = WFRAG(gu_slot(j + 1), s - 21);
        if constexpr (s == 0 && j == 2) lds_signal(L, S_WSEEN, wave);
        if constexpr (j == 6 && s == 22) *(volatile LAS u32*)(L + L_FLG + S_DONE4 * 16 + wave * 4) = 5u;
        if constexpr (s == 0 && j >= 3) *(volatile LAS u32*)(L + L_FLG + S_DONE4 * 16 + wave * 4) = j - 2u;
        sfor<0, 48>([&](auto EC) LAM {
          constexpr int e = EC.value, i = e & 7, a = e >> 3, k = 28 * (j - 2) + s - act0(e);
          if constexpr (k >= 0 && k < 10) {
            const float ga = acc[a % 3][i];
            if constexpr (k == 0) y[i] = ga * ga;
            if constexpr (k == 1) y[i] = fmaf(y[i], k1, k0);
            if constexpr (k == 2) y[i] = ga * y[i];
            if constexpr (k == 3) y[i] = fexp2(y[i]);
            if constexpr (k == 4) y[i] = fmaf(y[i], ve, ve);
            if constexpr (k == 6) { gu[i] = ga * acc[a % 3][i + 8]; PIN(gu[i]); }
            if constexpr (k == 5) y[i] = frcp(y[i]);
            if constexpr (k == 7) y[i] = y[i] * gu[i];
            if constexpr (k < 5 || k == 5 || k == 7) PIN(y[i]);
            if constexpr (k == 8 && (e & 1)) { u32 p_ = pk(y[i - 1], y[i]); if (i == 7) p_ = filled(p_); PIN(p_); yb[i >> 1] = p_; }
            if constexpr (k == 8 && e == 7) WAITVM(0);
            if constexpr (k == 8 && i == 7) ST16(rs, yb, lane * 16, hho + a * 8192);
            if constexpr (k == 9 && e == 7) FLAG(pvo, 1);
          }
        });
        constexpr int cq0 = j == 2 ? 17 : 13;
        if constexpr (j < 6 && s >= cq0 && s < cq0 + 4) CQ(j + 1, s - cq0);
        if constexpr (j < 6 && s >= cq0 + 6 && s < 27) {
          sfor<0, 16>([&](auto EC) LAM {
            constexpr int e = EC.value;
            if constexpr (cq0 + 6 + e * (21 - cq0) / 16 == s) { float v_ = acc[(j + 1) % 3][e] * nmu; PIN(v_); acc[(j + 1) % 3][e] = v_; }
          });
        }
        SCHED();
      });
      if (j == 3) STAMP(26);
      if (j == 2) STAMP(29);
      if (j == 4) STAMP(30);
      if (j == 5) STAMP(31);
    };
#define HBW(q) asm volatile("s_waitcnt vmcnt(0)" : "+v"(hb[4 * (q)]), "+v"(hb[4 * (q) + 1]), "+v"(hb[4 * (q) + 2]), "+v"(hb[4 * (q) + 3]) :: "memory")
#define HBBAD(q) (!__all(max(max(hb[4 * (q)].w, hb[4 * (q) + 1].w), max(hb[4 * (q) + 2].w, hb[4 * (q) + 3].w)) != EMPTY))
#define HBC(q, c_) asm volatile("s_bitcmp0_b32 %6, %7\n\ts_cbranch_scc1 1f\n\tglobal_load_dwordx4 %0, %4, %5 sc1\n\tglobal_load_dwordx4 %1, %4, %5 offset:1024 sc1\n\t" \
      "global_load_dwordx4 %2, %4, %5 offset:2048 sc1\n\tglobal_load_dwordx4 %3, %4, %5 offset:3072 sc1\n1:" : "+v"(hb[4 * (q)]), "+v"(hb[4 * (q) + 1]), "+v"(hb[4 * (q) + 2]), "+v"(hb[4 * (q) + 3]) \
      : "v"(lane * 16), "s"(ws + WS_H + (T32 * 28 + 4 * (q)) * 1024), "s"(c_), "n"(q) : "memory", "scc")
#define REDO(m_) do { STA("+v"); _Pragma("unroll") for (int q = 0; q < 7; ++q) HBC(q, m_); STW(0); _Pragma("unroll") for (int q = 0; q < 7; ++q) HBW(q); } while (0)
#define FIX() do { _Pragma("nounroll") for (u32 sp = 0; sp < SPIN_CAP; ++sp) { u32 m = 0; \
        _Pragma("unroll") for (int q = 0; q < 7; ++q) m |= HBBAD(q) ? 1u << q : 0u; \
        if (!m && !STBAD()) break; if (sp) __builtin_amdgcn_s_sleep(RSL); m = __builtin_amdgcn_readfirstlane(m); REDO(m); } } while (0)
    auto first0 = [&]() LAM {
#pragma unroll
      for (int k = 0; k < 8; ++k) { const f32x4 c_ = *(LAS f32x4*)(csl + k * 32); acc[k >> 2][4 * (k & 3)] = c_[0]; acc[k >> 2][4 * (k & 3) + 1] = c_[1]; acc[k >> 2][4 * (k & 3) + 2] = c_[2]; acc[k >> 2][4 * (k & 3) + 3] = c_[3]; }
#pragma unroll
      for (int s = 0; s < 7; ++s) ring[s] = WFRAG(s & 1, s >> 1);
    };
    auto first = [&](auto LC) LAM {
      if (!LC.value) first0();
      f32x2 sf[7];
#pragma unroll
      for (int i = 0; i < 7; ++i) sf[i] = __builtin_bit_cast(f32x2, st[i]);
      float s1 = ((sf[0].x + sf[1].x) + (sf[2].x + sf[3].x)) + ((sf[4].x + sf[5].x) + sf[6].x);
      float s2 = ((sf[0].y + sf[1].y) + (sf[2].y + sf[3].y)) + ((sf[4].y + sf[5].y) + sf[6].y);
      { f32x2 a = sw32(s1); s1 = a.x + a.y; a = sw32(s2); s2 = a.x + a.y; }
      nmu = s1 * (-1.f / 448.f); ve = fmaxf(fmaf(-nmu, nmu, s2 * (1.f / 448.f)), 0.f) + 1e-5f;
      const float rstd = __builtin_amdgcn_rsqf(ve);
      k0 = -2.30220819f * rstd; k1 = -0.10294324f * rstd * rstd * rstd;
#pragma unroll
      for (int i = 0; i < 32; ++i) acc[i >> 4][i & 15] *= nmu;
      mx = 0;
      __builtin_amdgcn_s_setprio(2);
      SCHED();
      gu01(LC);
    };
    first0();
    SCHED();
    STW(HLA);
    if (__builtin_expect(STBAD(), 0)) {
#pragma unroll
      for (int q = 0; q < HLA / 4; ++q) HBW(q);
#pragma nounroll
      for (u32 sp = 0; sp < SPIN_CAP; ++sp) {
        u32 m = 0;
#pragma unroll
        for (int i = 0; i < 7; ++i) { const u64 bm = __builtin_amdgcn_ballot_w64(st[i].y == EMPTY); m |= ((u32)bm ? 1u << (i >> 1) : 0u) | ((u32)(bm >> 32) ? 1u << ((7 + i) >> 1) : 0u); }
        m = __builtin_amdgcn_readfirstlane(m);
        if (!m) break;
        if (sp) __builtin_amdgcn_s_sleep(RSL);
        STA("+v");
#pragma unroll
        for (int q = 0; q < HLA / 4; ++q) HBC(q, m);
        STW(0);
#pragma unroll
        for (int q = 0; q < HLA / 4; ++q) HBW(q);
      }
    }
    first(IC<1>{});
    if (__builtin_expect(!__all(mx != EMPTY), 0)) { FIX(); first(IC<0>{}); }
    asm volatile("s_waitcnt vmcnt(%1)" : "+v"(fcl) : "n"(7 - NCL) : "memory");
    if (__builtin_expect(!__all(fcl == tag + 1), 0)) poll_flags(ws, WS_FH + (T32 * 32 + 28 + p4) * 128, lane < 4, tag);
    gu_block(IC<2>{}); gu_block(IC<3>{}); gu_block(IC<4>{}); gu_block(IC<5>{}); gu_block(IC<6>{});
#pragma unroll
    for (int i = 0; i < 8; ++i) { const float ga = acc[0][i]; y[i] = ga * acc[0][i + 8] * frcp(fmaf(fexp2(ga * fmaf(ga * ga, k1, k0)), ve, ve)); }
    yb = u32x4{pk(y[0], y[1]), pk(y[2], y[3]), pk(y[4], y[5]), filled(pk(y[6], y[7]))};
    ST16(rs, yb, lane * 16, hho + 6 * 8192);
    *(volatile LAS u32*)(L + L_FLG + S_DONE4 * 16 + wave * 4) = 6u;
    __builtin_amdgcn_s_setprio(0);
    STAMP(19);
    if (r2 < 14) {
      u32x4 w6[8][4], h6[8];
      u32 a6[2], a7[2], s7 = g < 2 ? 7168 : 1024;
      a6[0] = 3 * SLOT + (g >> 1) * 2048 + (g & 1) * 512 + q4 * 128 + 64 * (q4 >> 1) + 16 * p4; a6[1] = a6[0] ^ 64;
      a7[0] = a6[0] + (g < 2 ? 5120 : L_EX + 4096 - 3 * SLOT - 2048); a7[1] = a7[0] ^ 64;
      u32 v6 = (g >> 1) * 2048 + (tb >> 1) * 1024 + ((g & 1) * 32 + (tb & 1) * 16 + l15) * 16;
      asm volatile("" : "+v"(a6[0]), "+v"(a6[1]), "+v"(a7[0]), "+v"(a7[1]), "+v"(s7), "+v"(v6));
#define H6AT(i) "v"(v6), "s"(ws + WS_HH + ((tile2 * 2 + tg) * 4 + ((i) >> 1)) * 57344 + 6 * 8192 + 2048), "n"(((i) & 1) * 4096 - 2048)
      lds_wait(L, S_CONS);
      STAMP2(15);
#pragma unroll
      for (int ks = 0; ks < 8; ++ks) {
#pragma unroll
        for (int k = 0; k < 4; ++k) {
          LAS char* p_ = L + a6[k >> 1] + (ks >> 1) * 7168 + (ks & 1) * 4096 + 8 * (k & 1);
          const u32x2 x_ = trread(p_), y_ = trread(ks & 1 ? L + a7[k >> 1] + 8 * (k & 1) : p_ + 1024);
          w6[ks][k] = u32x4{x_.x, x_.y, y_.x, y_.y};
        }
        if (ks & 1) { a7[0] += s7; a7[1] += s7; }
      }
      SCHED();
      STAMP2(16);
      lds_wait<1>(L, S_SPARE);
#pragma unroll
      for (int i = 0; i < 8; ++i) asm volatile("global_load_dwordx4 %0, %1, %2 offset:%3 sc1" : "=v"(h6[i]) : H6AT(i) : "memory");
      STAMP2(18);
#define H6W() asm volatile("s_waitcnt vmcnt(0)" : "+v"(h6[0]), "+v"(h6[1]), "+v"(h6[2]), "+v"(h6[3]), "+v"(h6[4]), "+v"(h6[5]), "+v"(h6[6]), "+v"(h6[7]) :: "memory")
#define H6FIX(i0) do { H6W(); _Pragma("nounroll") for (u32 sp = 0; sp < SPIN_CAP; ++sp) { u32 m = 0; \
          _Pragma("unroll") for (int u = i0; u < 8; ++u) m |= __builtin_amdgcn_ballot_w64(h6[u].w == EMPTY) ? 1u << u : 0u; \
          m = __builtin_amdgcn_readfirstlane(m); if (!m) break; if (sp) __builtin_amdgcn_s_sleep(RSL); \
          _Pragma("unroll") for (int u = i0; u < 8; ++u) asm volatile("s_bitcmp0_b32 %4, %5\n\ts_cbranch_scc1 1f\n\tglobal_load_dwordx4 %0, %1, %2 offset:%3 sc1\n1:" : "+v"(h6[u]) \
            : H6AT(u), "s"(m), "n"(u) : "memory", "scc"); \
          H6W(); } } while (0)
#pragma unroll
      for (int n = 0; n < 8; n += 2) {
        asm volatile("s_waitcnt vmcnt(%2)" : "+v"(h6[n]), "+v"(h6[n + 1]) : "n"(6 - n) : "memory");
        if (__builtin_expect(!__all(max(h6[n].w, h6[n + 1].w) != EMPTY), 0)) H6FIX(n);
#pragma unroll
        for (int k = 0; k < 8; ++k) t6[~k & 3] = mfma16(w6[n + (k >> 2)][~k & 3], h6[n + (k >> 2)], n + (k >> 2) ? t6[~k & 3] : z4);
        SCHED();
      }
      STAMP2(31);
      *(LAS f32x4*)(L + w2 * 2048 + lane * 16) = t6[2]; *(LAS f32x4*)(L + w2 * 2048 + 1024 + lane * 16) = t6[3];
      STAMP(20);
    }
  } else {
    lds_wait2(L, S_HFA, S_HFB);
    if (r2 >= 14) GUBLK(4);
#define REFILL() do { lds_wait<BSL>(L, S_WSEEN); GUBLK(5); GUBLK(6); } while (0)
    if (r2 >= 14) {
      WAITVM(0); lds_signal(L, S_WIN3, w2);
      REFILL();
      WAITVM(0); lds_signal(L, S_REFILL, w2); lds_signal(L, S_FREE01, w2);
    } else {
      u64 pv;
#define PEEK() do { pv = tag + 7; asm volatile("s_mov_b64 exec, 0xff\n\tglobal_atomic_add_x2 %0, %1, %2, %3 sc0\n\ts_mov_b64 exec, -1" : "+v"(pv) : "v"(po), "v"(0ull), "s"(ws) : "memory"); } while (0)
#define PEEKED(k_, n_) do { asm volatile("s_waitcnt vmcnt(%1)" : "+v"(pv) : "n"(k_) : "memory"); PMIN(pv); NEEDP(n_); } while (0)
      {
        u32x2 e[24][2];
        asm volatile("" : "+v"(da_), "+v"(db_), "+v"(dc_), "+v"(dd_));
#define WBL(N0, N1, NS, SL) sfor<N0, N1>([&](auto NC) LAM { constexpr int n = NC.value, k = n - (N0); \
          if constexpr (k < (NS)) dma_wd1<n, (SL) * SLOT + k * 1024>(rd, dvo, w2 * 7168); else dma_wd1<n, L_EX + (k - (NS)) * 4096>(rd, dvo, w2 * 1024); })
#define WBR(N0, N1, NS, SL) do { sfor<N0, N1>([&](auto NC) LAM { constexpr int n = NC.value, k = n - (N0); \
          if constexpr (k >= (NS)) { e[n][0] = trread(L + dc_ + (k - (NS)) * 4096); e[n][1] = trread(L + dd_ + (k - (NS)) * 4096); } \
          else { e[n][0] = trread(L + da_ + ((SL) - 2) * SLOT + k * 1024); e[n][1] = trread(L + db_ + ((SL) - 2) * SLOT + k * 1024); } }); \
          asm volatile("s_waitcnt lgkmcnt(0)" : "+v"(e[(N1) - 1][1]) :: "memory"); } while (0)
        WBL(0, 11, 7, 4);
        WAITVM(0);
        STAMP2(24);
        WBR(0, 11, 7, 4);
        WBL(11, 15, 0, 0);
        WAITVM(0);
        STAMP2(25);
        WBR(11, 15, 0, 0);
        WBL(15, 19, 0, 0);
        GUBLK(4);
        WAITVM(7);
        WBR(15, 19, 0, 0);
        WBL(19, 23, 0, 0);
        STAMP2(27);
        lds_wait<BSL>(L, S_WSEEN);
        GUBLK(5);
        GUBLK(6);
        WAITVM(14);
        lds_signal(L, S_WIN3, w2);
        STAMP2(26);
        WBR(19, 23, 0, 0);
        dma_wd1<23, L_EX + 3 * 4096>(rd, dvo, w2 * 1024);
        FREE(1);
        PEEK();
        WDLOAD(3, 0);
        WAITVM(17);
        lds_signal(L, S_REFILL, w2);
        STAMP2(5);
        PEEKED(8, 0);
        lds_signal(L, S_FREE01, w2);
        e[23][0] = trread(L + dc_ + 3 * 4096); e[23][1] = trread(L + dd_ + 3 * 4096);
#pragma unroll
        for (int n = 0; n < 24; ++n) wdf[n >> 3][n & 7] = u32x4{e[n & ~1][n & 1].x, e[n & ~1][n & 1].y, e[n | 1][n & 1].x, e[n | 1][n & 1].y};
      }
      STAMP2(23);
      f32x16 acc[2][2];
      u32x4 hq[2][8];
      STAMP2(16);
#pragma unroll
      for (int k = 0; k < 3; ++k) asm volatile("" : "+v"(wdf[k][0]), "+v"(wdf[k][1]), "+v"(wdf[k][2]), "+v"(wdf[k][3]), "+v"(wdf[k][4]), "+v"(wdf[k][5]), "+v"(wdf[k][6]), "+v"(wdf[k][7]) :: "memory");
      NEEDP(1);
      STAMP2(1);
      HQA("=v", 0, 0);
      FREE(2);
      HQA("=v", 1, 1);
      WDLOAD(6, 1);
      STAMP2(2);
      FREE(3);
#define WFR(dst, bf, i) do { LAS char* p_ = L + ((i) & 1 ? db_ : da_) + (bf) * SLOT + ((i) >> 1) * 2048; \
          const u32x2 x_ = trread(p_), y_ = trread((i) < 6 ? p_ + 1024 : L + ((i) & 1 ? dd_ : dc_) + (bf) * 4096); dst = u32x4{x_.x, x_.y, y_.x, y_.y}; } while (0)
      auto round = [&](auto JC) LAM {
        constexpr int J = JC.value, q = J & 1, w = J % 3, nb = 2 - 2 * J;
        __builtin_amdgcn_s_setprio(J == 1 || J == 2 ? 1 : 3);
        sfor<0, 18>([&](auto MC) LAM {
          constexpr int m = MC.value, i = m - 8 - (m > 13 ? 2 : 0);
          if constexpr (m < 16) macc<J == 0 && m < 4 ? 0 : 1>(acc[(m >> 1) & 1][m & 1], wdf[w][2 * (m >> 2) + (m & 1)], hq[q][2 * (m >> 2) + ((m >> 1) & 1)]);
          SCHED();
          if constexpr (J < 2 && m < 7) dma_wd1<(J + 4) * 8 + m, (nb + 2) * SLOT + m * 1024>(rd, dvo, w2 * 7168);
          if constexpr (J < 2 && m == 7) dma_wd1<(J + 4) * 8 + 7, L_EX + nb * 4096>(rd, dvo, w2 * 1024);
          if constexpr ((J == 2 || J == 4) && m < 8) WFR(wdf[J >> 1][m], 4 - J, m);
          if constexpr (J >= 1 && J < 4 && m == 7) FREE(J + 3);
          if constexpr (J >= 2 && m == 14) HQOK(q ^ 1, J + 1, J < 4 ? 6 : 0);
          if constexpr (J < 4 && m >= 8 && m != 14 && m != 15) {
            asm volatile("global_load_dwordx4 %0, %1, %2 offset:%3 sc1" : "=v"(hq[q][i]) : "v"(lane * 16), "s"(hs + (J + 2) * 8192), "n"(i * 1024 - 4096) : "memory");
            if constexpr (J == 0) WFR(wdf[0][i], 0, i);
          }
          if constexpr (J == 3 && m == 17) lds_signal(L, S_SPARE, w2);
          SCHED();
        });
        if (J < 2) __builtin_amdgcn_s_setprio(0);
      };
      HQOK(0, 0, 16);
      round(IC<0>{});
      STAMP2(8);
      HQOK(1, 1, 16);
      lds_signal(L, S_CONS, w2);
      asm volatile("s_waitcnt lgkmcnt(0)" : "+v"(wdf[0][7]) :: "memory");
      round(IC<1>{});
      STAMP2(9);
      HQOK(0, 2, 16);
      round(IC<2>{});
      STAMP2(10);
      round(IC<3>{});
      STAMP2(11);
      round(IC<4>{});
      STAMP2(3);
      u32 hb0 = 2 * SLOT + w2 * 7168 + lane * 16, hb7 = L_EX + w2 * 1024 + lane * 16;
      asm volatile("" : "+v"(hb0), "+v"(hb7));
      STAMP2(4);
#pragma unroll
      for (int m = 0; m < 16 + OLAG; ++m) {
        if (m < 16) acc[m >> 3][(m >> 2) & 1] = mfma32(wdf[2][2 * (m & 3) + ((m >> 2) & 1)], hq[1][2 * (m & 3) + (m >> 3)], acc[m >> 3][(m >> 2) & 1]);
        SCHED();
        if (m >= OLAG) {
          const int n = m - OLAG, T = n >> 3, k = (n >> 2) & 1, q = n & 3, bf = 2 - 2 * T;
          *(LAS f32x4*)(L + ((n & 7) < 7 ? hb0 + bf * SLOT + (n & 7) * 1024 : hb7 + bf * 4096)) = f32x4{acc[T][k][4 * q], acc[T][k][4 * q + 1], acc[T][k][4 * q + 2], acc[T][k][4 * q + 3]};
        }
        SCHED();
      }
      STAMP(20);
    }
  }
  if (r2 < 14) {
    float c[8] = {blo(hres.x), bhi(hres.x), blo(hres.y), bhi(hres.y), blo(hres.z), bhi(hres.z), blo(hres.w), bhi(hres.w)};
    u32 ln;
    asm volatile("v_mbcnt_lo_u32_b32 %0, -1, 0\n\tv_mbcnt_hi_u32_b32 %0, -1, %0" : "=v"(ln));
    const u32 l15 = ln & 15, g = ln >> 4;
    f32x4 v0 = {c[0], c[1], c[2], c[3]}, v1 = {c[4], c[5], c[6], c[7]};
    const bool p7 = kh && g == 3;
    u32 rq = (p7 ? L_EX + (2 - (tb & 2)) * 4096 : (4 - (tb & 2)) * SLOT + (4 * kh + g) * 1024) + ((tb & 1) * 16 + l15) * 16, rx = w2 * 2048 + ln * 16;
    const u32 st7 = p7 ? 1024 : 7168;
    if (A) { v0 += t6[0]; v1 += t6[1]; }
    asm volatile("" : "+v"(rq), "+v"(rx), "+v"(v0), "+v"(v1));
    BAR();
    STAMP2(7);
    if (!A) { v0 += *(LAS f32x4*)(L + rx); v1 += *(LAS f32x4*)(L + rx + 1024); }
#pragma unroll
    for (int w = 0; w < 4; ++w) { v0 += *(LAS f32x4*)(L + rq + w * st7); v1 += *(LAS f32x4*)(L + rq + w * st7 + 512); }
#pragma unroll
    for (int i = 0; i < 4; ++i) { c[i] = v0[i]; c[4 + i] = v1[i]; }
    { const u32x4 ov = {pk(c[0], c[1]), pk(c[2], c[3]), pk(c[4], c[5]), pk(c[6], c[7])}; asm volatile("global_store_dwordx4 %0, %1, %2 sc1\n\ts_nop 1" :: "v"(xo), "v"(ov), "s"(out) : "memory"); }
    if (A && b == 0 && tid == 0) __hip_atomic_store((GAS u64*)(ws + WS_E), tag, RLX_AGENT);
  }
  STAMP(21);
  };
  if (wave < 4) role(IC<1>{}); else role(IC<0>{});
}

extern "C" void kernel_launch(void* const* d_in, const int* in_sizes, int n_in, void* d_out, int out_size, void* d_ws, size_t ws_size, hipStream_t stream) {
  (void)hipFuncSetAttribute((const void*)fused_block, hipFuncAttributeMaxDynamicSharedMemorySize, LDS_BYTES);
  fused_block<<<256, 512, LDS_BYTES, stream>>>((const char*)d_in[0], (const char*)d_in[1], (const char*)d_in[2], (const char*)d_in[3], (const char*)d_in[5], (const char*)d_in[6],
                                               (const char*)d_in[7], (char*)d_out, (char*)d_ws);
}
```
